```python
import torch

def _softmax(x, axis=-1):
    return torch.softmax(x, dim=axis)

def _gelu(x):
    return torch.nn.functional.gelu(x, approximate='tanh')

def _silu(x):
    return torch.nn.functional.silu(x)

def _sigmoid(x):
    return torch.sigmoid(x)

def _layernorm(x, axis=-1, eps=1e-05):
    m = x.mean(dim=axis, keepdim=True)
    v = x.var(dim=axis, keepdim=True, unbiased=False)
    return (x - m) / torch.sqrt(v + eps)

def _scatter_add(tgt, idx, src, axis=0):
    return tgt.clone().index_add_(axis, idx, src)

def _segment_sum(src, seg, n):
    out = torch.zeros((n,) + src.shape[1:], dtype=src.dtype)
    return out.index_add_(0, seg, src)

def _pad(x, pw):
    flat = tuple((v for pair in reversed(pw) for v in pair))
    return torch.nn.functional.pad(x, flat)
kernel_name = 'adaLN_cond__ssm_conv_s4__mixer_gmlp__prompt'

def setup_inputs():

    _w = lambda a: (lambda t: t.to(torch.bfloat16) if t.is_floating_point() else t)(torch.as_tensor(a))

    def _A_setup():
        B, L, D, N = (1, 2048, 640, 128)
        A_re = -(lambda _x: torch.exp(torch.as_tensor(_x)))((lambda *s: torch.randn(*s, dtype=torch.bfloat16) * 3.2)(D, N))
        A_im = torch.pi * (lambda *s: torch.randn(*s, dtype=torch.bfloat16) * 0.1)(D, N)
        B_re = (lambda *s: torch.randn(*s, dtype=torch.bfloat16) * 0.2)(D, N)
        B_im = (lambda *s: torch.randn(*s, dtype=torch.bfloat16) * 0.2)(D, N)
        C_re = (lambda *s: torch.randn(*s, dtype=torch.bfloat16) * 0.2)(D, N)
        C_im = (lambda *s: torch.randn(*s, dtype=torch.bfloat16) * 0.2)(D, N)
        log_dt = (lambda *s: torch.randn(*s, dtype=torch.bfloat16) * 0.1)(D) * 0.5 - 4.0
        Dskip = (lambda *s: torch.randn(*s, dtype=torch.bfloat16) * 0.1)(D)
        return {'u': _w((lambda *s: torch.randn(*s, dtype=torch.bfloat16) * 0.05)(B, L, D)), 'A_re': _w(A_re), 'A_im': _w(A_im), 'B_re': _w(B_re), 'B_im': _w(B_im), 'C_re': _w(C_re), 'C_im': _w(C_im), 'log_dt': _w(log_dt), 'Dskip': _w(Dskip)}
    _A_in = _A_setup()

    def _B_setup():
        B, S, D, F = (1, 2048, 640, 1728)
        Fh = F // 2
        return {'x': _w((lambda *s: torch.randn(*s, dtype=torch.bfloat16) * 0.05)(B, S, D)), 'w_in': _w((lambda *s: torch.randn(*s, dtype=torch.bfloat16) * 0.2)(D, 2 * Fh)), 'w_sp': _w(0.01 * (lambda *s: torch.randn(*s, dtype=torch.bfloat16) * 0.2)(S, S)), 'b_sp': _w(1.0 + 0.01 * (lambda *s: torch.randn(*s, dtype=torch.bfloat16) * 0.05)(S)), 'w_out': _w((lambda *s: torch.randn(*s, dtype=torch.bfloat16) * 0.05)(Fh, D))}
    _B_in = _B_setup()
    return {'A_u': _A_in['u'], 'A_A_re': _A_in['A_re'], 'A_A_im': _A_in['A_im'], 'A_B_re': _A_in['B_re'], 'A_B_im': _A_in['B_im'], 'A_C_re': _A_in['C_re'], 'A_C_im': _A_in['C_im'], 'A_log_dt': _A_in['log_dt'], 'A_Dskip': _A_in['Dskip'], 'B_x': _B_in['x'], 'B_w_in': _B_in['w_in'], 'B_w_sp': _B_in['w_sp'], 'B_b_sp': _B_in['b_sp'], 'B_w_out': _B_in['w_out'], 'c': _w((lambda *s: torch.randn(*s, dtype=torch.bfloat16) * 0.1)(1, 640) * 8.0), 'w_mod': _w((lambda *s: torch.randn(*s, dtype=torch.bfloat16) * 0.1)(640, 6 * 640) * 640 ** (-0.5) * 8.0)}

def reference(A_u, A_A_re, A_A_im, A_B_re, A_B_im, A_C_re, A_C_im, A_log_dt, A_Dskip, B_x, B_w_in, B_w_sp, B_b_sp, B_w_out, c, w_mod):

    def _A_ref(u, A_re, A_im, B_re, B_im, C_re, C_im, log_dt, Dskip):
        Bsz, L, D = u.shape
        dt = torch.exp(log_dt)[:, None]
        A = A_re + 1j * A_im
        Bc = B_re + 1j * B_im
        Cc = C_re + 1j * C_im
        logdA = dt * A
        dB = dt * Bc
        l = (lambda x, dt: x.to(dt))(torch.arange(L), A_re.dtype)
        P = torch.exp(logdA[:, :, None] * l[None, None, :])
        K = torch.einsum('dn,dnl,dn->dl', Cc, P, dB).real
        L2 = 2 * L
        Kp = _pad(K, ((0, 0), (0, L)))
        up = (lambda x, *d: x.permute(*d))(u, 0, 2, 1)
        up = _pad(up, ((0, 0), (0, 0), (0, L)))
        Kf = (lambda x, axes=(-2, -1): torch.fft.rfft2(x.float(), dim=axes))(Kp[:, None, :], axes=(-2, -1))
        Uf = (lambda x, axes=(-2, -1): torch.fft.rfft2(x.float(), dim=axes))(up[:, :, None, :], axes=(-2, -1))
        Yf = Uf * Kf[None, :, :, :]
        y = (lambda x, s, axes=(-2, -1): torch.fft.irfft2(x, s=s, dim=axes).to(torch.bfloat16))(Yf, (1, L2), axes=(-2, -1))[:, :, 0, :L]
        y = (lambda x, *d: x.permute(*d))(y, 0, 2, 1) + u * Dskip[None, None, :]
        return y

    def _B_ref(x, w_in, w_sp, b_sp, w_out):
        B, S, D = x.shape
        F = w_in.shape[-1]
        Fh = F // 2
        z = _gelu(torch.einsum('bsd,df->bsf', x, w_in))
        z1 = z[..., :Fh].clone()
        z2 = _layernorm(z[..., Fh:], axis=-1)
        g = torch.einsum('ts,bsf->btf', w_sp, z2) + b_sp[None, :, None]
        return torch.einsum('bsf,fd->bsd', z1 * g, w_out)
    x = A_u
    mod = (_silu(c) @ w_mod).reshape(c.shape[0], 6, 1, -1)
    sa, ca, ga, sb, cb, gb = (mod[:, 0].clone(), mod[:, 1].clone(), mod[:, 2].clone(), mod[:, 3].clone(), mod[:, 4].clone(), mod[:, 5].clone())
    h = _layernorm(x, axis=-1) * (1 + ca) + sa
    x = x + ga * _A_ref(h, A_A_re, A_A_im, A_B_re, A_B_im, A_C_re, A_C_im, A_log_dt, A_Dskip)
    h = _layernorm(x, axis=-1) * (1 + cb) + sb
    x = x + gb * _B_ref(h, B_w_in, B_w_sp, B_b_sp, B_w_out)
    return x
```

```cpp
#include <hip/hip_runtime.h>
#include <hip/hip_bf16.h>
#include <stdint.h>

#define GAS __attribute__((address_space(1)))
#define LAS __attribute__((address_space(3)))
typedef unsigned v4u __attribute__((ext_vector_type(4)));
typedef unsigned v2u __attribute__((ext_vector_type(2)));
typedef float f32x2 __attribute__((ext_vector_type(2)));
typedef float f32x4 __attribute__((ext_vector_type(4)));
typedef float f32x16 __attribute__((ext_vector_type(16)));
typedef __bf16 bf16x2 __attribute__((ext_vector_type(2)));
typedef __bf16 bf16x8 __attribute__((ext_vector_type(8)));
typedef _Float16 f16x2 __attribute__((ext_vector_type(2)));
typedef _Float16 f16x8 __attribute__((ext_vector_type(8)));
typedef short s16x4 __attribute__((ext_vector_type(4)));
typedef unsigned short u16;

constexpr int L = 2048, D = 640, NS = 128, F = 1728, FH = 864;

constexpr size_t WS_MOD = 0;
constexpr size_t WS_ST1 = 16384;
constexpr size_t WS_ST3 = WS_ST1 + 16384;
constexpr size_t WS_U = WS_ST3 + 32768;
constexpr size_t WS_XT = WS_U + 16384;
constexpr size_t WS_K = WS_XT + (size_t)D * L * 2;
constexpr size_t WS_X2T = WS_K + (size_t)D * L * 2;
constexpr size_t WS_X2R = WS_X2T + (size_t)D * L * 2;
constexpr size_t WS_WIN = WS_X2R + (size_t)D * L * 2;
constexpr size_t WS_WOUT = WS_WIN + (size_t)D * F * 2;
constexpr size_t WS_Z1 = WS_WOUT + (size_t)FH * D * 2;
constexpr size_t WS_Z2 = WS_Z1 + (size_t)L * FH * 2;
constexpr size_t WS_V = WS_Z2 + (size_t)L * FH * 2;
constexpr size_t WS_END = WS_V + (size_t)L * FH * 2;

__device__ __forceinline__ float bflo(unsigned u) { return __uint_as_float(u << 16); }
__device__ __forceinline__ float bfhi(unsigned u) { return __uint_as_float(u & 0xffff0000u); }
__device__ __forceinline__ float bf2f(u16 v) { return __uint_as_float((unsigned)v << 16); }
__device__ __forceinline__ unsigned pkbf(float a, float b) {
  f32x2 v = {a, b};
  return __builtin_bit_cast(unsigned, __builtin_convertvector(v, bf16x2));
}
__device__ __forceinline__ unsigned pkh(float a, float b) {
  f32x2 v = {a, b};
  return __builtin_bit_cast(unsigned, __builtin_convertvector(v, f16x2));
}
__device__ __forceinline__ float bfr(float a) { return bflo(pkbf(a, a)); }
__device__ __forceinline__ v2u trread(LAS const char* p) {
  return __builtin_bit_cast(v2u, __builtin_amdgcn_ds_read_tr16_b64_v4i16((LAS s16x4*)p));
}
__device__ __forceinline__ f32x4 mfma16(v4u a, v4u b, f32x4 c) {
  return __builtin_amdgcn_mfma_f32_16x16x32_bf16(__builtin_bit_cast(bf16x8, a), __builtin_bit_cast(bf16x8, b), c, 0, 0, 0);
}
__device__ __forceinline__ f32x16 mfma32(v4u a, v4u b, f32x16 c) {
  return __builtin_amdgcn_mfma_f32_32x32x16_bf16(__builtin_bit_cast(bf16x8, a), __builtin_bit_cast(bf16x8, b), c, 0, 0, 0);
}
__device__ __forceinline__ f32x16 mfma32h(v4u a, v4u b, f32x16 c) {
  return __builtin_amdgcn_mfma_f32_32x32x16_f16(__builtin_bit_cast(f16x8, a), __builtin_bit_cast(f16x8, b), c, 0, 0, 0);
}
#define GLDS16(src, dst) __builtin_amdgcn_global_load_lds((const GAS void*)(src), (LAS void*)(dst), 16, 0, 0)
#define SCHED_BAR() __builtin_amdgcn_sched_barrier(0)

__device__ __forceinline__ void ln_scalars(float mean, float var, float& m_b, float& rinv) {
  m_b = bfr(mean);
  float s = bfr(__builtin_sqrtf(bfr(bfr(var) + 1e-5f)));
  rinv = 1.0f / s;
}

struct cplx { float r, i; };
__device__ __forceinline__ cplx cmul(cplx a, cplx b) { return {a.r * b.r - a.i * b.i, a.r * b.i + a.i * b.r}; }

constexpr int K1_KG = 320, K1_LN = 64, K1_MOD = 60, K1_WO = 27;
constexpr int K1_GRID = K1_KG + K1_LN + K1_MOD + K1_WO;
constexpr int TP = 1312;
constexpr int K1_LDS = 4 * 16384 + 4 * 2576;

__global__ __launch_bounds__(256) void k1_prep(const u16* __restrict__ x, const u16* __restrict__ Are, const u16* __restrict__ Aim,
                                               const u16* __restrict__ Bre, const u16* __restrict__ Bim, const u16* __restrict__ Cre,
                                               const u16* __restrict__ Cim, const u16* __restrict__ logdt, const u16* __restrict__ wout,
                                               const u16* __restrict__ cvec, const u16* __restrict__ wmod, char* __restrict__ ws) {
  __shared__ __attribute__((aligned(16))) char smem[K1_LDS];
  LAS char* lds = (LAS char*)smem;
  const int tid = threadIdx.x, lane = tid & 63, wave = __builtin_amdgcn_readfirstlane(tid >> 6);
  int b = blockIdx.x;
  if (b < K1_KG) {
    const int d = 2 * b + (wave >> 1), n = (wave & 1) * 64 + lane, o = d * NS + n;
    const float are = bf2f(Are[o]), aim = bf2f(Aim[o]), bre = bf2f(Bre[o]), bim = bf2f(Bim[o]), cre = bf2f(Cre[o]), cim = bf2f(Cim[o]);
    const float dt = bfr(expf(bf2f(logdt[d])));
    const float ar2 = dt * are * 1.4426950408889634f, ai2 = dt * aim * 0.15915494309189535f;
    cplx W = cmul(cplx{cre, cim}, cplx{dt * bre, dt * bim});
    float mx = fmaxf(fabsf(W.r), fabsf(W.i));
#pragma unroll
    for (int s = 1; s < 64; s <<= 1) mx = fmaxf(mx, __shfl_xor(mx, s));
    int ex = (int)((__float_as_uint(mx) >> 23) & 0xff);
    ex = ex < 20 ? 20 : (ex > 240 ? 240 : ex);
    const float sc = __uint_as_float((unsigned)(267 - ex) << 23);
    const float isc = __uint_as_float((unsigned)(ex - 13) << 23);
    W.r *= sc; W.i *= sc;
    auto E = [&](float xx) {
      float mag = __builtin_amdgcn_exp2f(ar2 * xx), ph = __builtin_amdgcn_fractf(ai2 * xx);
      return cplx{mag * __builtin_amdgcn_cosf(ph), mag * __builtin_amdgcn_sinf(ph)};
    };
    LAS char* Qi = lds + wave * 16384;
    LAS char* Hi = Qi + 8192;
    const int wbase = lane * 128, sw = lane & 7;
    {
      cplx e8 = E(8.f), e64 = E(64.f), e128 = E(128.f), e192 = E(192.f);
      cplx q[8];
      q[0] = {1.f, 0.f}; q[1] = e8; q[2] = cmul(e8, e8); q[3] = cmul(q[2], e8); q[4] = cmul(q[2], q[2]);
      q[5] = cmul(q[4], e8); q[6] = cmul(q[4], q[2]); q[7] = cmul(q[4], q[3]);
#pragma unroll
      for (int g = 0; g < 4; ++g) {
        cplx v[8];
#pragma unroll
        for (int j = 0; j < 8; ++j) v[j] = g == 0 ? q[j] : cmul(q[j], g == 1 ? e64 : (g == 2 ? e128 : e192));
        v4u re = {pkh(v[0].r, v[1].r), pkh(v[2].r, v[3].r), pkh(v[4].r, v[5].r), pkh(v[6].r, v[7].r)};
        v4u im = {pkh(v[0].i, v[1].i), pkh(v[2].i, v[3].i), pkh(v[4].i, v[5].i), pkh(v[6].i, v[7].i)};
        *(LAS v4u*)(Qi + wbase + ((g ^ sw) << 4)) = re;
        *(LAS v4u*)(Qi + wbase + (((4 + g) ^ sw) << 4)) = im;
      }
    }
    {
      cplx e1 = E(1.f), e2 = cmul(e1, e1), e4 = cmul(e2, e2);
      cplx h[24];
      h[0] = W; h[1] = cmul(W, e1); h[2] = cmul(W, e2); h[3] = cmul(h[2], e1);
      h[4] = cmul(W, e4); h[5] = cmul(h[4], e1); h[6] = cmul(h[4], e2); h[7] = cmul(h[6], e1);
      cplx g1 = E(256.f), g2 = E(512.f), g3 = E(768.f), g4 = E(1024.f);
      h[8] = cmul(h[0], g1); h[9] = cmul(h[2], g1); h[10] = cmul(h[4], g1); h[11] = cmul(h[6], g1);
      h[12] = cmul(h[0], g2); h[13] = cmul(h[4], g2); h[14] = cmul(h[0], g3); h[15] = cmul(h[4], g3);
      h[16] = cmul(h[0], g4); h[17] = cmul(h[8], g4); h[18] = cmul(h[12], g4); h[19] = cmul(h[14], g4);
      h[20] = cmul(h[16], g4); h[21] = {0.f, 0.f}; h[22] = {0.f, 0.f}; h[23] = {0.f, 0.f};
#pragma unroll
      for (int g = 0; g < 3; ++g) {
        const cplx* v = h + 8 * g;
        v4u re = {pkh(v[0].r, v[1].r), pkh(v[2].r, v[3].r), pkh(v[4].r, v[5].r), pkh(v[6].r, v[7].r)};
        v4u im = {pkh(-v[0].i, -v[1].i), pkh(-v[2].i, -v[3].i), pkh(-v[4].i, -v[5].i), pkh(-v[6].i, -v[7].i)};
        *(LAS v4u*)(Hi + wbase + ((g ^ sw) << 4)) = re;
        *(LAS v4u*)(Hi + wbase + (((4 + g) ^ sw) << 4)) = im;
      }
      v4u z = {0u, 0u, 0u, 0u};
      *(LAS v4u*)(Hi + wbase + ((3 ^ sw) << 4)) = z;
      *(LAS v4u*)(Hi + wbase + ((7 ^ sw) << 4)) = z;
    }
    __syncthreads();
    f32x16 acc = {};
    {
      const int hh = lane >> 5, blk = (lane >> 4) & 1, qq = (lane & 15) >> 2, p = lane & 3;
#pragma unroll
      for (int ks = 0; ks < 8; ++ks) {
        v2u a[2], bb[2];
#pragma unroll
        for (int t = 0; t < 2; ++t) {
          const int k = 16 * ks + 8 * hh + 4 * t + qq, mode = k >> 1, c = k & 1;
          const int off = mode * 128 + (((c * 4 + 2 * blk + (p >> 1)) ^ (mode & 7)) << 4) + (p & 1) * 8;
          a[t] = trread(Hi + off);
          bb[t] = trread(Qi + off);
        }
        acc = mfma32h(v4u{a[0].x, a[0].y, a[1].x, a[1].y}, v4u{bb[0].x, bb[0].y, bb[1].x, bb[1].y}, acc);
      }
    }
    LAS float* T = (LAS float*)(lds + 65536 + wave * 2576);
    {
      const int q = lane & 31, hi = lane >> 5;
#pragma unroll
      for (int r = 0; r < 4; ++r) T[(r + 4 * hi) + 8 * q] = acc[r] * isc;
      if (hi == 0) {
#pragma unroll
        for (int r = 0; r < 4; ++r) T[256 + r + 4 * q] = acc[4 + r] * isc;
#pragma unroll
        for (int r = 0; r < 4; ++r) T[512 + 32 * r + q] = acc[8 + r] * isc;
      } else {
#pragma unroll
        for (int r = 0; r < 4; ++r) T[384 + 64 * (r >> 1) + (r & 1) + 2 * q] = acc[4 + r] * isc;
        if (q == 0) T[640] = acc[8] * isc;
      }
    }
    __syncthreads();
    {
      const LAS float* T0 = (const LAS float*)(lds + 65536 + (wave & 2) * 2576);
      const LAS float* T1 = T0 + 644;
      u16* Kd = (u16*)(ws + WS_K) + (size_t)d * L;
      const int th = tid & 127;
#pragma unroll
      for (int pc = 0; pc < 2; ++pc) {
        const int l0 = (th + 128 * pc) * 8;
        float kv[8];
#pragma unroll
        for (int j = 0; j < 8; ++j) {
          const int l = l0 + j;
          const int idx = l < 128 ? l : (int)(pkbf((float)l, 0.f) & 0xffffu) - 0x4300 + 128;
          kv[j] = T0[idx] + T1[idx];
        }
        *(v4u*)(Kd + l0) = v4u{pkbf(kv[0], kv[1]), pkbf(kv[2], kv[3]), pkbf(kv[4], kv[5]), pkbf(kv[6], kv[7])};
      }
    }
    return;
  }
  b -= K1_KG;
  if (b < K1_LN + K1_WO) {
    const bool isx = b < K1_LN;
    const int slab = isx ? b : b - K1_LN;
    const u16* src = (isx ? x : wout) + (size_t)slab * 32 * D;
    const int r = tid >> 3, s = tid & 7;
    v4u v[10];
#pragma unroll
    for (int i = 0; i < 10; ++i) v[i] = *(const v4u*)(src + r * D + (s + 8 * i) * 8);
#pragma unroll
    for (int i = 0; i < 10; ++i) *(LAS v4u*)(lds + r * TP + (s + 8 * i) * 16) = v[i];
    if (isx) {
      float sum = 0.f;
#pragma unroll
      for (int i = 0; i < 10; ++i)
#pragma unroll
        for (int j = 0; j < 4; ++j) sum += bflo(v[i][j]) + bfhi(v[i][j]);
      sum += __shfl_xor(sum, 1); sum += __shfl_xor(sum, 2); sum += __shfl_xor(sum, 4);
      const float mean = sum * (1.0f / D);
      float sq = 0.f;
#pragma unroll
      for (int i = 0; i < 10; ++i)
#pragma unroll
        for (int j = 0; j < 4; ++j) {
          float a = bflo(v[i][j]) - mean, c = bfhi(v[i][j]) - mean;
          sq += a * a + c * c;
        }
      sq += __shfl_xor(sq, 1); sq += __shfl_xor(sq, 2); sq += __shfl_xor(sq, 4);
      float mb, rinv;
      ln_scalars(mean, sq * (1.0f / D), mb, rinv);
      if (s == 0) ((f32x2*)(ws + WS_ST1))[slab * 32 + r] = f32x2{mb, rinv};
      if (tid < 32) ((v4u*)(ws + WS_ST3))[slab * 32 + tid] = v4u{0u, 0u, 0u, 0u};
    }
    __syncthreads();
    const int q = lane >> 4, rr = (lane & 15) >> 2, p = lane & 3, i16 = lane & 15;
    const int a0 = (8 * q + rr) * TP + p * 8;
#pragma unroll
    for (int j = 0; j < 10; ++j) {
      const int sub = wave + 4 * j;
      v2u lo = trread(lds + a0 + sub * 32), hi = trread(lds + a0 + 4 * TP + sub * 32);
      v4u o = {lo.x, lo.y, hi.x, hi.y};
      if (isx) *(v4u*)((u16*)(ws + WS_XT) + (size_t)(16 * sub + i16) * L + slab * 32 + 8 * q) = o;
      else *(v4u*)((u16*)(ws + WS_WOUT) + ((size_t)(sub * 27 + slab) * 64 + lane) * 8) = o;
    }
    return;
  }
  b -= K1_LN + K1_WO;
  {
    const int c0 = 64 * b, seg = tid & 7, rg = tid >> 3;
    v4u w[20];
#pragma unroll
    for (int i = 0; i < 20; ++i) w[i] = *(const v4u*)(wmod + (size_t)(rg + 32 * i) * (6 * D) + c0 + seg * 8);
    LAS float* sc = (LAS float*)lds;
    for (int k = tid; k < D; k += 256) {
      float cv = bf2f(cvec[k]);
      sc[k] = bfr(cv / (1.0f + expf(-cv)));
    }
    __syncthreads();
    float acc[8] = {};
#pragma unroll
    for (int i = 0; i < 20; ++i) {
      const float s = sc[rg + 32 * i];
#pragma unroll
      for (int j = 0; j < 4; ++j) { acc[2 * j] += s * bflo(w[i][j]); acc[2 * j + 1] += s * bfhi(w[i][j]); }
    }
#pragma unroll
    for (int j = 0; j < 8; ++j) { acc[j] += __shfl_xor(acc[j], 8); acc[j] += __shfl_xor(acc[j], 16); acc[j] += __shfl_xor(acc[j], 32); }
    LAS float* red = (LAS float*)(lds + 4096);
    if (lane < 8) {
#pragma unroll
      for (int j = 0; j < 8; ++j) red[wave * 64 + lane * 8 + j] = acc[j];
    }
    __syncthreads();
    if (tid < 64) ((float*)(ws + WS_MOD))[c0 + tid] = bfr((red[tid] + red[64 + tid]) + (red[128 + tid] + red[192 + tid]));
  }
}

constexpr int K2_WP = 27, K2_GRID = K2_WP + D;
constexpr int K2_KOFF = 0, K2_UOFF = 4224, K2_UP = 1536, K2_COFF = K2_UOFF + 4 * K2_UP;
constexpr int K2_LDS = K2_COFF + 4 * 64 * 144;

__global__ __launch_bounds__(256) void k2_conv(const u16* __restrict__ dskip, const u16* __restrict__ win, char* __restrict__ ws) {
  __shared__ __attribute__((aligned(16))) char smem[K2_LDS];
  LAS char* lds = (LAS char*)smem;
  const int tid = threadIdx.x, lane = tid & 63, wave = __builtin_amdgcn_readfirstlane(tid >> 6);
  const float* mod = (const float*)(ws + WS_MOD);
  if (blockIdx.x < K2_WP) {
    const int c0 = 64 * blockIdx.x, seg = tid & 7, rg = tid >> 3;
    v4u w[20];
#pragma unroll
    for (int i = 0; i < 20; ++i) w[i] = *(const v4u*)(win + (size_t)(rg + 32 * i) * F + c0 + seg * 8);
    float u1[8] = {}, u2[8] = {};
#pragma unroll
    for (int i = 0; i < 20; ++i) {
      const float c1 = bfr(1.0f + mod[4 * D + rg + 32 * i]), sb = mod[3 * D + rg + 32 * i];
#pragma unroll
      for (int j = 0; j < 4; ++j) {
        const float a = bflo(w[i][j]), c = bfhi(w[i][j]);
        const unsigned pk = pkbf(a * c1, c * c1);
        u2[2 * j] += sb * a; u2[2 * j + 1] += sb * c;
        u1[2 * j] += bflo(pk); u1[2 * j + 1] += bfhi(pk);
        w[i][j] = pk;
      }
    }
    const int q = lane >> 4, rr = (lane & 15) >> 2, p = lane & 3;
    u16* WT = (u16*)(ws + WS_WIN);
    constexpr int WPITCH = 144;
#pragma unroll
    for (int pass = 0; pass < 4; ++pass) {
      if (pass) __syncthreads();
#pragma unroll
      for (int i = 0; i < 5; ++i) *(LAS v4u*)(lds + (rg + 32 * i) * WPITCH + seg * 16) = w[pass * 5 + i];
      __syncthreads();
      const int colb = (32 * (wave >> 1) + 8 * p + 4 * (wave & 1)) * 2;
#pragma unroll
      for (int i = 0; i < 5; ++i) {
        const int a0 = (32 * i + 8 * q + rr) * WPITCH + colb;
        v2u lo = trread(lds + a0), hi = trread(lds + a0 + 4 * WPITCH);
        const int sub = 4 * blockIdx.x + wave, ks = pass * 5 + i;
        *(v4u*)(WT + ((size_t)(sub * 20 + ks) * 64 + lane) * 8) = v4u{lo.x, lo.y, hi.x, hi.y};
      }
    }
#pragma unroll
    for (int j = 0; j < 8; ++j) {
      u1[j] += __shfl_xor(u1[j], 8); u1[j] += __shfl_xor(u1[j], 16); u1[j] += __shfl_xor(u1[j], 32);
      u2[j] += __shfl_xor(u2[j], 8); u2[j] += __shfl_xor(u2[j], 16); u2[j] += __shfl_xor(u2[j], 32);
    }
    LAS float* red = (LAS float*)(lds + 24576);
    if (lane < 8) {
#pragma unroll
      for (int j = 0; j < 8; ++j) { red[wave * 128 + lane * 8 + j] = u1[j]; red[wave * 128 + 64 + lane * 8 + j] = u2[j]; }
    }
    __syncthreads();
    if (tid < 128) {
      const int which = tid >> 6, cl = tid & 63;
      const float s = (red[tid] + red[128 + tid]) + (red[256 + tid] + red[384 + tid]);
      const int sub = 2 * (cl >> 5) + ((cl >> 2) & 1), i = ((cl >> 3) & 3) * 4 + (cl & 3);
      ((float*)(ws + WS_U))[which * F + (4 * blockIdx.x + sub) * 16 + i] = s;
    }
    return;
  }
  const int d = blockIdx.x - K2_WP;
  const v4u kv = *(const v4u*)((const u16*)(ws + WS_K) + (size_t)d * L + tid * 8);
  const v4u xv = *(const v4u*)((const u16*)(ws + WS_XT) + (size_t)d * L + tid * 8);
  f32x4 st[4];
#pragma unroll
  for (int j = 0; j < 4; ++j) st[j] = ((const f32x4*)(ws + WS_ST1))[tid * 4 + j];
  const float sa = mod[d], c1a = bfr(1.0f + mod[D + d]), ga = mod[2 * D + d], dsk = bf2f(dskip[d]);
  if (tid < 4) *(LAS v4u*)(lds + K2_KOFF + tid * 16) = v4u{0u, 0u, 0u, 0u};
  if (tid < 128) *(LAS v4u*)(lds + K2_UOFF + (tid >> 5) * K2_UP + (tid & 31) * 16) = v4u{0u, 0u, 0u, 0u};
  *(LAS v4u*)(lds + K2_KOFF + 64 + tid * 16) = kv;
  float xf[8], h[8];
#pragma unroll
  for (int j = 0; j < 4; ++j) { xf[2 * j] = bflo(xv[j]); xf[2 * j + 1] = bfhi(xv[j]); }
#pragma unroll
  for (int j = 0; j < 8; ++j) {
    const float m = st[j >> 1][(j & 1) * 2], ri = st[j >> 1][(j & 1) * 2 + 1];
    const float xn = bfr(bfr(xf[j] - m) * ri);
    h[j] = bfr(bfr(xn * c1a) + sa);
  }
#pragma unroll
  for (int e = 0; e < 4; ++e) *(LAS unsigned*)(lds + K2_UOFF + e * K2_UP + 512 + tid * 4) = pkbf(h[e], h[e + 4]);
  __syncthreads();
  f32x16 y0 = {}, y1 = {};
  {
    const int hh = lane >> 5, blk = (lane >> 4) & 1, qq = (lane & 15) >> 2, p = lane & 3, col = lane & 31;
    LAS const char* ab = lds + K2_KOFF + (32 + 16 * blk + 4 * p - 4 * qq + 32 * hh) * 2 - 32;
    LAS const char* bb = lds + K2_UOFF + wave * K2_UP + (256 + 8 * (col - hh) - 240) * 2;
#pragma unroll
    for (int ks = 0; ks < 16; ++ks) {
      v2u a0l = trread(ab + ks * 128 + 32), a0h = trread(ab + ks * 128);
      v2u a1l = trread(ab + (ks + 16) * 128 + 32), a1h = trread(ab + (ks + 16) * 128);
      v4u b0 = *(LAS const v4u*)(bb + (15 - ks) * 32);
      v4u b1 = *(LAS const v4u*)(bb + (31 - ks) * 32);
      v4u a0 = {a0l.x, a0l.y, a0h.x, a0h.y}, a1 = {a1l.x, a1l.y, a1h.x, a1h.y};
      y0 = mfma32(a0, b0, y0);
      y1 = mfma32(a0, b1, y1);
      y1 = mfma32(a1, b0, y1);
    }
    LAS char* cb = lds + K2_COFF + (wave * 64 + col) * 144 + hh * 16;
#pragma unroll
    for (int g = 0; g < 4; ++g) {
      *(LAS f32x4*)(cb + g * 32) = f32x4{y0[4 * g], y0[4 * g + 1], y0[4 * g + 2], y0[4 * g + 3]};
      *(LAS f32x4*)(cb + 32 * 144 + g * 32) = f32x4{y1[4 * g], y1[4 * g + 1], y1[4 * g + 2], y1[4 * g + 3]};
    }
  }
  __syncthreads();
  float y[8];
  {
    const int c1 = 2 * tid, c0 = tid ? c1 - 1 : 0;
    const int o0 = (c0 >> 3) * 144 + (c0 & 7) * 16, o1 = (c1 >> 3) * 144 + (c1 & 7) * 16;
    LAS const char* cb = lds + K2_COFF;
#pragma unroll
    for (int e = 0; e < 4; ++e) {
      f32x4 w1 = *(LAS const f32x4*)(cb + e * 64 * 144 + o1), w2 = *(LAS const f32x4*)(cb + e * 64 * 144 + o1 + 16);
      float w[12];
#pragma unroll
      for (int j = 0; j < 4; ++j) { w[4 + j] = w1[j]; w[8 + j] = w2[j]; }
      if (e) {
        f32x4 w0 = *(LAS const f32x4*)(cb + e * 64 * 144 + o0);
#pragma unroll
        for (int j = 0; j < 4; ++j) w[j] = tid ? w0[j] : 0.f;
      }
#pragma unroll
      for (int j = 0; j < 8; ++j) y[j] = e ? y[j] + w[4 + j - e] : w[4 + j];
    }
  }
  float o[8];
#pragma unroll
  for (int j = 0; j < 8; ++j) {
    const float yy = bfr(bfr(y[j]) + bfr(h[j] * dsk));
    o[j] = xf[j] + bfr(ga * yy);
  }
  u16* dst = (u16*)(ws + WS_X2T) + (size_t)d * L + (tid >> 2) * 32 + (tid & 3) * 4;
  *(v2u*)dst = v2u{pkbf(o[0], o[1]), pkbf(o[2], o[3])};
  *(v2u*)(dst + 16) = v2u{pkbf(o[4], o[5]), pkbf(o[6], o[7])};
}

constexpr int K3_STAGE = 30720, K3_NSLOT = 5, K3_TAB = K3_STAGE * K3_NSLOT, K3_LDS = K3_TAB + 2048;

__device__ __forceinline__ float gelu_f(float x) {
  const float p = __builtin_fmaf(x * x, -0.044715f * 2.302208198f, -2.302208198f);
  const float e = __builtin_amdgcn_exp2f(x * p);
  return x * __builtin_amdgcn_rcpf(1.0f + e);
}

template <int NSUB, bool STATS, bool Z2>
__device__ __forceinline__ void k3_body(LAS char* lds, char* ws, int mt, int nt, int sub0, int cnt, int wave, int lane, int tid) {
  const int G = wave & 3, n0 = STATS ? 4 : 0;
  const bool four = wave + 8 < 2 * cnt;
  const char* asrc;
  {
    const int row = 8 * wave + (lane >> 4), pos = lane & 15;
    const int f0 = (row & 3) | (((row >> 3) & 1) << 2);
    asrc = ws + WS_X2T + (size_t)row * (L * 2) + mt * 256 + ((pos ^ (f0 << 1)) << 4);
  }
  const char* bsrc0 = ws + WS_WIN + ((size_t)((sub0 + (wave >> 1)) * 20 + (wave & 1)) * 64 + lane) * 16;
  const char* bsrc1 = bsrc0 + (size_t)4 * 20 * 1024;
  auto issue = [&](int s) {
    LAS char* slot = lds + (s % K3_NSLOT) * K3_STAGE;
    const char* a = asrc + (size_t)s * 64 * (L * 2);
    GLDS16(a, slot + wave * 2048);
    GLDS16(a + 4 * (L * 2), slot + wave * 2048 + 1024);
    GLDS16(bsrc0 + s * 2048, slot + 16384 + wave * 1024);
    if (four) GLDS16(bsrc1 + s * 2048, slot + 16384 + (wave + 8) * 1024);
  };
#pragma unroll
  for (int s = 0; s < 4; ++s) issue(s);
  const int q = lane >> 4, rr = (lane & 15) >> 2, p = lane & 3, i16 = lane & 15;
  int aoff[2];
#pragma unroll
  for (int ab = 0; ab < 2; ++ab) {
    const int f0 = rr | ((q & 1) << 2);
    aoff[ab] = (8 * q + rr) * 256 + (((4 * G + 2 * ab + (p >> 1)) ^ (f0 << 1)) << 4) + (p & 1) * 8;
  }
  const int boff = 16384 + n0 * 2048 + lane * 16;
  f32x4 acc[NSUB][2];
#pragma unroll
  for (int n = 0; n < NSUB; ++n) { acc[n][0] = f32x4{0.f, 0.f, 0.f, 0.f}; acc[n][1] = f32x4{0.f, 0.f, 0.f, 0.f}; }
  float ssum[2] = {0.f, 0.f}, ssq[2] = {0.f, 0.f};
  v4u keep[2][2];
#pragma unroll
  for (int a = 0; a < 2; ++a) { keep[a][0] = v4u{0u, 0u, 0u, 0u}; keep[a][1] = v4u{0u, 0u, 0u, 0u}; }
#pragma unroll
  for (int s = 0; s < 10; ++s) {
    {
      const int ahead = (9 - s) < 3 ? (9 - s) : 3;
      if (four) {
        if (ahead == 3) asm volatile("s_waitcnt vmcnt(12)" ::: "memory");
        else if (ahead == 2) asm volatile("s_waitcnt vmcnt(8)" ::: "memory");
        else if (ahead == 1) asm volatile("s_waitcnt vmcnt(4)" ::: "memory");
        else asm volatile("s_waitcnt vmcnt(0)" ::: "memory");
      } else {
        if (ahead == 3) asm volatile("s_waitcnt vmcnt(9)" ::: "memory");
        else if (ahead == 2) asm volatile("s_waitcnt vmcnt(6)" ::: "memory");
        else if (ahead == 1) asm volatile("s_waitcnt vmcnt(3)" ::: "memory");
        else asm volatile("s_waitcnt vmcnt(0)" ::: "memory");
      }
    }
    asm volatile("s_waitcnt lgkmcnt(0)" ::: "memory");
    __builtin_amdgcn_s_barrier();
    asm volatile("" ::: "memory");
    if (s + 4 < 10) issue(s + 4);
    LAS const char* slot = lds + (s % K3_NSLOT) * K3_STAGE;
#pragma unroll
    for (int kk = 0; kk < 2; ++kk) {
      v4u af[2], bf[NSUB];
#pragma unroll
      for (int ab = 0; ab < 2; ++ab) {
        v2u lo = trread(slot + aoff[ab] + kk * 8192), hi = trread(slot + aoff[ab] + kk * 8192 + 1024);
        af[ab] = v4u{lo.x, lo.y, hi.x, hi.y};
      }
#pragma unroll
      for (int n = 0; n < NSUB; ++n) bf[n] = *(LAS const v4u*)(slot + boff + n * 2048 + kk * 1024);
#pragma unroll
      for (int n = 0; n < NSUB; ++n)
#pragma unroll
        for (int ab = 0; ab < 2; ++ab) acc[n][ab] = Z2 ? mfma16(af[ab], bf[n], acc[n][ab]) : mfma16(bf[n], af[ab], acc[n][ab]);
      if (STATS) {
#pragma unroll
        for (int ab = 0; ab < 2; ++ab)
#pragma unroll
          for (int j = 0; j < 4; ++j) {
            const float a = bflo(af[ab][j]), c = bfhi(af[ab][j]);
            ssum[ab] += a + c;
            ssq[ab] = __builtin_fmaf(a, a, __builtin_fmaf(c, c, ssq[ab]));
          }
      } else {
        const int ks = 2 * s + kk;
        if ((ks & 15) == nt) { keep[ks >> 4][0] = af[0]; keep[ks >> 4][1] = af[1]; }
      }
    }
  }
  LAS f32x2* tab = (LAS f32x2*)(lds + K3_TAB);
  if (STATS) {
#pragma unroll
    for (int ab = 0; ab < 2; ++ab) {
      float s1 = ssum[ab], s2 = ssq[ab];
      s1 += __shfl_xor(s1, 16); s1 += __shfl_xor(s1, 32);
      s2 += __shfl_xor(s2, 16); s2 += __shfl_xor(s2, 32);
      const float mean = s1 * (1.0f / D);
      const float var = fmaxf(s2 * (1.0f / D) - mean * mean, 0.f);
      float mb, rinv;
      ln_scalars(mean, var, mb, rinv);
      if (lane < 16) tab[(G * 2 + ab) * 16 + lane] = f32x2{rinv, rinv * mb};
    }
  } else {
    u16* x2r = (u16*)(ws + WS_X2R);
#pragma unroll
    for (int ab = 0; ab < 2; ++ab) {
      const int t = mt * 128 + 32 * G + 8 * (i16 >> 2) + 4 * ab + (i16 & 3);
      *(v4u*)(x2r + (size_t)t * D + nt * 32 + 8 * q) = keep[0][ab];
      if (nt < 4) *(v4u*)(x2r + (size_t)t * D + (nt + 16) * 32 + 8 * q) = keep[1][ab];
    }
  }
  __syncthreads();
  const float* U = (const float*)(ws + WS_U);
  if (Z2) {
    float rs[8], rm[8], s1[8], s2[8];
#pragma unroll
    for (int ab = 0; ab < 2; ++ab)
#pragma unroll
      for (int j = 0; j < 4; ++j) {
        f32x2 v = tab[(G * 2 + ab) * 16 + 4 * q + j];
        rs[ab * 4 + j] = v.x; rm[ab * 4 + j] = v.y; s1[ab * 4 + j] = 0.f; s2[ab * 4 + j] = 0.f;
      }
#pragma unroll
    for (int n = 0; n < NSUB; ++n) {
      const int sub = sub0 + n0 + n;
      const float u1 = U[sub * 16 + i16], u2 = U[F + sub * 16 + i16];
      float z[8];
#pragma unroll
      for (int ab = 0; ab < 2; ++ab)
#pragma unroll
        for (int j = 0; j < 4; ++j) z[ab * 4 + j] = gelu_f(__builtin_fmaf(rs[ab * 4 + j], acc[n][ab][j], __builtin_fmaf(-rm[ab * 4 + j], u1, u2)));
      v4u o = {pkbf(z[0], z[1]), pkbf(z[2], z[3]), pkbf(z[4], z[5]), pkbf(z[6], z[7])};
#pragma unroll
      for (int j = 0; j < 4; ++j) {
        const float a = bflo(o[j]), c = bfhi(o[j]);
        s1[2 * j] += a; s1[2 * j + 1] += c;
        s2[2 * j] = __builtin_fmaf(a, a, s2[2 * j]); s2[2 * j + 1] = __builtin_fmaf(c, c, s2[2 * j + 1]);
      }
      *(v4u*)(ws + WS_Z2 + ((size_t)((sub - 54) * 64 + mt * 4 + G) * 64 + lane) * 16) = o;
    }
#pragma unroll
    for (int j = 0; j < 8; ++j) {
#pragma unroll
      for (int sft = 1; sft < 16; sft <<= 1) { s1[j] += __shfl_xor(s1[j], sft); s2[j] += __shfl_xor(s2[j], sft); }
    }
    float mine = 0.f;
#pragma unroll
    for (int j = 0; j < 8; ++j) { if (i16 == 2 * j) mine = s1[j]; if (i16 == 2 * j + 1) mine = s2[j]; }
    double* st3 = (double*)(ws + WS_ST3);
    unsafeAtomicAdd(st3 + (size_t)(mt * 128 + 32 * G + 8 * q) * 2 + i16, (double)mine);
  } else {
    f32x2 rv[2];
#pragma unroll
    for (int ab = 0; ab < 2; ++ab) rv[ab] = tab[(G * 2 + ab) * 16 + i16];
#pragma unroll
    for (int n = 0; n < NSUB; ++n) {
      const int sub = sub0 + n0 + n;
      const f32x4 u1 = *(const f32x4*)(U + sub * 16 + 4 * q), u2 = *(const f32x4*)(U + F + sub * 16 + 4 * q);
#pragma unroll
      for (int ab = 0; ab < 2; ++ab) {
        float z[4];
#pragma unroll
        for (int j = 0; j < 4; ++j) z[j] = gelu_f(__builtin_fmaf(rv[ab].x, acc[n][ab][j], __builtin_fmaf(-rv[ab].y, u1[j], u2[j])));
        const int t16 = mt * 8 + 2 * G + ab;
        *(v2u*)(ws + WS_Z1 + ((size_t)(t16 * 27 + (sub >> 1)) * 64 + lane) * 16 + (sub & 1) * 8) = v2u{pkbf(z[0], z[1]), pkbf(z[2], z[3])};
      }
    }
  }
}

__global__ __launch_bounds__(512, 2) void k3_gemm1(char* __restrict__ ws) {
  extern __shared__ __attribute__((aligned(16))) char smem[];
  LAS char* lds = (LAS char*)smem;
  const int tid = threadIdx.x, lane = tid & 63, wave = __builtin_amdgcn_readfirstlane(tid >> 6);
  const int xcd = blockIdx.x & 7, idx = blockIdx.x >> 3;
  const int mt = (xcd >> 1) * 4 + (idx & 3), nt = (idx >> 2) * 2 + (xcd & 1);
  const int j = nt & 7, cnt = j < 6 ? 7 : 6, sub0 = (nt >> 3) * 54 + (j < 6 ? 7 * j : 42 + 6 * (j - 6));
  if (nt >= 8) {
    if (wave < 4) k3_body<4, false, true>(lds, ws, mt, nt, sub0, cnt, wave, lane, tid);
    else if (cnt == 7) k3_body<3, true, true>(lds, ws, mt, nt, sub0, cnt, wave, lane, tid);
    else k3_body<2, true, true>(lds, ws, mt, nt, sub0, cnt, wave, lane, tid);
  } else {
    if (wave < 4) k3_body<4, false, false>(lds, ws, mt, nt, sub0, cnt, wave, lane, tid);
    else if (cnt == 7) k3_body<3, true, false>(lds, ws, mt, nt, sub0, cnt, wave, lane, tid);
    else k3_body<2, true, false>(lds, ws, mt, nt, sub0, cnt, wave, lane, tid);
  }
}

constexpr int K4_RT = 0, K4_MT = 8192, K4_RED = K4_MT + 3 * 4096 + 64, K4_LDS = K4_RED + 4 * 3 * 8 * 1024;
constexpr int K4_R = 4;

template <int NSUB>
__device__ __forceinline__ void k4_body(LAS char* lds, const u16* __restrict__ wsp, const u16* __restrict__ bsp, char* ws, int mt, int sub0, int wave, int lane, int tid) {
  const int q = lane >> 4, i16 = lane & 15;
  const GAS char* wp[4];
#pragma unroll
  for (int m = 0; m < 4; ++m) {
    const int t = mt * 64 + 32 * (m >> 1) + 8 * (i16 >> 2) + 4 * (m & 1) + (i16 & 3);
    wp[m] = (const GAS char*)wsp + (size_t)t * (L * 2) + wave * 1024 + q * 16;
  }
  const GAS char* zp = (const GAS char*)ws + WS_Z2 + ((size_t)(sub0 * 64 + wave * 16) * 64 + lane) * 16;
  v4u wr[K4_R][4], zr[K4_R][NSUB];
  auto load = [&](int ks, int slot) {
#pragma unroll
    for (int m = 0; m < 4; ++m) wr[slot][m] = *(const GAS v4u*)(wp[m] + ks * 64);
#pragma unroll
    for (int n = 0; n < NSUB; ++n) zr[slot][n] = *(const GAS v4u*)(zp + (size_t)n * 65536 + ks * 1024);
  };
  v4u sv[8];
#pragma unroll
  for (int i = 0; i < 8; ++i) sv[i] = *(const GAS v4u*)((const GAS char*)ws + WS_ST3 + (size_t)(wave * 512 + i * 64 + lane) * 16);
#pragma unroll
  for (int r = 0; r < K4_R; ++r) load(r, r);
  SCHED_BAR();
  {
    LAS float* rt = (LAS float*)(lds + K4_RT) + wave * 512;
    LAS u16* mh = (LAS u16*)(lds + K4_MT) + wave * 512;
    LAS u16* ml = mh + 2048;
#pragma unroll
    for (int i = 0; i < 8; ++i) {
      const double s1 = __builtin_bit_cast(double, v2u{sv[i].x, sv[i].y}), s2 = __builtin_bit_cast(double, v2u{sv[i].z, sv[i].w});
      const double mean = s1 * (1.0 / FH);
      const float var = fmaxf((float)(s2 * (1.0 / FH) - mean * mean), 0.f);
      float mb, rinv;
      ln_scalars((float)mean, var, mb, rinv);
      rt[i * 64 + lane] = rinv;
      mh[i * 64 + lane] = (u16)(__float_as_uint(mb) >> 16);
      ml[i * 64 + lane] = 0;
    }
  }
  LAS const char* mfp = lds + K4_MT + (i16 == 0 ? 0 : 4096) + wave * 1024 + q * 16;
  LAS const char* rtp = lds + K4_RT + wave * 2048 + q * 32;
  f32x4 acc[NSUB + 1][4];
#pragma unroll
  for (int n = 0; n <= NSUB; ++n)
#pragma unroll
    for (int m = 0; m < 4; ++m) acc[n][m] = f32x4{0.f, 0.f, 0.f, 0.f};
#pragma unroll
  for (int ks = 0; ks < 16; ++ks) {
    const int slot = ks % K4_R;
    const f32x4 r0 = *(LAS const f32x4*)(rtp + ks * 128), r1 = *(LAS const f32x4*)(rtp + ks * 128 + 16);
    const v4u mf = *(LAS const v4u*)(mfp + ks * 64);
    v4u ws_[4], zc[NSUB];
#pragma unroll
    for (int m = 0; m < 4; ++m) {
      const v4u w = wr[slot][m];
      ws_[m] = v4u{pkbf(bflo(w.x) * r0.x, bfhi(w.x) * r0.y), pkbf(bflo(w.y) * r0.z, bfhi(w.y) * r0.w),
                   pkbf(bflo(w.z) * r1.x, bfhi(w.z) * r1.y), pkbf(bflo(w.w) * r1.z, bfhi(w.w) * r1.w)};
    }
#pragma unroll
    for (int n = 0; n < NSUB; ++n) zc[n] = zr[slot][n];
#pragma unroll
    for (int m = 0; m < 4; ++m) {
#pragma unroll
      for (int n = 0; n < NSUB; ++n) acc[n][m] = mfma16(zc[n], ws_[m], acc[n][m]);
      acc[NSUB][m] = mfma16(mf, ws_[m], acc[NSUB][m]);
    }
    if (ks + K4_R < 16) load(ks + K4_R, slot);
    SCHED_BAR();
  }
  const int t16 = mt * 4 + wave;
  v2u z1v[NSUB];
#pragma unroll
  for (int n = 0; n < NSUB; ++n) {
    const int sub = sub0 + n;
    z1v[n] = *(const GAS v2u*)((const GAS char*)ws + WS_Z1 + ((size_t)(t16 * 27 + (sub >> 1)) * 64 + lane) * 16 + (sub & 1) * 8);
  }
  const float bias = bf2f(bsp[mt * 64 + 32 * (wave >> 1) + 8 * (i16 >> 2) + 4 * (wave & 1) + (i16 & 3)]);
  LAS char* red = lds + K4_RED;
#pragma unroll
  for (int m = 0; m < 4; ++m) {
    if (m != wave) {
      const int sl = wave - (wave > m ? 1 : 0);
#pragma unroll
      for (int n = 0; n <= NSUB; ++n) *(LAS f32x4*)(red + ((m * 3 + sl) * 8 + n) * 1024 + lane * 16) = acc[n][m];
    }
  }
  f32x4 fin[NSUB + 1];
#pragma unroll
  for (int n = 0; n <= NSUB; ++n) fin[n] = wave == 0 ? acc[n][0] : (wave == 1 ? acc[n][1] : (wave == 2 ? acc[n][2] : acc[n][3]));
  __syncthreads();
#pragma unroll
  for (int sl = 0; sl < 3; ++sl)
#pragma unroll
    for (int n = 0; n <= NSUB; ++n) fin[n] += *(LAS const f32x4*)(red + ((wave * 3 + sl) * 8 + n) * 1024 + lane * 16);
  const float cm = __shfl(fin[NSUB][0], i16);
#pragma unroll
  for (int n = 0; n < NSUB; ++n) {
    const int sub = sub0 + n;
    float g[4];
#pragma unroll
    for (int j = 0; j < 4; ++j) g[j] = bfr(bfr(fin[n][j] - cm) + bias);
    const v2u o = {pkbf(bflo(z1v[n].x) * g[0], bfhi(z1v[n].x) * g[1]), pkbf(bflo(z1v[n].y) * g[2], bfhi(z1v[n].y) * g[3])};
    *(GAS v2u*)((GAS char*)ws + WS_V + ((size_t)(t16 * 27 + (sub >> 1)) * 64 + lane) * 16 + (sub & 1) * 8) = o;
  }
}

__global__ __launch_bounds__(256, 1) void k4_gemm2(const u16* __restrict__ wsp, const u16* __restrict__ bsp, char* __restrict__ ws) {
  extern __shared__ __attribute__((aligned(16))) char smem[];
  LAS char* lds = (LAS char*)smem;
  const int tid = threadIdx.x, lane = tid & 63, wave = __builtin_amdgcn_readfirstlane(tid >> 6);
  const int xcd = blockIdx.x & 7, idx = blockIdx.x >> 3;
  const int mt = (xcd >> 1) * 8 + (idx & 7), j = (xcd & 1) * 4 + (idx >> 3);
  if (j < 6) k4_body<7>(lds, wsp, bsp, ws, mt, 7 * j, wave, lane, tid);
  else k4_body<6>(lds, wsp, bsp, ws, mt, 42 + 6 * (j - 6), wave, lane, tid);
}

constexpr int K5_LDS = 4 * 3 * 5 * 1024;
constexpr int K5_R = 5;

template <int NK>
__device__ __forceinline__ void k5_loop(f32x4 (&acc)[5][4], const GAS char* vp, const GAS char* wp) {
  v4u vr[K5_R][4], wr[K5_R][5];
  auto load = [&](int ks, int slot) {
#pragma unroll
    for (int m = 0; m < 4; ++m) vr[slot][m] = *(const GAS v4u*)(vp + (size_t)m * 27 * 1024 + ks * 1024);
#pragma unroll
    for (int n = 0; n < 5; ++n) wr[slot][n] = *(const GAS v4u*)(wp + (size_t)n * 27 * 1024 + ks * 1024);
  };
#pragma unroll
  for (int r = 0; r < K5_R; ++r) load(r, r);
  SCHED_BAR();
#pragma unroll
  for (int ks = 0; ks < NK; ++ks) {
    const int slot = ks % K5_R;
#pragma unroll
    for (int m = 0; m < 4; ++m)
#pragma unroll
      for (int n = 0; n < 5; ++n) acc[n][m] = mfma16(wr[slot][n], vr[slot][m], acc[n][m]);
    if (ks + K5_R < NK) load(ks + K5_R, slot);
    SCHED_BAR();
  }
}

__global__ __launch_bounds__(256, 1) void k5_gemm3(char* __restrict__ ws, u16* __restrict__ out) {
  __shared__ __attribute__((aligned(16))) char smem[K5_LDS];
  LAS char* lds = (LAS char*)smem;
  const int tid = threadIdx.x, lane = tid & 63, wave = __builtin_amdgcn_readfirstlane(tid >> 6);
  const int xcd = blockIdx.x & 7, idx = blockIdx.x >> 3;
  const int mt = (xcd >> 1) * 8 + (idx & 7), nt = (xcd & 1) * 4 + (idx >> 3);
  const int q = lane >> 4, i16 = lane & 15;
  const GAS char* vp = (const GAS char*)ws + WS_V + ((size_t)(mt * 4 * 27 + wave * 7) * 64 + lane) * 16;
  const GAS char* wp = (const GAS char*)ws + WS_WOUT + ((size_t)(nt * 5 * 27 + wave * 7) * 64 + lane) * 16;
  f32x4 acc[5][4];
#pragma unroll
  for (int n = 0; n < 5; ++n)
#pragma unroll
    for (int m = 0; m < 4; ++m) acc[n][m] = f32x4{0.f, 0.f, 0.f, 0.f};
  if (wave < 3) k5_loop<7>(acc, vp, wp);
  else k5_loop<6>(acc, vp, wp);
  const int t = mt * 64 + 32 * (wave >> 1) + 8 * (i16 >> 2) + 4 * (wave & 1) + (i16 & 3);
  const int d0 = nt * 80 + 4 * q;
  v2u x2v[5];
  f32x4 gb[5];
#pragma unroll
  for (int n = 0; n < 5; ++n) {
    x2v[n] = *(const GAS v2u*)((const GAS char*)ws + WS_X2R + ((size_t)t * D + d0 + 16 * n) * 2);
    gb[n] = *(const GAS f32x4*)((const GAS char*)ws + WS_MOD + (size_t)(5 * D + d0 + 16 * n) * 4);
  }
#pragma unroll
  for (int m = 0; m < 4; ++m) {
    if (m != wave) {
      const int sl = wave - (wave > m ? 1 : 0);
#pragma unroll
      for (int n = 0; n < 5; ++n) *(LAS f32x4*)(lds + ((m * 3 + sl) * 5 + n) * 1024 + lane * 16) = acc[n][m];
    }
  }
  f32x4 fin[5];
#pragma unroll
  for (int n = 0; n < 5; ++n) fin[n] = wave == 0 ? acc[n][0] : (wave == 1 ? acc[n][1] : (wave == 2 ? acc[n][2] : acc[n][3]));
  __syncthreads();
#pragma unroll
  for (int sl = 0; sl < 3; ++sl)
#pragma unroll
    for (int n = 0; n < 5; ++n) fin[n] += *(LAS const f32x4*)(lds + ((wave * 3 + sl) * 5 + n) * 1024 + lane * 16);
#pragma unroll
  for (int n = 0; n < 5; ++n) {
    const float o0 = bflo(x2v[n].x) + bfr(gb[n][0] * bfr(fin[n][0])), o1 = bfhi(x2v[n].x) + bfr(gb[n][1] * bfr(fin[n][1]));
    const float o2 = bflo(x2v[n].y) + bfr(gb[n][2] * bfr(fin[n][2])), o3 = bfhi(x2v[n].y) + bfr(gb[n][3] * bfr(fin[n][3]));
    *(GAS v2u*)((GAS char*)out + ((size_t)t * D + d0 + 16 * n) * 2) = v2u{pkbf(o0, o1), pkbf(o2, o3)};
  }
}

extern "C" void kernel_launch(void* const* d_in, const int* in_sizes, int n_in, void* d_out, int out_size, void* d_ws, size_t ws_size,
                              hipStream_t stream) {
  const u16* x = (const u16*)d_in[0];
  char* ws = (char*)d_ws;
  (void)hipFuncSetAttribute((const void*)k3_gemm1, hipFuncAttributeMaxDynamicSharedMemorySize, K3_LDS);
  (void)hipFuncSetAttribute((const void*)k4_gemm2, hipFuncAttributeMaxDynamicSharedMemorySize, K4_LDS);
  k1_prep<<<K1_GRID, 256, 0, stream>>>(x, (const u16*)d_in[1], (const u16*)d_in[2], (const u16*)d_in[3], (const u16*)d_in[4],
                                       (const u16*)d_in[5], (const u16*)d_in[6], (const u16*)d_in[7], (const u16*)d_in[13],
                                       (const u16*)d_in[14], (const u16*)d_in[15], ws);
  k2_conv<<<K2_GRID, 256, 0, stream>>>((const u16*)d_in[8], (const u16*)d_in[10], ws);
  k3_gemm1<<<256, 512, K3_LDS, stream>>>(ws);
  k4_gemm2<<<256, 256, K4_LDS, stream>>>((const u16*)d_in[11], (const u16*)d_in[12], ws);
  k5_gemm3<<<256, 256, 0, stream>>>(ws, (u16*)d_out);
}
```

```cpp
#include <hip/hip_runtime.h>
#include <hip/hip_bf16.h>
#include <stdint.h>

#define GAS __attribute__((address_space(1)))
#define LAS __attribute__((address_space(3)))
typedef unsigned v4u __attribute__((ext_vector_type(4)));
typedef unsigned v2u __attribute__((ext_vector_type(2)));
typedef float f32x2 __attribute__((ext_vector_type(2)));
typedef float f32x4 __attribute__((ext_vector_type(4)));
typedef float f32x16 __attribute__((ext_vector_type(16)));
typedef __bf16 bf16x2 __attribute__((ext_vector_type(2)));
typedef __bf16 bf16x8 __attribute__((ext_vector_type(8)));
typedef _Float16 f16x2 __attribute__((ext_vector_type(2)));
typedef _Float16 f16x8 __attribute__((ext_vector_type(8)));
typedef short s16x4 __attribute__((ext_vector_type(4)));
typedef unsigned short u16;

constexpr int L = 2048, D = 640, NS = 128, F = 1728, FH = 864;

constexpr size_t CTL_E1 = 0;
constexpr size_t CTL_E1B = 196608;
constexpr size_t CTL_E2 = 262144;
constexpr size_t CTL_E3 = 294912;
constexpr size_t CTL_ST3 = 327680;
constexpr size_t CTL_F0 = CTL_ST3 + 262144;
constexpr size_t CTL_EPOCH = CTL_F0 + 32768;
constexpr size_t WS_MOD = CTL_EPOCH + 4096;
constexpr size_t WS_ST1 = WS_MOD + 32768;
constexpr size_t WS_U = WS_ST1 + 16384;
constexpr size_t WS_XT = WS_U + 65536;
constexpr size_t WS_X2T = WS_XT + (size_t)D * L * 2;
constexpr size_t WS_WIN = WS_X2T + (size_t)D * L * 2;
constexpr size_t WS_WOUT = WS_WIN + (size_t)D * F * 2;
constexpr size_t WS_Z2 = WS_WOUT + (size_t)FH * D * 2;
constexpr size_t WS_V = WS_Z2 + (size_t)L * FH * 2;
constexpr size_t WS_END = WS_V + (size_t)L * FH * 2;

__device__ __forceinline__ float bflo(unsigned u) { return __uint_as_float(u << 16); }
__device__ __forceinline__ float bfhi(unsigned u) { return __uint_as_float(u & 0xffff0000u); }
__device__ __forceinline__ float bf2f(u16 v) { return __uint_as_float((unsigned)v << 16); }
__device__ __forceinline__ unsigned pkbf(float a, float b) {
  f32x2 v = {a, b};
  return __builtin_bit_cast(unsigned, __builtin_convertvector(v, bf16x2));
}
__device__ __forceinline__ unsigned pkh(float a, float b) {
  f32x2 v = {a, b};
  return __builtin_bit_cast(unsigned, __builtin_convertvector(v, f16x2));
}
__device__ __forceinline__ float bfr(float a) { return bflo(pkbf(a, a)); }
__device__ __forceinline__ v2u trread(LAS const char* p) {
  return __builtin_bit_cast(v2u, __builtin_amdgcn_ds_read_tr16_b64_v4i16((LAS s16x4*)p));
}
__device__ __forceinline__ f32x4 mfma16(v4u a, v4u b, f32x4 c) {
  return __builtin_amdgcn_mfma_f32_16x16x32_bf16(__builtin_bit_cast(bf16x8, a), __builtin_bit_cast(bf16x8, b), c, 0, 0, 0);
}
__device__ __forceinline__ f32x16 mfma32(v4u a, v4u b, f32x16 c) {
  return __builtin_amdgcn_mfma_f32_32x32x16_bf16(__builtin_bit_cast(bf16x8, a), __builtin_bit_cast(bf16x8, b), c, 0, 0, 0);
}
__device__ __forceinline__ f32x16 mfma32h(v4u a, v4u b, f32x16 c) {
  return __builtin_amdgcn_mfma_f32_32x32x16_f16(__builtin_bit_cast(f16x8, a), __builtin_bit_cast(f16x8, b), c, 0, 0, 0);
}
template <bool DEV>
__device__ __forceinline__ void glds16(const void* sbase, unsigned voff, unsigned dst) {
  unsigned keep;
  if (DEV) asm volatile("s_mov_b32 %0, m0\n\ts_mov_b32 m0, %3\n\ts_nop 0\n\tglobal_load_lds_dwordx4 %1, %2 sc1\n\ts_mov_b32 m0, %0"
                        : "=&s"(keep) : "v"(voff), "s"(sbase), "s"(dst) : "memory");
  else asm volatile("s_mov_b32 %0, m0\n\ts_mov_b32 m0, %3\n\ts_nop 0\n\tglobal_load_lds_dwordx4 %1, %2\n\ts_mov_b32 m0, %0"
                    : "=&s"(keep) : "v"(voff), "s"(sbase), "s"(dst) : "memory");
}
template <int N> __device__ __forceinline__ void waitv() { asm volatile("s_waitcnt vmcnt(%0)" :: "i"(N) : "memory"); }
__device__ __forceinline__ float dot2bf(unsigned a, unsigned b, float c) {
  return __builtin_amdgcn_fdot2_f32_bf16(__builtin_bit_cast(bf16x2, a), __builtin_bit_cast(bf16x2, b), c, false);
}
#define SCHED_BAR() __builtin_amdgcn_sched_barrier(0)
#define STAMP_DECL
#define STAMP(i) do {} while (0)
#define STAMP_FLUSH(kid) do {} while (0)
#define STAMP_ARG
#define STAMP_PASS
#define CBAR() do { SCHED_BAR(); __builtin_amdgcn_s_waitcnt(0xC07F); asm volatile("" ::: "memory"); __builtin_amdgcn_s_barrier(); asm volatile("" ::: "memory"); } while (0)
#define LBAR() __builtin_amdgcn_s_barrier()

template <int CTRL> __device__ __forceinline__ float dppf(float v) {
  return __builtin_bit_cast(float, __builtin_amdgcn_update_dpp(0, __builtin_bit_cast(int, v), CTRL, 0xf, 0xf, true));
}
template <int N, int CTRL> __device__ __forceinline__ void treduce_dpp(float* v, bool up) {
#pragma unroll
  for (int e = 0; e < N; ++e) {
    const float a = v[e], b = v[e + N];
    const float send = up ? a : b, keep = up ? b : a;
    v[e] = keep + dppf<CTRL>(send);
  }
}
template <int N> __device__ __forceinline__ void treduce_xor(float* v, bool up, int mask) {
#pragma unroll
  for (int e = 0; e < N; ++e) {
    const float a = v[e], b = v[e + N];
    const float send = up ? a : b, keep = up ? b : a;
    v[e] = keep + __shfl_xor(send, mask);
  }
}

__device__ __forceinline__ float ln_std(float var) { return __builtin_amdgcn_sqrtf(bfr(bfr(var) + 1e-5f)); }
__device__ __forceinline__ void ln_scalars(float mean, float var, float& m_b, float& rinv) {
  m_b = bfr(mean);
  rinv = __builtin_amdgcn_rcpf(bfr(ln_std(var)));
}

struct cplx { float r, i; };
__device__ __forceinline__ cplx cmul(cplx a, cplx b) { return {a.r * b.r - a.i * b.i, a.r * b.i + a.i * b.r}; }

typedef GAS unsigned gu32;
#define RLX_AGENT __ATOMIC_RELAXED, __HIP_MEMORY_SCOPE_AGENT
constexpr unsigned SPIN_CAP = 1u << 16;
constexpr int M_LDS = 163840, M_CTL = M_LDS - 64;
constexpr int C_KOFF = 0, C_UOFF = 4224, C_UP = 1536, C_COFF = C_UOFF + 4 * C_UP, C_GRP = C_COFF + 4 * 64 * 144;
constexpr int G1_BOFF = 81920, G1_BST = 14336, G1_NSLOT = 5, G1_TAB = G1_BOFF + G1_BST * G1_NSLOT;
constexpr int G1_T_LN2 = G1_TAB, G1_T_U = G1_T_LN2 + 512, G1_T_P = G1_T_U + 2048, G1_T_Z = G1_T_P + 2048;
constexpr int G2_NW = 9, G2_NZ = 6, G2_ZOFF = G2_NW * 8192, G2_ZST = 14336;
constexpr int G3_RED = 135 * 1024, G3_GB = G3_RED + 20480;
static_assert(3 * C_GRP <= M_CTL && G1_T_Z + 512 <= M_CTL && G2_ZOFF + G2_NZ * G2_ZST <= M_CTL && G3_GB + 320 <= M_CTL, "lds");

__device__ __forceinline__ void st128(__amdgpu_buffer_rsrc_t rs, unsigned off, v4u v) { __builtin_amdgcn_raw_buffer_store_b128(v, rs, off, 0, 16); }
__device__ __forceinline__ v4u ld128(__amdgpu_buffer_rsrc_t rs, unsigned off) { return __builtin_amdgcn_raw_buffer_load_b128(rs, off, 0, 16); }

__device__ __forceinline__ bool wave_arrive(LAS char* lds, int word, unsigned n, int lane) {
  waitv<0>();
  unsigned old = 0;
  if (lane == 0) old = __hip_atomic_fetch_add((LAS unsigned*)(lds + M_CTL) + word, 1u, __ATOMIC_RELAXED, __HIP_MEMORY_SCOPE_WORKGROUP);
  return (unsigned)__builtin_amdgcn_readfirstlane(old) == n - 1;
}
__device__ __forceinline__ void lds_wait(LAS char* lds0, int word, unsigned target) {
  LAS unsigned* p = (LAS unsigned*)(lds0 + M_CTL) + word;
  while ((unsigned)__builtin_amdgcn_readfirstlane(__hip_atomic_load(p, __ATOMIC_RELAXED, __HIP_MEMORY_SCOPE_WORKGROUP)) < target) __builtin_amdgcn_s_sleep(0);
  asm volatile("" ::: "memory");
}
__device__ __forceinline__ void lds_post(LAS char* lds0, int word, int lane) {
  SCHED_BAR();
  asm volatile("" ::: "memory");
  if (lane == 0) __hip_atomic_fetch_add((LAS unsigned*)(lds0 + M_CTL) + word, 1u, __ATOMIC_RELAXED, __HIP_MEMORY_SCOPE_WORKGROUP);
  asm volatile("" ::: "memory");
}
__device__ __forceinline__ void group_sync(LAS char* lds0, int word, unsigned target, int lane) { lds_post(lds0, word, lane); lds_wait(lds0, word, target); }
template <int NREP, int NSH> __device__ __forceinline__ void signal(char* base, int shard, int lane) {
  if (lane < NREP) __hip_atomic_fetch_add((gu32*)(base + (lane * NSH + shard) * 256), 1u, RLX_AGENT);
}
template <int NSH> __device__ __forceinline__ unsigned poll_issue(char* base, int rep, int lane) {
  return __hip_atomic_load((gu32*)(base + (rep * NSH + (lane & (NSH - 1))) * 256), RLX_AGENT);
}
template <int NSH> __device__ __forceinline__ void poll_finish(unsigned c, char* base, int rep, unsigned total, int lane) {
  for (unsigned spin = 0; spin < SPIN_CAP; ++spin) {
#pragma unroll
    for (int s = 1; s < NSH; s <<= 1) c += __shfl_xor(c, s);
    if ((unsigned)__builtin_amdgcn_readfirstlane(c) >= total) break;
    __builtin_amdgcn_s_sleep(1);
    c = poll_issue<NSH>(base, rep, lane);
  }
  __builtin_amdgcn_fence(__ATOMIC_ACQUIRE, "wavefront");
}
template <int NSH> __device__ __forceinline__ void wait_count(char* base, int rep, unsigned total, int lane) {
  poll_finish<NSH>(poll_issue<NSH>(base, rep, lane), base, rep, total, lane);
}

__device__ __forceinline__ float gelu_f(float x) {
  const float p = __builtin_fmaf(x * x, -0.044715f * 2.302208198f, -2.302208198f);
  const float e = __builtin_amdgcn_exp2f(x * p);
  return x * __builtin_amdgcn_rcpf(1.0f + e);
}

__device__ __forceinline__ float ld32f(__amdgpu_buffer_rsrc_t rs, unsigned off) { return __uint_as_float(__builtin_amdgcn_raw_buffer_load_b32(rs, off, 0, 16)); }
__device__ __forceinline__ void st32(__amdgpu_buffer_rsrc_t rs, unsigned off, unsigned v) { __builtin_amdgcn_raw_buffer_store_b32(v, rs, off, 0, 16); }

struct Args {
  const u16 *x, *Are, *Aim, *Bre, *Bim, *Cre, *Cim, *logdt, *dskip, *win, *wsp, *bsp, *wout, *cvec, *wmod;
  char* ws; u16* out;
};

__device__ __forceinline__ unsigned read_tag(const char* ws) {
  unsigned e;
  asm volatile("s_load_dword %0, %1, 0x0\n\ts_waitcnt lgkmcnt(0)" : "=s"(e) : "s"(ws + CTL_EPOCH) : "memory");
  return e + 1u;
}
typedef GAS unsigned long long gu64;
__device__ __forceinline__ void put_granule(char* p, unsigned v, unsigned tag) { __hip_atomic_store((gu64*)p, ((unsigned long long)tag << 32) | v, RLX_AGENT); }
template <int N, class F> __device__ __forceinline__ void get_granules(unsigned (&v)[N], unsigned tag, F addr) {
  for (unsigned spin = 0; spin < SPIN_CAP; ++spin) {
    unsigned long long x[N];
#pragma unroll
    for (int k = 0; k < N; ++k) x[k] = __hip_atomic_load((gu64*)addr(k), RLX_AGENT);
    bool ok = true;
#pragma unroll
    for (int k = 0; k < N; ++k) { v[k] = (unsigned)x[k]; ok &= (unsigned)(x[k] >> 32) == tag; }
    if (__all(ok)) break;
    __builtin_amdgcn_s_sleep(2);
  }
}

__device__ __forceinline__ void conv_group(LAS char* lds, LAS char* lds0, int bt, const Args& A, __amdgpu_buffer_rsrc_t rs, unsigned& tag,
                                           int wg, int d, int tid, int wave, int lane STAMP_ARG) {
  char* ws = A.ws;
  float S[8];
  {
    const int half = wave & 1, o = d * NS + half * 64 + lane;
    const unsigned vare = A.Are[o], vaim = A.Aim[o], vdt = A.logdt[d], vbre = A.Bre[o], vbim = A.Bim[o], vcre = A.Cre[o], vcim = A.Cim[o];
    CBAR();
    tag = read_tag(ws);
    if (tid < 4) *(LAS v4u*)(lds + C_KOFF + tid * 16) = v4u{0u, 0u, 0u, 0u};
    const float dt = bfr(expf(bflo(vdt)));
    const float ar2 = dt * bflo(vare) * 1.4426950408889634f, ai2 = dt * bflo(vaim) * 0.15915494309189535f;
    auto E = [&](float xx) {
      const float mag = __builtin_amdgcn_exp2f(ar2 * xx), ph = __builtin_amdgcn_fractf(ai2 * xx);
      return cplx{mag * __builtin_amdgcn_cosf(ph), mag * __builtin_amdgcn_sinf(ph)};
    };
    LAS char* img = lds + C_COFF + wave * 8192 + lane * 128;
    const int sw = lane & 7;
    float isc = 0.f;
    if (wave < 2) {
      const cplx e8 = E(8.f), e64 = E(64.f), e128 = E(128.f), e192 = E(192.f);
      cplx q[8];
      q[0] = {1.f, 0.f}; q[1] = e8; q[2] = cmul(e8, e8); q[3] = cmul(q[2], e8); q[4] = cmul(q[2], q[2]);
      q[5] = cmul(q[4], e8); q[6] = cmul(q[4], q[2]); q[7] = cmul(q[4], q[3]);
#pragma unroll
      for (int g = 0; g < 4; ++g) {
        cplx v[8];
#pragma unroll
        for (int j = 0; j < 8; ++j) v[j] = g == 0 ? q[j] : cmul(q[j], g == 1 ? e64 : (g == 2 ? e128 : e192));
        *(LAS v4u*)(img + ((g ^ sw) << 4)) = v4u{pkh(v[0].r, v[1].r), pkh(v[2].r, v[3].r), pkh(v[4].r, v[5].r), pkh(v[6].r, v[7].r)};
        *(LAS v4u*)(img + (((4 + g) ^ sw) << 4)) = v4u{pkh(v[0].i, v[1].i), pkh(v[2].i, v[3].i), pkh(v[4].i, v[5].i), pkh(v[6].i, v[7].i)};
      }
    } else {
      cplx W = cmul(cplx{bflo(vcre), bflo(vcim)}, cplx{dt * bflo(vbre), dt * bflo(vbim)});
      float mx = fmaxf(fabsf(W.r), fabsf(W.i));
      mx = fmaxf(mx, dppf<0xb1>(mx)); mx = fmaxf(mx, dppf<0x4e>(mx)); mx = fmaxf(mx, dppf<0x141>(mx)); mx = fmaxf(mx, dppf<0x140>(mx));
      const unsigned mb = __float_as_uint(mx);
      const unsigned m01 = max((unsigned)__builtin_amdgcn_readlane(mb, 0), (unsigned)__builtin_amdgcn_readlane(mb, 16));
      const unsigned m23 = max((unsigned)__builtin_amdgcn_readlane(mb, 32), (unsigned)__builtin_amdgcn_readlane(mb, 48));
      int ex = (int)((max(m01, m23) >> 23) & 0xff);
      ex = ex < 20 ? 20 : (ex > 240 ? 240 : ex);
      const float sc = __uint_as_float((unsigned)(267 - ex) << 23);
      isc = __uint_as_float((unsigned)(ex - 13) << 23);
      W.r *= sc; W.i *= sc;
      const cplx e1 = E(1.f), e2 = cmul(e1, e1), e4 = cmul(e2, e2);
      cplx h[24];
      h[0] = W; h[1] = cmul(W, e1); h[2] = cmul(W, e2); h[3] = cmul(h[2], e1);
      h[4] = cmul(W, e4); h[5] = cmul(h[4], e1); h[6] = cmul(h[4], e2); h[7] = cmul(h[6], e1);
      const cplx g1 = E(256.f), g2 = E(512.f), g3 = E(768.f), g4 = E(1024.f);
      h[8] = cmul(h[0], g1); h[9] = cmul(h[2], g1); h[10] = cmul(h[4], g1); h[11] = cmul(h[6], g1);
      h[12] = cmul(h[0], g2); h[13] = cmul(h[4], g2); h[14] = cmul(h[0], g3); h[15] = cmul(h[4], g3);
      h[16] = cmul(h[0], g4); h[17] = cmul(h[8], g4); h[18] = cmul(h[12], g4); h[19] = cmul(h[14], g4);
      h[20] = cmul(h[16], g4); h[21] = {0.f, 0.f}; h[22] = {0.f, 0.f}; h[23] = {0.f, 0.f};
#pragma unroll
      for (int g = 0; g < 3; ++g) {
        const cplx* v = h + 8 * g;
        *(LAS v4u*)(img + ((g ^ sw) << 4)) = v4u{pkh(v[0].r, v[1].r), pkh(v[2].r, v[3].r), pkh(v[4].r, v[5].r), pkh(v[6].r, v[7].r)};
        *(LAS v4u*)(img + (((4 + g) ^ sw) << 4)) = v4u{pkh(-v[0].i, -v[1].i), pkh(-v[2].i, -v[3].i), pkh(-v[4].i, -v[5].i), pkh(-v[6].i, -v[7].i)};
      }
      *(LAS v4u*)(img + ((3 ^ sw) << 4)) = v4u{0u, 0u, 0u, 0u};
      *(LAS v4u*)(img + ((7 ^ sw) << 4)) = v4u{0u, 0u, 0u, 0u};
    }
    STAMP(1);
    group_sync(lds0, 5 + bt, 4, lane);
    if (wave >= 2) {
      const int hh = lane >> 5, blk = (lane >> 4) & 1, qq = (lane & 15) >> 2, p = lane & 3;
      LAS const char* Qi = lds + C_COFF + half * 8192;
      v2u a[8][2], bb[8][2];
#pragma unroll
      for (int ks = 0; ks < 8; ++ks)
#pragma unroll
        for (int t = 0; t < 2; ++t) {
          const int k = 16 * ks + 8 * hh + 4 * t + qq, mode = k >> 1, c = k & 1;
          const int off = mode * 128 + (((c * 4 + 2 * blk + (p >> 1)) ^ (mode & 7)) << 4) + (p & 1) * 8;
          a[ks][t] = trread(Qi + 16384 + off);
          bb[ks][t] = trread(Qi + off);
        }
      SCHED_BAR();
      f32x16 acc = {};
#pragma unroll
      for (int ks = 0; ks < 8; ++ks) acc = mfma32h(v4u{a[ks][0].x, a[ks][0].y, a[ks][1].x, a[ks][1].y}, v4u{bb[ks][0].x, bb[ks][0].y, bb[ks][1].x, bb[ks][1].y}, acc);
      LAS float* T = (LAS float*)(lds + C_UOFF) + half;
      const int q = lane & 31, hi = lane >> 5;
      auto put = [&](int idx, float v) { T[(idx + (idx >> 3)) * 2] = v * isc; };
#pragma unroll
      for (int r = 0; r < 4; ++r) put((r + 4 * hi) + 8 * q, acc[r]);
#pragma unroll
      for (int r = 0; r < 4; ++r) put(hi ? 384 + 64 * (r >> 1) + (r & 1) + 2 * q : 256 + r + 4 * q, acc[4 + r]);
      if (hi == 0) {
#pragma unroll
        for (int r = 0; r < 4; ++r) put(512 + 32 * r + q, acc[8 + r]);
      } else if (q == 0) put(640, acc[8]);
    }
    group_sync(lds0, 5 + bt, 8, lane);
    {
      f32x2 tv[8];
#pragma unroll
      for (int j = 0; j < 8; ++j) {
        const int l = tid * 8 + j;
        const int idx = l < 128 ? l : (int)(pkbf((float)l, 0.f) & 0xffffu) - 0x4300 + 128;
        tv[j] = *(LAS const f32x2*)(lds + C_UOFF + (idx + (idx >> 3)) * 8);
      }
      SCHED_BAR();
      float kv[8];
#pragma unroll
      for (int j = 0; j < 8; ++j) kv[j] = tv[j][0] + tv[j][1];
      *(LAS v4u*)(lds + C_KOFF + 64 + tid * 16) = v4u{pkbf(kv[0], kv[1]), pkbf(kv[2], kv[3]), pkbf(kv[4], kv[5]), pkbf(kv[6], kv[7])};
      S[0] = kv[0];
#pragma unroll
      for (int j = 1; j < 8; ++j) S[j] = S[j - 1] + kv[j];
      float inc = S[7];
      inc += dppf<0x111>(inc); inc += dppf<0x112>(inc); inc += dppf<0x114>(inc); inc += dppf<0x118>(inc);
      inc += __builtin_bit_cast(float, __builtin_amdgcn_update_dpp(0, __builtin_bit_cast(int, inc), 0x142, 0xa, 0xf, false));
      inc += __builtin_bit_cast(float, __builtin_amdgcn_update_dpp(0, __builtin_bit_cast(int, inc), 0x143, 0xc, 0xf, false));
      const float exc = inc - S[7];
#pragma unroll
      for (int j = 0; j < 8; ++j) S[j] += exc;
      if (lane == 63) ((LAS float*)(lds + C_KOFF + 4160))[wave] = inc;
    }
  }
  STAMP(2);
  lds_post(lds0, 5 + bt, lane);
  if (bt == 0 && wave == 0) {
    for (unsigned spin = 0; spin < SPIN_CAP; ++spin) {
      const v4u f = ld128(rs, (unsigned)CTL_F0 + (wg & 31) * 1024 + lane * 16);
      if (__all(f.x == tag && f.y == ~tag && f.z == tag && f.w == ~tag)) break;
      __builtin_amdgcn_s_sleep(4);
    }
    __builtin_amdgcn_fence(__ATOMIC_ACQUIRE, "wavefront");
    lds_post(lds0, 8, lane);
  } else lds_wait(lds0, 8, 1);
  const v4u xv = ld128(rs, (unsigned)WS_XT + (tid * D + d) * 16);
  v4u st[2];
#pragma unroll
  for (int i = 0; i < 2; ++i) st[i] = ld128(rs, (unsigned)WS_ST1 + (i * 256 + tid) * 16);
  lds_wait(lds0, 5 + bt, 12);
  STAMP(3);
  float xf[8], xn[8];
  {
#pragma unroll
    for (int j = 0; j < 4; ++j) { xf[2 * j] = bflo(xv[j]); xf[2 * j + 1] = bfhi(xv[j]); }
    const f32x4 wt = *(LAS const f32x4*)(lds + C_KOFF + 4160);
    if (tid < 128) *(LAS v4u*)(lds + C_UOFF + (tid >> 5) * C_UP + (tid & 31) * 16) = v4u{0u, 0u, 0u, 0u};
#pragma unroll
    for (int j = 0; j < 8; ++j) {
      const unsigned sj = st[j >> 2][j & 3];
      xn[j] = bfr(bfr(xf[j] - bflo(sj)) * __builtin_amdgcn_rcpf(bfhi(sj)));
    }
#pragma unroll
    for (int e = 0; e < 4; ++e) *(LAS unsigned*)(lds + C_UOFF + e * C_UP + 512 + tid * 4) = (__float_as_uint(xn[e]) >> 16) | (__float_as_uint(xn[e + 4]) & 0xffff0000u);
    const float off = (wave > 0 ? wt[0] : 0.f) + (wave > 1 ? wt[1] : 0.f) + (wave > 2 ? wt[2] : 0.f);
#pragma unroll
    for (int j = 0; j < 8; ++j) S[j] += off;
  }
  const u16 dskv = A.dskip[d];
  unsigned long long mg[3];
#pragma unroll
  for (int k = 0; k < 3; ++k) mg[k] = __hip_atomic_load((gu64*)(ws + WS_MOD + (size_t)(k * D + d) * 8), RLX_AGENT);
  SCHED_BAR();
  const unsigned pv = *(const unsigned*)((const char*)A.wsp + ((size_t)(d & 511) * 256 + tid) * 64);
  STAMP(5);
  group_sync(lds0, 5 + bt, 16, lane);
  STAMP(6);
  f32x16 y0 = {}, y1 = {};
  {
    const int hh = lane >> 5, blk = (lane >> 4) & 1, qq = (lane & 15) >> 2, p = lane & 3, col = lane & 31;
    LAS const char* ab = lds + C_KOFF + (32 + 16 * blk + 4 * p - 4 * qq + 32 * hh) * 2 - 32;
    LAS const char* bb = lds + C_UOFF + wave * C_UP + (256 + 8 * (col - hh) - 240) * 2;
    struct CF { v2u a0l, a0h, a1l, a1h; v4u b0, b1; };
    CF cf[3];
    auto rdc = [&](int ks, CF& f) {
      f.a0l = trread(ab + ks * 128 + 32); f.a0h = trread(ab + ks * 128);
      f.b0 = *(LAS const v4u*)(bb + (15 - ks) * 32);
      f.b1 = *(LAS const v4u*)(bb + (31 - ks) * 32);
      f.a1l = trread(ab + (ks + 16) * 128 + 32); f.a1h = trread(ab + (ks + 16) * 128);
    };
    rdc(0, cf[0]); rdc(1, cf[1]);
#pragma unroll
    for (int ks = 0; ks < 16; ++ks) {
      if (ks + 2 < 16) rdc(ks + 2, cf[(ks + 2) % 3]);
      SCHED_BAR();
      const CF& f = cf[ks % 3];
      const v4u a0 = {f.a0l.x, f.a0l.y, f.a0h.x, f.a0h.y}, a1 = {f.a1l.x, f.a1l.y, f.a1h.x, f.a1h.y};
      y0 = mfma32(a0, f.b0, y0);
      y1 = mfma32(a0, f.b1, y1);
      y1 = mfma32(a1, f.b0, y1);
      SCHED_BAR();
    }
    asm volatile("" :: "v"(y0), "v"(y1));
    STAMP(7);
    LAS char* cb = lds + C_COFF + (wave * 64 + col) * 144 + hh * 16;
#pragma unroll
    for (int g = 0; g < 4; ++g) {
      *(LAS f32x4*)(cb + g * 32) = f32x4{y0[4 * g], y0[4 * g + 1], y0[4 * g + 2], y0[4 * g + 3]};
      *(LAS f32x4*)(cb + 32 * 144 + g * 32) = f32x4{y1[4 * g], y1[4 * g + 1], y1[4 * g + 2], y1[4 * g + 3]};
    }
  }
  lds_post(lds0, 5 + bt, lane);
  unsigned mv[3] = {(unsigned)mg[0], (unsigned)mg[1], (unsigned)mg[2]};
  if (!__all((unsigned)(mg[0] >> 32) == tag && (unsigned)(mg[1] >> 32) == tag && (unsigned)(mg[2] >> 32) == tag))
    get_granules<3>(mv, tag, [&](int k) { return ws + WS_MOD + (size_t)(k * D + d) * 8; });
  const float sa = __uint_as_float(mv[0]), c1a = bfr(1.0f + __uint_as_float(mv[1])), ga = __uint_as_float(mv[2]), dsk = bf2f(dskv);
#pragma unroll
  for (int j = 0; j < 8; ++j) { xn[j] = bfr(bfr(bfr(xn[j] * c1a) + sa) * dsk); S[j] *= sa; }
  STAMP(8);
  lds_wait(lds0, 5 + bt, 20);
  STAMP(9);
  float y[8];
  {
    const int c1 = 2 * tid, c0 = tid ? c1 - 1 : 0;
    const int o0 = (c0 >> 3) * 144 + (c0 & 7) * 16, o1 = (c1 >> 3) * 144 + (c1 & 7) * 16;
    LAS const char* cb = lds + C_COFF;
    f32x4 w0[4], w1[4], w2[4];
#pragma unroll
    for (int e = 0; e < 4; ++e) {
      w1[e] = *(LAS const f32x4*)(cb + e * 64 * 144 + o1); w2[e] = *(LAS const f32x4*)(cb + e * 64 * 144 + o1 + 16);
      if (e) w0[e] = *(LAS const f32x4*)(cb + e * 64 * 144 + o0);
    }
    SCHED_BAR();
#pragma unroll
    for (int e = 0; e < 4; ++e) {
      float w[12];
#pragma unroll
      for (int j = 0; j < 4; ++j) { w[4 + j] = w1[e][j]; w[8 + j] = w2[e][j]; w[j] = (e && tid) ? w0[e][j] : 0.f; }
#pragma unroll
      for (int j = 0; j < 8; ++j) y[j] = e ? y[j] + w[4 + j - e] : w[4 + j];
    }
  }
  float o[8];
#pragma unroll
  for (int j = 0; j < 8; ++j) {
    const float yy = bfr(bfr(__builtin_fmaf(c1a, y[j], S[j])) + xn[j]);
    o[j] = xf[j] + bfr(ga * yy);
  }
  {
    const unsigned a0 = pkbf(o[0], o[1]), a1 = pkbf(o[2], o[3]), b0 = pkbf(o[4], o[5]), b1 = pkbf(o[6], o[7]);
    const bool odd = tid & 1;
    const unsigned s0 = odd ? a0 : b0, s1 = odd ? a1 : b1;
    const unsigned r0 = __builtin_amdgcn_update_dpp(0, s0, 0xb1, 0xf, 0xf, true), r1 = __builtin_amdgcn_update_dpp(0, s1, 0xb1, 0xf, 0xf, true);
    const v4u ov = odd ? v4u{r0, r1, b0, b1} : v4u{a0, a1, r0, r1};
    st128(rs, (unsigned)WS_X2T + d * (L * 2) + (tid >> 2) * 64 + (tid & 1) * 32 + (tid & 2) * 8, ov);
  }
  STAMP(10);
  asm volatile("" :: "v"(pv));
  STAMP(11);
  if (wave_arrive(lds0, bt, 4, lane)) signal<32, 8>(ws + CTL_E1 + bt * 65536, wg & 7, lane);
}

constexpr int TP = 1312;

__device__ __forceinline__ void aux_group(LAS char* lds, LAS char* lds0, const Args& A, __amdgpu_buffer_rsrc_t rs, int u, int tid, int wave, int lane STAMP_ARG) {
  char* ws = A.ws;
  unsigned gs = 0, tag;
  {
    CBAR();
    const int r = tid >> 4, s = tid & 15;
    v4u v[5], w[10];
#pragma unroll
    for (int i = 0; i < 5; ++i) v[i] = *(const v4u*)(A.x + (size_t)(u * 16 + r) * D + (s + 16 * i) * 8);
    SCHED_BAR();
    {
      const int c = (u * 4 + wave) * 3 + lane - 32;
      st128(rs, lane < 32 ? (unsigned)CTL_ST3 + ((u * 4 + wave) * 32 + lane) * 16 : (lane < 35 && c < 1280 ? (unsigned)c * 256 : 0x7ffffff0u), v4u{0u, 0u, 0u, 0u});
    }
    const int c0 = 32 * (u < 120 ? u : 119), seg = tid & 3, rg = tid >> 2;
    u16 cv[10];
#pragma unroll
    for (int i = 0; i < 10; ++i) cv[i] = A.cvec[rg + 64 * i];
#pragma unroll
    for (int i = 0; i < 10; ++i) w[i] = *(const v4u*)(A.wmod + (size_t)(rg + 64 * i) * (6 * D) + c0 + seg * 8);
    SCHED_BAR();
    tag = read_tag(ws);
    SCHED_BAR();
    LAS char* img = lds + 1024;
#pragma unroll
    for (int i = 0; i < 5; ++i) *(LAS v4u*)(img + r * TP + (s + 16 * i) * 16) = v[i];
    lds_post(lds0, 7, lane); gs += 4;
    STAMP(3);
    float s0 = 0.f, s1 = 0.f, q0 = 0.f, q1 = 0.f;
#pragma unroll
    for (int i = 0; i < 5; ++i) {
      s0 = dot2bf(v[i][0], 0x3f803f80u, s0); s1 = dot2bf(v[i][1], 0x3f803f80u, s1); s0 = dot2bf(v[i][2], 0x3f803f80u, s0); s1 = dot2bf(v[i][3], 0x3f803f80u, s1);
      q0 = dot2bf(v[i][0], v[i][0], q0); q1 = dot2bf(v[i][1], v[i][1], q1); q0 = dot2bf(v[i][2], v[i][2], q0); q1 = dot2bf(v[i][3], v[i][3], q1);
    }
    float sum = s0 + s1, sq = q0 + q1;
    sum += dppf<0xb1>(sum); sq += dppf<0xb1>(sq); sum += dppf<0x4e>(sum); sq += dppf<0x4e>(sq);
    sum += dppf<0x141>(sum); sq += dppf<0x141>(sq); sum += dppf<0x140>(sum); sq += dppf<0x140>(sq);
    const float mean = sum * (1.0f / D);
    const int t = u * 16 + r;
    const unsigned stv = pkbf(mean, ln_std(fmaxf(sq * (1.0f / D) - mean * mean, 0.f)));
    if (s == 0) st32(rs, (unsigned)WS_ST1 + ((((t >> 2) & 1) * 256 + (t >> 3)) * 4 + (t & 3)) * 4, stv);
    lds_wait(lds0, 7, gs);
    {
      const int q = lane >> 4, a0 = (8 * (q & 1) + ((lane & 15) >> 2)) * TP + (lane & 3) * 8 + (wave * 2 + (q >> 1)) * 32;
      v2u lo[5], hi[5];
#pragma unroll
      for (int j = 0; j < 5; ++j) { lo[j] = trread(img + a0 + j * 256); hi[j] = trread(img + a0 + 4 * TP + j * 256); }
#pragma unroll
      for (int j = 0; j < 5; ++j)
        st128(rs, (unsigned)WS_XT + ((u * 2 + (q & 1)) * D + (8 * j + wave * 2 + (q >> 1)) * 16 + (lane & 15)) * 16, v4u{lo[j].x, lo[j].y, hi[j].x, hi[j].y});
    }
    SCHED_BAR();
    STAMP(8);
    float acc[8] = {};
#pragma unroll
    for (int i = 0; i < 10; ++i) {
      const float c = bf2f(cv[i]);
      const float sl = bfr(c * __builtin_amdgcn_rcpf(1.0f + __builtin_amdgcn_exp2f(-1.4426950408889634f * c)));
#pragma unroll
      for (int j = 0; j < 4; ++j) { acc[2 * j] += sl * bflo(w[i][j]); acc[2 * j + 1] += sl * bfhi(w[i][j]); }
    }
    SCHED_BAR();
    if (wave_arrive(lds0, 2, 4, lane) && lane < 32)
      __hip_atomic_store((gu64*)(ws + CTL_F0 + lane * 1024 + u * 8), ((unsigned long long)~tag << 32) | tag, RLX_AGENT);
    STAMP(1);
    treduce_xor<4>(acc, (lane & 32) != 0, 32);
    treduce_xor<2>(acc, (lane & 16) != 0, 16);
    treduce_dpp<1, 0x128>(acc, (lane & 8) != 0);
    acc[0] += __shfl_xor(acc[0], 4);
    LAS float* red = (LAS float*)lds;
    if (!(lane & 4)) red[wave * 32 + seg * 8 + (lane >> 3)] = acc[0];
    group_sync(lds0, 7, gs += 4, lane);
    if (tid < 32 && u < 120) put_granule(ws + WS_MOD + (size_t)(c0 + tid) * 8, __float_as_uint(bfr((red[tid] + red[32 + tid]) + (red[64 + tid] + red[96 + tid]))), tag);
  }
  STAMP(2);
  if (u < 108) {
    const int cbk = u >> 2, part = u & 3;
    const int c0 = 64 * cbk, r0 = 160 * part, seg = tid & 7, rg = tid >> 3;
    v4u w[5];
    int dd[5];
#pragma unroll
    for (int i = 0; i < 5; ++i) { dd[i] = r0 + rg + 32 * i; w[i] = *(const v4u*)(A.win + (size_t)dd[i] * F + c0 + seg * 8); }
    unsigned mv[10];
    get_granules<10>(mv, tag, [&](int k) { return ws + WS_MOD + (size_t)((3 + (k & 1)) * D + dd[k >> 1]) * 8; });
    float v[16] = {};
    constexpr int WPITCH = 144;
    LAS char* img = lds + 1024;
#pragma unroll
    for (int i = 0; i < 5; ++i) {
      const float c1 = bfr(1.0f + __uint_as_float(mv[2 * i + 1])), sb = __uint_as_float(mv[2 * i]);
#pragma unroll
      for (int j = 0; j < 4; ++j) {
        const float a = bflo(w[i][j]), c = bfhi(w[i][j]);
        const unsigned pk = pkbf(a * c1, c * c1);
        v[8 + 2 * j] += sb * a; v[8 + 2 * j + 1] += sb * c;
        v[2 * j] += bflo(pk); v[2 * j + 1] += bfhi(pk);
        w[i][j] = pk;
      }
      *(LAS v4u*)(img + (rg + 32 * i) * WPITCH + seg * 16) = w[i];
    }
    treduce_xor<8>(v, (lane & 32) != 0, 32);
    treduce_xor<4>(v, (lane & 16) != 0, 16);
    treduce_dpp<2, 0x128>(v, (lane & 8) != 0);
    LAS float* red = (LAS float*)(lds + 24576 + 1024);
    {
      const int which = lane >> 5, j = ((lane >> 4) & 1) * 4 + ((lane >> 3) & 1) * 2;
      *(LAS f32x2*)(red + wave * 128 + which * 64 + seg * 8 + j) = f32x2{v[0], v[1]};
    }
    group_sync(lds0, 7, gs += 4, lane);
    const int q = lane >> 4, rr = (lane & 15) >> 2, p = lane & 3;
    const int colb = (32 * (wave >> 1) + 8 * p + 4 * (wave & 1)) * 2;
#pragma unroll
    for (int i = 0; i < 5; ++i) {
      const int a0 = (32 * i + 8 * q + rr) * WPITCH + colb;
      v2u lo = trread(img + a0), hi = trread(img + a0 + 4 * WPITCH);
      st128(rs, (unsigned)WS_WIN + (((4 * cbk + wave) * 20 + part * 5 + i) * 64 + lane) * 16, v4u{lo.x, lo.y, hi.x, hi.y});
    }
    if (tid < 128) {
      const int which = tid >> 6, o = tid & 63, sub = o >> 4, i = o & 15;
      const int cl = 32 * (sub >> 1) + 8 * (i >> 2) + 4 * (sub & 1) + (i & 3), ix = which * 64 + cl;
      st32(rs, (unsigned)WS_U + ((part * 2 + which) * F + c0 + o) * 4, __float_as_uint((red[ix] + red[128 + ix]) + (red[256 + ix] + red[384 + ix])));
    }
  } else {
    LAS char* img = lds + 1024;
#pragma unroll 1
    for (int slab = u - 108; slab < 27; slab += 20) {
      const int r = tid >> 3, s = tid & 7;
      const int half = (r >> 2) & 1, jj = slab < 24 ? slab / 3 : 2 * (slab - 24) + half;
      const int sub = (jj < 6 ? 7 * jj : 42 + 6 * (jj - 6)) + (slab < 24 ? 2 * (slab - 3 * jj) + half : 6);
      const u16* src = A.wout + (size_t)(32 * (sub >> 1) + 8 * (r >> 3) + 4 * (sub & 1) + (r & 3)) * D;
      v4u v[10];
#pragma unroll
      for (int i = 0; i < 10; ++i) v[i] = *(const v4u*)(src + (s + 8 * i) * 8);
      group_sync(lds0, 7, gs += 4, lane);
#pragma unroll
      for (int i = 0; i < 10; ++i) *(LAS v4u*)(img + r * TP + (s + 8 * i) * 16) = v[i];
      group_sync(lds0, 7, gs += 4, lane);
      const int a0 = (8 * (lane >> 4) + ((lane & 15) >> 2)) * TP + (lane & 3) * 8 + wave * 32;
#pragma unroll
      for (int j = 0; j < 10; ++j) {
        const v2u lo = trread(img + a0 + j * 128), hi = trread(img + a0 + 4 * TP + j * 128);
        st128(rs, (unsigned)WS_WOUT + (((wave + 4 * j) * 27 + slab) * 64 + lane) * 16, v4u{lo.x, lo.y, hi.x, hi.y});
      }
    }
  }
  lds_wait(lds0, 8, 1);
  if (wave_arrive(lds0, 2, 8, lane)) signal<32, 8>(ws + CTL_E1B, u & 7, lane);
}

__device__ __forceinline__ int g1_aoff(int row, int G, int ab, int p) {
  const int f = ((row >> 1) & 1) | (((row >> 3) & 1) << 1);
  return row * 128 + (((4 * G + 2 * ab + (p >> 1)) ^ (f << 1)) << 4) + (p & 1) * 8;
}

template <int NB>
__device__ __forceinline__ void loader_path(LAS char* lds, char* ws, const u16* __restrict__ wsp, int wg, int mt, int j, int sub0, int lw, int lane) {
  const unsigned lbase = (unsigned)(size_t)lds, vo = lane * 16;
  {
    unsigned avo[2];
#pragma unroll
    for (int h = 0; h < 2; ++h) {
      const int row = lane >> 3, pos = lane & 7, f = ((row >> 1) & 1) | (h << 1);
      avo[h] = row * (L * 2) + ((pos ^ (f << 1)) << 4);
    }
    const char* asb = ws + WS_X2T + (size_t)(16 * lw) * (L * 2) + mt * 128;
    const char* bsb = ws + WS_WIN + (size_t)((sub0 + (lw >> 1)) * 20 + (lw & 1)) * 1024;
    const unsigned bb = lbase + G1_BOFF;
    auto issueB = [&](int s, unsigned slot) {
      const char* bs = bsb + (s < 10 ? 54 * 20 * 1024 + s * 2048 : (s - 10) * 2048);
#pragma unroll
      for (int i = 0; i < NB; ++i) glds16<true>(bs + i * 2 * 20 * 1024, vo, slot + (lw + 4 * i) * 1024);
    };
    auto issueA = [&](int s0, int s1) {
#pragma unroll 1
      for (int s = s0; s < s1; ++s)
#pragma unroll
        for (int h = 0; h < 2; ++h) glds16<true>(asb + (size_t)(s * 64 + h * 8) * (L * 2), avo[h], lbase + s * 8192 + lw * 2048 + h * 1024);
    };
    group_sync(lds, 5, 24, lane);
    wait_count<8>(ws + CTL_E1B, wg & 31, 128, lane);
#pragma unroll
    for (int s = 0; s < 4; ++s) issueB(s, bb + (s + 1) * G1_BST);
    LBAR();
    issueA(0, 4); issueB(4, bb);
    waitv<6 + NB>(); LBAR();
    waitv<4 + NB>(); LBAR(); issueB(5, bb + G1_BST);
    waitv<2 + 2 * NB>(); LBAR(); issueA(4, 8); issueB(6, bb + 2 * G1_BST);
    waitv<8 + 3 * NB>(); LBAR(); issueB(7, bb + 3 * G1_BST);
    waitv<6 + 2 * NB>(); LBAR(); issueB(8, bb + 4 * G1_BST);
    waitv<4 + 3 * NB>(); LBAR(); issueA(8, 10); issueB(9, bb);
    waitv<4 + 3 * NB>(); LBAR(); issueB(10, bb + G1_BST);
    waitv<4 + 3 * NB>(); LBAR(); issueB(11, bb + 2 * G1_BST);
    waitv<2 + 3 * NB>(); LBAR(); issueB(12, bb + 3 * G1_BST);
    int nxt = 4 * G1_BST;
#pragma unroll 1
    for (int i = 9; i <= 15; ++i) {
      waitv<NB * 3>(); LBAR();
      issueB(i + 4, bb + nxt);
      nxt += G1_BST; if (nxt == G1_NSLOT * G1_BST) nxt = 0;
    }
    waitv<NB * 3>(); LBAR();
    waitv<NB * 2>(); LBAR();
    waitv<NB * 1>(); LBAR();
    waitv<0>(); LBAR();
    LBAR();
  }
  {
    unsigned wvo[2];
#pragma unroll
    for (int h = 0; h < 2; ++h) {
      const int i = 8 * h + (lane >> 3);
      wvo[h] = (8 * (i >> 2) + (i & 3)) * (L * 2) + (((lane & 7) ^ (i >> 1)) << 4);
    }
    const char* wsb = (const char*)wsp + (size_t)(mt * 64 + 32 * (lw >> 1) + 4 * (lw & 1)) * (L * 2);
    const char* zsb = ws + WS_Z2 + (size_t)((sub0 + (lw >> 1)) * 64 + (lw & 1)) * 1024;
    auto issueW = [&](int s, unsigned slot) {
      glds16<false>(wsb + s * 128, wvo[0], slot + lw * 2048);
      glds16<false>(wsb + s * 128, wvo[1], slot + lw * 2048 + 1024);
    };
    auto issueZ = [&](int s, unsigned slot) {
#pragma unroll
      for (int i = 0; i < NB; ++i) glds16<true>(zsb + i * 2 * 65536 + s * 2048, vo, slot + (lw + 4 * i) * 1024);
    };
    const unsigned zb = lbase + G2_ZOFF;
#pragma unroll 1
    for (int s = 0; s < G2_NW; ++s) issueW(s, lbase + s * 8192);
    LBAR();
#pragma unroll 1
    for (int s = 0; s < G2_NZ; ++s) issueZ(s, zb + s * G2_ZST);
    waitv<NB * 5>(); LBAR();
    int zn = 0, wn = 0;
    auto step = [&](int i, bool dz, bool dw) {
      if (dz) { issueZ(i + 5, zb + zn); zn += G2_ZST; if (zn == G2_NZ * G2_ZST) zn = 0; }
      if (dw) { issueW(i + 8, lbase + wn); wn += 8192; if (wn == G2_NW * 8192) wn = 0; }
    };
    waitv<NB * 4>(); LBAR(); step(1, true, true);
    waitv<NB * 4 + 2>(); LBAR(); step(2, true, true);
    waitv<NB * 4 + 4>(); LBAR(); step(3, true, true);
    waitv<NB * 4 + 6>(); LBAR(); step(4, true, true);
    waitv<NB * 4 + 8>(); LBAR(); step(5, true, true);
#pragma unroll 1
    for (int i = 6; i <= 23; ++i) { waitv<NB * 4 + 10>(); LBAR(); step(i, true, true); }
    waitv<NB * 4 + 10>(); LBAR(); step(24, true, false);
    waitv<NB * 4 + 8>(); LBAR(); step(25, true, false);
    waitv<NB * 4 + 6>(); LBAR(); step(26, true, false);
    waitv<NB * 4 + 4>(); LBAR();
    waitv<NB * 3 + 2>(); LBAR();
    waitv<NB * 2>(); LBAR();
    waitv<NB * 1>(); LBAR();
    waitv<0>(); LBAR();
    LBAR();
  }
  {
    const char* osb = ws + WS_WOUT + (size_t)(j * 135 + lw) * 1024;
#pragma unroll 1
    for (int p = 0; p < 33; ++p) glds16<true>(osb + p * 4096, vo, lbase + (lw + 4 * p) * 1024);
    if (lw < 3) glds16<true>(osb + 33 * 4096, vo, lbase + (lw + 132) * 1024);
    waitv<0>();
    LBAR();
  }
}

template <int NSUB, bool STATS>
__device__ __forceinline__ void consumer_g12(LAS char* lds, char* ws, __amdgpu_buffer_rsrc_t rs, const u16* __restrict__ bsp, int wg, int mt, int j, int sub0, int cnt,
                                             int wave, int tid, int lane, v2u (&x2v)[3] STAMP_ARG) {
  constexpr int NS1 = NSUB > 0 ? NSUB : 1;
  const int G = wave >> 2, ng = G ? 3 - (wave & 3) : (wave & 3);
  const int q = lane >> 4, i16 = lane & 15, rr = i16 >> 2, p = lane & 3, n0 = 2 * ng;
  float bias[2];
#pragma unroll
  for (int m = 0; m < 2; ++m) bias[m] = bf2f(bsp[mt * 64 + 32 * G + 8 * (i16 >> 2) + 4 * m + (i16 & 3)]);
  if (wave == 3) wait_count<8>(ws + CTL_E1, wg & 31, 256, lane);
  STAMP(4);
  CBAR();
  {
    const int c = tid & 127, which = (tid >> 7) & 1, half = tid >> 8;
    if (c < cnt * 16) {
      float up[4];
#pragma unroll
      for (int k = 0; k < 4; ++k)
        up[k] = __uint_as_float(__builtin_amdgcn_raw_buffer_load_b32(rs, (unsigned)WS_U + ((k * 2 + which) * F + (sub0 + (half ? 0 : 54)) * 16 + c) * 4, 0, 16));
      ((LAS float*)(lds + G1_T_U))[tid] = (up[0] + up[1]) + (up[2] + up[3]);
    }
  }
  v2u z1v[NS1][2];
  {
    const int aoff0 = g1_aoff(8 * q + rr, G, 0, p), aoff1 = g1_aoff(8 * q + rr, G, 1, p);
    const int boff = G1_BOFF + n0 * 2048 + lane * 16;
    struct Frags { v4u a[2][2]; v4u b[2][NS1]; };
    f32x4 acc2[NS1][2], acc1[NS1][2], asum[2], asq[2];
    int acur = 0, bcur = G1_BST;
    auto rd = [&](Frags& f) {
#pragma unroll
      for (int kk = 0; kk < 2; ++kk) {
        const v2u l0 = trread(lds + acur + aoff0 + kk * 4096), h0 = trread(lds + acur + aoff0 + kk * 4096 + 512);
        const v2u l1 = trread(lds + acur + aoff1 + kk * 4096), h1 = trread(lds + acur + aoff1 + kk * 4096 + 512);
        f.a[kk][0] = v4u{l0.x, l0.y, h0.x, h0.y}; f.a[kk][1] = v4u{l1.x, l1.y, h1.x, h1.y};
#pragma unroll
        for (int n = 0; n < NSUB; ++n) f.b[kk][n] = *(LAS const v4u*)(lds + bcur + boff + n * 2048 + kk * 1024);
      }
      acur += 8192; if (acur == 81920) acur = 0;
      bcur += G1_BST; if (bcur == G1_NSLOT * G1_BST) bcur = 0;
      SCHED_BAR();
    };
    auto pin1 = [&]() {
#pragma unroll
      for (int n = 0; n < NSUB; ++n) asm volatile("" : "+v"(acc1[n][0]), "+v"(acc1[n][1]));
    };
#pragma unroll
    for (int ab = 0; ab < 2; ++ab) {
      asum[ab] = f32x4{0.f, 0.f, 0.f, 0.f}; asq[ab] = f32x4{0.f, 0.f, 0.f, 0.f};
#pragma unroll
      for (int n = 0; n < NS1; ++n) { acc2[n][ab] = f32x4{0.f, 0.f, 0.f, 0.f}; acc1[n][ab] = f32x4{0.f, 0.f, 0.f, 0.f}; }
    }
    const v4u ones = {0x3f803f80u, 0x3f803f80u, 0x3f803f80u, 0x3f803f80u};
    auto mm2 = [&](const Frags& f) {
#pragma unroll
      for (int kk = 0; kk < 2; ++kk)
#pragma unroll
        for (int ab = 0; ab < 2; ++ab) {
#pragma unroll
          for (int n = 0; n < NSUB; ++n) acc2[n][ab] = mfma16(f.a[kk][ab], f.b[kk][n], acc2[n][ab]);
          if (STATS) { asum[ab] = mfma16(f.a[kk][ab], ones, asum[ab]); asq[ab] = mfma16(f.a[kk][ab], f.a[kk][ab], asq[ab]); }
        }
    };
    auto mm1 = [&](const Frags& f) {
#pragma unroll
      for (int kk = 0; kk < 2; ++kk)
#pragma unroll
        for (int ab = 0; ab < 2; ++ab)
#pragma unroll
          for (int n = 0; n < NSUB; ++n) acc1[n][ab] = mfma16(f.b[kk][n], f.a[kk][ab], acc1[n][ab]);
    };
    Frags f0, f1;
    CBAR(); rd(f0);
    STAMP(5);
    const bool poller = STATS && G == 0;
    unsigned pc = 0;
    if (poller) pc = poll_issue<8>(ws + CTL_E1 + 65536, wg & 31, lane);
#pragma unroll 1
    for (int it = 0; it < 5; ++it) {
      if (poller && it == 2) poll_finish<8>(pc, ws + CTL_E1 + 2 * 65536, wg & 31, 128, lane);
      CBAR(); rd(f1); mm2(f0);
      if (poller && it == 0) poll_finish<8>(pc, ws + CTL_E1 + 65536, wg & 31, 256, lane);
      if (poller && it == 1) pc = poll_issue<8>(ws + CTL_E1 + 2 * 65536, wg & 31, lane);
      CBAR(); rd(f0); mm2(f1);
    }
    LAS f32x2* tab = (LAS f32x2*)(lds + G1_T_LN2);
    if (STATS) {
#pragma unroll
      for (int ab = 0; ab < 2; ++ab) {
        const float a0 = asum[ab][0], a1 = asum[ab][1], a2 = asum[ab][2], a3 = asum[ab][3];
        const float b0 = asq[ab][0], b1 = asq[ab][1], b2 = asq[ab][2], b3 = asq[ab][3];
        const float s1 = (p & 2) ? ((p & 1) ? a3 : a2) : ((p & 1) ? a1 : a0), s2 = (p & 2) ? ((p & 1) ? b3 : b2) : ((p & 1) ? b1 : b0);
        const float mean = s1 * (1.0f / D);
        float mb, rinv;
        ln_scalars(mean, fmaxf(s2 * (1.0f / D) - mean * mean, 0.f), mb, rinv);
        if (rr == q) tab[(G * 2 + ab) * 16 + i16] = f32x2{rinv, rinv * mb};
      }
    }
    {
      const int nb = G ? 3 : 0;
#pragma unroll
      for (int n = 0; n < 3; ++n) {
        const int row = 80 * j + 16 * (nb + n < 5 ? nb + n : 4) + 4 * q + rr;
        x2v[n] = trread(lds + g1_aoff(row, (wave >> 1) & 1, wave & 1, p));
      }
    }
    STAMP(6);
    CBAR(); rd(f1); mm1(f0);
    v4u zq[NS1];
    {
      LAS const float* ut = (LAS const float*)(lds + G1_T_U);
      float v[16];
#pragma unroll
      for (int e = 0; e < 16; ++e) v[e] = 0.f;
      if (NSUB > 0) {
        f32x2 rv[8];
#pragma unroll
        for (int e = 0; e < 8; ++e) rv[e] = tab[(G * 2 + (e >> 2)) * 16 + 4 * q + (e & 3)];
#pragma unroll
        for (int n = 0; n < NSUB; ++n) {
          const float c1 = ut[(n0 + n) * 16 + i16], c2 = ut[128 + (n0 + n) * 16 + i16];
          float z[8];
#pragma unroll
          for (int e = 0; e < 8; ++e) z[e] = gelu_f(__builtin_fmaf(rv[e].x, acc2[n][e >> 2][e & 3], __builtin_fmaf(-rv[e].y, c1, c2)));
          zq[n] = v4u{pkbf(z[0], z[1]), pkbf(z[2], z[3]), pkbf(z[4], z[5]), pkbf(z[6], z[7])};
#pragma unroll
          for (int e = 0; e < 4; ++e) {
            const float a = bflo(zq[n][e]), c = bfhi(zq[n][e]);
            v[4 * e] += a; v[4 * e + 1] = __builtin_fmaf(a, a, v[4 * e + 1]);
            v[4 * e + 2] += c; v[4 * e + 3] = __builtin_fmaf(c, c, v[4 * e + 3]);
          }
        }
        treduce_dpp<8, 0x140>(v, (i16 & 8) != 0);
        treduce_dpp<4, 0x141>(v, (i16 & 4) != 0);
        treduce_dpp<2, 0x4e>(v, (i16 & 2) != 0);
        treduce_dpp<1, 0xb1>(v, (i16 & 1) != 0);
      }
      ((LAS float*)(lds + G1_T_P))[ng * 128 + G * 64 + lane] = v[0];
    }
    CBAR(); pin1(); rd(f0); mm1(f1);
    GAS unsigned long long* gr = (GAS unsigned long long*)(ws + CTL_ST3) + (size_t)mt * 1024 + G * 32;
    if (STATS) {
      LAS const float* pp = (LAS const float*)(lds + G1_T_P) + G * 64 + lane;
      const float sv = (pp[0] + pp[128]) + (pp[256] + pp[384]);
      __hip_atomic_store(gr + (j * 2 + (lane & 1)) * 64 + (lane >> 1), (1ull << 32) | __float_as_uint(sv), RLX_AGENT);
    }
    CBAR(); pin1(); rd(f1); mm1(f0);
    CBAR(); pin1(); rd(f0); mm1(f1);
    unsigned long long gx[8];
    if (STATS) {
#pragma unroll
      for (int k = 0; k < 8; ++k) gx[k] = __hip_atomic_load(gr + (k * 2 + (lane >> 5)) * 64 + (lane & 31), RLX_AGENT);
    }
    CBAR(); pin1(); rd(f1); mm1(f0);
    if (STATS) {
      float sv;
      for (unsigned spin = 0;; ++spin) {
        bool ok = true;
        sv = 0.f;
#pragma unroll
        for (int k = 0; k < 8; ++k) { ok &= (gx[k] >> 32) == 1ull; sv += __uint_as_float((unsigned)gx[k]); }
        if (__all(ok) || spin > SPIN_CAP) break;
        __builtin_amdgcn_s_sleep(1);
#pragma unroll
        for (int k = 0; k < 8; ++k) gx[k] = __hip_atomic_load(gr + (k * 2 + (lane >> 5)) * 64 + (lane & 31), RLX_AGENT);
      }
      const float s2 = __shfl_xor(sv, 32);
      const float mean = sv * (1.0f / FH);
      float mb, rinv;
      ln_scalars(mean, fmaxf(s2 * (1.0f / FH) - mean * mean, 0.f), mb, rinv);
      if (lane < 32) ((LAS f32x2*)(lds + G1_T_Z))[32 * G + lane] = f32x2{mb, rinv};
    }
    CBAR(); pin1(); rd(f0); mm1(f1);
    if (NSUB > 0) {
      f32x2 zs[8];
#pragma unroll
      for (int e = 0; e < 8; ++e) zs[e] = ((LAS const f32x2*)(lds + G1_T_Z))[32 * G + 8 * q + e];
#pragma unroll
      for (int n = 0; n < NSUB; ++n) {
        v4u o;
#pragma unroll
        for (int e = 0; e < 4; ++e)
          o[e] = pkbf(bfr(bflo(zq[n][e]) - zs[2 * e].x) * zs[2 * e].y, bfr(bfhi(zq[n][e]) - zs[2 * e + 1].x) * zs[2 * e + 1].y);
        st128(rs, (unsigned)WS_Z2 + (((sub0 + n0 + n) * 64 + mt * 2 + G) * 64 + lane) * 16, o);
      }
    }
    CBAR(); pin1(); rd(f1); mm1(f0);
    CBAR(); pin1(); rd(f0); mm1(f1);
    CBAR(); pin1(); rd(f1); mm1(f0);
    if (wave_arrive(lds, 3, 8, lane)) signal<4, 4>(ws + CTL_E2 + j * 4096, mt & 3, lane);
    CBAR(); pin1();
    mm1(f1);
    STAMP(7);
    {
      LAS const float* ut = (LAS const float*)(lds + G1_T_U) + 256;
      f32x2 rv[2];
#pragma unroll
      for (int ab = 0; ab < 2; ++ab) rv[ab] = tab[(G * 2 + ab) * 16 + i16];
#pragma unroll
      for (int n = 0; n < NSUB; ++n) {
        const f32x4 u1 = *(LAS const f32x4*)(ut + (n0 + n) * 16 + 4 * q), u2 = *(LAS const f32x4*)(ut + 128 + (n0 + n) * 16 + 4 * q);
#pragma unroll
        for (int ab = 0; ab < 2; ++ab) {
          float z[4];
#pragma unroll
          for (int e = 0; e < 4; ++e) z[e] = gelu_f(__builtin_fmaf(rv[ab].x, acc1[n][ab][e], __builtin_fmaf(-rv[ab].y, u1[e], u2[e])));
          z1v[n][ab] = v2u{pkbf(z[0], z[1]), pkbf(z[2], z[3])};
        }
      }
    }
  }
  if (wave == 3) wait_count<4>(ws + CTL_E2 + j * 4096, (mt >> 2) & 3, 32, lane);
  CBAR();
  STAMP(9);
  {
    f32x4 acc[NS1][2];
#pragma unroll
    for (int n = 0; n < NS1; ++n) { acc[n][0] = f32x4{0.f, 0.f, 0.f, 0.f}; acc[n][1] = f32x4{0.f, 0.f, 0.f, 0.f}; }
    int woff[2];
#pragma unroll
    for (int kk = 0; kk < 2; ++kk) woff[kk] = G * 4096 + i16 * 128 + (((4 * kk + q) ^ (i16 >> 1)) << 4);
    const int zoff = G2_ZOFF + n0 * 2048 + lane * 16;
    struct Frags { v4u w[2][2]; v4u z[2][NS1]; };
    int wc = 0, zc = 0;
    auto rd = [&](Frags& f) {
      if (NSUB > 0) {
#pragma unroll
        for (int kk = 0; kk < 2; ++kk) {
#pragma unroll
          for (int m = 0; m < 2; ++m) f.w[kk][m] = *(LAS const v4u*)(lds + wc + woff[kk] + m * 2048);
#pragma unroll
          for (int n = 0; n < NSUB; ++n) f.z[kk][n] = *(LAS const v4u*)(lds + zc + zoff + n * 2048 + kk * 1024);
        }
        wc += 8192; if (wc == G2_NW * 8192) wc = 0;
        zc += G2_ZST; if (zc == G2_NZ * G2_ZST) zc = 0;
      }
      SCHED_BAR();
    };
    auto mm = [&](const Frags& f) {
#pragma unroll
      for (int kk = 0; kk < 2; ++kk)
#pragma unroll
        for (int n = 0; n < NSUB; ++n)
#pragma unroll
          for (int m = 0; m < 2; ++m) acc[n][m] = mfma16(f.z[kk][n], f.w[kk][m], acc[n][m]);
    };
    Frags f0, f1;
    CBAR(); rd(f0);
    STAMP(10);
#pragma unroll 1
    for (int it = 0; it < 15; ++it) {
      CBAR(); rd(f1); mm(f0);
      CBAR(); rd(f0); mm(f1);
    }
    CBAR(); rd(f1); mm(f0);
    mm(f1);
    STAMP(11);
#pragma unroll
    for (int m = 0; m < 2; ++m) {
      v2u o[NS1];
#pragma unroll
      for (int n = 0; n < NSUB; ++n) {
        float g[4];
#pragma unroll
        for (int e = 0; e < 4; ++e) g[e] = bfr(bfr(acc[n][m][e]) + bias[m]);
        const v2u zz = z1v[n][m];
        o[n] = v2u{pkbf(bflo(zz.x) * g[0], bfhi(zz.x) * g[1]), pkbf(bflo(zz.y) * g[2], bfhi(zz.y) * g[3])};
      }
      const unsigned vb = (unsigned)WS_V + (mt * 4 + G * 2 + m) * 27 * 1024 + lane * 16;
      if (NSUB == 2) st128(rs, vb + (3 * j + ng) * 1024, v4u{o[0].x, o[0].y, o[NS1 - 1].x, o[NS1 - 1].y});
      if (NSUB == 1) __builtin_amdgcn_raw_buffer_store_b64(o[0], rs, vb + (24 + (j >> 1)) * 1024 + (j & 1) * 8, 0, 16);
    }
    CBAR();
    if (wave_arrive(lds, 4, 8, lane)) signal<1, 1>(ws + CTL_E3 + mt * 256, 0, lane);
  }
}

template <int KH>
__device__ __forceinline__ void consumer_g3(LAS char* lds, char* ws, __amdgpu_buffer_rsrc_t rs, u16* __restrict__ out, int mt, int j, int m, int lane, const v2u (&x2v)[3] STAMP_ARG) {
  constexpr int K0 = KH ? 14 : 0, NK = KH ? 13 : 14, NF = KH ? 2 : 3, N0 = KH ? 3 : 0;
  const int q = lane >> 4, i16 = lane & 15;
  v4u vf[NK];
#pragma unroll
  for (int k = 0; k < NK; ++k) vf[k] = ld128(rs, (unsigned)WS_V + ((mt * 4 + m) * 27 + K0 + k) * 1024 + lane * 16);
  SCHED_BAR();
  f32x4 acc[5];
#pragma unroll
  for (int n = 0; n < 5; ++n) acc[n] = f32x4{0.f, 0.f, 0.f, 0.f};
  LAS const char* wp = lds + K0 * 1024 + lane * 16;
  LAS const char* wp2 = wp + 54 * 1024;
  LAS const char* wp4 = wp + 108 * 1024;
  v4u wf[3][5];
  auto rdw = [&](int k, v4u (&w)[5]) {
    w[0] = *(LAS const v4u*)(wp + k * 1024); w[1] = *(LAS const v4u*)(wp + (27 + k) * 1024);
    w[2] = *(LAS const v4u*)(wp2 + k * 1024); w[3] = *(LAS const v4u*)(wp2 + (27 + k) * 1024);
    w[4] = *(LAS const v4u*)(wp4 + k * 1024);
  };
  rdw(0, wf[0]); rdw(1, wf[1]);
#pragma unroll
  for (int k = 0; k < NK; ++k) {
    if (k + 2 < NK) rdw(k + 2, wf[(k + 2) % 3]);
    SCHED_BAR();
#pragma unroll
    for (int n = 0; n < 5; ++n) acc[n] = mfma16(wf[k % 3][n], vf[k], acc[n]);
    SCHED_BAR();
  }
  STAMP(13);
  f32x4 gb[NF];
#pragma unroll
  for (int n = 0; n < NF; ++n) gb[n] = *(LAS const f32x4*)(lds + G3_GB + (16 * (N0 + n) + 4 * q) * 4);
  LAS char* red = lds + G3_RED + m * 5120 + lane * 16;
#pragma unroll
  for (int n = 0; n < 5; ++n) if ((n >= 3) != (KH != 0)) *(LAS f32x4*)(red + n * 1024) = acc[n];
  CBAR();
  const int t = mt * 64 + 32 * (m >> 1) + 8 * (i16 >> 2) + 4 * (m & 1) + (i16 & 3);
#pragma unroll
  for (int n = 0; n < NF; ++n) {
    const f32x4 o = *(LAS const f32x4*)(red + (N0 + n) * 1024);
    const f32x4 a = KH ? o + acc[N0 + n] : acc[N0 + n] + o;
    const float o0 = bflo(x2v[n].x) + bfr(gb[n][0] * bfr(a[0])), o1 = bfhi(x2v[n].x) + bfr(gb[n][1] * bfr(a[1]));
    const float o2 = bflo(x2v[n].y) + bfr(gb[n][2] * bfr(a[2])), o3 = bfhi(x2v[n].y) + bfr(gb[n][3] * bfr(a[3]));
    *(v2u*)((char*)out + ((size_t)t * D + 80 * j + 16 * (N0 + n) + 4 * q) * 2) = v2u{pkbf(o0, o1), pkbf(o2, o3)};
  }
}

__global__ __launch_bounds__(768, 3) void k_all(Args A) {
  extern __shared__ __attribute__((aligned(16))) char smem[];
  LAS char* lds = (LAS char*)smem;
  char* ws = A.ws;
  const int tid = threadIdx.x, lane = tid & 63, wave = __builtin_amdgcn_readfirstlane(tid >> 6), wg = blockIdx.x;
  STAMP_DECL; STAMP(0);
  const __amdgpu_buffer_rsrc_t rs = __builtin_amdgcn_make_buffer_rsrc((void*)ws, 0, (int)WS_END, 0x00020000);
  unsigned tag = 0;
  if (tid < 16) ((LAS unsigned*)(lds + M_CTL))[tid] = 0u;
  {
    const int grp = wave >> 2, bt = grp == 2 ? 0 : grp + 1, d = wg + 256 * bt;
    LAS char* gl = lds + (grp == 2 ? G1_TAB - C_GRP : grp * C_GRP);
    if (bt == 0) __builtin_amdgcn_s_setprio(2);
    if (bt == 1) __builtin_amdgcn_s_setprio(1);
    if (d < D) conv_group(gl, lds, bt, A, rs, tag, wg, 256 * bt + (wg & 7) * (bt < 2 ? 32 : 16) + (wg >> 3), tid & 255, wave & 3, lane STAMP_PASS);
    else { __builtin_amdgcn_s_setprio(3); aux_group(gl, lds, A, rs, d - D, tid & 255, wave & 3, lane STAMP_PASS); }
    __builtin_amdgcn_s_setprio(0);
  }
  const int xcd = wg & 7, idx = wg >> 3;
  const int mt = (xcd >> 1) * 8 + (idx & 7), j = (xcd & 1) * 4 + (idx >> 3);
  const int cnt = j < 6 ? 7 : 6, sub0 = j < 6 ? 7 * j : 42 + 6 * (j - 6);
  if (wave >= 8) {
    STAMP(4);
    const int lw = wave - 8;
    if (lw + 12 < 2 * cnt) loader_path<4>(lds, ws, A.wsp, wg, mt, j, sub0, lw, lane);
    else loader_path<3>(lds, ws, A.wsp, wg, mt, j, sub0, lw, lane);
    if (wg == 0 && tid == 512) *(volatile unsigned*)(ws + CTL_EPOCH) = tag;
    STAMP_FLUSH(1);
    return;
  }
  v2u x2v[3];
  const int ng = wave >> 2 ? 3 - (wave & 3) : (wave & 3);
  if (ng < 3) consumer_g12<2, false>(lds, ws, rs, A.bsp, wg, mt, j, sub0, cnt, wave, tid, lane, x2v STAMP_PASS);
  else if (cnt == 7) consumer_g12<1, true>(lds, ws, rs, A.bsp, wg, mt, j, sub0, cnt, wave, tid, lane, x2v STAMP_PASS);
  else consumer_g12<0, true>(lds, ws, rs, A.bsp, wg, mt, j, sub0, cnt, wave, tid, lane, x2v STAMP_PASS);
  if (wave == 1 || wave == 2) {
    const int c = tid - 64;
    if (c < 80) ((LAS float*)(lds + G3_GB))[c] = ld32f(rs, (unsigned)WS_MOD + (5 * D + 80 * j + c) * 8);
  }
  if (wave == 0) wait_count<1>(ws + CTL_E3 + mt * 256, 0, 8, lane);
  CBAR();
  STAMP(12);
  if (wave < 4) consumer_g3<0>(lds, ws, rs, A.out, mt, j, wave & 3, lane, x2v STAMP_PASS);
  else consumer_g3<1>(lds, ws, rs, A.out, mt, j, wave & 3, lane, x2v STAMP_PASS);
  STAMP(14);
  STAMP_FLUSH(1);
}

extern "C" void kernel_launch(void* const* d_in, const int* in_sizes, int n_in, void* d_out, int out_size, void* d_ws, size_t ws_size,
                              hipStream_t stream) {
  Args A;
  A.x = (const u16*)d_in[0]; A.Are = (const u16*)d_in[1]; A.Aim = (const u16*)d_in[2]; A.Bre = (const u16*)d_in[3]; A.Bim = (const u16*)d_in[4];
  A.Cre = (const u16*)d_in[5]; A.Cim = (const u16*)d_in[6]; A.logdt = (const u16*)d_in[7]; A.dskip = (const u16*)d_in[8];
  A.win = (const u16*)d_in[10]; A.wsp = (const u16*)d_in[11]; A.bsp = (const u16*)d_in[12]; A.wout = (const u16*)d_in[13];
  A.cvec = (const u16*)d_in[14]; A.wmod = (const u16*)d_in[15];
  A.ws = (char*)d_ws; A.out = (u16*)d_out;
  (void)hipFuncSetAttribute((const void*)k_all, hipFuncAttributeMaxDynamicSharedMemorySize, M_LDS);
  k_all<<<256, 768, M_LDS, stream>>>(A);
}
```

```cpp
#include <hip/hip_runtime.h>
#include <hip/hip_bf16.h>
#include <stdint.h>

#define GAS __attribute__((address_space(1)))
#define LAS __attribute__((address_space(3)))
typedef unsigned v4u __attribute__((ext_vector_type(4)));
typedef unsigned v2u __attribute__((ext_vector_type(2)));
typedef float f32x2 __attribute__((ext_vector_type(2)));
typedef float f32x4 __attribute__((ext_vector_type(4)));
typedef float f32x16 __attribute__((ext_vector_type(16)));
typedef __bf16 bf16x2 __attribute__((ext_vector_type(2)));
typedef __bf16 bf16x8 __attribute__((ext_vector_type(8)));
typedef _Float16 f16x2 __attribute__((ext_vector_type(2)));
typedef _Float16 f16x8 __attribute__((ext_vector_type(8)));
typedef short s16x4 __attribute__((ext_vector_type(4)));
typedef unsigned short u16;

constexpr int L = 2048, D = 640, NS = 128, F = 1728, FH = 864;

constexpr size_t WS_CTL = 0;
constexpr size_t CTL_E1 = 0;
constexpr size_t CTL_E1B = 196608;
constexpr size_t CTL_E2 = 262144;
constexpr size_t CTL_E3 = 294912;
constexpr size_t CTL_ST3 = 327680;
constexpr size_t CTL_BYTES = CTL_ST3 + 262144;
constexpr size_t WS_MOD = CTL_BYTES;
constexpr size_t WS_ST1 = WS_MOD + 16384;
constexpr size_t WS_U = WS_ST1 + 16384;
constexpr size_t WS_XT = WS_U + 65536;
constexpr size_t WS_K = WS_XT + (size_t)D * L * 2;
constexpr size_t WS_X2T = WS_K + (size_t)D * L * 2;
constexpr size_t WS_WIN = WS_X2T + (size_t)D * L * 2;
constexpr size_t WS_WOUT = WS_WIN + (size_t)D * F * 2;
constexpr size_t WS_Z2 = WS_WOUT + (size_t)FH * D * 2;
constexpr size_t WS_V = WS_Z2 + (size_t)L * FH * 2;
constexpr size_t WS_END = WS_V + (size_t)L * FH * 2;

__device__ __forceinline__ float bflo(unsigned u) { return __uint_as_float(u << 16); }
__device__ __forceinline__ float bfhi(unsigned u) { return __uint_as_float(u & 0xffff0000u); }
__device__ __forceinline__ float bf2f(u16 v) { return __uint_as_float((unsigned)v << 16); }
__device__ __forceinline__ unsigned pkbf(float a, float b) {
  f32x2 v = {a, b};
  return __builtin_bit_cast(unsigned, __builtin_convertvector(v, bf16x2));
}
__device__ __forceinline__ unsigned pkh(float a, float b) {
  f32x2 v = {a, b};
  return __builtin_bit_cast(unsigned, __builtin_convertvector(v, f16x2));
}
__device__ __forceinline__ float bfr(float a) { return bflo(pkbf(a, a)); }
__device__ __forceinline__ v2u trread(LAS const char* p) {
  return __builtin_bit_cast(v2u, __builtin_amdgcn_ds_read_tr16_b64_v4i16((LAS s16x4*)p));
}
__device__ __forceinline__ f32x4 mfma16(v4u a, v4u b, f32x4 c) {
  return __builtin_amdgcn_mfma_f32_16x16x32_bf16(__builtin_bit_cast(bf16x8, a), __builtin_bit_cast(bf16x8, b), c, 0, 0, 0);
}
__device__ __forceinline__ f32x16 mfma32(v4u a, v4u b, f32x16 c) {
  return __builtin_amdgcn_mfma_f32_32x32x16_bf16(__builtin_bit_cast(bf16x8, a), __builtin_bit_cast(bf16x8, b), c, 0, 0, 0);
}
__device__ __forceinline__ f32x16 mfma32h(v4u a, v4u b, f32x16 c) {
  return __builtin_amdgcn_mfma_f32_32x32x16_f16(__builtin_bit_cast(f16x8, a), __builtin_bit_cast(f16x8, b), c, 0, 0, 0);
}
template <bool DEV>
__device__ __forceinline__ void glds16(const void* sbase, unsigned voff, unsigned dst) {
  unsigned keep;
  if (DEV) asm volatile("s_mov_b32 %0, m0\n\ts_mov_b32 m0, %3\n\ts_nop 0\n\tglobal_load_lds_dwordx4 %1, %2 sc1\n\ts_mov_b32 m0, %0"
                        : "=&s"(keep) : "v"(voff), "s"(sbase), "s"(dst) : "memory");
  else asm volatile("s_mov_b32 %0, m0\n\ts_mov_b32 m0, %3\n\ts_nop 0\n\tglobal_load_lds_dwordx4 %1, %2\n\ts_mov_b32 m0, %0"
                    : "=&s"(keep) : "v"(voff), "s"(sbase), "s"(dst) : "memory");
}
template <int N> __device__ __forceinline__ void waitv() { asm volatile("s_waitcnt vmcnt(%0)" :: "i"(N) : "memory"); }
__device__ __forceinline__ float dot2bf(unsigned a, unsigned b, float c) {
  return __builtin_amdgcn_fdot2_f32_bf16(__builtin_bit_cast(bf16x2, a), __builtin_bit_cast(bf16x2, b), c, false);
}
#define SCHED_BAR() __builtin_amdgcn_sched_barrier(0)
#define STAMP_DECL
#define STAMP(i) do {} while (0)
#define STAMP_FLUSH(kid) do {} while (0)
#define STAMP_ARG
#define STAMP_PASS
#define CBAR() do { SCHED_BAR(); __builtin_amdgcn_s_waitcnt(0xC07F); asm volatile("" ::: "memory"); __builtin_amdgcn_s_barrier(); asm volatile("" ::: "memory"); } while (0)
#define LBAR() __builtin_amdgcn_s_barrier()

template <int CTRL> __device__ __forceinline__ float dppf(float v) {
  return __builtin_bit_cast(float, __builtin_amdgcn_update_dpp(0, __builtin_bit_cast(int, v), CTRL, 0xf, 0xf, true));
}
template <int N, int CTRL> __device__ __forceinline__ void treduce_dpp(float* v, bool up) {
#pragma unroll
  for (int e = 0; e < N; ++e) {
    const float a = v[e], b = v[e + N];
    const float send = up ? a : b, keep = up ? b : a;
    v[e] = keep + dppf<CTRL>(send);
  }
}
template <int N> __device__ __forceinline__ void treduce_xor(float* v, bool up, int mask) {
#pragma unroll
  for (int e = 0; e < N; ++e) {
    const float a = v[e], b = v[e + N];
    const float send = up ? a : b, keep = up ? b : a;
    v[e] = keep + __shfl_xor(send, mask);
  }
}

__device__ __forceinline__ float ln_std(float var) { return __builtin_amdgcn_sqrtf(bfr(bfr(var) + 1e-5f)); }
__device__ __forceinline__ void ln_scalars(float mean, float var, float& m_b, float& rinv) {
  m_b = bfr(mean);
  rinv = __builtin_amdgcn_rcpf(bfr(ln_std(var)));
}

struct cplx { float r, i; };
__device__ __forceinline__ cplx cmul(cplx a, cplx b) { return {a.r * b.r - a.i * b.i, a.r * b.i + a.i * b.r}; }

constexpr int K1_KG = 320, K1_LN = 64, K1_MOD = 60, K1_WO = 27;
constexpr int K1_GRID = K1_KG + K1_LN + K1_MOD + K1_WO;
constexpr int TP = 1312;
constexpr int K1_LDS = 4 * 16384 + 4 * 2576;

__global__ __launch_bounds__(256) void k1_prep(const u16* __restrict__ x, const u16* __restrict__ Are, const u16* __restrict__ Aim,
                                               const u16* __restrict__ Bre, const u16* __restrict__ Bim, const u16* __restrict__ Cre,
                                               const u16* __restrict__ Cim, const u16* __restrict__ logdt, const u16* __restrict__ wout,
                                               const u16* __restrict__ cvec, const u16* __restrict__ wmod, char* __restrict__ ws) {
  __shared__ __attribute__((aligned(16))) char smem[K1_LDS];
  LAS char* lds = (LAS char*)smem;
  const int tid = threadIdx.x, lane = tid & 63, wave = __builtin_amdgcn_readfirstlane(tid >> 6);
  int b = blockIdx.x;
  STAMP_DECL; STAMP(0);
  if (b < K1_KG) {
    const int d = 2 * b + (wave >> 1), n = (wave & 1) * 64 + lane, o = d * NS + n;
    const float are = bf2f(Are[o]), aim = bf2f(Aim[o]), bre = bf2f(Bre[o]), bim = bf2f(Bim[o]), cre = bf2f(Cre[o]), cim = bf2f(Cim[o]);
    const float dt = bfr(expf(bf2f(logdt[d])));
    const float ar2 = dt * are * 1.4426950408889634f, ai2 = dt * aim * 0.15915494309189535f;
    cplx W = cmul(cplx{cre, cim}, cplx{dt * bre, dt * bim});
    STAMP(1);
    float mx = fmaxf(fabsf(W.r), fabsf(W.i));
#pragma unroll
    for (int s = 1; s < 64; s <<= 1) mx = fmaxf(mx, __shfl_xor(mx, s));
    int ex = (int)((__float_as_uint(mx) >> 23) & 0xff);
    ex = ex < 20 ? 20 : (ex > 240 ? 240 : ex);
    const float sc = __uint_as_float((unsigned)(267 - ex) << 23);
    const float isc = __uint_as_float((unsigned)(ex - 13) << 23);
    W.r *= sc; W.i *= sc;
    auto E = [&](float xx) {
      float mag = __builtin_amdgcn_exp2f(ar2 * xx), ph = __builtin_amdgcn_fractf(ai2 * xx);
      return cplx{mag * __builtin_amdgcn_cosf(ph), mag * __builtin_amdgcn_sinf(ph)};
    };
    LAS char* Qi = lds + wave * 16384;
    LAS char* Hi = Qi + 8192;
    const int wbase = lane * 128, sw = lane & 7;
    {
      cplx e8 = E(8.f), e64 = E(64.f), e128 = E(128.f), e192 = E(192.f);
      cplx q[8];
      q[0] = {1.f, 0.f}; q[1] = e8; q[2] = cmul(e8, e8); q[3] = cmul(q[2], e8); q[4] = cmul(q[2], q[2]);
      q[5] = cmul(q[4], e8); q[6] = cmul(q[4], q[2]); q[7] = cmul(q[4], q[3]);
#pragma unroll
      for (int g = 0; g < 4; ++g) {
        cplx v[8];
#pragma unroll
        for (int j = 0; j < 8; ++j) v[j] = g == 0 ? q[j] : cmul(q[j], g == 1 ? e64 : (g == 2 ? e128 : e192));
        v4u re = {pkh(v[0].r, v[1].r), pkh(v[2].r, v[3].r), pkh(v[4].r, v[5].r), pkh(v[6].r, v[7].r)};
        v4u im = {pkh(v[0].i, v[1].i), pkh(v[2].i, v[3].i), pkh(v[4].i, v[5].i), pkh(v[6].i, v[7].i)};
        *(LAS v4u*)(Qi + wbase + ((g ^ sw) << 4)) = re;
        *(LAS v4u*)(Qi + wbase + (((4 + g) ^ sw) << 4)) = im;
      }
    }
    {
      cplx e1 = E(1.f), e2 = cmul(e1, e1), e4 = cmul(e2, e2);
      cplx h[24];
      h[0] = W; h[1] = cmul(W, e1); h[2] = cmul(W, e2); h[3] = cmul(h[2], e1);
      h[4] = cmul(W, e4); h[5] = cmul(h[4], e1); h[6] = cmul(h[4], e2); h[7] = cmul(h[6], e1);
      cplx g1 = E(256.f), g2 = E(512.f), g3 = E(768.f), g4 = E(1024.f);
      h[8] = cmul(h[0], g1); h[9] = cmul(h[2], g1); h[10] = cmul(h[4], g1); h[11] = cmul(h[6], g1);
      h[12] = cmul(h[0], g2); h[13] = cmul(h[4], g2); h[14] = cmul(h[0], g3); h[15] = cmul(h[4], g3);
      h[16] = cmul(h[0], g4); h[17] = cmul(h[8], g4); h[18] = cmul(h[12], g4); h[19] = cmul(h[14], g4);
      h[20] = cmul(h[16], g4); h[21] = {0.f, 0.f}; h[22] = {0.f, 0.f}; h[23] = {0.f, 0.f};
#pragma unroll
      for (int g = 0; g < 3; ++g) {
        const cplx* v = h + 8 * g;
        v4u re = {pkh(v[0].r, v[1].r), pkh(v[2].r, v[3].r), pkh(v[4].r, v[5].r), pkh(v[6].r, v[7].r)};
        v4u im = {pkh(-v[0].i, -v[1].i), pkh(-v[2].i, -v[3].i), pkh(-v[4].i, -v[5].i), pkh(-v[6].i, -v[7].i)};
        *(LAS v4u*)(Hi + wbase + ((g ^ sw) << 4)) = re;
        *(LAS v4u*)(Hi + wbase + (((4 + g) ^ sw) << 4)) = im;
      }
      v4u z = {0u, 0u, 0u, 0u};
      *(LAS v4u*)(Hi + wbase + ((3 ^ sw) << 4)) = z;
      *(LAS v4u*)(Hi + wbase + ((7 ^ sw) << 4)) = z;
    }
    STAMP(2);
    __syncthreads();
    f32x16 acc = {};
    {
      const int hh = lane >> 5, blk = (lane >> 4) & 1, qq = (lane & 15) >> 2, p = lane & 3;
#pragma unroll
      for (int ks = 0; ks < 8; ++ks) {
        v2u a[2], bb[2];
#pragma unroll
        for (int t = 0; t < 2; ++t) {
          const int k = 16 * ks + 8 * hh + 4 * t + qq, mode = k >> 1, c = k & 1;
          const int off = mode * 128 + (((c * 4 + 2 * blk + (p >> 1)) ^ (mode & 7)) << 4) + (p & 1) * 8;
          a[t] = trread(Hi + off);
          bb[t] = trread(Qi + off);
        }
        acc = mfma32h(v4u{a[0].x, a[0].y, a[1].x, a[1].y}, v4u{bb[0].x, bb[0].y, bb[1].x, bb[1].y}, acc);
      }
    }
    LAS float* T = (LAS float*)(lds + 65536 + wave * 2576);
    {
      const int q = lane & 31, hi = lane >> 5;
#pragma unroll
      for (int r = 0; r < 4; ++r) T[(r + 4 * hi) + 8 * q] = acc[r] * isc;
      if (hi == 0) {
#pragma unroll
        for (int r = 0; r < 4; ++r) T[256 + r + 4 * q] = acc[4 + r] * isc;
#pragma unroll
        for (int r = 0; r < 4; ++r) T[512 + 32 * r + q] = acc[8 + r] * isc;
      } else {
#pragma unroll
        for (int r = 0; r < 4; ++r) T[384 + 64 * (r >> 1) + (r & 1) + 2 * q] = acc[4 + r] * isc;
        if (q == 0) T[640] = acc[8] * isc;
      }
    }
    STAMP(3);
    __syncthreads();
    {
      const LAS float* T0 = (const LAS float*)(lds + 65536 + (wave & 2) * 2576);
      const LAS float* T1 = T0 + 644;
      u16* Kd = (u16*)(ws + WS_K) + (size_t)d * L;
      const int th = tid & 127;
#pragma unroll
      for (int pc = 0; pc < 2; ++pc) {
        const int l0 = (th + 128 * pc) * 8;
        float kv[8];
#pragma unroll
        for (int j = 0; j < 8; ++j) {
          const int l = l0 + j;
          const int idx = l < 128 ? l : (int)(pkbf((float)l, 0.f) & 0xffffu) - 0x4300 + 128;
          kv[j] = T0[idx] + T1[idx];
        }
        *(v4u*)(Kd + l0) = v4u{pkbf(kv[0], kv[1]), pkbf(kv[2], kv[3]), pkbf(kv[4], kv[5]), pkbf(kv[6], kv[7])};
      }
    }
    STAMP(4);
    STAMP_FLUSH(0);
    return;
  }
  b -= K1_KG;
  if (b < K1_LN + K1_WO) {
    const bool isx = b < K1_LN;
    const int slab = isx ? b : b - K1_LN;
    const int r = tid >> 3, s = tid & 7;
    const u16* src;
    if (isx) src = x + (size_t)(slab * 32 + r) * D;
    else {
      const int half = (r >> 2) & 1, jj = slab < 24 ? slab / 3 : 2 * (slab - 24) + half;
      const int sub = (jj < 6 ? 7 * jj : 42 + 6 * (jj - 6)) + (slab < 24 ? 2 * (slab - 3 * jj) + half : 6);
      src = wout + (size_t)(32 * (sub >> 1) + 8 * (r >> 3) + 4 * (sub & 1) + (r & 3)) * D;
    }
    v4u v[10];
#pragma unroll
    for (int i = 0; i < 10; ++i) v[i] = *(const v4u*)(src + (s + 8 * i) * 8);
#pragma unroll
    for (int i = 0; i < 10; ++i) *(LAS v4u*)(lds + r * TP + (s + 8 * i) * 16) = v[i];
    STAMP(1);
    STAMP(2);
    __syncthreads();
    const int q = lane >> 4, rr = (lane & 15) >> 2, p = lane & 3, i16 = lane & 15;
    const int a0 = (8 * q + rr) * TP + p * 8;
#pragma unroll
    for (int j = 0; j < 10; ++j) {
      const int sub = wave + 4 * j;
      v2u lo = trread(lds + a0 + sub * 32), hi = trread(lds + a0 + 4 * TP + sub * 32);
      v4u o = {lo.x, lo.y, hi.x, hi.y};
      if (isx) *(v4u*)((u16*)(ws + WS_XT) + (size_t)(16 * sub + i16) * L + slab * 32 + 8 * q) = o;
      else *(v4u*)((u16*)(ws + WS_WOUT) + ((size_t)(sub * 27 + slab) * 64 + lane) * 8) = o;
    }
    STAMP(3);
    if (isx) {
      float s0 = 0.f, s1 = 0.f, q0 = 0.f, q1 = 0.f;
#pragma unroll
      for (int i = 0; i < 10; ++i) {
        s0 = dot2bf(v[i][0], 0x3f803f80u, s0); s1 = dot2bf(v[i][1], 0x3f803f80u, s1); s0 = dot2bf(v[i][2], 0x3f803f80u, s0); s1 = dot2bf(v[i][3], 0x3f803f80u, s1);
        q0 = dot2bf(v[i][0], v[i][0], q0); q1 = dot2bf(v[i][1], v[i][1], q1); q0 = dot2bf(v[i][2], v[i][2], q0); q1 = dot2bf(v[i][3], v[i][3], q1);
      }
      float sum = s0 + s1, sq = q0 + q1;
      sum += __shfl_xor(sum, 1); sq += __shfl_xor(sq, 1); sum += __shfl_xor(sum, 2); sq += __shfl_xor(sq, 2); sum += __shfl_xor(sum, 4); sq += __shfl_xor(sq, 4);
      const float mean = sum * (1.0f / D);
      const int t = slab * 32 + r;
      if (s == 0) ((unsigned*)(ws + WS_ST1))[((t >> 2) & 1) * 1024 + (t >> 3) * 4 + (t & 3)] = pkbf(mean, ln_std(fmaxf(sq * (1.0f / D) - mean * mean, 0.f)));
      ((v4u*)(ws + CTL_ST3))[slab * 256 + tid] = v4u{0u, 0u, 0u, 0u};
      if (tid < 20) *(unsigned*)(ws + WS_CTL + (slab * 20 + tid) * 256) = 0u;
    }
    STAMP(4);
    STAMP_FLUSH(0);
    return;
  }
  b -= K1_LN + K1_WO;
  {
    const int c0 = 64 * b, seg = tid & 7, rg = tid >> 3;
    v4u w[20];
#pragma unroll
    for (int i = 0; i < 20; ++i) w[i] = *(const v4u*)(wmod + (size_t)(rg + 32 * i) * (6 * D) + c0 + seg * 8);
    LAS float* sc = (LAS float*)lds;
    for (int k = tid; k < D; k += 256) {
      float cv = bf2f(cvec[k]);
      sc[k] = bfr(cv / (1.0f + expf(-cv)));
    }
    __syncthreads();
    STAMP(1);
    float acc[8] = {};
#pragma unroll
    for (int i = 0; i < 20; ++i) {
      const float s = sc[rg + 32 * i];
#pragma unroll
      for (int j = 0; j < 4; ++j) { acc[2 * j] += s * bflo(w[i][j]); acc[2 * j + 1] += s * bfhi(w[i][j]); }
    }
#pragma unroll
    for (int j = 0; j < 8; ++j) { acc[j] += __shfl_xor(acc[j], 8); acc[j] += __shfl_xor(acc[j], 16); acc[j] += __shfl_xor(acc[j], 32); }
    LAS float* red = (LAS float*)(lds + 4096);
    if (lane < 8) {
#pragma unroll
      for (int j = 0; j < 8; ++j) red[wave * 64 + lane * 8 + j] = acc[j];
    }
    __syncthreads();
    if (tid < 64) ((float*)(ws + WS_MOD))[c0 + tid] = bfr((red[tid] + red[64 + tid]) + (red[128 + tid] + red[192 + tid]));
    STAMP_FLUSH(0);
  }
}

typedef GAS unsigned gu32;
#define RLX_AGENT __ATOMIC_RELAXED, __HIP_MEMORY_SCOPE_AGENT
constexpr unsigned SPIN_CAP = 1u << 16;
constexpr int M_LDS = 163840, M_CTL = M_LDS - 64;
constexpr int C_KOFF = 0, C_UOFF = 4224, C_UP = 1536, C_COFF = C_UOFF + 4 * C_UP, C_GRP = C_COFF + 4 * 64 * 144;
constexpr int G1_BOFF = 81920, G1_BST = 14336, G1_NSLOT = 5, G1_TAB = G1_BOFF + G1_BST * G1_NSLOT;
constexpr int G1_T_LN2 = G1_TAB, G1_T_U = G1_T_LN2 + 512, G1_T_P = G1_T_U + 2048, G1_T_Z = G1_T_P + 2048;
constexpr int G2_NW = 9, G2_NZ = 6, G2_ZOFF = G2_NW * 8192, G2_ZST = 14336;
constexpr int G3_RED = 135 * 1024;
static_assert(3 * C_GRP <= M_CTL && G1_T_Z + 512 <= M_CTL && G2_ZOFF + G2_NZ * G2_ZST <= M_CTL && G3_RED + 20480 <= M_CTL, "lds");

__device__ __forceinline__ void st128(__amdgpu_buffer_rsrc_t rs, unsigned off, v4u v) { __builtin_amdgcn_raw_buffer_store_b128(v, rs, off, 0, 16); }
__device__ __forceinline__ v4u ld128(__amdgpu_buffer_rsrc_t rs, unsigned off) { return __builtin_amdgcn_raw_buffer_load_b128(rs, off, 0, 16); }

__device__ __forceinline__ bool wave_arrive(LAS char* lds, int word, unsigned n, int lane) {
  waitv<0>();
  unsigned old = 0;
  if (lane == 0) old = __hip_atomic_fetch_add((LAS unsigned*)(lds + M_CTL) + word, 1u, __ATOMIC_RELAXED, __HIP_MEMORY_SCOPE_WORKGROUP);
  return (unsigned)__builtin_amdgcn_readfirstlane(old) == n - 1;
}
__device__ __forceinline__ void group_sync(LAS char* lds0, int word, unsigned target, int lane) {
  SCHED_BAR();
  __builtin_amdgcn_s_waitcnt(0xC07F);
  asm volatile("" ::: "memory");
  LAS unsigned* p = (LAS unsigned*)(lds0 + M_CTL) + word;
  if (lane == 0) __hip_atomic_fetch_add(p, 1u, __ATOMIC_RELAXED, __HIP_MEMORY_SCOPE_WORKGROUP);
  while ((unsigned)__builtin_amdgcn_readfirstlane(__hip_atomic_load(p, __ATOMIC_RELAXED, __HIP_MEMORY_SCOPE_WORKGROUP)) < target) __builtin_amdgcn_s_sleep(1);
  asm volatile("" ::: "memory");
}
template <int NREP, int NSH> __device__ __forceinline__ void signal(char* base, int shard, int lane) {
  if (lane < NREP) __hip_atomic_fetch_add((gu32*)(base + (lane * NSH + shard) * 256), 1u, RLX_AGENT);
}
template <int NSH> __device__ __forceinline__ unsigned poll_issue(char* base, int rep, int lane) {
  return __hip_atomic_load((gu32*)(base + (rep * NSH + (lane & (NSH - 1))) * 256), RLX_AGENT);
}
template <int NSH> __device__ __forceinline__ void poll_finish(unsigned c, char* base, int rep, unsigned total, int lane) {
  for (unsigned spin = 0; spin < SPIN_CAP; ++spin) {
#pragma unroll
    for (int s = 1; s < NSH; s <<= 1) c += __shfl_xor(c, s);
    if ((unsigned)__builtin_amdgcn_readfirstlane(c) >= total) break;
    __builtin_amdgcn_s_sleep(1);
    c = poll_issue<NSH>(base, rep, lane);
  }
  __builtin_amdgcn_fence(__ATOMIC_ACQUIRE, "wavefront");
}
template <int NSH> __device__ __forceinline__ void wait_count(char* base, int rep, unsigned total, int lane) {
  poll_finish<NSH>(poll_issue<NSH>(base, rep, lane), base, rep, total, lane);
}

__device__ __forceinline__ float gelu_f(float x) {
  const float p = __builtin_fmaf(x * x, -0.044715f * 2.302208198f, -2.302208198f);
  const float e = __builtin_amdgcn_exp2f(x * p);
  return x * __builtin_amdgcn_rcpf(1.0f + e);
}

__device__ __forceinline__ void conv_group(LAS char* lds, LAS char* lds0, int bt, char* ws, __amdgpu_buffer_rsrc_t rs, const u16* __restrict__ dskip, const u16* __restrict__ wsp,
                                           int d, int tid, int wave, int lane STAMP_ARG) {
  const float* mod = (const float*)(ws + WS_MOD);
  const v4u kv = *(const v4u*)((const u16*)(ws + WS_K) + (size_t)d * L + tid * 8);
  const v4u xv = *(const v4u*)((const u16*)(ws + WS_XT) + (size_t)d * L + tid * 8);
  v4u st[2];
#pragma unroll
  for (int j = 0; j < 2; ++j) st[j] = ((const v4u*)(ws + WS_ST1))[j * 256 + tid];
  const float sa = mod[d], ca = mod[D + d], ga = mod[2 * D + d];
  const u16 dskv = dskip[d];
  CBAR();
  const float c1a = bfr(1.0f + ca), dsk = bf2f(dskv);
  if (tid < 4) *(LAS v4u*)(lds + C_KOFF + tid * 16) = v4u{0u, 0u, 0u, 0u};
  if (tid < 128) *(LAS v4u*)(lds + C_UOFF + (tid >> 5) * C_UP + (tid & 31) * 16) = v4u{0u, 0u, 0u, 0u};
  *(LAS v4u*)(lds + C_KOFF + 64 + tid * 16) = kv;
  float xf[8], h[8];
#pragma unroll
  for (int j = 0; j < 4; ++j) { xf[2 * j] = bflo(xv[j]); xf[2 * j + 1] = bfhi(xv[j]); }
#pragma unroll
  for (int j = 0; j < 8; ++j) {
    const float m = bflo(st[j >> 2][j & 3]), ri = __builtin_amdgcn_rcpf(bfhi(st[j >> 2][j & 3]));
    const float xn = bfr(bfr(xf[j] - m) * ri);
    h[j] = bfr(bfr(xn * c1a) + sa);
  }
#pragma unroll
  for (int e = 0; e < 4; ++e) *(LAS unsigned*)(lds + C_UOFF + e * C_UP + 512 + tid * 4) = pkbf(h[e], h[e + 4]);
#pragma unroll
  for (int j = 0; j < 8; ++j) h[j] = bfr(h[j] * dsk);
  SCHED_BAR();
  const unsigned pv = *(const unsigned*)((const char*)wsp + ((size_t)(d & 511) * 256 + tid) * 64);
  STAMP(1);
  group_sync(lds0, 5 + bt, 4, lane);
  f32x16 y0 = {}, y1 = {};
  {
    const int hh = lane >> 5, blk = (lane >> 4) & 1, qq = (lane & 15) >> 2, p = lane & 3, col = lane & 31;
    LAS const char* ab = lds + C_KOFF + (32 + 16 * blk + 4 * p - 4 * qq + 32 * hh) * 2 - 32;
    LAS const char* bb = lds + C_UOFF + wave * C_UP + (256 + 8 * (col - hh) - 240) * 2;
    struct CF { v2u a0l, a0h, a1l, a1h; v4u b0, b1; };
    CF cf[3];
    auto rdc = [&](int ks, CF& f) {
      f.a0l = trread(ab + ks * 128 + 32); f.a0h = trread(ab + ks * 128);
      f.b0 = *(LAS const v4u*)(bb + (15 - ks) * 32);
      f.b1 = *(LAS const v4u*)(bb + (31 - ks) * 32);
      f.a1l = trread(ab + (ks + 16) * 128 + 32); f.a1h = trread(ab + (ks + 16) * 128);
    };
    rdc(0, cf[0]); rdc(1, cf[1]);
#pragma unroll
    for (int ks = 0; ks < 16; ++ks) {
      if (ks + 2 < 16) rdc(ks + 2, cf[(ks + 2) % 3]);
      SCHED_BAR();
      const CF& f = cf[ks % 3];
      const v4u a0 = {f.a0l.x, f.a0l.y, f.a0h.x, f.a0h.y}, a1 = {f.a1l.x, f.a1l.y, f.a1h.x, f.a1h.y};
      y0 = mfma32(a0, f.b0, y0);
      y1 = mfma32(a0, f.b1, y1);
      y1 = mfma32(a1, f.b0, y1);
      SCHED_BAR();
    }
    LAS char* cb = lds + C_COFF + (wave * 64 + col) * 144 + hh * 16;
#pragma unroll
    for (int g = 0; g < 4; ++g) {
      *(LAS f32x4*)(cb + g * 32) = f32x4{y0[4 * g], y0[4 * g + 1], y0[4 * g + 2], y0[4 * g + 3]};
      *(LAS f32x4*)(cb + 32 * 144 + g * 32) = f32x4{y1[4 * g], y1[4 * g + 1], y1[4 * g + 2], y1[4 * g + 3]};
    }
  }
  STAMP(2);
  group_sync(lds0, 5 + bt, 8, lane);
  float y[8];
  {
    const int c1 = 2 * tid, c0 = tid ? c1 - 1 : 0;
    const int o0 = (c0 >> 3) * 144 + (c0 & 7) * 16, o1 = (c1 >> 3) * 144 + (c1 & 7) * 16;
    LAS const char* cb = lds + C_COFF;
    f32x4 w0[4], w1[4], w2[4];
#pragma unroll
    for (int e = 0; e < 4; ++e) {
      w1[e] = *(LAS const f32x4*)(cb + e * 64 * 144 + o1); w2[e] = *(LAS const f32x4*)(cb + e * 64 * 144 + o1 + 16);
      if (e) w0[e] = *(LAS const f32x4*)(cb + e * 64 * 144 + o0);
    }
    SCHED_BAR();
#pragma unroll
    for (int e = 0; e < 4; ++e) {
      float w[12];
#pragma unroll
      for (int j = 0; j < 4; ++j) { w[4 + j] = w1[e][j]; w[8 + j] = w2[e][j]; w[j] = (e && tid) ? w0[e][j] : 0.f; }
#pragma unroll
      for (int j = 0; j < 8; ++j) y[j] = e ? y[j] + w[4 + j - e] : w[4 + j];
    }
  }
  float o[8];
#pragma unroll
  for (int j = 0; j < 8; ++j) {
    const float yy = bfr(bfr(y[j]) + h[j]);
    o[j] = xf[j] + bfr(ga * yy);
  }
  {
    const unsigned a0 = pkbf(o[0], o[1]), a1 = pkbf(o[2], o[3]), b0 = pkbf(o[4], o[5]), b1 = pkbf(o[6], o[7]);
    const bool odd = tid & 1;
    const unsigned s0 = odd ? a0 : b0, s1 = odd ? a1 : b1;
    const unsigned r0 = __builtin_amdgcn_update_dpp(0, s0, 0xb1, 0xf, 0xf, true), r1 = __builtin_amdgcn_update_dpp(0, s1, 0xb1, 0xf, 0xf, true);
    const v4u ov = odd ? v4u{r0, r1, b0, b1} : v4u{a0, a1, r0, r1};
    st128(rs, (unsigned)(WS_X2T + (size_t)d * (L * 2)) + (tid >> 2) * 64 + (tid & 1) * 32 + (tid & 2) * 8, ov);
  }
  STAMP(8);
  asm volatile("" :: "v"(pv));
  if (wave_arrive(lds0, bt, 4, lane)) signal<32, 8>(ws + CTL_E1 + bt * 65536, d & 7, lane);
}

__device__ __forceinline__ void wprep_group(LAS char* lds, LAS char* lds0, char* ws, __amdgpu_buffer_rsrc_t rs, const u16* __restrict__ win, int unit, int tid, int wave, int lane) {
  const float* mod = (const float*)(ws + WS_MOD);
  const int cbk = unit >> 2, part = unit & 3;
  const int c0 = 64 * cbk, r0 = 160 * part, seg = tid & 7, rg = tid >> 3;
  v4u w[5];
  float cbv[5], sbv[5];
#pragma unroll
  for (int i = 0; i < 5; ++i) {
    w[i] = *(const v4u*)(win + (size_t)(r0 + rg + 32 * i) * F + c0 + seg * 8);
    cbv[i] = mod[4 * D + r0 + rg + 32 * i]; sbv[i] = mod[3 * D + r0 + rg + 32 * i];
  }
  CBAR();
  float v[16] = {};
  constexpr int WPITCH = 144;
#pragma unroll
  for (int i = 0; i < 5; ++i) {
    const float c1 = bfr(1.0f + cbv[i]), sb = sbv[i];
#pragma unroll
    for (int j = 0; j < 4; ++j) {
      const float a = bflo(w[i][j]), c = bfhi(w[i][j]);
      const unsigned pk = pkbf(a * c1, c * c1);
      v[8 + 2 * j] += sb * a; v[8 + 2 * j + 1] += sb * c;
      v[2 * j] += bflo(pk); v[2 * j + 1] += bfhi(pk);
      w[i][j] = pk;
    }
    *(LAS v4u*)(lds + (rg + 32 * i) * WPITCH + seg * 16) = w[i];
  }
  treduce_xor<8>(v, (lane & 32) != 0, 32);
  treduce_xor<4>(v, (lane & 16) != 0, 16);
  treduce_dpp<2, 0x128>(v, (lane & 8) != 0);
  LAS float* red = (LAS float*)(lds + 24576);
  {
    const int which = lane >> 5, j = ((lane >> 4) & 1) * 4 + ((lane >> 3) & 1) * 2;
    *(LAS f32x2*)(red + wave * 128 + which * 64 + seg * 8 + j) = f32x2{v[0], v[1]};
  }
  group_sync(lds0, 7, 4, lane);
  const int q = lane >> 4, rr = (lane & 15) >> 2, p = lane & 3;
  const int colb = (32 * (wave >> 1) + 8 * p + 4 * (wave & 1)) * 2;
#pragma unroll
  for (int i = 0; i < 5; ++i) {
    const int a0 = (32 * i + 8 * q + rr) * WPITCH + colb;
    v2u lo = trread(lds + a0), hi = trread(lds + a0 + 4 * WPITCH);
    st128(rs, (unsigned)WS_WIN + (((4 * cbk + wave) * 20 + part * 5 + i) * 64 + lane) * 16, v4u{lo.x, lo.y, hi.x, hi.y});
  }
  if (tid < 128) {
    const int which = tid >> 6, o = tid & 63, sub = o >> 4, i = o & 15;
    const int cl = 32 * (sub >> 1) + 8 * (i >> 2) + 4 * (sub & 1) + (i & 3), ix = which * 64 + cl;
    const float s = (red[ix] + red[128 + ix]) + (red[256 + ix] + red[384 + ix]);
    __builtin_amdgcn_raw_buffer_store_b32(__float_as_uint(s), rs, (unsigned)WS_U + ((part * 2 + which) * F + c0 + o) * 4, 0, 16);
  }
  if (wave_arrive(lds0, 2, 4, lane)) signal<32, 8>(ws + CTL_E1B, unit & 7, lane);
}

__device__ __forceinline__ int g1_aoff(int row, int G, int ab, int p) {
  const int f = ((row >> 1) & 1) | (((row >> 3) & 1) << 1);
  return row * 128 + (((4 * G + 2 * ab + (p >> 1)) ^ (f << 1)) << 4) + (p & 1) * 8;
}

template <int NB>
__device__ __forceinline__ void loader_path(LAS char* lds, char* ws, const u16* __restrict__ wsp, int wg, int mt, int j, int sub0, int lw, int lane) {
  const unsigned lbase = (unsigned)(size_t)lds, vo = lane * 16;
  {
    unsigned avo[2];
#pragma unroll
    for (int h = 0; h < 2; ++h) {
      const int row = lane >> 3, pos = lane & 7, f = ((row >> 1) & 1) | (h << 1);
      avo[h] = row * (L * 2) + ((pos ^ (f << 1)) << 4);
    }
    const char* asb = ws + WS_X2T + (size_t)(16 * lw) * (L * 2) + mt * 128;
    const char* bsb = ws + WS_WIN + (size_t)((sub0 + (lw >> 1)) * 20 + (lw & 1)) * 1024;
    const unsigned bb = lbase + G1_BOFF;
    auto issueB = [&](int s, unsigned slot) {
      const char* bs = bsb + (s < 10 ? 54 * 20 * 1024 + s * 2048 : (s - 10) * 2048);
#pragma unroll
      for (int i = 0; i < NB; ++i) glds16<true>(bs + i * 2 * 20 * 1024, vo, slot + (lw + 4 * i) * 1024);
    };
    auto issueA = [&](int s0, int s1) {
#pragma unroll 1
      for (int s = s0; s < s1; ++s)
#pragma unroll
        for (int h = 0; h < 2; ++h) glds16<true>(asb + (size_t)(s * 64 + h * 8) * (L * 2), avo[h], lbase + s * 8192 + lw * 2048 + h * 1024);
    };
    group_sync(lds, 5, 12, lane);
    wait_count<8>(ws + CTL_E1B, wg & 31, 108, lane);
#pragma unroll
    for (int s = 0; s < 4; ++s) issueB(s, bb + (s + 1) * G1_BST);
    LBAR();
    issueA(0, 4); issueB(4, bb);
    waitv<6 + NB>(); LBAR();
    waitv<4 + NB>(); LBAR(); issueB(5, bb + G1_BST);
    waitv<2 + 2 * NB>(); LBAR(); issueA(4, 8); issueB(6, bb + 2 * G1_BST);
    waitv<8 + 3 * NB>(); LBAR(); issueB(7, bb + 3 * G1_BST);
    waitv<6 + 2 * NB>(); LBAR(); issueB(8, bb + 4 * G1_BST);
    waitv<4 + 3 * NB>(); LBAR(); issueA(8, 10); issueB(9, bb);
    waitv<4 + 3 * NB>(); LBAR(); issueB(10, bb + G1_BST);
    waitv<4 + 3 * NB>(); LBAR(); issueB(11, bb + 2 * G1_BST);
    waitv<2 + 3 * NB>(); LBAR(); issueB(12, bb + 3 * G1_BST);
    int nxt = 4 * G1_BST;
#pragma unroll 1
    for (int i = 9; i <= 15; ++i) {
      waitv<NB * 3>(); LBAR();
      issueB(i + 4, bb + nxt);
      nxt += G1_BST; if (nxt == G1_NSLOT * G1_BST) nxt = 0;
    }
    waitv<NB * 3>(); LBAR();
    waitv<NB * 2>(); LBAR();
    waitv<NB * 1>(); LBAR();
    waitv<0>(); LBAR();
    LBAR();
  }
  {
    unsigned wvo[2];
#pragma unroll
    for (int h = 0; h < 2; ++h) {
      const int i = 8 * h + (lane >> 3);
      wvo[h] = (8 * (i >> 2) + (i & 3)) * (L * 2) + (((lane & 7) ^ (i >> 1)) << 4);
    }
    const char* wsb = (const char*)wsp + (size_t)(mt * 64 + 32 * (lw >> 1) + 4 * (lw & 1)) * (L * 2);
    const char* zsb = ws + WS_Z2 + (size_t)((sub0 + (lw >> 1)) * 64 + (lw & 1)) * 1024;
    auto issueW = [&](int s, unsigned slot) {
      glds16<false>(wsb + s * 128, wvo[0], slot + lw * 2048);
      glds16<false>(wsb + s * 128, wvo[1], slot + lw * 2048 + 1024);
    };
    auto issueZ = [&](int s, unsigned slot) {
#pragma unroll
      for (int i = 0; i < NB; ++i) glds16<true>(zsb + i * 2 * 65536 + s * 2048, vo, slot + (lw + 4 * i) * 1024);
    };
    const unsigned zb = lbase + G2_ZOFF;
#pragma unroll 1
    for (int s = 0; s < G2_NW; ++s) issueW(s, lbase + s * 8192);
    LBAR();
#pragma unroll 1
    for (int s = 0; s < G2_NZ; ++s) issueZ(s, zb + s * G2_ZST);
    waitv<NB * 5>(); LBAR();
    int zn = 0, wn = 0;
    auto step = [&](int i, bool dz, bool dw) {
      if (dz) { issueZ(i + 5, zb + zn); zn += G2_ZST; if (zn == G2_NZ * G2_ZST) zn = 0; }
      if (dw) { issueW(i + 8, lbase + wn); wn += 8192; if (wn == G2_NW * 8192) wn = 0; }
    };
    waitv<NB * 4>(); LBAR(); step(1, true, true);
    waitv<NB * 4 + 2>(); LBAR(); step(2, true, true);
    waitv<NB * 4 + 4>(); LBAR(); step(3, true, true);
    waitv<NB * 4 + 6>(); LBAR(); step(4, true, true);
    waitv<NB * 4 + 8>(); LBAR(); step(5, true, true);
#pragma unroll 1
    for (int i = 6; i <= 23; ++i) { waitv<NB * 4 + 10>(); LBAR(); step(i, true, true); }
    waitv<NB * 4 + 10>(); LBAR(); step(24, true, false);
    waitv<NB * 4 + 8>(); LBAR(); step(25, true, false);
    waitv<NB * 4 + 6>(); LBAR(); step(26, true, false);
    waitv<NB * 4 + 4>(); LBAR();
    waitv<NB * 3 + 2>(); LBAR();
    waitv<NB * 2>(); LBAR();
    waitv<NB * 1>(); LBAR();
    waitv<0>(); LBAR();
    LBAR();
  }
  {
    const char* osb = ws + WS_WOUT + (size_t)(j * 135 + lw) * 1024;
#pragma unroll 1
    for (int p = 0; p < 33; ++p) glds16<false>(osb + p * 4096, vo, lbase + (lw + 4 * p) * 1024);
    if (lw < 3) glds16<false>(osb + 33 * 4096, vo, lbase + (lw + 132) * 1024);
    waitv<0>();
    LBAR();
  }
}

template <int NSUB, bool STATS>
__device__ __forceinline__ void consumer_g12(LAS char* lds, char* ws, __amdgpu_buffer_rsrc_t rs, const u16* __restrict__ bsp, int wg, int mt, int j, int sub0, int cnt,
                                             int wave, int tid, int lane, v2u (&x2v)[3] STAMP_ARG) {
  constexpr int NS1 = NSUB > 0 ? NSUB : 1;
  const int G = wave >> 2, ng = G ? 3 - (wave & 3) : (wave & 3);
  const int q = lane >> 4, i16 = lane & 15, rr = i16 >> 2, p = lane & 3, n0 = 2 * ng;
  float bias[2];
#pragma unroll
  for (int m = 0; m < 2; ++m) bias[m] = bf2f(bsp[mt * 64 + 32 * G + 8 * (i16 >> 2) + 4 * m + (i16 & 3)]);
  if (wave == 3) wait_count<8>(ws + CTL_E1, wg & 31, 256, lane);
  STAMP(4);
  CBAR();
  {
    const int c = tid & 127, which = (tid >> 7) & 1, half = tid >> 8;
    if (c < cnt * 16) {
      float up[4];
#pragma unroll
      for (int k = 0; k < 4; ++k)
        up[k] = __uint_as_float(__builtin_amdgcn_raw_buffer_load_b32(rs, (unsigned)WS_U + ((k * 2 + which) * F + (sub0 + (half ? 0 : 54)) * 16 + c) * 4, 0, 16));
      ((LAS float*)(lds + G1_T_U))[tid] = (up[0] + up[1]) + (up[2] + up[3]);
    }
  }
  v2u z1v[NS1][2];
  {
    const int aoff0 = g1_aoff(8 * q + rr, G, 0, p), aoff1 = g1_aoff(8 * q + rr, G, 1, p);
    const int boff = G1_BOFF + n0 * 2048 + lane * 16;
    struct Frags { v4u a[2][2]; v4u b[2][NS1]; };
    f32x4 acc2[NS1][2], acc1[NS1][2], asum[2], asq[2];
    int acur = 0, bcur = G1_BST;
    auto rd = [&](Frags& f) {
#pragma unroll
      for (int kk = 0; kk < 2; ++kk) {
        const v2u l0 = trread(lds + acur + aoff0 + kk * 4096), h0 = trread(lds + acur + aoff0 + kk * 4096 + 512);
        const v2u l1 = trread(lds + acur + aoff1 + kk * 4096), h1 = trread(lds + acur + aoff1 + kk * 4096 + 512);
        f.a[kk][0] = v4u{l0.x, l0.y, h0.x, h0.y}; f.a[kk][1] = v4u{l1.x, l1.y, h1.x, h1.y};
#pragma unroll
        for (int n = 0; n < NSUB; ++n) f.b[kk][n] = *(LAS const v4u*)(lds + bcur + boff + n * 2048 + kk * 1024);
      }
      acur += 8192; if (acur == 81920) acur = 0;
      bcur += G1_BST; if (bcur == G1_NSLOT * G1_BST) bcur = 0;
      SCHED_BAR();
    };
    auto pin1 = [&]() {
#pragma unroll
      for (int n = 0; n < NSUB; ++n) asm volatile("" : "+v"(acc1[n][0]), "+v"(acc1[n][1]));
    };
#pragma unroll
    for (int ab = 0; ab < 2; ++ab) {
      asum[ab] = f32x4{0.f, 0.f, 0.f, 0.f}; asq[ab] = f32x4{0.f, 0.f, 0.f, 0.f};
#pragma unroll
      for (int n = 0; n < NS1; ++n) { acc2[n][ab] = f32x4{0.f, 0.f, 0.f, 0.f}; acc1[n][ab] = f32x4{0.f, 0.f, 0.f, 0.f}; }
    }
    const v4u ones = {0x3f803f80u, 0x3f803f80u, 0x3f803f80u, 0x3f803f80u};
    auto mm2 = [&](const Frags& f) {
#pragma unroll
      for (int kk = 0; kk < 2; ++kk)
#pragma unroll
        for (int ab = 0; ab < 2; ++ab) {
#pragma unroll
          for (int n = 0; n < NSUB; ++n) acc2[n][ab] = mfma16(f.a[kk][ab], f.b[kk][n], acc2[n][ab]);
          if (STATS) { asum[ab] = mfma16(f.a[kk][ab], ones, asum[ab]); asq[ab] = mfma16(f.a[kk][ab], f.a[kk][ab], asq[ab]); }
        }
    };
    auto mm1 = [&](const Frags& f) {
#pragma unroll
      for (int kk = 0; kk < 2; ++kk)
#pragma unroll
        for (int ab = 0; ab < 2; ++ab)
#pragma unroll
          for (int n = 0; n < NSUB; ++n) acc1[n][ab] = mfma16(f.b[kk][n], f.a[kk][ab], acc1[n][ab]);
    };
    Frags f0, f1;
    CBAR(); rd(f0);
    STAMP(5);
    const bool poller = STATS && G == 0;
    unsigned pc = 0;
    if (poller) pc = poll_issue<8>(ws + CTL_E1 + 65536, wg & 31, lane);
#pragma unroll 1
    for (int it = 0; it < 5; ++it) {
      if (poller && it == 2) poll_finish<8>(pc, ws + CTL_E1 + 2 * 65536, wg & 31, 128, lane);
      CBAR(); rd(f1); mm2(f0);
      if (poller && it == 0) poll_finish<8>(pc, ws + CTL_E1 + 65536, wg & 31, 256, lane);
      if (poller && it == 1) pc = poll_issue<8>(ws + CTL_E1 + 2 * 65536, wg & 31, lane);
      CBAR(); rd(f0); mm2(f1);
    }
    LAS f32x2* tab = (LAS f32x2*)(lds + G1_T_LN2);
    if (STATS) {
#pragma unroll
      for (int ab = 0; ab < 2; ++ab) {
        const float a0 = asum[ab][0], a1 = asum[ab][1], a2 = asum[ab][2], a3 = asum[ab][3];
        const float b0 = asq[ab][0], b1 = asq[ab][1], b2 = asq[ab][2], b3 = asq[ab][3];
        const float s1 = (p & 2) ? ((p & 1) ? a3 : a2) : ((p & 1) ? a1 : a0), s2 = (p & 2) ? ((p & 1) ? b3 : b2) : ((p & 1) ? b1 : b0);
        const float mean = s1 * (1.0f / D);
        float mb, rinv;
        ln_scalars(mean, fmaxf(s2 * (1.0f / D) - mean * mean, 0.f), mb, rinv);
        if (rr == q) tab[(G * 2 + ab) * 16 + i16] = f32x2{rinv, rinv * mb};
      }
    }
    {
      const int nb = G ? 3 : 0;
#pragma unroll
      for (int n = 0; n < 3; ++n) {
        const int row = 80 * j + 16 * (nb + n < 5 ? nb + n : 4) + 4 * q + rr;
        x2v[n] = trread(lds + g1_aoff(row, (wave >> 1) & 1, wave & 1, p));
      }
    }
    STAMP(6);
    CBAR(); rd(f1); mm1(f0);
    v4u zq[NS1];
    {
      LAS const float* ut = (LAS const float*)(lds + G1_T_U);
      float v[16];
#pragma unroll
      for (int e = 0; e < 16; ++e) v[e] = 0.f;
      if (NSUB > 0) {
        f32x2 rv[8];
#pragma unroll
        for (int e = 0; e < 8; ++e) rv[e] = tab[(G * 2 + (e >> 2)) * 16 + 4 * q + (e & 3)];
#pragma unroll
        for (int n = 0; n < NSUB; ++n) {
          const float c1 = ut[(n0 + n) * 16 + i16], c2 = ut[128 + (n0 + n) * 16 + i16];
          float z[8];
#pragma unroll
          for (int e = 0; e < 8; ++e) z[e] = gelu_f(__builtin_fmaf(rv[e].x, acc2[n][e >> 2][e & 3], __builtin_fmaf(-rv[e].y, c1, c2)));
          zq[n] = v4u{pkbf(z[0], z[1]), pkbf(z[2], z[3]), pkbf(z[4], z[5]), pkbf(z[6], z[7])};
#pragma unroll
          for (int e = 0; e < 4; ++e) {
            const float a = bflo(zq[n][e]), c = bfhi(zq[n][e]);
            v[4 * e] += a; v[4 * e + 1] = __builtin_fmaf(a, a, v[4 * e + 1]);
            v[4 * e + 2] += c; v[4 * e + 3] = __builtin_fmaf(c, c, v[4 * e + 3]);
          }
        }
        treduce_dpp<8, 0x140>(v, (i16 & 8) != 0);
        treduce_dpp<4, 0x141>(v, (i16 & 4) != 0);
        treduce_dpp<2, 0x4e>(v, (i16 & 2) != 0);
        treduce_dpp<1, 0xb1>(v, (i16 & 1) != 0);
      }
      ((LAS float*)(lds + G1_T_P))[ng * 128 + G * 64 + lane] = v[0];
    }
    CBAR(); pin1(); rd(f0); mm1(f1);
    GAS unsigned long long* gr = (GAS unsigned long long*)(ws + CTL_ST3) + (size_t)mt * 1024 + G * 32;
    if (STATS) {
      LAS const float* pp = (LAS const float*)(lds + G1_T_P) + G * 64 + lane;
      const float sv = (pp[0] + pp[128]) + (pp[256] + pp[384]);
      __hip_atomic_store(gr + (j * 2 + (lane & 1)) * 64 + (lane >> 1), (1ull << 32) | __float_as_uint(sv), RLX_AGENT);
    }
    CBAR(); pin1(); rd(f1); mm1(f0);
    CBAR(); pin1(); rd(f0); mm1(f1);
    unsigned long long gx[8];
    if (STATS) {
#pragma unroll
      for (int k = 0; k < 8; ++k) gx[k] = __hip_atomic_load(gr + (k * 2 + (lane >> 5)) * 64 + (lane & 31), RLX_AGENT);
    }
    CBAR(); pin1(); rd(f1); mm1(f0);
    if (STATS) {
      float sv;
      for (unsigned spin = 0;; ++spin) {
        bool ok = true;
        sv = 0.f;
#pragma unroll
        for (int k = 0; k < 8; ++k) { ok &= (gx[k] >> 32) == 1ull; sv += __uint_as_float((unsigned)gx[k]); }
        if (__all(ok) || spin > SPIN_CAP) break;
        __builtin_amdgcn_s_sleep(1);
#pragma unroll
        for (int k = 0; k < 8; ++k) gx[k] = __hip_atomic_load(gr + (k * 2 + (lane >> 5)) * 64 + (lane & 31), RLX_AGENT);
      }
      const float s2 = __shfl_xor(sv, 32);
      const float mean = sv * (1.0f / FH);
      float mb, rinv;
      ln_scalars(mean, fmaxf(s2 * (1.0f / FH) - mean * mean, 0.f), mb, rinv);
      if (lane < 32) ((LAS f32x2*)(lds + G1_T_Z))[32 * G + lane] = f32x2{mb, rinv};
    }
    CBAR(); pin1(); rd(f0); mm1(f1);
    if (NSUB > 0) {
      f32x2 zs[8];
#pragma unroll
      for (int e = 0; e < 8; ++e) zs[e] = ((LAS const f32x2*)(lds + G1_T_Z))[32 * G + 8 * q + e];
#pragma unroll
      for (int n = 0; n < NSUB; ++n) {
        v4u o;
#pragma unroll
        for (int e = 0; e < 4; ++e)
          o[e] = pkbf(bfr(bflo(zq[n][e]) - zs[2 * e].x) * zs[2 * e].y, bfr(bfhi(zq[n][e]) - zs[2 * e + 1].x) * zs[2 * e + 1].y);
        st128(rs, (unsigned)WS_Z2 + (((sub0 + n0 + n) * 64 + mt * 2 + G) * 64 + lane) * 16, o);
      }
    }
    CBAR(); pin1(); rd(f1); mm1(f0);
    CBAR(); pin1(); rd(f0); mm1(f1);
    CBAR(); pin1(); rd(f1); mm1(f0);
    if (wave_arrive(lds, 3, 8, lane)) signal<4, 4>(ws + CTL_E2 + j * 4096, mt & 3, lane);
    CBAR(); pin1();
    mm1(f1);
    STAMP(7);
    {
      LAS const float* ut = (LAS const float*)(lds + G1_T_U) + 256;
      f32x2 rv[2];
#pragma unroll
      for (int ab = 0; ab < 2; ++ab) rv[ab] = tab[(G * 2 + ab) * 16 + i16];
#pragma unroll
      for (int n = 0; n < NSUB; ++n) {
        const f32x4 u1 = *(LAS const f32x4*)(ut + (n0 + n) * 16 + 4 * q), u2 = *(LAS const f32x4*)(ut + 128 + (n0 + n) * 16 + 4 * q);
#pragma unroll
        for (int ab = 0; ab < 2; ++ab) {
          float z[4];
#pragma unroll
          for (int e = 0; e < 4; ++e) z[e] = gelu_f(__builtin_fmaf(rv[ab].x, acc1[n][ab][e], __builtin_fmaf(-rv[ab].y, u1[e], u2[e])));
          z1v[n][ab] = v2u{pkbf(z[0], z[1]), pkbf(z[2], z[3])};
        }
      }
    }
  }
  if (wave == 3) wait_count<4>(ws + CTL_E2 + j * 4096, (mt >> 2) & 3, 32, lane);
  CBAR();
  STAMP(9);
  {
    f32x4 acc[NS1][2];
#pragma unroll
    for (int n = 0; n < NS1; ++n) { acc[n][0] = f32x4{0.f, 0.f, 0.f, 0.f}; acc[n][1] = f32x4{0.f, 0.f, 0.f, 0.f}; }
    int woff[2];
#pragma unroll
    for (int kk = 0; kk < 2; ++kk) woff[kk] = G * 4096 + i16 * 128 + (((4 * kk + q) ^ (i16 >> 1)) << 4);
    const int zoff = G2_ZOFF + n0 * 2048 + lane * 16;
    struct Frags { v4u w[2][2]; v4u z[2][NS1]; };
    int wc = 0, zc = 0;
    auto rd = [&](Frags& f) {
      if (NSUB > 0) {
#pragma unroll
        for (int kk = 0; kk < 2; ++kk) {
#pragma unroll
          for (int m = 0; m < 2; ++m) f.w[kk][m] = *(LAS const v4u*)(lds + wc + woff[kk] + m * 2048);
#pragma unroll
          for (int n = 0; n < NSUB; ++n) f.z[kk][n] = *(LAS const v4u*)(lds + zc + zoff + n * 2048 + kk * 1024);
        }
        wc += 8192; if (wc == G2_NW * 8192) wc = 0;
        zc += G2_ZST; if (zc == G2_NZ * G2_ZST) zc = 0;
      }
      SCHED_BAR();
    };
    auto mm = [&](const Frags& f) {
#pragma unroll
      for (int kk = 0; kk < 2; ++kk)
#pragma unroll
        for (int n = 0; n < NSUB; ++n)
#pragma unroll
          for (int m = 0; m < 2; ++m) acc[n][m] = mfma16(f.z[kk][n], f.w[kk][m], acc[n][m]);
    };
    Frags f0, f1;
    CBAR(); rd(f0);
    STAMP(10);
#pragma unroll 1
    for (int it = 0; it < 15; ++it) {
      CBAR(); rd(f1); mm(f0);
      CBAR(); rd(f0); mm(f1);
    }
    CBAR(); rd(f1); mm(f0);
    mm(f1);
    STAMP(11);
#pragma unroll
    for (int m = 0; m < 2; ++m) {
      v2u o[NS1];
#pragma unroll
      for (int n = 0; n < NSUB; ++n) {
        float g[4];
#pragma unroll
        for (int e = 0; e < 4; ++e) g[e] = bfr(bfr(acc[n][m][e]) + bias[m]);
        const v2u zz = z1v[n][m];
        o[n] = v2u{pkbf(bflo(zz.x) * g[0], bfhi(zz.x) * g[1]), pkbf(bflo(zz.y) * g[2], bfhi(zz.y) * g[3])};
      }
      const unsigned vb = (unsigned)WS_V + (mt * 4 + G * 2 + m) * 27 * 1024 + lane * 16;
      if (NSUB == 2) st128(rs, vb + (3 * j + ng) * 1024, v4u{o[0].x, o[0].y, o[NS1 - 1].x, o[NS1 - 1].y});
      if (NSUB == 1) __builtin_amdgcn_raw_buffer_store_b64(o[0], rs, vb + (24 + (j >> 1)) * 1024 + (j & 1) * 8, 0, 16);
    }
    CBAR();
    if (wave_arrive(lds, 4, 8, lane)) signal<1, 1>(ws + CTL_E3 + mt * 256, 0, lane);
  }
}

template <int KH>
__device__ __forceinline__ void consumer_g3(LAS char* lds, char* ws, __amdgpu_buffer_rsrc_t rs, u16* __restrict__ out, int mt, int j, int m, int lane, const v2u (&x2v)[3], const f32x4 (&gb)[3] STAMP_ARG) {
  constexpr int K0 = KH ? 14 : 0, NK = KH ? 13 : 14, NF = KH ? 2 : 3, N0 = KH ? 3 : 0;
  const int q = lane >> 4, i16 = lane & 15;
  v4u vf[NK];
#pragma unroll
  for (int k = 0; k < NK; ++k) vf[k] = ld128(rs, (unsigned)WS_V + ((mt * 4 + m) * 27 + K0 + k) * 1024 + lane * 16);
  SCHED_BAR();
  f32x4 acc[5];
#pragma unroll
  for (int n = 0; n < 5; ++n) acc[n] = f32x4{0.f, 0.f, 0.f, 0.f};
  LAS const char* wp = lds + K0 * 1024 + lane * 16;
  LAS const char* wp2 = wp + 54 * 1024;
  LAS const char* wp4 = wp + 108 * 1024;
  v4u wf[3][5];
  auto rdw = [&](int k, v4u (&w)[5]) {
    w[0] = *(LAS const v4u*)(wp + k * 1024); w[1] = *(LAS const v4u*)(wp + (27 + k) * 1024);
    w[2] = *(LAS const v4u*)(wp2 + k * 1024); w[3] = *(LAS const v4u*)(wp2 + (27 + k) * 1024);
    w[4] = *(LAS const v4u*)(wp4 + k * 1024);
  };
  rdw(0, wf[0]); rdw(1, wf[1]);
#pragma unroll
  for (int k = 0; k < NK; ++k) {
    if (k + 2 < NK) rdw(k + 2, wf[(k + 2) % 3]);
    SCHED_BAR();
#pragma unroll
    for (int n = 0; n < 5; ++n) acc[n] = mfma16(wf[k % 3][n], vf[k], acc[n]);
    SCHED_BAR();
  }
  STAMP(13);
  LAS char* red = lds + G3_RED + m * 5120 + lane * 16;
#pragma unroll
  for (int n = 0; n < 5; ++n) if ((n >= 3) != (KH != 0)) *(LAS f32x4*)(red + n * 1024) = acc[n];
  CBAR();
  const int t = mt * 64 + 32 * (m >> 1) + 8 * (i16 >> 2) + 4 * (m & 1) + (i16 & 3);
#pragma unroll
  for (int n = 0; n < NF; ++n) {
    const f32x4 o = *(LAS const f32x4*)(red + (N0 + n) * 1024);
    const f32x4 a = KH ? o + acc[N0 + n] : acc[N0 + n] + o;
    const float o0 = bflo(x2v[n].x) + bfr(gb[n][0] * bfr(a[0])), o1 = bfhi(x2v[n].x) + bfr(gb[n][1] * bfr(a[1]));
    const float o2 = bflo(x2v[n].y) + bfr(gb[n][2] * bfr(a[2])), o3 = bfhi(x2v[n].y) + bfr(gb[n][3] * bfr(a[3]));
    *(v2u*)((char*)out + ((size_t)t * D + 80 * j + 16 * (N0 + n) + 4 * q) * 2) = v2u{pkbf(o0, o1), pkbf(o2, o3)};
  }
}

__global__ __launch_bounds__(768, 3) void k2_mega(const u16* __restrict__ dskip, const u16* __restrict__ win, const u16* __restrict__ wsp,
                                                  const u16* __restrict__ bsp, char* __restrict__ ws, u16* __restrict__ out) {
  extern __shared__ __attribute__((aligned(16))) char smem[];
  LAS char* lds = (LAS char*)smem;
  const int tid = threadIdx.x, lane = tid & 63, wave = __builtin_amdgcn_readfirstlane(tid >> 6), wg = blockIdx.x;
  STAMP_DECL; STAMP(0);
  const __amdgpu_buffer_rsrc_t rs = __builtin_amdgcn_make_buffer_rsrc((void*)ws, 0, (int)WS_END, 0x00020000);
  if (tid < 8) ((LAS unsigned*)(lds + M_CTL))[tid] = 0u;
  {
    const int grp = wave >> 2, bt = grp == 2 ? 0 : grp + 1, d = wg + 256 * bt;
    LAS char* gl = lds + (grp == 2 ? G1_TAB - C_GRP : grp * C_GRP);
    if (bt == 0) __builtin_amdgcn_s_setprio(2);
    if (bt == 1) __builtin_amdgcn_s_setprio(1);
    if (d < D) conv_group(gl, lds, bt, ws, rs, dskip, wsp, d, tid & 255, wave & 3, lane STAMP_PASS);
    else if (d < D + 108) { __builtin_amdgcn_s_setprio(3); wprep_group(gl, lds, ws, rs, win, d - D, tid & 255, wave & 3, lane); }
    else CBAR();
    __builtin_amdgcn_s_setprio(0);
  }
  STAMP(3);
  const int xcd = wg & 7, idx = wg >> 3;
  const int mt = (xcd >> 1) * 8 + (idx & 7), j = (xcd & 1) * 4 + (idx >> 3);
  const int cnt = j < 6 ? 7 : 6, sub0 = j < 6 ? 7 * j : 42 + 6 * (j - 6);
  if (wave >= 8) {
    const int lw = wave - 8;
    if (lw + 12 < 2 * cnt) loader_path<4>(lds, ws, wsp, wg, mt, j, sub0, lw, lane);
    else loader_path<3>(lds, ws, wsp, wg, mt, j, sub0, lw, lane);
    STAMP_FLUSH(1);
    return;
  }
  v2u x2v[3];
  const int ng = wave >> 2 ? 3 - (wave & 3) : (wave & 3);
  if (ng < 3) consumer_g12<2, false>(lds, ws, rs, bsp, wg, mt, j, sub0, cnt, wave, tid, lane, x2v STAMP_PASS);
  else if (cnt == 7) consumer_g12<1, true>(lds, ws, rs, bsp, wg, mt, j, sub0, cnt, wave, tid, lane, x2v STAMP_PASS);
  else consumer_g12<0, true>(lds, ws, rs, bsp, wg, mt, j, sub0, cnt, wave, tid, lane, x2v STAMP_PASS);
  f32x4 gb[3];
  {
    const int nb = wave >> 2 ? 3 : 0;
#pragma unroll
    for (int n = 0; n < 3; ++n) gb[n] = *(const f32x4*)(ws + WS_MOD + (size_t)(5 * D + 80 * j + 16 * (nb + n < 5 ? nb + n : 4) + 4 * (lane >> 4)) * 4);
  }
  if (wave == 0) wait_count<1>(ws + CTL_E3 + mt * 256, 0, 8, lane);
  CBAR();
  STAMP(12);
  if (wave < 4) consumer_g3<0>(lds, ws, rs, out, mt, j, wave & 3, lane, x2v, gb STAMP_PASS);
  else consumer_g3<1>(lds, ws, rs, out, mt, j, wave & 3, lane, x2v, gb STAMP_PASS);
  STAMP(14);
  STAMP_FLUSH(1);
}

extern "C" void kernel_launch(void* const* d_in, const int* in_sizes, int n_in, void* d_out, int out_size, void* d_ws, size_t ws_size,
                              hipStream_t stream) {
  const u16* x = (const u16*)d_in[0];
  char* ws = (char*)d_ws;
  (void)hipFuncSetAttribute((const void*)k2_mega, hipFuncAttributeMaxDynamicSharedMemorySize, M_LDS);
  k1_prep<<<K1_GRID, 256, 0, stream>>>(x, (const u16*)d_in[1], (const u16*)d_in[2], (const u16*)d_in[3], (const u16*)d_in[4],
                                       (const u16*)d_in[5], (const u16*)d_in[6], (const u16*)d_in[7], (const u16*)d_in[13],
                                       (const u16*)d_in[14], (const u16*)d_in[15], ws);
  k2_mega<<<256, 768, M_LDS, stream>>>((const u16*)d_in[8], (const u16*)d_in[10], (const u16*)d_in[11], (const u16*)d_in[12], ws, (u16*)d_out);
}
```

```cpp
#include <hip/hip_runtime.h>
#include <hip/hip_bf16.h>
#include <stdint.h>

#define NT " nt"
#define NTLOAD(p) __builtin_nontemporal_load(p)
#define GAS __attribute__((address_space(1)))
#define LAS __attribute__((address_space(3)))
typedef unsigned v4u __attribute__((ext_vector_type(4)));
typedef unsigned v2u __attribute__((ext_vector_type(2)));
typedef float f32x2 __attribute__((ext_vector_type(2)));
typedef float f32x4 __attribute__((ext_vector_type(4)));
typedef float f32x16 __attribute__((ext_vector_type(16)));
typedef __bf16 bf16x2 __attribute__((ext_vector_type(2)));
typedef __bf16 bf16x8 __attribute__((ext_vector_type(8)));
typedef _Float16 f16x2 __attribute__((ext_vector_type(2)));
typedef _Float16 f16x8 __attribute__((ext_vector_type(8)));
typedef short s16x4 __attribute__((ext_vector_type(4)));
typedef unsigned short u16;

#if defined(__HIP_DEVICE_COMPILE__) && !defined(KEEP_PK)
#pragma clang attribute push(__attribute__((target("no-packed-fp32-ops"))), apply_to = function)
#endif
__device__ __forceinline__ float u2f(unsigned u) { return __builtin_bit_cast(float, u); }
__device__ __forceinline__ unsigned f2u(float f) { return __builtin_bit_cast(unsigned, f); }
__device__ __forceinline__ int lane_id() { return __builtin_amdgcn_mbcnt_hi(~0u, __builtin_amdgcn_mbcnt_lo(~0u, 0u)); }
__device__ __forceinline__ unsigned shx(unsigned v, int m) { return __builtin_amdgcn_ds_bpermute((lane_id() ^ m) << 2, v); }
__device__ __forceinline__ float shx(float v, int m) { return u2f(shx(f2u(v), m)); }
__device__ __forceinline__ bool wall(bool p) { return __builtin_amdgcn_ballot_w64(!p) == 0ull; }
__device__ __forceinline__ float exp_acc(float x) {
  const float hi = x * 1.4426950408889634f;
  const float lo = __builtin_fmaf(x, 1.4426950408889634f, -hi) + x * 1.925963033500414e-8f;
  const float e = __builtin_amdgcn_exp2f(hi);
  return __builtin_fmaf(e, lo * 0.6931471805599453f, e);
}

constexpr int L = 2048, D = 640, NS = 128, F = 1728, FH = 864;

constexpr size_t CTL_E1 = 0;
constexpr size_t CTL_E1B = 196608;
constexpr size_t CTL_E2 = 262144;
constexpr size_t CTL_E3 = 294912;
constexpr size_t CTL_EW = 303104;
constexpr size_t CTL_ST3 = 327680;
constexpr size_t CTL_F0 = CTL_ST3 + 262144;
constexpr size_t CTL_EPOCH = CTL_F0 + 32768;
constexpr size_t WS_MOD = CTL_EPOCH + 4096;
constexpr size_t WS_ST1 = WS_MOD + 131072;
constexpr size_t WS_U = WS_ST1 + 16384;
constexpr size_t WS_XT = WS_U + 65536;
constexpr size_t WS_X2T = WS_XT + (size_t)D * L * 2;
constexpr size_t WS_WIN = WS_X2T + (size_t)D * L * 2;
constexpr size_t WS_WOUT = WS_WIN + (size_t)D * F * 2;
constexpr size_t WS_Z2 = WS_WOUT + (size_t)FH * D * 2;
constexpr size_t WS_V = WS_Z2 + (size_t)L * FH * 2;
constexpr size_t WS_END = WS_V + (size_t)L * FH * 2;

__device__ __forceinline__ float bflo(unsigned u) { return u2f(u << 16); }
__device__ __forceinline__ float bfhi(unsigned u) { return u2f(u & 0xffff0000u); }
__device__ __forceinline__ float bf2f(u16 v) { return u2f((unsigned)v << 16); }
__device__ __forceinline__ unsigned pkbf(float a, float b) {
  f32x2 v = {a, b};
  return __builtin_bit_cast(unsigned, __builtin_convertvector(v, bf16x2));
}
__device__ __forceinline__ unsigned pkh(float a, float b) {
  f32x2 v = {a, b};
  return __builtin_bit_cast(unsigned, __builtin_convertvector(v, f16x2));
}
__device__ __forceinline__ float bfr(float a) { return bflo(pkbf(a, a)); }
__device__ __forceinline__ v2u trread(LAS const char* p) {
  return __builtin_bit_cast(v2u, __builtin_amdgcn_ds_read_tr16_b64_v4i16((LAS s16x4*)p));
}
__device__ __forceinline__ f32x4 mfma16(v4u a, v4u b, f32x4 c) {
  return __builtin_amdgcn_mfma_f32_16x16x32_bf16(__builtin_bit_cast(bf16x8, a), __builtin_bit_cast(bf16x8, b), c, 0, 0, 0);
}
__device__ __forceinline__ f32x16 mfma32(v4u a, v4u b, f32x16 c) {
  return __builtin_amdgcn_mfma_f32_32x32x16_bf16(__builtin_bit_cast(bf16x8, a), __builtin_bit_cast(bf16x8, b), c, 0, 0, 0);
}
__device__ __forceinline__ f32x16 mfma32h(v4u a, v4u b, f32x16 c) {
  return __builtin_amdgcn_mfma_f32_32x32x16_f16(__builtin_bit_cast(f16x8, a), __builtin_bit_cast(f16x8, b), c, 0, 0, 0);
}
template <bool DEV>
__device__ __forceinline__ void glds16(const void* sbase, unsigned voff, unsigned dst) {
  unsigned keep;
  if (DEV) asm volatile("s_mov_b32 %0, m0\n\ts_mov_b32 m0, %3\n\ts_nop 0\n\tglobal_load_lds_dwordx4 %1, %2 sc1\n\ts_mov_b32 m0, %0"
                        : "=&s"(keep) : "v"(voff), "s"(sbase), "s"(dst) : "memory");
  else asm volatile("s_mov_b32 %0, m0\n\ts_mov_b32 m0, %3\n\ts_nop 0\n\tglobal_load_lds_dwordx4 %1, %2\n\ts_mov_b32 m0, %0"
                    : "=&s"(keep) : "v"(voff), "s"(sbase), "s"(dst) : "memory");
}
__device__ __forceinline__ void glds16nt(const void* sbase, unsigned voff, unsigned dst) {
  unsigned keep;
  asm volatile("s_mov_b32 %0, m0\n\ts_mov_b32 m0, %3\n\ts_nop 0\n\tglobal_load_lds_dwordx4 %1, %2" NT "\n\ts_mov_b32 m0, %0"
               : "=&s"(keep) : "v"(voff), "s"(sbase), "s"(dst) : "memory");
}
template <int N> __device__ __forceinline__ void waitv() { asm volatile("s_waitcnt vmcnt(%0)" :: "i"(N) : "memory"); }
__device__ __forceinline__ float dot2bf(unsigned a, unsigned b, float c) {
  return __builtin_amdgcn_fdot2_f32_bf16(__builtin_bit_cast(bf16x2, a), __builtin_bit_cast(bf16x2, b), c, false);
}
#define SCHED_BAR() __builtin_amdgcn_sched_barrier(0)
#define STAMP_DECL
#define STAMP(i) do {} while (0)
#define STAMP_FLUSH(kid) do {} while (0)
#define STAMP_ARG
#define STAMP_PASS
#define CBAR() do { SCHED_BAR(); __builtin_amdgcn_s_waitcnt(0xC07F); asm volatile("" ::: "memory"); __builtin_amdgcn_s_barrier(); asm volatile("" ::: "memory"); } while (0)
#define LBAR() __builtin_amdgcn_s_barrier()

template <int CTRL> __device__ __forceinline__ float dppf(float v) {
  return __builtin_bit_cast(float, __builtin_amdgcn_update_dpp(0, __builtin_bit_cast(int, v), CTRL, 0xf, 0xf, true));
}
template <int N, int CTRL> __device__ __forceinline__ void treduce_dpp(float* v, bool up) {
#pragma unroll
  for (int e = 0; e < N; ++e) {
    const float a = v[e], b = v[e + N];
    const float send = up ? a : b, keep = up ? b : a;
    v[e] = keep + dppf<CTRL>(send);
  }
}
template <int N> __device__ __forceinline__ void treduce_xor(float* v, bool up, int mask) {
#pragma unroll
  for (int e = 0; e < N; ++e) {
    const float a = v[e], b = v[e + N];
    const float send = up ? a : b, keep = up ? b : a;
    v[e] = keep + shx(send, mask);
  }
}

__device__ __forceinline__ float ln_std(float var) { return __builtin_amdgcn_sqrtf(bfr(bfr(var) + 1e-5f)); }
__device__ __forceinline__ void ln_scalars(float mean, float var, float& m_b, float& rinv) {
  m_b = bfr(mean);
  rinv = __builtin_amdgcn_rcpf(bfr(ln_std(var)));
}

struct cplx { float r, i; };
__device__ __forceinline__ cplx cmul(cplx a, cplx b) { return {a.r * b.r - a.i * b.i, a.r * b.i + a.i * b.r}; }

typedef GAS unsigned gu32;
#define RLX_AGENT __ATOMIC_RELAXED, __HIP_MEMORY_SCOPE_AGENT
#define F0_REP 8
#define V_DELAY 16
#define LSTAMP(i) do {} while (0)
#define POLL_SLEEP 6
#define E1_REP 32
constexpr unsigned SPIN_CAP = 1u << 16;
constexpr int M_LDS = 163840, M_CTL = M_LDS - 64;
constexpr int CODE_SECTORS = 1050;
constexpr int C_KOFF = 0, C_UOFF = 4224, C_UP = 1536, C_COFF = C_UOFF + 4 * C_UP, C_GRP = C_COFF + 4 * 64 * 144;
constexpr int G1_BOFF = 81920, G1_BST = 14336, G1_NSLOT = 5, G1_TAB = G1_BOFF + G1_BST * G1_NSLOT;
constexpr int G1_T_LN2 = G1_TAB, G1_T_U = G1_T_LN2 + 512, G1_T_P = G1_T_U + 2048, G1_T_Z = G1_T_P + 2048;
constexpr int G2_NW = 9, G2_NZ = 6, G2_ZOFF = G2_NW * 8192, G2_ZST = 14336;
constexpr int G3_RED = 135 * 1024, G3_GB = 161024;
static_assert(3 * C_GRP <= M_CTL && G1_T_Z + 512 <= M_CTL && G2_ZOFF + G2_NZ * G2_ZST <= M_CTL && G3_GB + 320 <= M_CTL, "lds");

__device__ __forceinline__ void st128(__amdgpu_buffer_rsrc_t rs, unsigned off, v4u v) { __builtin_amdgcn_raw_buffer_store_b128(v, rs, off, 0, 16); }
__device__ __forceinline__ v4u ld128(__amdgpu_buffer_rsrc_t rs, unsigned off) { return __builtin_amdgcn_raw_buffer_load_b128(rs, off, 0, 16); }

__device__ __forceinline__ bool wave_arrive(LAS char* lds, int word, unsigned n, int lane) {
  waitv<0>();
  unsigned old = 0;
  if (lane == 0) old = __hip_atomic_fetch_add((LAS unsigned*)(lds + M_CTL) + word, 1u, __ATOMIC_RELAXED, __HIP_MEMORY_SCOPE_WORKGROUP);
  return (unsigned)__builtin_amdgcn_readfirstlane(old) == n - 1;
}
__device__ __forceinline__ void lds_wait(LAS char* lds0, int word, unsigned target) {
  LAS unsigned* p = (LAS unsigned*)(lds0 + M_CTL) + word;
  while ((unsigned)__builtin_amdgcn_readfirstlane(__hip_atomic_load(p, __ATOMIC_RELAXED, __HIP_MEMORY_SCOPE_WORKGROUP)) < target) __builtin_amdgcn_s_sleep(0);
  asm volatile("" ::: "memory");
}
__device__ __forceinline__ void lds_post(LAS char* lds0, int word, int lane) {
  SCHED_BAR();
  asm volatile("" ::: "memory");
  if (lane == 0) __hip_atomic_fetch_add((LAS unsigned*)(lds0 + M_CTL) + word, 1u, __ATOMIC_RELAXED, __HIP_MEMORY_SCOPE_WORKGROUP);
  asm volatile("" ::: "memory");
}
__device__ __forceinline__ void group_sync(LAS char* lds0, int word, unsigned target, int lane) { lds_post(lds0, word, lane); lds_wait(lds0, word, target); }
template <int NREP, int NSH> __device__ __forceinline__ void signal(char* base, int shard, int lane) {
  if (lane < NREP) __hip_atomic_fetch_add((gu32*)(base + (lane * NSH + shard) * 256), 1u, RLX_AGENT);
}
template <int NSH> __device__ __forceinline__ unsigned poll_issue(char* base, int rep, int lane) {
  return __hip_atomic_load((gu32*)(base + (rep * NSH + (lane & (NSH - 1))) * 256), RLX_AGENT);
}
template <int NSH> __device__ __forceinline__ void poll_finish(unsigned c, char* base, int rep, unsigned total, int lane) {
  for (unsigned spin = 0; spin < SPIN_CAP; ++spin) {
#pragma unroll
    for (int s = 1; s < NSH; s <<= 1) c += shx(c, s);
    if ((unsigned)__builtin_amdgcn_readfirstlane(c) >= total) break;
    __builtin_amdgcn_s_sleep(POLL_SLEEP);
    c = poll_issue<NSH>(base, rep, lane);
  }
  __builtin_amdgcn_fence(__ATOMIC_ACQUIRE, "wavefront");
}
template <int NSH> __device__ __forceinline__ void wait_count(char* base, int rep, unsigned total, int lane) {
  poll_finish<NSH>(poll_issue<NSH>(base, rep, lane), base, rep, total, lane);
}

__device__ __forceinline__ float gelu_f(float x) {
  const float p = __builtin_fmaf(x * x, -0.044715f * 2.302208198f, -2.302208198f);
  const float e = __builtin_amdgcn_exp2f(x * p);
  return x * __builtin_amdgcn_rcpf(1.0f + e);
}

__device__ __forceinline__ float ld32f(__amdgpu_buffer_rsrc_t rs, unsigned off) { return u2f(__builtin_amdgcn_raw_buffer_load_b32(rs, off, 0, 16)); }
__device__ __forceinline__ void st32(__amdgpu_buffer_rsrc_t rs, unsigned off, unsigned v) { __builtin_amdgcn_raw_buffer_store_b32(v, rs, off, 0, 16); }

struct Args {
  const u16 *x, *Are, *Aim, *Bre, *Bim, *Cre, *Cim, *logdt, *dskip, *win, *wsp, *bsp, *wout, *cvec, *wmod;
  char* ws; u16* out;
};

typedef GAS unsigned long long gu64;
__device__ __forceinline__ void put_granule(char* p, unsigned v, unsigned tag) { __hip_atomic_store((gu64*)p, ((unsigned long long)tag << 32) | v, RLX_AGENT); }
template <int N, class F> __device__ __forceinline__ void get_granules(unsigned (&v)[N], unsigned tag, F addr) {
  for (unsigned spin = 0; spin < SPIN_CAP; ++spin) {
    unsigned long long x[N];
#pragma unroll
    for (int k = 0; k < N; ++k) x[k] = __hip_atomic_load((gu64*)addr(k), RLX_AGENT);
    bool ok = true;
#pragma unroll
    for (int k = 0; k < N; ++k) { v[k] = (unsigned)x[k]; ok &= (unsigned)(x[k] >> 32) == tag; }
    if (wall(ok)) break;
    __builtin_amdgcn_s_sleep(8);
  }
}

__device__ __forceinline__ void conv_group(LAS char* lds, LAS char* lds0, int bt, const Args& A, __amdgpu_buffer_rsrc_t rs, unsigned epoch, unsigned& tag, unsigned (&pre)[10],
                                           int wg, int d, int tid, int wave, int lane STAMP_ARG) {
  char* ws = A.ws;
  float S[8], soff;
  v4u xv, st[2];
  {
    const int half = wave & 1;
    const bool domod = bt == 2 && wg < 120;
    v4u mw[10];
    unsigned mc;
    const int mpart = wg & 3, mcol = ((wg >> 2) + 15) % 30 * 128;
#define LDV4(dst, base, voff) asm volatile("global_load_dwordx4 %0, %1, %2" NT : "=v"(dst) : "v"(voff), "s"(base) : "memory")
#define LDU16(dst, base, voff) asm volatile("global_load_ushort %0, %1, %2" NT : "=v"(dst) : "v"(voff), "s"(base) : "memory")
    if (domod) {
      LDU16(mc, A.cvec, (mpart * 160 + (tid < 160 ? tid : 0)) * 2);
#pragma unroll
      for (int i = 0; i < 10; ++i) LDV4(mw[i], (const char*)A.wmod + (size_t)(mpart * 160 + i * 16) * (6 * D * 2) + mcol * 2, (tid >> 4) * (6 * D * 2) + (tid & 15) * 16);
    }
    int xoff[8];
#pragma unroll
    for (int j = 0; j < 8; ++j) {
      const int l = tid * 8 + j;
      const int idx = l < 128 ? l : (int)(pkbf((float)l, 0.f) & 0xffffu) - 0x4300 + 128;
      xoff[j] = (idx + (idx >> 3)) * 8;
    }
    asm volatile("" : "+v"(xoff[0]), "+v"(xoff[1]), "+v"(xoff[2]), "+v"(xoff[3]), "+v"(xoff[4]), "+v"(xoff[5]), "+v"(xoff[6]), "+v"(xoff[7]));
    if (domod) waitv<12>(); else waitv<1>();
    asm volatile("v_readfirstlane_b32 %0, %1\n\ts_nop 3" : "=s"(tag) : "v"(epoch) : "memory");
    tag += 1u;
    asm volatile("" : "+v"(pre[0]), "+v"(pre[1]), "+v"(pre[2]), "+v"(pre[3]), "+v"(pre[4]), "+v"(pre[5]), "+v"(pre[6]));
    const unsigned vare = pre[0], vaim = pre[1], vdt = pre[2], vbre = pre[3], vbim = pre[4], vcre = pre[5], vcim = pre[6];
    if (domod) {
      __builtin_amdgcn_s_setprio(3);
      LAS float* msc = (LAS float*)(lds + C_COFF + 32768);
      waitv<10>();
      asm volatile("" : "+v"(mc));
      {
        const float c = bflo(mc);
        msc[tid] = bfr(c * __builtin_amdgcn_rcpf(1.0f + __builtin_amdgcn_exp2f(-1.4426950408889634f * c)));
      }
      group_sync(lds0, 9, 4, lane);
      float sv[10];
#pragma unroll
      for (int i = 0; i < 10; ++i) sv[i] = msc[(tid >> 4) + 16 * i];
      waitv<0>();
      asm volatile("" : "+v"(mw[0]), "+v"(mw[1]), "+v"(mw[2]), "+v"(mw[3]), "+v"(mw[4]), "+v"(mw[5]), "+v"(mw[6]), "+v"(mw[7]), "+v"(mw[8]), "+v"(mw[9]));
      float acc[8] = {};
#pragma unroll
      for (int i = 0; i < 10; ++i)
#pragma unroll
        for (int j = 0; j < 4; ++j) { acc[2 * j] += sv[i] * bflo(mw[i][j]); acc[2 * j + 1] += sv[i] * bfhi(mw[i][j]); }
      treduce_xor<4>(acc, (lane & 32) != 0, 32);
      treduce_xor<2>(acc, (lane & 16) != 0, 16);
      LAS float* red = msc + 256;
      *(LAS f32x2*)(red + wave * 128 + (lane & 15) * 8 + (lane >> 4) * 2) = f32x2{acc[0], acc[1]};
      lds_post(lds0, 9, lane);
      if (wave >= 2) {
        lds_wait(lds0, 9, 8);
        const int c = tid - 128;
        put_granule(ws + WS_MOD + (size_t)(mpart * 3840 + mcol + c) * 8, f2u((red[c] + red[128 + c]) + (red[256 + c] + red[384 + c])), tag);
      }
      __builtin_amdgcn_s_setprio(0);
    }
    if (tid < 4) *(LAS v4u*)(lds + C_KOFF + tid * 16) = v4u{0u, 0u, 0u, 0u};
    const float dt = bfr(exp_acc(bflo(vdt)));
    const float ar2 = dt * bflo(vare) * 1.4426950408889634f, ai2 = dt * bflo(vaim) * 0.15915494309189535f;
    auto E = [&](float xx) {
      const float mag = __builtin_amdgcn_exp2f(ar2 * xx), ph = __builtin_amdgcn_fractf(ai2 * xx);
      return cplx{mag * __builtin_amdgcn_cosf(ph), mag * __builtin_amdgcn_sinf(ph)};
    };
    LAS char* img = lds + C_COFF + wave * 8192 + lane * 128;
    const int sw = lane & 7;
    float isc = 0.f;
    if (wave < 2) {
      const cplx e8 = E(8.f), e64 = E(64.f), e128 = E(128.f), e192 = E(192.f);
      cplx q[8];
      q[0] = {1.f, 0.f}; q[1] = e8; q[2] = cmul(e8, e8); q[3] = cmul(q[2], e8); q[4] = cmul(q[2], q[2]);
      q[5] = cmul(q[4], e8); q[6] = cmul(q[4], q[2]); q[7] = cmul(q[4], q[3]);
#pragma unroll
      for (int g = 0; g < 4; ++g) {
        cplx v[8];
#pragma unroll
        for (int j = 0; j < 8; ++j) v[j] = g == 0 ? q[j] : cmul(q[j], g == 1 ? e64 : (g == 2 ? e128 : e192));
        *(LAS v4u*)(img + ((g ^ sw) << 4)) = v4u{pkh(v[0].r, v[1].r), pkh(v[2].r, v[3].r), pkh(v[4].r, v[5].r), pkh(v[6].r, v[7].r)};
        *(LAS v4u*)(img + (((4 + g) ^ sw) << 4)) = v4u{pkh(v[0].i, v[1].i), pkh(v[2].i, v[3].i), pkh(v[4].i, v[5].i), pkh(v[6].i, v[7].i)};
      }
    } else {
      cplx W = cmul(cplx{bflo(vcre), bflo(vcim)}, cplx{dt * bflo(vbre), dt * bflo(vbim)});
      float mx = __builtin_fmaxf(__builtin_fabsf(W.r), __builtin_fabsf(W.i));
      mx = __builtin_fmaxf(mx, dppf<0xb1>(mx)); mx = __builtin_fmaxf(mx, dppf<0x4e>(mx)); mx = __builtin_fmaxf(mx, dppf<0x141>(mx)); mx = __builtin_fmaxf(mx, dppf<0x140>(mx));
      const unsigned mb = f2u(mx);
      const unsigned m01 = __builtin_elementwise_max((unsigned)__builtin_amdgcn_readlane(mb, 0), (unsigned)__builtin_amdgcn_readlane(mb, 16));
      const unsigned m23 = __builtin_elementwise_max((unsigned)__builtin_amdgcn_readlane(mb, 32), (unsigned)__builtin_amdgcn_readlane(mb, 48));
      int ex = (int)((__builtin_elementwise_max(m01, m23) >> 23) & 0xff);
      ex = ex < 20 ? 20 : (ex > 240 ? 240 : ex);
      const float sc = u2f((unsigned)(267 - ex) << 23);
      isc = u2f((unsigned)(ex - 13) << 23);
      W.r *= sc; W.i *= sc;
      const cplx e1 = E(1.f), e2 = cmul(e1, e1), e4 = cmul(e2, e2);
      cplx h[24];
      h[0] = W; h[1] = cmul(W, e1); h[2] = cmul(W, e2); h[3] = cmul(h[2], e1);
      h[4] = cmul(W, e4); h[5] = cmul(h[4], e1); h[6] = cmul(h[4], e2); h[7] = cmul(h[6], e1);
      const cplx g1 = E(256.f), g2 = E(512.f), g3 = E(768.f), g4 = E(1024.f);
      h[8] = cmul(h[0], g1); h[9] = cmul(h[2], g1); h[10] = cmul(h[4], g1); h[11] = cmul(h[6], g1);
      h[12] = cmul(h[0], g2); h[13] = cmul(h[4], g2); h[14] = cmul(h[0], g3); h[15] = cmul(h[4], g3);
      h[16] = cmul(h[0], g4); h[17] = cmul(h[8], g4); h[18] = cmul(h[12], g4); h[19] = cmul(h[14], g4);
      h[20] = cmul(h[16], g4); h[21] = {0.f, 0.f}; h[22] = {0.f, 0.f}; h[23] = {0.f, 0.f};
#pragma unroll
      for (int g = 0; g < 3; ++g) {
        const cplx* v = h + 8 * g;
        *(LAS v4u*)(img + ((g ^ sw) << 4)) = v4u{pkh(v[0].r, v[1].r), pkh(v[2].r, v[3].r), pkh(v[4].r, v[5].r), pkh(v[6].r, v[7].r)};
        *(LAS v4u*)(img + (((4 + g) ^ sw) << 4)) = v4u{pkh(-v[0].i, -v[1].i), pkh(-v[2].i, -v[3].i), pkh(-v[4].i, -v[5].i), pkh(-v[6].i, -v[7].i)};
      }
      *(LAS v4u*)(img + ((3 ^ sw) << 4)) = v4u{0u, 0u, 0u, 0u};
      *(LAS v4u*)(img + ((7 ^ sw) << 4)) = v4u{0u, 0u, 0u, 0u};
    }
    STAMP(1);
    group_sync(lds0, 5 + bt, 4, lane);
    if (wave >= 2) {
      const int hh = lane >> 5, blk = (lane >> 4) & 1, qq = (lane & 15) >> 2, p = lane & 3;
      LAS const char* Qi = lds + C_COFF + half * 8192;
      v2u a[8][2], bb[8][2];
#pragma unroll
      for (int ks = 0; ks < 8; ++ks)
#pragma unroll
        for (int t = 0; t < 2; ++t) {
          const int k = 16 * ks + 8 * hh + 4 * t + qq, mode = k >> 1, c = k & 1;
          const int off = mode * 128 + (((c * 4 + 2 * blk + (p >> 1)) ^ (mode & 7)) << 4) + (p & 1) * 8;
          a[ks][t] = trread(Qi + 16384 + off);
          bb[ks][t] = trread(Qi + off);
        }
      SCHED_BAR();
      f32x16 acc = {};
#pragma unroll
      for (int ks = 0; ks < 8; ++ks) acc = mfma32h(v4u{a[ks][0].x, a[ks][0].y, a[ks][1].x, a[ks][1].y}, v4u{bb[ks][0].x, bb[ks][0].y, bb[ks][1].x, bb[ks][1].y}, acc);
      LAS float* T = (LAS float*)(lds + C_UOFF) + half;
      const int q = lane & 31, hi = lane >> 5;
      auto put = [&](int idx, float v) { T[(idx + (idx >> 3)) * 2] = v * isc; };
#pragma unroll
      for (int r = 0; r < 4; ++r) put((r + 4 * hi) + 8 * q, acc[r]);
#pragma unroll
      for (int r = 0; r < 4; ++r) put(hi ? 384 + 64 * (r >> 1) + (r & 1) + 2 * q : 256 + r + 4 * q, acc[4 + r]);
      if (hi == 0) {
#pragma unroll
        for (int r = 0; r < 4; ++r) put(512 + 32 * r + q, acc[8 + r]);
      } else if (q == 0) put(640, acc[8]);
    }
    lds_post(lds0, 5 + bt, lane);
    asm volatile("s_waitcnt vmcnt(0)" : "+v"(pre[7]) :: "memory");
    if (bt == 0 && wave == 0) {
      for (unsigned spin = 0; spin < SPIN_CAP; ++spin) {
        asm volatile("" ::: "memory");
        const v4u f = ld128(rs, (unsigned)CTL_F0 + (wg & (F0_REP - 1)) * 1024 + lane * 16);
        if (wall(f.x == tag && f.y == ~tag && f.z == tag && f.w == ~tag)) break;
        __builtin_amdgcn_s_sleep(POLL_SLEEP);
      }
      __builtin_amdgcn_fence(__ATOMIC_ACQUIRE, "wavefront");
      lds_post(lds0, 8, lane);
    } else lds_wait(lds0, 8, 1);
    xv = ld128(rs, (unsigned)WS_XT + (tid * D + d) * 16);
#pragma unroll
    for (int i = 0; i < 2; ++i) st[i] = ld128(rs, (unsigned)WS_ST1 + (i * 256 + tid) * 16);
    SCHED_BAR();
    STAMP(2);
    lds_wait(lds0, 5 + bt, 8);
    {
      f32x2 tv[8];
#pragma unroll
      for (int j = 0; j < 8; ++j) tv[j] = *(LAS const f32x2*)(lds + C_UOFF + xoff[j]);
      SCHED_BAR();
      float kv[8];
#pragma unroll
      for (int j = 0; j < 8; ++j) kv[j] = tv[j][0] + tv[j][1];
      *(LAS v4u*)(lds + C_KOFF + 64 + tid * 16) = v4u{pkbf(kv[0], kv[1]), pkbf(kv[2], kv[3]), pkbf(kv[4], kv[5]), pkbf(kv[6], kv[7])};
      S[0] = kv[0];
#pragma unroll
      for (int j = 1; j < 8; ++j) S[j] = S[j - 1] + kv[j];
      float inc = S[7];
      inc += dppf<0x111>(inc); inc += dppf<0x112>(inc); inc += dppf<0x114>(inc); inc += dppf<0x118>(inc);
      inc += __builtin_bit_cast(float, __builtin_amdgcn_update_dpp(0, __builtin_bit_cast(int, inc), 0x142, 0xa, 0xf, false));
      inc += __builtin_bit_cast(float, __builtin_amdgcn_update_dpp(0, __builtin_bit_cast(int, inc), 0x143, 0xc, 0xf, false));
      soff = inc - S[7];
      if (lane == 63) ((LAS float*)(lds + C_KOFF + 4160))[wave] = inc;
    }
  }
  lds_post(lds0, 5 + bt, lane);
  lds_wait(lds0, 5 + bt, 12);
  STAMP(3);
  float xf[8], xn[8];
  {
#pragma unroll
    for (int j = 0; j < 4; ++j) { xf[2 * j] = bflo(xv[j]); xf[2 * j + 1] = bfhi(xv[j]); }
    const f32x4 wt = *(LAS const f32x4*)(lds + C_KOFF + 4160);
    if (tid < 128) *(LAS v4u*)(lds + C_UOFF + (tid >> 5) * C_UP + (tid & 31) * 16) = v4u{0u, 0u, 0u, 0u};
#pragma unroll
    for (int j = 0; j < 8; ++j) {
      const unsigned sj = st[j >> 2][j & 3];
      xn[j] = (xf[j] - bflo(sj)) * __builtin_amdgcn_rcpf(bfhi(sj));
    }
#pragma unroll
    for (int e = 0; e < 4; ++e) *(LAS unsigned*)(lds + C_UOFF + e * C_UP + 512 + tid * 4) = pkbf(xn[e], xn[e + 4]);
    soff += (wave > 0 ? wt[0] : 0.f) + (wave > 1 ? wt[1] : 0.f) + (wave > 2 ? wt[2] : 0.f);
  }
  const u16 dskv = A.dskip[d];
  char* mgp = ws + WS_MOD + (size_t)((lane & 3) * 3840 + (lane < 12 ? lane >> 2 : 0) * D + d) * 8;
  const unsigned long long mg = __hip_atomic_load((gu64*)mgp, RLX_AGENT);
  STAMP(5);
  group_sync(lds0, 5 + bt, 16, lane);
  STAMP(6);
  f32x16 y0 = {}, y1 = {};
  {
    const int hh = lane >> 5, blk = (lane >> 4) & 1, qq = (lane & 15) >> 2, p = lane & 3, col = lane & 31;
    LAS const char* ab = lds + C_KOFF + (32 + 16 * blk + 4 * p - 4 * qq + 32 * hh) * 2 - 32;
    LAS const char* bb = lds + C_UOFF + wave * C_UP + (256 + 8 * (col - hh) - 240) * 2;
    struct CF { v2u a0l, a0h, a1l, a1h; v4u b0, b1; };
    CF cf[3];
    auto rdc = [&](int ks, CF& f) {
      f.a0l = trread(ab + ks * 128 + 32); f.a0h = trread(ab + ks * 128);
      f.b0 = *(LAS const v4u*)(bb + (15 - ks) * 32);
      f.b1 = *(LAS const v4u*)(bb + (31 - ks) * 32);
      f.a1l = trread(ab + (ks + 16) * 128 + 32); f.a1h = trread(ab + (ks + 16) * 128);
    };
    rdc(0, cf[0]); rdc(1, cf[1]);
#pragma unroll
    for (int ks = 0; ks < 16; ++ks) {
      if (ks + 2 < 16) rdc(ks + 2, cf[(ks + 2) % 3]);
      SCHED_BAR();
      const CF& f = cf[ks % 3];
      const v4u a0 = {f.a0l.x, f.a0l.y, f.a0h.x, f.a0h.y}, a1 = {f.a1l.x, f.a1l.y, f.a1h.x, f.a1h.y};
      y0 = mfma32(a0, f.b0, y0);
      y1 = mfma32(a0, f.b1, y1);
      y1 = mfma32(a1, f.b0, y1);
      SCHED_BAR();
    }
    asm volatile("" :: "v"(y0), "v"(y1));
    STAMP(7);
    LAS char* cb = lds + C_COFF + (wave * 64 + col) * 144 + hh * 16;
#pragma unroll
    for (int g = 0; g < 4; ++g) {
      *(LAS f32x4*)(cb + g * 32) = f32x4{y0[4 * g], y0[4 * g + 1], y0[4 * g + 2], y0[4 * g + 3]};
      *(LAS f32x4*)(cb + 32 * 144 + g * 32) = f32x4{y1[4 * g], y1[4 * g + 1], y1[4 * g + 2], y1[4 * g + 3]};
    }
  }
  lds_post(lds0, 5 + bt, lane);
  unsigned mv[1] = {(unsigned)mg};
  if (!wall((unsigned)(mg >> 32) == tag)) get_granules<1>(mv, tag, [&](int) { return mgp; });
  float msum = u2f(mv[0]);
  msum += dppf<0xb1>(msum); msum += dppf<0x4e>(msum);
  const unsigned mb = f2u(bfr(msum));
  const float sa = u2f(__builtin_amdgcn_readlane(mb, 0)), c1a = bfr(1.0f + u2f(__builtin_amdgcn_readlane(mb, 4))), ga = u2f(__builtin_amdgcn_readlane(mb, 8)), dsk = bf2f(dskv);
  const float gc = ga * c1a, gs_ = ga * sa, gd = gc * dsk, g0 = gs_ * (soff + dsk);
#pragma unroll
  for (int j = 0; j < 8; ++j) xf[j] = __builtin_fmaf(gd, xn[j], __builtin_fmaf(gs_, S[j], xf[j] + g0));
  STAMP(8);
  lds_wait(lds0, 5 + bt, 20);
  STAMP(9);
  float y[8];
  {
    const int c1 = 2 * tid, c0 = tid ? c1 - 1 : 0;
    const int o0 = (c0 >> 3) * 144 + (c0 & 7) * 16, o1 = (c1 >> 3) * 144 + (c1 & 7) * 16;
    LAS const char* cb = lds + C_COFF;
    f32x4 w0[4], w1[4], w2[4];
#pragma unroll
    for (int e = 0; e < 4; ++e) {
      w1[e] = *(LAS const f32x4*)(cb + e * 64 * 144 + o1); w2[e] = *(LAS const f32x4*)(cb + e * 64 * 144 + o1 + 16);
      if (e) w0[e] = *(LAS const f32x4*)(cb + e * 64 * 144 + o0);
    }
    SCHED_BAR();
#pragma unroll
    for (int e = 0; e < 4; ++e) {
      float w[12];
#pragma unroll
      for (int j = 0; j < 4; ++j) { w[4 + j] = w1[e][j]; w[8 + j] = w2[e][j]; w[j] = (e && tid) ? w0[e][j] : 0.f; }
#pragma unroll
      for (int j = 0; j < 8; ++j) y[j] = e ? y[j] + w[4 + j - e] : w[4 + j];
    }
  }
  float o[8];
#pragma unroll
  for (int j = 0; j < 8; ++j) o[j] = __builtin_fmaf(gc, y[j], xf[j]);
  {
    const unsigned a0 = pkbf(o[0], o[1]), a1 = pkbf(o[2], o[3]), b0 = pkbf(o[4], o[5]), b1 = pkbf(o[6], o[7]);
    const bool odd = tid & 1;
    const unsigned s0 = odd ? a0 : b0, s1 = odd ? a1 : b1;
    const unsigned r0 = __builtin_amdgcn_update_dpp(0, s0, 0xb1, 0xf, 0xf, true), r1 = __builtin_amdgcn_update_dpp(0, s1, 0xb1, 0xf, 0xf, true);
    const v4u ov = odd ? v4u{r0, r1, b0, b1} : v4u{a0, a1, r0, r1};
    st128(rs, (unsigned)WS_X2T + d * (L * 2) + (tid >> 2) * 64 + (tid & 1) * 32 + (tid & 2) * 8, ov);
  }
  STAMP(10);
  if (wave_arrive(lds0, bt, 4, lane)) signal<E1_REP, 8>(ws + CTL_E1 + bt * 65536, wg & 7, lane);
}

constexpr int TP = 1312;

__device__ __forceinline__ void aux_group(LAS char* lds, LAS char* lds0, const Args& A, __amdgpu_buffer_rsrc_t rs, unsigned epoch, int u, int tid, int wave, int lane STAMP_ARG) {
  char* ws = A.ws;
  unsigned gs = 0;
  v4u wb[5];
  unsigned tag;
  asm volatile("s_waitcnt vmcnt(1)\n\tv_readfirstlane_b32 %0, %1\n\ts_nop 3" : "=s"(tag) : "v"(epoch) : "memory");
  tag += 1u;
  auto load_wb = [&]() {
    const u16* bp = A.win + (size_t)(160 * (u & 3) + (tid >> 3)) * F + 64 * ((u < 108 ? u : 0) >> 2) + (tid & 7) * 8;
#pragma unroll
    for (int i = 0; i < 5; ++i) wb[i] = NTLOAD((const v4u*)(bp + (size_t)i * 32 * F));
  };
  {
    LAS const char* img = lds + 8192;
    group_sync(lds0, 7, gs += 4, lane);
    STAMP(3);
    const int r = tid >> 4, s = tid & 15;
    v4u v[5];
    {
      const int q = lane >> 4, rr = (lane & 15) >> 2, p = lane & 3, f = (rr | ((q & 1) << 2)) << 1;
      v2u lo[5], hi[5];
#pragma unroll
      for (int j = 0; j < 5; ++j) {
        const int a0 = (2 * (q & 1) * 5 + j) * 1024 + rr * 256 + ((((2 * wave + (q >> 1)) * 2 + (p >> 1)) ^ f) << 4) + (p & 1) * 8;
        lo[j] = trread(img + a0); hi[j] = trread(img + a0 + 5120);
      }
#pragma unroll
      for (int i = 0; i < 5; ++i) v[i] = *(LAS const v4u*)(img + (wave * 5 + i) * 1024 + lane * 16);
      SCHED_BAR();
#pragma unroll
      for (int j = 0; j < 5; ++j)
        st128(rs, (unsigned)WS_XT + ((u * 2 + (q & 1)) * D + (8 * j + 2 * wave + (q >> 1)) * 16 + (lane & 15)) * 16, v4u{lo[j].x, lo[j].y, hi[j].x, hi[j].y});
      SCHED_BAR();
    }
    float s0 = 0.f, s1 = 0.f, q0 = 0.f, q1 = 0.f;
#pragma unroll
    for (int i = 0; i < 5; ++i) {
      s0 = dot2bf(v[i][0], 0x3f803f80u, s0); s1 = dot2bf(v[i][1], 0x3f803f80u, s1); s0 = dot2bf(v[i][2], 0x3f803f80u, s0); s1 = dot2bf(v[i][3], 0x3f803f80u, s1);
      q0 = dot2bf(v[i][0], v[i][0], q0); q1 = dot2bf(v[i][1], v[i][1], q1); q0 = dot2bf(v[i][2], v[i][2], q0); q1 = dot2bf(v[i][3], v[i][3], q1);
    }
    float sum = s0 + s1, sq = q0 + q1;
    sum += dppf<0xb1>(sum); sq += dppf<0xb1>(sq); sum += dppf<0x4e>(sum); sq += dppf<0x4e>(sq);
    sum += dppf<0x141>(sum); sq += dppf<0x141>(sq); sum += dppf<0x140>(sum); sq += dppf<0x140>(sq);
    const float mean = sum * (1.0f / D);
    const int t = u * 16 + r;
    st32(rs, s == 0 ? (unsigned)WS_ST1 + ((((t >> 2) & 1) * 256 + (t >> 3)) * 4 + (t & 3)) * 4 : 0x7ffffff0u, pkbf(mean, ln_std(__builtin_fmaxf(sq * (1.0f / D) - mean * mean, 0.f))));
    STAMP(8);
    if (wave_arrive(lds0, 9, 4, lane) && lane < F0_REP)
      __hip_atomic_store((gu64*)(ws + CTL_F0 + lane * 1024 + u * 8), ((unsigned long long)~tag << 32) | tag, RLX_AGENT);
    STAMP(1);
  }
  load_wb();
  group_sync(lds0, 7, gs += 4, lane);
  if (u < 108) {
    const int cbk = u >> 2, part = u & 3;
    const int c0 = 64 * cbk, r0 = 160 * part, seg = tid & 7, rg = tid >> 3;
    v4u w[5];
#pragma unroll
    for (int i = 0; i < 5; ++i) w[i] = wb[i];
    LAS f32x2* mt2 = (LAS f32x2*)(lds + 28672);
    if (wave < 3) {
      unsigned mv[8];
      const int rw = r0 + (tid < 160 ? tid : 159);
      get_granules<8>(mv, tag, [&](int k) { return ws + WS_MOD + (size_t)((k & 3) * 3840 + (3 + (k >> 2)) * D + rw) * 8; });
      mt2[tid] = f32x2{bfr((u2f(mv[0]) + u2f(mv[1])) + (u2f(mv[2]) + u2f(mv[3]))), bfr(1.0f + bfr((u2f(mv[4]) + u2f(mv[5])) + (u2f(mv[6]) + u2f(mv[7]))))};
    }
    group_sync(lds0, 7, gs += 4, lane);
    STAMP(2);
    float v[16] = {};
    constexpr int WPITCH = 144;
    LAS char* img = lds + 1024;
#pragma unroll
    for (int i = 0; i < 5; ++i) {
      const f32x2 ms = mt2[rg + 32 * i];
      const float c1 = ms.y, sb = ms.x;
#pragma unroll
      for (int j = 0; j < 4; ++j) {
        const float a = bflo(w[i][j]), c = bfhi(w[i][j]);
        const unsigned pk = pkbf(a * c1, c * c1);
        v[8 + 2 * j] += sb * a; v[8 + 2 * j + 1] += sb * c;
        v[2 * j] += bflo(pk); v[2 * j + 1] += bfhi(pk);
        w[i][j] = pk;
      }
      *(LAS v4u*)(img + (rg + 32 * i) * WPITCH + seg * 16) = w[i];
    }
    treduce_xor<8>(v, (lane & 32) != 0, 32);
    treduce_xor<4>(v, (lane & 16) != 0, 16);
    treduce_dpp<2, 0x128>(v, (lane & 8) != 0);
    LAS float* red = (LAS float*)(lds + 24576 + 1024);
    {
      const int which = lane >> 5, j = ((lane >> 4) & 1) * 4 + ((lane >> 3) & 1) * 2;
      *(LAS f32x2*)(red + wave * 128 + which * 64 + seg * 8 + j) = f32x2{v[0], v[1]};
    }
    group_sync(lds0, 7, gs += 4, lane);
    STAMP(3);
    const int q = lane >> 4, rr = (lane & 15) >> 2, p = lane & 3;
    const int colb = (32 * (wave >> 1) + 8 * p + 4 * (wave & 1)) * 2;
#pragma unroll
    for (int i = 0; i < 5; ++i) {
      const int a0 = (32 * i + 8 * q + rr) * WPITCH + colb;
      v2u lo = trread(img + a0), hi = trread(img + a0 + 4 * WPITCH);
      st128(rs, (unsigned)WS_WIN + (((4 * cbk + wave) * 20 + part * 5 + i) * 64 + lane) * 16, v4u{lo.x, lo.y, hi.x, hi.y});
    }
    if (tid < 128) {
      const int which = tid >> 6, o = tid & 63, sub = o >> 4, i = o & 15;
      const int cl = 32 * (sub >> 1) + 8 * (i >> 2) + 4 * (sub & 1) + (i & 3), ix = which * 64 + cl;
      st32(rs, (unsigned)WS_U + ((part * 2 + which) * F + c0 + o) * 4, f2u((red[ix] + red[128 + ix]) + (red[256 + ix] + red[384 + ix])));
    }
  } else {
    LAS char* img = lds + 1024;
    __builtin_amdgcn_s_setprio(0);
#pragma unroll 1
    for (int slab = u - 108; slab < 27; slab += 20) {
      const int r = tid >> 3, s = tid & 7;
      const int half = (r >> 2) & 1, jj = slab < 24 ? slab / 3 : 2 * (slab - 24) + half;
      const int sub = (jj < 6 ? 7 * jj : 42 + 6 * (jj - 6)) + (slab < 24 ? 2 * (slab - 3 * jj) + half : 6);
      const u16* src = A.wout + (size_t)(32 * (sub >> 1) + 8 * (r >> 3) + 4 * (sub & 1) + (r & 3)) * D;
      v4u v[10];
#pragma unroll
      for (int i = 0; i < 10; ++i) v[i] = NTLOAD((const v4u*)(src + (s + 8 * i) * 8));
      group_sync(lds0, 7, gs += 4, lane);
#pragma unroll
      for (int i = 0; i < 10; ++i) *(LAS v4u*)(img + r * TP + (s + 8 * i) * 16) = v[i];
      group_sync(lds0, 7, gs += 4, lane);
      const int a0 = (8 * (lane >> 4) + ((lane & 15) >> 2)) * TP + (lane & 3) * 8 + wave * 32;
#pragma unroll
      for (int j = 0; j < 10; ++j) {
        const v2u lo = trread(img + a0 + j * 128), hi = trread(img + a0 + 4 * TP + j * 128);
        st128(rs, (unsigned)WS_WOUT + (((wave + 4 * j) * 27 + slab) * 64 + lane) * 16, v4u{lo.x, lo.y, hi.x, hi.y});
      }
    }
  }
  STAMP(8);
  lds_wait(lds0, 8, 1);
  if (wave_arrive(lds0, 2, 4, lane)) {
    if (u < 108) signal<E1_REP, 8>(ws + CTL_E1B, u & 7, lane);
    else signal<32, 1>(ws + CTL_EW, 0, lane);
  }
}

__device__ __forceinline__ int g1_aoff(int row, int G, int ab, int p) {
  const int f = ((row >> 1) & 1) | (((row >> 3) & 1) << 1);
  return row * 128 + (((4 * G + 2 * ab + (p >> 1)) ^ (f << 1)) << 4) + (p & 1) * 8;
}

template <int NB>
__device__ __forceinline__ void loader_path(LAS char* lds, char* ws, __amdgpu_buffer_rsrc_t rs, const u16* __restrict__ wsp, int wg, int mt, int j, int sub0, int lw, int lane) {
  const unsigned lbase = (unsigned)(size_t)lds, vo = lane * 16;
  {
    unsigned avo[2];
#pragma unroll
    for (int h = 0; h < 2; ++h) {
      const int row = lane >> 3, pos = lane & 7, f = ((row >> 1) & 1) | (h << 1);
      avo[h] = row * (L * 2) + ((pos ^ (f << 1)) << 4);
    }
    const char* asb = ws + WS_X2T + (size_t)(16 * lw) * (L * 2) + mt * 128;
    const char* bsb = ws + WS_WIN + (size_t)((sub0 + (lw >> 1)) * 20 + (lw & 1)) * 1024;
    const unsigned bb = lbase + G1_BOFF;
    auto issueB = [&](int s, unsigned slot) {
      const char* bs = bsb + (s < 10 ? 54 * 20 * 1024 + s * 2048 : (s - 10) * 2048);
#pragma unroll
      for (int i = 0; i < NB; ++i) glds16<true>(bs + i * 2 * 20 * 1024, vo, slot + (lw + 4 * i) * 1024);
    };
    auto issueA = [&](int s0, int s1) {
#pragma unroll 1
      for (int s = s0; s < s1; ++s)
#pragma unroll
        for (int h = 0; h < 2; ++h) glds16<true>(asb + (size_t)(s * 64 + h * 8) * (L * 2), avo[h], lbase + s * 8192 + lw * 2048 + h * 1024);
    };
    {
      const unsigned vb = (unsigned)WS_V + (mt * 4 + lw) * 27 * 1024 + lane * 16;
#pragma unroll
      for (int k = 0; k < 3; ++k) st128(rs, vb + (3 * j + k) * 1024, v4u{~0u, ~0u, ~0u, ~0u});
      if (j < 6) __builtin_amdgcn_raw_buffer_store_b64(v2u{~0u, ~0u}, rs, vb + (24 + (j >> 1)) * 1024 + (j & 1) * 8, 0, 16);
    }
    group_sync(lds, 5, 24, lane);
    if (lw == 0) { wait_count<8>(ws + CTL_E1B, wg & (E1_REP - 1), 108, lane); lds_post(lds, 10, lane); }
    else lds_wait(lds, 10, 1);
    STAMP(13);
#pragma unroll
    for (int s = 0; s < 4; ++s) issueB(s, bb + (s + 1) * G1_BST);
    STAMP(12);
    LBAR();
    LSTAMP(1); STAMP(14);
    issueA(0, 4); issueB(4, bb);
    waitv<6 + NB>(); LBAR(); LSTAMP(2);
    waitv<4 + NB>(); LBAR(); issueB(5, bb + G1_BST);
    waitv<2 + 2 * NB>(); LBAR(); LSTAMP(3); issueA(4, 8); issueB(6, bb + 2 * G1_BST);
    waitv<8 + 3 * NB>(); LBAR(); issueB(7, bb + 3 * G1_BST);
    waitv<6 + 2 * NB>(); LBAR(); LSTAMP(4); issueB(8, bb + 4 * G1_BST);
    waitv<4 + 3 * NB>(); LBAR(); LSTAMP(5); issueA(8, 10); issueB(9, bb);
    waitv<4 + 3 * NB>(); LBAR(); issueB(10, bb + G1_BST);
    waitv<4 + 3 * NB>(); LBAR(); issueB(11, bb + 2 * G1_BST);
    waitv<2 + 3 * NB>(); LBAR(); LSTAMP(6); issueB(12, bb + 3 * G1_BST);
    int nxt = 4 * G1_BST;
#pragma unroll 1
    for (int i = 9; i <= 15; ++i) {
      waitv<NB * 3>(); LBAR();
      if (i >= 11) LSTAMP(i - 4);
      issueB(i + 4, bb + nxt);
      nxt += G1_BST; if (nxt == G1_NSLOT * G1_BST) nxt = 0;
    }
    waitv<NB * 3>(); LBAR(); LSTAMP(12);
    waitv<NB * 2>(); LBAR(); LSTAMP(13);
    waitv<NB * 1>(); LBAR();
    waitv<0>(); LBAR(); LSTAMP(14);
    LBAR();
  }
  const char* osb = ws + WS_WOUT + (size_t)j * 135 * 1024;
  if (lw >= 2) {
    unsigned wvo[2];
#pragma unroll
    for (int h = 0; h < 2; ++h) {
      const int i = 8 * h + (lane >> 3);
      wvo[h] = (8 * (i >> 2) + (i & 3)) * (L * 2) + (((lane & 7) ^ (i >> 1)) << 4);
    }
    const char* wsb = (const char*)wsp + (size_t)(mt * 64 + 32 * (lw & 1)) * (L * 2);
    auto issueW = [&](int s, unsigned slot) {
#pragma unroll
      for (int i = 0; i < 4; ++i) glds16<false>(wsb + (i >> 1) * 4 * (L * 2) + s * 128, wvo[i & 1], slot + (lw & 1) * 4096 + i * 1024);
    };
#pragma unroll 1
    for (int s = 0; s < G2_NW; ++s) issueW(s, lbase + s * 8192);
    LBAR();
    waitv<32>(); LBAR();
    int wn = 0;
#pragma unroll 1
    for (int i = 1; i <= 23; ++i) { waitv<28>(); LBAR(); issueW(i + 8, lbase + wn); wn += 8192; if (wn == G2_NW * 8192) wn = 0; }
#pragma unroll 1
    for (int i = 24; i <= 32; ++i) {
      waitv<28>(); LBAR();
#pragma unroll
      for (int k = 0; k < 4; ++k) glds16<true>(osb + wn + (lw & 1) * 4096 + k * 1024, vo, lbase + wn + (lw & 1) * 4096 + k * 1024);
      wn += 8192; if (wn == G2_NW * 8192) wn = 0;
    }
  } else {
    constexpr int NZ = NB == 4 ? 7 : 6;
    const char* zsb = ws + WS_Z2 + (size_t)(sub0 * 64 + lw) * 1024;
    auto issueZ = [&](int s, unsigned slot) {
#pragma unroll
      for (int i = 0; i < NZ; ++i) glds16<true>(zsb + i * 65536 + s * 2048, vo, slot + (lw + 2 * i) * 1024);
    };
    const unsigned zb = lbase + G2_ZOFF;
    LBAR();
#pragma unroll 1
    for (int s = 0; s < G2_NZ; ++s) issueZ(s, zb + s * G2_ZST);
    waitv<NZ * 5>(); LBAR();
    int zn = 0;
#pragma unroll 1
    for (int i = 1; i <= 26; ++i) { waitv<NZ * 4>(); LBAR(); issueZ(i + 5, zb + zn); zn += G2_ZST; if (zn == G2_NZ * G2_ZST) zn = 0; }
    auto issueO = [&](int slot, int n) {
      const int o = G2_ZOFF + slot * G2_ZST + lw * 1024;
#pragma unroll
      for (int k = 0; k < 7; ++k) if (k < n) glds16<true>(osb + o + k * 2048, vo, lbase + o + k * 2048);
    };
    waitv<NZ * 4>(); LBAR(); issueO(2, 7);
    waitv<NZ * 3 + 7>(); LBAR(); issueO(3, 7);
    waitv<NZ * 2 + 14>(); LBAR(); issueO(4, 3);
    waitv<NZ + 17>(); LBAR(); if (lw == 0) glds16<true>(osb + 134 * 1024, vo, lbase + 134 * 1024);
    waitv<17>(); LBAR(); issueO(0, 7);
    LBAR(); issueO(1, 7);
  }
  waitv<0>();
  LBAR();
}

template <int NSUB, bool STATS>
__device__ __forceinline__ void consumer_g12(LAS char* lds, char* ws, __amdgpu_buffer_rsrc_t rs, const u16* __restrict__ bsp, const u16* __restrict__ wsp, int wg, int mt, int j, int sub0, int cnt,
                                             int wave, int tid, int lane, v2u (&x2v)[3] STAMP_ARG) {
  constexpr int NS1 = NSUB > 0 ? NSUB : 1;
  const int G = wave >> 2, ng = G ? 3 - (wave & 3) : (wave & 3);
  const int q = lane >> 4, i16 = lane & 15, rr = i16 >> 2, p = lane & 3, n0 = 2 * ng;
  float bias[2];
#pragma unroll
  for (int m = 0; m < 2; ++m) bias[m] = bf2f(bsp[mt * 64 + 32 * G + 8 * (i16 >> 2) + 4 * m + (i16 & 3)]);
  if (wave == 3) wait_count<8>(ws + CTL_E1, wg & (E1_REP - 1), 256, lane);
  STAMP(4);
  CBAR();
  {
    const int c = tid & 127, which = (tid >> 7) & 1, half = tid >> 8;
    if (c < cnt * 16) {
      float up[4];
#pragma unroll
      for (int k = 0; k < 4; ++k)
        up[k] = u2f(__builtin_amdgcn_raw_buffer_load_b32(rs, (unsigned)WS_U + ((k * 2 + which) * F + (sub0 + (half ? 0 : 54)) * 16 + c) * 4, 0, 16));
      ((LAS float*)(lds + G1_T_U))[tid] = (up[0] + up[1]) + (up[2] + up[3]);
    }
  }
  v2u z1v[NS1][2];
  {
    const int aoff0 = g1_aoff(8 * q + rr, G, 0, p), aoff1 = g1_aoff(8 * q + rr, G, 1, p);
    const int boff = G1_BOFF + n0 * 2048 + lane * 16;
    struct Frags { v4u a[2][2]; v4u b[2][NS1]; };
    f32x4 acc2[NS1][2], acc1[NS1][2], asum[2], asq[2];
    int acur = 0, bcur = G1_BST;
    auto rd = [&](Frags& f) {
#pragma unroll
      for (int kk = 0; kk < 2; ++kk) {
        const v2u l0 = trread(lds + acur + aoff0 + kk * 4096), h0 = trread(lds + acur + aoff0 + kk * 4096 + 512);
        const v2u l1 = trread(lds + acur + aoff1 + kk * 4096), h1 = trread(lds + acur + aoff1 + kk * 4096 + 512);
        f.a[kk][0] = v4u{l0.x, l0.y, h0.x, h0.y}; f.a[kk][1] = v4u{l1.x, l1.y, h1.x, h1.y};
#pragma unroll
        for (int n = 0; n < NSUB; ++n) f.b[kk][n] = *(LAS const v4u*)(lds + bcur + boff + n * 2048 + kk * 1024);
      }
      acur += 8192; if (acur == 81920) acur = 0;
      bcur += G1_BST; if (bcur == G1_NSLOT * G1_BST) bcur = 0;
      SCHED_BAR();
    };
    auto pin1 = [&]() {
#pragma unroll
      for (int n = 0; n < NSUB; ++n) asm volatile("" : "+v"(acc1[n][0]), "+v"(acc1[n][1]));
    };
#pragma unroll
    for (int ab = 0; ab < 2; ++ab) {
      asum[ab] = f32x4{0.f, 0.f, 0.f, 0.f}; asq[ab] = f32x4{0.f, 0.f, 0.f, 0.f};
#pragma unroll
      for (int n = 0; n < NS1; ++n) { acc2[n][ab] = f32x4{0.f, 0.f, 0.f, 0.f}; acc1[n][ab] = f32x4{0.f, 0.f, 0.f, 0.f}; }
    }
    const v4u ones = {0x3f803f80u, 0x3f803f80u, 0x3f803f80u, 0x3f803f80u};
    auto mm2 = [&](const Frags& f) {
#pragma unroll
      for (int kk = 0; kk < 2; ++kk)
#pragma unroll
        for (int ab = 0; ab < 2; ++ab) {
#pragma unroll
          for (int n = 0; n < NSUB; ++n) acc2[n][ab] = mfma16(f.a[kk][ab], f.b[kk][n], acc2[n][ab]);
          if (STATS) { asum[ab] = mfma16(f.a[kk][ab], ones, asum[ab]); asq[ab] = mfma16(f.a[kk][ab], f.a[kk][ab], asq[ab]); }
        }
    };
    auto mm1 = [&](const Frags& f) {
#pragma unroll
      for (int kk = 0; kk < 2; ++kk)
#pragma unroll
        for (int ab = 0; ab < 2; ++ab)
#pragma unroll
          for (int n = 0; n < NSUB; ++n) acc1[n][ab] = mfma16(f.b[kk][n], f.a[kk][ab], acc1[n][ab]);
    };
    Frags f0, f1;
    CBAR(); rd(f0);
    STAMP(5);
    const bool poller = STATS && G == 0;
    unsigned pc = 0, pv = 0;
    if (poller) pc = poll_issue<8>(ws + CTL_E1 + 65536, wg & (E1_REP - 1), lane);
#pragma unroll 1
    for (int it = 0; it < 5; ++it) {
      if (poller && it == 2) poll_finish<8>(pc, ws + CTL_E1 + 2 * 65536, wg & (E1_REP - 1), 128, lane);
      if (!STATS && ng < 2 && it == 1 + G * 2 + ng)
        pv = *(const unsigned*)((const char*)wsp + (size_t)(mt * 64 + (G * 2 + ng) * 16 + (lane >> 2)) * (L * 2) + j * 512 + (lane & 3) * 128);
      CBAR(); rd(f1); mm2(f0);
      if (poller && it == 0) poll_finish<8>(pc, ws + CTL_E1 + 65536, wg & (E1_REP - 1), 256, lane);
      if (poller && it == 1) pc = poll_issue<8>(ws + CTL_E1 + 2 * 65536, wg & (E1_REP - 1), lane);
      CBAR(); rd(f0); mm2(f1);
    }
    LAS f32x2* tab = (LAS f32x2*)(lds + G1_T_LN2);
    if (STATS) {
#pragma unroll
      for (int ab = 0; ab < 2; ++ab) {
        const float a0 = asum[ab][0], a1 = asum[ab][1], a2 = asum[ab][2], a3 = asum[ab][3];
        const float b0 = asq[ab][0], b1 = asq[ab][1], b2 = asq[ab][2], b3 = asq[ab][3];
        const float s1 = (p & 2) ? ((p & 1) ? a3 : a2) : ((p & 1) ? a1 : a0), s2 = (p & 2) ? ((p & 1) ? b3 : b2) : ((p & 1) ? b1 : b0);
        const float mean = s1 * (1.0f / D);
        float mb, rinv;
        ln_scalars(mean, __builtin_fmaxf(s2 * (1.0f / D) - mean * mean, 0.f), mb, rinv);
        if (rr == q) tab[(G * 2 + ab) * 16 + i16] = f32x2{rinv, rinv * mb};
      }
    }
    {
      const int nb = G ? 3 : 0;
#pragma unroll
      for (int n = 0; n < 3; ++n) {
        const int row = 80 * j + 16 * (nb + n < 5 ? nb + n : 4) + 4 * q + rr;
        x2v[n] = trread(lds + g1_aoff(row, (wave >> 1) & 1, wave & 1, p));
      }
    }
    STAMP(6);
    CBAR(); rd(f1); mm1(f0);
    v4u zq[NS1];
    {
      LAS const float* ut = (LAS const float*)(lds + G1_T_U);
      float v[16];
#pragma unroll
      for (int e = 0; e < 16; ++e) v[e] = 0.f;
      if (NSUB > 0) {
        f32x2 rv[8];
#pragma unroll
        for (int e = 0; e < 8; ++e) rv[e] = tab[(G * 2 + (e >> 2)) * 16 + 4 * q + (e & 3)];
#pragma unroll
        for (int n = 0; n < NSUB; ++n) {
          const float c1 = ut[(n0 + n) * 16 + i16], c2 = ut[128 + (n0 + n) * 16 + i16];
          float z[8];
#pragma unroll
          for (int e = 0; e < 8; ++e) z[e] = gelu_f(__builtin_fmaf(rv[e].x, acc2[n][e >> 2][e & 3], __builtin_fmaf(-rv[e].y, c1, c2)));
          zq[n] = v4u{pkbf(z[0], z[1]), pkbf(z[2], z[3]), pkbf(z[4], z[5]), pkbf(z[6], z[7])};
#pragma unroll
          for (int e = 0; e < 4; ++e) {
            const float a = bflo(zq[n][e]), c = bfhi(zq[n][e]);
            v[4 * e] += a; v[4 * e + 1] = __builtin_fmaf(a, a, v[4 * e + 1]);
            v[4 * e + 2] += c; v[4 * e + 3] = __builtin_fmaf(c, c, v[4 * e + 3]);
          }
        }
        treduce_dpp<8, 0x140>(v, (i16 & 8) != 0);
        treduce_dpp<4, 0x141>(v, (i16 & 4) != 0);
        treduce_dpp<2, 0x4e>(v, (i16 & 2) != 0);
        treduce_dpp<1, 0xb1>(v, (i16 & 1) != 0);
      }
      ((LAS float*)(lds + G1_T_P))[ng * 128 + G * 64 + lane] = v[0];
    }
    CBAR(); pin1(); rd(f0); mm1(f1);
    GAS unsigned long long* gr = (GAS unsigned long long*)(ws + CTL_ST3) + (size_t)mt * 1024 + G * 32;
    if (STATS) {
      LAS const float* pp = (LAS const float*)(lds + G1_T_P) + G * 64 + lane;
      const float sv = (pp[0] + pp[128]) + (pp[256] + pp[384]);
      __hip_atomic_store(gr + (j * 2 + (lane & 1)) * 64 + (lane >> 1), (1ull << 32) | f2u(sv), RLX_AGENT);
    }
    CBAR(); pin1(); rd(f1); mm1(f0);
    CBAR(); pin1(); rd(f0); mm1(f1);
    unsigned long long gx[8];
    if (STATS) {
#pragma unroll
      for (int k = 0; k < 8; ++k) gx[k] = __hip_atomic_load(gr + (k * 2 + (lane >> 5)) * 64 + (lane & 31), RLX_AGENT);
    }
    CBAR(); pin1(); rd(f1); mm1(f0);
    if (STATS) {
      float sv;
      for (unsigned spin = 0;; ++spin) {
        bool ok = true;
        sv = 0.f;
#pragma unroll
        for (int k = 0; k < 8; ++k) { ok &= (gx[k] >> 32) == 1ull; sv += u2f((unsigned)gx[k]); }
        if (wall(ok) || spin > SPIN_CAP) break;
        __builtin_amdgcn_s_sleep(1);
#pragma unroll
        for (int k = 0; k < 8; ++k) gx[k] = __hip_atomic_load(gr + (k * 2 + (lane >> 5)) * 64 + (lane & 31), RLX_AGENT);
      }
      const float s2 = shx(sv, 32);
      const float mean = sv * (1.0f / FH);
      float mb, rinv;
      ln_scalars(mean, __builtin_fmaxf(s2 * (1.0f / FH) - mean * mean, 0.f), mb, rinv);
      if (lane < 32) ((LAS f32x2*)(lds + G1_T_Z))[32 * G + lane] = f32x2{-mb * rinv, rinv};
    }
    CBAR(); pin1(); rd(f0); mm1(f1);
    if (NSUB > 0) {
      f32x2 zs[8];
#pragma unroll
      for (int e = 0; e < 8; ++e) zs[e] = ((LAS const f32x2*)(lds + G1_T_Z))[32 * G + 8 * q + e];
#pragma unroll
      for (int n = 0; n < NSUB; ++n) {
        v4u o;
#pragma unroll
        for (int e = 0; e < 4; ++e)
          o[e] = pkbf(__builtin_fmaf(bflo(zq[n][e]), zs[2 * e].y, zs[2 * e].x), __builtin_fmaf(bfhi(zq[n][e]), zs[2 * e + 1].y, zs[2 * e + 1].x));
        st128(rs, (unsigned)WS_Z2 + (((sub0 + n0 + n) * 64 + mt * 2 + G) * 64 + lane) * 16, o);
      }
    }
    CBAR(); pin1(); rd(f1); mm1(f0);
    CBAR(); pin1(); rd(f0); mm1(f1);
    CBAR(); pin1(); rd(f1); mm1(f0);
    asm volatile("" :: "v"(pv));
    if (wave_arrive(lds, 3, 8, lane)) signal<4, 4>(ws + CTL_E2 + j * 4096, mt & 3, lane);
    CBAR(); pin1();
    mm1(f1);
    STAMP(7);
    {
      LAS const float* ut = (LAS const float*)(lds + G1_T_U) + 256;
      f32x2 rv[2];
#pragma unroll
      for (int ab = 0; ab < 2; ++ab) rv[ab] = tab[(G * 2 + ab) * 16 + i16];
#pragma unroll
      for (int n = 0; n < NSUB; ++n) {
        const f32x4 u1 = *(LAS const f32x4*)(ut + (n0 + n) * 16 + 4 * q), u2 = *(LAS const f32x4*)(ut + 128 + (n0 + n) * 16 + 4 * q);
#pragma unroll
        for (int ab = 0; ab < 2; ++ab) {
          float z[4];
#pragma unroll
          for (int e = 0; e < 4; ++e) z[e] = gelu_f(__builtin_fmaf(rv[ab].x, acc1[n][ab][e], __builtin_fmaf(-rv[ab].y, u1[e], u2[e])));
          z1v[n][ab] = v2u{pkbf(z[0], z[1]), pkbf(z[2], z[3])};
        }
      }
    }
  }
  if (wave == 1 || wave == 2) {
    const int c = tid - 64;
    if (c < 80) {
      const unsigned o = (unsigned)WS_MOD + (5 * D + 80 * j + c) * 8;
      ((LAS float*)(lds + G3_GB))[c] = bfr((ld32f(rs, o) + ld32f(rs, o + 30720)) + (ld32f(rs, o + 61440) + ld32f(rs, o + 92160)));
    }
  }
  if (wave == 3) wait_count<4>(ws + CTL_E2 + j * 4096, (mt >> 2) & 3, 32, lane);
  if (wave == 4) wait_count<1>(ws + CTL_EW, wg & 31, 20, lane);
  CBAR();
  STAMP(9);
  {
    f32x4 acc[NS1][2];
#pragma unroll
    for (int n = 0; n < NS1; ++n) { acc[n][0] = f32x4{0.f, 0.f, 0.f, 0.f}; acc[n][1] = f32x4{0.f, 0.f, 0.f, 0.f}; }
    int woff[2];
#pragma unroll
    for (int kk = 0; kk < 2; ++kk) woff[kk] = G * 4096 + i16 * 128 + (((4 * kk + q) ^ (i16 >> 1)) << 4);
    const int zoff = G2_ZOFF + n0 * 2048 + lane * 16;
    struct Frags { v4u w[2][2]; v4u z[2][NS1]; };
    int wc = 0, zc = 0;
    auto rd = [&](Frags& f) {
      if (NSUB > 0) {
#pragma unroll
        for (int kk = 0; kk < 2; ++kk) {
#pragma unroll
          for (int m = 0; m < 2; ++m) f.w[kk][m] = *(LAS const v4u*)(lds + wc + woff[kk] + m * 2048);
#pragma unroll
          for (int n = 0; n < NSUB; ++n) f.z[kk][n] = *(LAS const v4u*)(lds + zc + zoff + n * 2048 + kk * 1024);
        }
        wc += 8192; if (wc == G2_NW * 8192) wc = 0;
        zc += G2_ZST; if (zc == G2_NZ * G2_ZST) zc = 0;
      }
      SCHED_BAR();
    };
    auto mm = [&](const Frags& f) {
#pragma unroll
      for (int kk = 0; kk < 2; ++kk)
#pragma unroll
        for (int n = 0; n < NSUB; ++n)
#pragma unroll
          for (int m = 0; m < 2; ++m) acc[n][m] = mfma16(f.z[kk][n], f.w[kk][m], acc[n][m]);
    };
    Frags f0, f1;
    CBAR(); rd(f0);
    STAMP(10);
#pragma unroll 1
    for (int it = 0; it < 15; ++it) {
      CBAR(); rd(f1); mm(f0);
      CBAR(); rd(f0); mm(f1);
    }
    CBAR(); rd(f1); mm(f0);
    mm(f1);
    STAMP(11);
#pragma unroll
    for (int m = 0; m < 2; ++m) {
      v2u o[NS1];
#pragma unroll
      for (int n = 0; n < NSUB; ++n) {
        float g[4];
#pragma unroll
        for (int e = 0; e < 4; ++e) g[e] = acc[n][m][e] + bias[m];
        const v2u zz = z1v[n][m];
        o[n] = v2u{pkbf(bflo(zz.x) * g[0], bfhi(zz.x) * g[1]), pkbf(bflo(zz.y) * g[2], bfhi(zz.y) * g[3])};
      }
      const unsigned vb = (unsigned)WS_V + (mt * 4 + G * 2 + m) * 27 * 1024 + lane * 16;
      if (NSUB == 2) st128(rs, vb + (3 * j + ng) * 1024, v4u{o[0].x, o[0].y, o[NS1 - 1].x, o[NS1 - 1].y});
      if (NSUB == 1) __builtin_amdgcn_raw_buffer_store_b64(o[0], rs, vb + (24 + (j >> 1)) * 1024 + (j & 1) * 8, 0, 16);
    }
    CBAR();
  }
}

template <int KH>
__device__ __forceinline__ void consumer_g3(LAS char* lds, char* ws, __amdgpu_buffer_rsrc_t rs, u16* __restrict__ out, int mt, int j, int m, int lane, const v2u (&x2v)[3] STAMP_ARG) {
  constexpr int K0 = KH ? 14 : 0, NK = KH ? 13 : 14, NF = KH ? 2 : 3, N0 = KH ? 3 : 0;
  const int q = lane >> 4, i16 = lane & 15;
  v4u vf[NK];
  __builtin_amdgcn_s_sleep(V_DELAY);
#pragma unroll
  for (int k = 0; k < NK; ++k) vf[k] = ld128(rs, (unsigned)WS_V + ((mt * 4 + m) * 27 + K0 + k) * 1024 + lane * 16);
  SCHED_BAR();
  CBAR();
  STAMP(12);
  f32x4 acc[5];
#pragma unroll
  for (int n = 0; n < 5; ++n) acc[n] = f32x4{0.f, 0.f, 0.f, 0.f};
  LAS const char* wp = lds + K0 * 1024 + lane * 16;
  LAS const char* wp2 = wp + 54 * 1024;
  LAS const char* wp4 = wp + 108 * 1024;
  v4u wf[3][5];
  auto rdw = [&](int k, v4u (&w)[5]) {
    w[0] = *(LAS const v4u*)(wp + k * 1024); w[1] = *(LAS const v4u*)(wp + (27 + k) * 1024);
    w[2] = *(LAS const v4u*)(wp2 + k * 1024); w[3] = *(LAS const v4u*)(wp2 + (27 + k) * 1024);
    w[4] = *(LAS const v4u*)(wp4 + k * 1024);
  };
  rdw(0, wf[0]); rdw(1, wf[1]);
#pragma unroll
  for (int k = 0; k < NK; ++k) {
    if (k + 2 < NK) rdw(k + 2, wf[(k + 2) % 3]);
    SCHED_BAR();
    if (__builtin_expect(!wall(__builtin_elementwise_max(vf[k].x, vf[k].z) != ~0u), 0)) {
      unsigned spin = 0;
      do { asm volatile("" ::: "memory"); vf[k] = ld128(rs, (unsigned)WS_V + ((mt * 4 + m) * 27 + K0 + k) * 1024 + lane * 16); }
      while (!wall(__builtin_elementwise_max(vf[k].x, vf[k].z) != ~0u) && ++spin < SPIN_CAP);
    }
#pragma unroll
    for (int n = 0; n < 5; ++n) acc[n] = mfma16(wf[k % 3][n], vf[k], acc[n]);
    asm volatile("" : "+v"(acc[0]), "+v"(acc[1]), "+v"(acc[2]), "+v"(acc[3]), "+v"(acc[4]));
    SCHED_BAR();
  }
  STAMP(13);
  f32x4 gb[NF];
#pragma unroll
  for (int n = 0; n < NF; ++n) gb[n] = *(LAS const f32x4*)(lds + G3_GB + (16 * (N0 + n) + 4 * q) * 4);
  LAS char* red = lds + G3_RED + m * 5120 + lane * 16;
#pragma unroll
  for (int n = 0; n < 5; ++n) if ((n >= 3) != (KH != 0)) *(LAS f32x4*)(red + n * 1024) = acc[n];
  CBAR();
  const int t = mt * 64 + 32 * (m >> 1) + 8 * (i16 >> 2) + 4 * (m & 1) + (i16 & 3);
#pragma unroll
  for (int n = 0; n < NF; ++n) {
    const f32x4 o = *(LAS const f32x4*)(red + (N0 + n) * 1024);
    const f32x4 a = KH ? o + acc[N0 + n] : acc[N0 + n] + o;
    const float o0 = __builtin_fmaf(gb[n][0], a[0], bflo(x2v[n].x)), o1 = __builtin_fmaf(gb[n][1], a[1], bfhi(x2v[n].x));
    const float o2 = __builtin_fmaf(gb[n][2], a[2], bflo(x2v[n].y)), o3 = __builtin_fmaf(gb[n][3], a[3], bfhi(x2v[n].y));
    *(v2u*)((char*)out + ((size_t)t * D + 80 * j + 16 * (N0 + n) + 4 * q) * 2) = v2u{pkbf(o0, o1), pkbf(o2, o3)};
  }
}

__global__ __launch_bounds__(768, 3) void k_all(Args A) {
  extern __shared__ __attribute__((aligned(16))) char smem[];
  unsigned long long pc;
  asm volatile("s_getpc_b64 %0" : "=s"(pc));
  LAS char* lds = (LAS char*)smem;
  char* ws = A.ws;
  const int tid = __builtin_amdgcn_workitem_id_x(), lane = tid & 63, wave = __builtin_amdgcn_readfirstlane(tid >> 6), wg = __builtin_amdgcn_workgroup_id_x();
  STAMP_DECL; STAMP(0);
  const __amdgpu_buffer_rsrc_t rs = __builtin_amdgcn_make_buffer_rsrc((void*)ws, 0, (int)WS_END, 0x00020000);
  asm volatile("" :: "s"(A.x), "s"(A.Are), "s"(A.Aim), "s"(A.Bre), "s"(A.Bim), "s"(A.Cre), "s"(A.Cim), "s"(A.logdt), "s"(A.dskip), "s"(A.win), "s"(A.wsp),
               "s"(A.bsp), "s"(A.wout), "s"(A.cvec), "s"(A.wmod), "s"(A.ws), "s"(A.out));
  const int grp = wave >> 2, bt = grp == 2 ? 0 : grp + 1, gw = wave & 3, gt = tid & 255;
  const bool isaux = bt == 2 && wg >= 128;
  const int u = wg - 128, d = 256 * bt + (wg & 7) * (bt < 2 ? 32 : 16) + (wg >> 3);
  unsigned epoch;
  asm volatile("global_load_dword %0, %1, %2" : "=v"(epoch) : "v"((unsigned)CTL_EPOCH), "s"(ws) : "memory");
  if (__builtin_expect(isaux, 1)) {
    const int rr = lane >> 4;
    const unsigned vo = rr * (D * 2) + (((lane & 15) ^ ((rr | ((gw >> 1) << 2)) << 1)) << 4);
    const char* xb = (const char*)A.x + (size_t)(u * 16 + gw * 4) * (D * 2);
#pragma unroll
    for (int i = 0; i < 5; ++i) glds16nt(xb + i * 256, vo, (unsigned)(size_t)lds + C_GRP + 8192 + (gw * 5 + i) * 1024);
    const int zc = (u * 4 + gw) * 3 + lane - 32;
    st128(rs, lane < 32 ? (unsigned)CTL_ST3 + ((u * 4 + gw) * 32 + lane) * 16 : (lane < 35 && zc < 1280 ? (unsigned)zc * 256 : 0x7ffffff0u), v4u{0u, 0u, 0u, 0u});
  }
  unsigned pre[10] = {};
  if (__builtin_expect(!isaux, 1)) {
    const unsigned o = (d * NS + (gw & 1) * 64 + lane) * 2;
    LDU16(pre[0], A.Are, o); LDU16(pre[1], A.Aim, o); LDU16(pre[2], A.logdt, d * 2); LDU16(pre[3], A.Bre, o);
    LDU16(pre[4], A.Bim, o); LDU16(pre[5], A.Cre, o); LDU16(pre[6], A.Cim, o);
    const int so = ((wg >> 3) * 4 + gw) * 10 + lane;
    const char* wp = (const char*)(pc & ~255ull) + (size_t)(so < CODE_SECTORS ? so : 0) * 64;
    const size_t po = ((size_t)((lane - 10) & 7) << 21) + wg * 256;
    const char* wq = lane < 18 ? ws + po : (lane < 22 ? (const char*)A.wsp + po : (const char*)A.out + ((lane & 1) << 21) + wg * 256);
    wp = lane >= 10 ? wq : wp;
    wp = bt == 1 && (lane < 10 || (gw == 0 && lane < 24)) ? wp : (const char*)A.logdt;
    asm volatile("global_load_dword %0, %1, off" : "=v"(pre[7]) : "v"(wp) : "memory");
  }
  STAMP(11);
  if (tid < 16) ((LAS unsigned*)(lds + M_CTL))[tid] = 0u;
  unsigned tag = 0;
  CBAR();
  {
    LAS char* gl = lds + (grp == 2 ? G1_TAB - C_GRP : grp * C_GRP);
    if (bt == 0) __builtin_amdgcn_s_setprio(2);
    if (bt == 1) __builtin_amdgcn_s_setprio(1);
    if (isaux) { __builtin_amdgcn_s_setprio(3); aux_group(gl, lds, A, rs, epoch, u, gt, gw, lane STAMP_PASS); }
    else conv_group(gl, lds, bt, A, rs, epoch, tag, pre, wg, d, gt, gw, lane STAMP_PASS);
    __builtin_amdgcn_s_setprio(0);
  }
  const int xcd = wg & 7, idx = wg >> 3;
  const int mt = (xcd >> 1) * 8 + (idx & 7), j = (xcd & 1) * 4 + (idx >> 3);
  const int cnt = j < 6 ? 7 : 6, sub0 = j < 6 ? 7 * j : 42 + 6 * (j - 6);
  if (wave >= 8) {
    STAMP(4);
    const int lw = wave - 8;
    if (lw + 12 < 2 * cnt) loader_path<4>(lds, ws, rs, A.wsp, wg, mt, j, sub0, lw, lane);
    else loader_path<3>(lds, ws, rs, A.wsp, wg, mt, j, sub0, lw, lane);
    if (wg == 0 && tid == 512) *(volatile unsigned*)(ws + CTL_EPOCH) = tag;
    STAMP_FLUSH(1);
    return;
  }
  v2u x2v[3];
  const int ng = wave >> 2 ? 3 - (wave & 3) : (wave & 3);
  if (ng < 3) consumer_g12<2, false>(lds, ws, rs, A.bsp, A.wsp, wg, mt, j, sub0, cnt, wave, tid, lane, x2v STAMP_PASS);
  else if (cnt == 7) consumer_g12<1, true>(lds, ws, rs, A.bsp, A.wsp, wg, mt, j, sub0, cnt, wave, tid, lane, x2v STAMP_PASS);
  else consumer_g12<0, true>(lds, ws, rs, A.bsp, A.wsp, wg, mt, j, sub0, cnt, wave, tid, lane, x2v STAMP_PASS);
  if (wave < 4) consumer_g3<0>(lds, ws, rs, A.out, mt, j, wave & 3, lane, x2v STAMP_PASS);
  else consumer_g3<1>(lds, ws, rs, A.out, mt, j, wave & 3, lane, x2v STAMP_PASS);
  STAMP(14);
  STAMP_FLUSH(1);
}

#if defined(__HIP_DEVICE_COMPILE__) && !defined(KEEP_PK)
#pragma clang attribute pop
#endif

extern "C" void kernel_launch(void* const* d_in, const int* in_sizes, int n_in, void* d_out, int out_size, void* d_ws, size_t ws_size,
                              hipStream_t stream) {
  Args A;
  A.x = (const u16*)d_in[0]; A.Are = (const u16*)d_in[1]; A.Aim = (const u16*)d_in[2]; A.Bre = (const u16*)d_in[3]; A.Bim = (const u16*)d_in[4];
  A.Cre = (const u16*)d_in[5]; A.Cim = (const u16*)d_in[6]; A.logdt = (const u16*)d_in[7]; A.dskip = (const u16*)d_in[8];
  A.win = (const u16*)d_in[10]; A.wsp = (const u16*)d_in[11]; A.bsp = (const u16*)d_in[12]; A.wout = (const u16*)d_in[13];
  A.cvec = (const u16*)d_in[14]; A.wmod = (const u16*)d_in[15];
  A.ws = (char*)d_ws; A.out = (u16*)d_out;
  (void)hipFuncSetAttribute((const void*)k_all, hipFuncAttributeMaxDynamicSharedMemorySize, M_LDS);
  k_all<<<256, 768, M_LDS, stream>>>(A);
}
```

```cpp
#include <hip/hip_runtime.h>
#include <hip/hip_bf16.h>
#include <stdint.h>

#define NT " nt"
#define NTLOAD(p) __builtin_nontemporal_load(p)
#define GAS __attribute__((address_space(1)))
#define LAS __attribute__((address_space(3)))
typedef unsigned v4u __attribute__((ext_vector_type(4)));
typedef unsigned v2u __attribute__((ext_vector_type(2)));
typedef float f32x2 __attribute__((ext_vector_type(2)));
typedef float f32x4 __attribute__((ext_vector_type(4)));
typedef float f32x16 __attribute__((ext_vector_type(16)));
typedef __bf16 bf16x2 __attribute__((ext_vector_type(2)));
typedef __bf16 bf16x8 __attribute__((ext_vector_type(8)));
typedef _Float16 f16x2 __attribute__((ext_vector_type(2)));
typedef _Float16 f16x8 __attribute__((ext_vector_type(8)));
typedef short s16x4 __attribute__((ext_vector_type(4)));
typedef unsigned short u16;

#if defined(__HIP_DEVICE_COMPILE__) && !defined(KEEP_PK)
#pragma clang attribute push(__attribute__((target("no-packed-fp32-ops"))), apply_to = function)
#endif
__device__ __forceinline__ float u2f(unsigned u) { return __builtin_bit_cast(float, u); }
__device__ __forceinline__ unsigned f2u(float f) { return __builtin_bit_cast(unsigned, f); }
__device__ __forceinline__ int lane_id() { return __builtin_amdgcn_mbcnt_hi(~0u, __builtin_amdgcn_mbcnt_lo(~0u, 0u)); }
__device__ __forceinline__ unsigned shx(unsigned v, int m) { return __builtin_amdgcn_ds_bpermute((lane_id() ^ m) << 2, v); }
__device__ __forceinline__ float shx(float v, int m) { return u2f(shx(f2u(v), m)); }
__device__ __forceinline__ bool wall(bool p) { return __builtin_amdgcn_ballot_w64(!p) == 0ull; }
__device__ __forceinline__ float exp_acc(float x) {
  const float hi = x * 1.4426950408889634f;
  const float lo = __builtin_fmaf(x, 1.4426950408889634f, -hi) + x * 1.925963033500414e-8f;
  const float e = __builtin_amdgcn_exp2f(hi);
  return __builtin_fmaf(e, lo * 0.6931471805599453f, e);
}

constexpr int L = 2048, D = 640, NS = 128, F = 1728, FH = 864;

constexpr size_t CTL_E1 = 0;
constexpr size_t CTL_E1B = 196608;
constexpr size_t CTL_E2 = 262144;
constexpr size_t CTL_E3 = 294912;
constexpr size_t CTL_EW = 303104;
constexpr size_t CTL_ST3 = 327680;
constexpr size_t CTL_F0 = CTL_ST3 + 262144;
constexpr size_t CTL_EPOCH = CTL_F0 + 32768;
constexpr size_t WS_MOD = CTL_EPOCH + 4096;
constexpr size_t WS_ST1 = WS_MOD + 131072;
constexpr size_t WS_U = WS_ST1 + 16384;
constexpr size_t WS_XT = WS_U + 65536;
constexpr size_t WS_X2T = WS_XT + (size_t)D * L * 2;
constexpr size_t WS_WIN = WS_X2T + (size_t)D * L * 2;
constexpr size_t WS_WOUT = WS_WIN + (size_t)D * F * 2;
constexpr size_t WS_Z2 = WS_WOUT + (size_t)FH * D * 2;
constexpr size_t WS_V = WS_Z2 + (size_t)L * FH * 2;
constexpr size_t WS_END = WS_V + (size_t)L * FH * 2;

__device__ __forceinline__ float bflo(unsigned u) { return u2f(u << 16); }
__device__ __forceinline__ float bfhi(unsigned u) { return u2f(u & 0xffff0000u); }
__device__ __forceinline__ float bf2f(u16 v) { return u2f((unsigned)v << 16); }
__device__ __forceinline__ unsigned pkbf(float a, float b) {
  f32x2 v = {a, b};
  return __builtin_bit_cast(unsigned, __builtin_convertvector(v, bf16x2));
}
__device__ __forceinline__ unsigned pkh(float a, float b) {
  f32x2 v = {a, b};
  return __builtin_bit_cast(unsigned, __builtin_convertvector(v, f16x2));
}
__device__ __forceinline__ float bfr(float a) { return bflo(pkbf(a, a)); }
__device__ __forceinline__ v2u trread(LAS const char* p) {
  return __builtin_bit_cast(v2u, __builtin_amdgcn_ds_read_tr16_b64_v4i16((LAS s16x4*)p));
}
__device__ __forceinline__ f32x4 mfma16(v4u a, v4u b, f32x4 c) {
  return __builtin_amdgcn_mfma_f32_16x16x32_bf16(__builtin_bit_cast(bf16x8, a), __builtin_bit_cast(bf16x8, b), c, 0, 0, 0);
}
__device__ __forceinline__ f32x16 mfma32(v4u a, v4u b, f32x16 c) {
  return __builtin_amdgcn_mfma_f32_32x32x16_bf16(__builtin_bit_cast(bf16x8, a), __builtin_bit_cast(bf16x8, b), c, 0, 0, 0);
}
__device__ __forceinline__ f32x16 mfma32h(v4u a, v4u b, f32x16 c) {
  return __builtin_amdgcn_mfma_f32_32x32x16_f16(__builtin_bit_cast(f16x8, a), __builtin_bit_cast(f16x8, b), c, 0, 0, 0);
}
template <bool DEV>
__device__ __forceinline__ void glds16(const void* sbase, unsigned voff, unsigned dst) {
  unsigned keep;
  if (DEV) asm volatile(
"s_mov_b32 %0, m0\n\ts_mov_b32 m0, %3\n\ts_nop 0\n\tglobal_load_lds_dwordx4 %1, %2 sc1\n\ts_mov_b32 m0, %0"
                        : "=&s"(keep) : "v"(voff), "s"(sbase), "s"(dst) : "memory");
  else asm volatile("s_mov_b32 %0, m0\n\ts_mov_b32 m0, %3\n\ts_nop 0\n\tglobal_load_lds_dwordx4 %1, %2\n\ts_mov_b32 m0, %0"
                    : "=&s"(keep) : "v"(voff), "s"(sbase), "s"(dst) : "memory");
}
__device__ __forceinline__ void glds16nt(const void* sbase, unsigned voff, unsigned dst) {
  unsigned keep;
  asm volatile("s_mov_b32 %0, m0\n\ts_mov_b32 m0, %3\n\ts_nop 0\n\tglobal_load_lds_dwordx4 %1, %2" NT "\n\ts_mov_b32 m0, %0"
               : "=&s"(keep) : "v"(voff), "s"(sbase), "s"(dst) : "memory");
}
template <int N> __device__ __forceinline__ void waitv() { asm volatile("s_waitcnt vmcnt(%0)" :: "i"(N) : "memory"); }
__device__ __forceinline__ float dot2bf(unsigned a, unsigned b, float c) {
  return __builtin_amdgcn_fdot2_f32_bf16(__builtin_bit_cast(bf16x2, a), __builtin_bit_cast(bf16x2, b), c, false);
}
#define SCHED_BAR() __builtin_amdgcn_sched_barrier(0)
#define STAMP_DECL
#define STAMP(i) do {} while (0)
#define STAMP_FLUSH(kid) do {} while (0)
#define STAMP_ARG
#define STAMP_PASS
#define CBAR() do { SCHED_BAR(); __builtin_amdgcn_s_waitcnt(0xC07F); asm volatile("" ::: "memory"); __builtin_amdgcn_s_barrier(); asm volatile("" ::: "memory"); } while (0)
#define LBAR() __builtin_amdgcn_s_barrier()

template <int CTRL> __device__ __forceinline__ float dppf(float v) {
  return __builtin_bit_cast(float, __builtin_amdgcn_update_dpp(0, __builtin_bit_cast(int, v), CTRL, 0xf, 0xf, true));
}
template <int N, int CTRL> __device__ __forceinline__ void treduce_dpp(float* v, bool up) {
#pragma unroll
  for (int e = 0; e < N; ++e) {
    const float a = v[e], b = v[e + N];
    const float send = up ? a : b, keep = up ? b : a;
    v[e] = keep + dppf<CTRL>(send);
  }
}
template <int N> __device__ __forceinline__ void treduce_xor(float* v, bool up, int mask) {
#pragma unroll
  for (int e = 0; e < N; ++e) {
    const float a = v[e], b = v[e + N];
    const float send = up ? a : b, keep = up ? b : a;
    v[e] = keep + shx(send, mask);
  }
}

__device__ __forceinline__ float ln_std(float var) { return __builtin_amdgcn_sqrtf(bfr(bfr(var) + 1e-5f)); }
__device__ __forceinline__ void ln_scalars(float mean, float var, float& m_b, float& rinv) {
  m_b = bfr(mean);
  rinv = __builtin_amdgcn_rcpf(bfr(ln_std(var)));
}

struct cplx { float r, i; };
__device__ __forceinline__ cplx cmul(cplx a, cplx b) { return {a.r * b.r - a.i * b.i, a.r * b.i + a.i * b.r}; }

typedef GAS unsigned gu32;
#define RLX_AGENT __ATOMIC_RELAXED, __HIP_MEMORY_SCOPE_AGENT
#define F0_REP 8
#define V_DELAY 20
#define V_WIN 5
#define LSTAMP(i) do {} while (0)
#define POLL_SLEEP 6
#define E1_REP 32
#define INTERLEAVE(n, r) do { _Pragma("unroll") for (int i_ = 0; i_ < (n); ++i_) { __builtin_amdgcn_sched_group_barrier(0x008, 1, 0); __builtin_amdgcn_sched_group_barrier(0x100, (r), 0); } \
    __builtin_amdgcn_sched_group_barrier(0x008, 8, 0); SCHED_BAR(); } while (0)
#define ABL_KK kk
constexpr unsigned SPIN_CAP = 1u << 16;
constexpr int M_LDS = 163840, M_CTL = M_LDS - 64;
constexpr int CODE_SECTORS = 1000;
constexpr int C_KOFF = 0, C_UOFF = 4224, C_UP = 1536, C_COFF = C_UOFF + 4 * C_UP, C_GRP = C_COFF + 4 * 64 * 144;
constexpr int G1_BOFF = 81920, G1_BST = 14336, G1_NSLOT = 5, G1_TAB = G1_BOFF + G1_BST * G1_NSLOT;
constexpr int G1_T_LN2 = G1_TAB, G1_T_U = G1_T_LN2 + 512, G1_T_P = G1_T_U + 2048, G1_T_Z = G1_T_P + 2048;
constexpr int G2_NW = 9, G2_NZ = 6, G2_ZOFF = G2_NW * 8192, G2_ZST = 14336;
constexpr int G3_RED = 135 * 1024, G3_GB = 161024;
static_assert(3 * C_GRP <= M_CTL && G1_T_Z + 512 <= M_CTL && G2_ZOFF + G2_NZ * G2_ZST <= M_CTL && G3_GB + 320 <= M_CTL, "lds");

__device__ __forceinline__ void st128(__amdgpu_buffer_rsrc_t rs, unsigned off, v4u v) { __builtin_amdgcn_raw_buffer_store_b128(v, rs, off, 0, 16); }
__device__ __forceinline__ v4u ld128(__amdgpu_buffer_rsrc_t rs, unsigned off) { return __builtin_amdgcn_raw_buffer_load_b128(rs, off, 0, 16); }

__device__ __forceinline__ bool wave_arrive(LAS char* lds, int word, unsigned n, int lane) {
  waitv<0>();
  unsigned old = 0;
  if (lane == 0) old = __hip_atomic_fetch_add((LAS unsigned*)(lds + M_CTL) + word, 1u, __ATOMIC_RELAXED, __HIP_MEMORY_SCOPE_WORKGROUP);
  return (unsigned)__builtin_amdgcn_readfirstlane(old) == n - 1;
}
__device__ __forceinline__ void lds_wait(LAS char* lds0, int word, unsigned target) {
  LAS unsigned* p = (LAS unsigned*)(lds0 + M_CTL) + word;
  while ((unsigned)__builtin_amdgcn_readfirstlane(__hip_atomic_load(p, __ATOMIC_RELAXED, __HIP_MEMORY_SCOPE_WORKGROUP)) < target) __builtin_amdgcn_s_sleep(0);
  asm volatile("" ::: "memory");
}
__device__ __forceinline__ void lds_post(LAS char* lds0, int word, int lane) {
  SCHED_BAR();
  asm volatile("" ::: "memory");
  if (lane == 0) __hip_atomic_fetch_add((LAS unsigned*)(lds0 + M_CTL) + word, 1u, __ATOMIC_RELAXED, __HIP_MEMORY_SCOPE_WORKGROUP);
  asm volatile("" ::: "memory");
}
__device__ __forceinline__ void group_sync(LAS char* lds0, int word, unsigned target, int lane) { lds_post(lds0, word, lane); lds_wait(lds0, word, target); }
template <int NREP, int NSH> __device__ __forceinline__ void signal(char* base, int shard, int lane) {
  if (lane < NREP) __hip_atomic_fetch_add((gu32*)(base + (lane * NSH + shard) * 256), 1u, RLX_AGENT);
}
template <int NSH> __device__ __forceinline__ bool count_ok(unsigned c, unsigned total) {
  if (NSH > 1) c += __builtin_amdgcn_update_dpp(0, c, 0xb1, 0xf, 0xf, true);
  if (NSH > 2) c += __builtin_amdgcn_update_dpp(0, c, 0x4e, 0xf, 0xf, true);
  if (NSH > 4) c += __builtin_amdgcn_update_dpp(0, c, 0x141, 0xf, 0xf, true);
  return (unsigned)__builtin_amdgcn_readfirstlane(c) >= total;
}
template <int NSH> __device__ __forceinline__ void wait_count(char* base, int rep, unsigned total, int lane) {
  gu32* p = (gu32*)(base + (rep * NSH + (lane & (NSH - 1))) * 256);
#pragma unroll 1
  for (unsigned spin = 0; spin < SPIN_CAP; ++spin) {
    if (count_ok<NSH>(__hip_atomic_load(p, RLX_AGENT), total)) break;
    __builtin_amdgcn_s_sleep(POLL_SLEEP);
  }
  __builtin_amdgcn_fence(__ATOMIC_ACQUIRE, "wavefront");
}
template <int NSH> __device__ __forceinline__ void poll_issue(unsigned& c, char* base, int rep, int lane) {
  c = __hip_atomic_load((gu32*)(base + (rep * NSH + (lane & (NSH - 1))) * 256), RLX_AGENT);
}
template <int NSH> __device__ __forceinline__ void poll_finish(unsigned c, char* base, int rep, unsigned total, int lane) {
  if (!count_ok<NSH>(c, total)) wait_count<NSH>(base, rep, total, lane);
  __builtin_amdgcn_fence(__ATOMIC_ACQUIRE, "wavefront");
}

__device__ __forceinline__ float gelu_f(float x) {
  const float p = __builtin_fmaf(x * x, -0.044715f * 2.302208198f, -2.302208198f);
  const float e = __builtin_amdgcn_exp2f(x * p);
  return x * __builtin_amdgcn_rcpf(1.0f + e);
}

__device__ __forceinline__ float ld32f(__amdgpu_buffer_rsrc_t rs, unsigned off) { return u2f(__builtin_amdgcn_raw_buffer_load_b32(rs, off, 0, 16)); }
__device__ __forceinline__ void st32(__amdgpu_buffer_rsrc_t rs, unsigned off, unsigned v) { __builtin_amdgcn_raw_buffer_store_b32(v, rs, off, 0, 16); }

struct Args {
  const u16 *x, *Are, *Aim, *Bre, *Bim, *Cre, *Cim, *logdt, *dskip, *win, *wsp, *bsp, *wout, *cvec, *wmod;
  char* ws; u16* out;
};

typedef GAS unsigned long long gu64;
__device__ __forceinline__ void put_granule(char* p, unsigned v, unsigned tag) { __hip_atomic_store((gu64*)p, ((unsigned long long)tag << 32) | v, RLX_AGENT); }
template <int N, class F> __device__ __forceinline__ void get_granules(unsigned (&v)[N], unsigned tag, F addr) {
#pragma unroll 1
  for (unsigned spin = 0; spin < SPIN_CAP; ++spin) {
    unsigned long long x[N];
#pragma unroll
    for (int k = 0; k < N; ++k) x[k] = __hip_atomic_load((gu64*)addr(k), RLX_AGENT);
    bool ok = true;
#pragma unroll
    for (int k = 0; k < N; ++k) { v[k] = (unsigned)x[k]; ok &= (unsigned)(x[k] >> 32) == tag; }
    if (wall(ok)) break;
    __builtin_amdgcn_s_sleep(8);
  }
}

__device__ __forceinline__ void conv_group(LAS char* lds, LAS char* lds0, int bt, const Args& A, __amdgpu_buffer_rsrc_t rs, unsigned epoch, unsigned& tag, unsigned (&pre)[10],
                                           int wg, int d, int tid, int wave, int lane STAMP_ARG) {
  char* ws = A.ws;
  float S[8], soff;
  v4u xv, st[2];
  {
    const int half = wave & 1;
    const bool domod = bt == 2 && wg < 120;
    v4u mw[10];
    unsigned mc;
    const int mpart = wg & 3, mcol = ((wg >> 2) + 15) % 30 * 128;
#define LDV4(dst, base, voff) asm volatile("global_load_dwordx4 %0, %1, %2" NT : "=v"(dst) : "v"(voff), "s"(base) : "memory")
#define LDU16(dst, base, voff) asm volatile("global_load_ushort %0, %1, %2" NT : "=v"(dst) : "v"(voff), "s"(base) : "memory")
    if (domod) {
      LDU16(mc, A.cvec, (mpart * 160 + (tid < 160 ? tid : 0)) * 2);
#pragma unroll
      for (int i = 0; i < 10; ++i) LDV4(mw[i], (const char*)A.wmod + (size_t)(mpart * 160 + i * 16) * (6 * D * 2) + mcol * 2, (tid >> 4) * (6 * D * 2) + (tid & 15) * 16);
    }
    int xoff[8];
#pragma unroll
    for (int j = 0; j < 8; ++j) {
      const int l = tid * 8 + j;
      const int idx = l < 128 ? l : (int)(pkbf((float)l, 0.f) & 0xffffu) - 0x4300 + 128;
      xoff[j] = (idx + (idx >> 3)) * 8;
    }
    asm volatile("" : "+v"(xoff[0]), "+v"(xoff[1]), "+v"(xoff[2]), "+v"(xoff[3]), "+v"(xoff[4]), "+v"(xoff[5]), "+v"(xoff[6]), "+v"(xoff[7]));
    if (domod) waitv<12>(); else waitv<1>();
    asm volatile("v_readfirstlane_b32 %0, %1\n\ts_nop 3" : "=s"(tag) : "v"(epoch) : "memory");
    tag += 1u;
    asm volatile("" : "+v"(pre[0]), "+v"(pre[1]), "+v"(pre[2]), "+v"(pre[3]), "+v"(pre[4]), "+v"(pre[5]), "+v"(pre[6]));
    const unsigned vare = pre[0], vaim = pre[1], vdt = pre[2], vbre = pre[3], vbim = pre[4], vcre = pre[5], vcim = pre[6];
    if (domod) {
      __builtin_amdgcn_s_setprio(3);
      LAS float* msc = (LAS float*)(lds + C_COFF + 32768);
      waitv<10>();
      asm volatile("" : "+v"(mc));
      {
        const float c = bflo(mc);
        msc[tid] = bfr(c * __builtin_amdgcn_rcpf(1.0f + __builtin_amdgcn_exp2f(-1.4426950408889634f * c)));
      }
      group_sync(lds0, 9, 4, lane);
      float sv[10];
#pragma unroll
      for (int i = 0; i < 10; ++i) sv[i] = msc[(tid >> 4) + 16 * i];
      waitv<0>();
      asm volatile("" : "+v"(mw[0]), "+v"(mw[1]), "+v"(mw[2]), "+v"(mw[3]), "+v"(mw[4]), "+v"(mw[5]), "+v"(mw[6]), "+v"(mw[7]), "+v"(mw[8]), "+v"(mw[9]));
      float acc[8] = {};
#pragma unroll
      for (int i = 0; i < 10; ++i)
#pragma unroll
        for (int j = 0; j < 4; ++j) { acc[2 * j] += sv[i] * bflo(mw[i][j]); acc[2 * j + 1] += sv[i] * bfhi(mw[i][j]); }
      treduce_xor<4>(acc, (lane & 32) != 0, 32);
      treduce_xor<2>(acc, (lane & 16) != 0, 16);
      LAS float* red = msc + 256;
      *(LAS f32x2*)(red + wave * 128 + (lane & 15) * 8 + (lane >> 4) * 2) = f32x2{acc[0], acc[1]};
      lds_post(lds0, 9, lane);
      if (wave >= 2) {
        lds_wait(lds0, 9, 8);
        const int c = tid - 128;
        put_granule(ws + WS_MOD + (size_t)(mpart * 3840 + mcol + c) * 8, f2u((red[c] + red[128 + c]) + (red[256 + c] + red[384 + c])), tag);
      }
      __builtin_amdgcn_s_setprio(0);
    }
    if (tid < 4) *(LAS v4u*)(lds + C_KOFF + tid * 16) = v4u{0u, 0u, 0u, 0u};
    const float dt = bfr(exp_acc(bflo(vdt)));
    const float ar2 = dt * bflo(vare) * 1.4426950408889634f, ai2 = dt * bflo(vaim) * 0.15915494309189535f;
    auto E = [&](float xx) {
      const float mag = __builtin_amdgcn_exp2f(ar2 * xx), ph = __builtin_amdgcn_fractf(ai2 * xx);
      return cplx{mag * __builtin_amdgcn_cosf(ph), mag * __builtin_amdgcn_sinf(ph)};
    };
    LAS char* img = lds + C_COFF + wave * 8192 + lane * 128;
    const int sw = lane & 7;
    float isc = 0.f;
    if (wave < 2) {
      const cplx e8 = E(8.f), e64 = E(64.f), e128 = E(128.f), e192 = E(192.f);
      cplx q[8];
      q[0] = {1.f, 0.f}; q[1] = e8; q[2] = cmul(e8, e8); q[3] = cmul(q[2], e8); q[4] = cmul(q[2], q[2]);
      q[5] = cmul(q[4], e8); q[6] = cmul(q[4], q[2]); q[7] = cmul(q[4], q[3]);
#pragma unroll
      for (int g = 0; g < 4; ++g) {
        cplx v[8];
#pragma unroll
        for (int j = 0; j < 8; ++j) v[j] = g == 0 ? q[j] : cmul(q[j], g == 1 ? e64 : (g == 2 ? e128 : e192));
        *(LAS v4u*)(img + ((g ^ sw) << 4)) = v4u{pkh(v[0].r, v[1].r), pkh(v[2].r, v[3].r), pkh(v[4].r, v[5].r), pkh(v[6].r, v[7].r)};
        *(LAS v4u*)(img + (((4 + g) ^ sw) << 4)) = v4u{pkh(v[0].i, v[1].i), pkh(v[2].i, v[3].i), pkh(v[4].i, v[5].i), pkh(v[6].i, v[7].i)};
      }
    } else {
      cplx W = cmul(cplx{bflo(vcre), bflo(vcim)}, cplx{dt * bflo(vbre), dt * bflo(vbim)});
      float mx = __builtin_fmaxf(__builtin_fabsf(W.r), __builtin_fabsf(W.i));
      mx = __builtin_fmaxf(mx, dppf<0xb1>(mx)); mx = __builtin_fmaxf(mx, dppf<0x4e>(mx)); mx = __builtin_fmaxf(mx, dppf<0x141>(mx)); mx = __builtin_fmaxf(mx, dppf<0x140>(mx));
      const unsigned mb = f2u(mx);
      const unsigned m01 = __builtin_elementwise_max((unsigned)__builtin_amdgcn_readlane(mb, 0), (unsigned)__builtin_amdgcn_readlane(mb, 16));
      const unsigned m23 = __builtin_elementwise_max((unsigned)__builtin_amdgcn_readlane(mb, 32), (unsigned)__builtin_amdgcn_readlane(mb, 48));
      int ex = (int)((__builtin_elementwise_max(m01, m23) >> 23) & 0xff);
      ex = ex < 20 ? 20 : (ex > 240 ? 240 : ex);
      const float sc = u2f((unsigned)(267 - ex) << 23);
      isc = u2f((unsigned)(ex - 13) << 23);
      W.r *= sc; W.i *= sc;
      const cplx e1 = E(1.f), e2 = cmul(e1, e1), e4 = cmul(e2, e2);
      cplx h[24];
      h[0] = W; h[1] = cmul(W, e1); h[2] = cmul(W, e2); h[3] = cmul(h[2], e1);
      h[4] = cmul(W, e4); h[5] = cmul(h[4], e1); h[6] = cmul(h[4], e2); h[7] = cmul(h[6], e1);
      const cplx g1 = E(256.f), g2 = E(512.f), g3 = E(768.f), g4 = E(1024.f);
      h[8] = cmul(h[0], g1); h[9] = cmul(h[2], g1); h[10] = cmul(h[4], g1); h[11] = cmul(h[6], g1);
      h[12] = cmul(h[0], g2); h[13] = cmul(h[4], g2); h[14] = cmul(h[0], g3); h[15] = cmul(h[4], g3);
      h[16] = cmul(h[0], g4); h[17] = cmul(h[8], g4); h[18] = cmul(h[12], g4); h[19] = cmul(h[14], g4);
      h[20] = cmul(h[16], g4); h[21] = {0.f, 0.f}; h[22] = {0.f, 0.f}; h[23] = {0.f, 0.f};
#pragma unroll
      for (int g = 0; g < 3; ++g) {
        const cplx* v = h + 8 * g;
        *(LAS v4u*)(img + ((g ^ sw) << 4)) = v4u{pkh(v[0].r, v[1].r), pkh(v[2].r, v[3].r), pkh(v[4].r, v[5].r), pkh(v[6].r, v[7].r)};
        *(LAS v4u*)(img + (((4 + g) ^ sw) << 4)) = v4u{pkh(-v[0].i, -v[1].i), pkh(-v[2].i, -v[3].i), pkh(-v[4].i, -v[5].i), pkh(-v[6].i, -v[7].i)};
      }
      *(LAS v4u*)(img + ((3 ^ sw) << 4)) = v4u{0u, 0u, 0u, 0u};
      *(LAS v4u*)(img + ((7 ^ sw) << 4)) = v4u{0u, 0u, 0u, 0u};
    }
    STAMP(1);
    group_sync(lds0, 5 + bt, 4, lane);
    if (wave >= 2) {
      const int hh = lane >> 5, blk = (lane >> 4) & 1, qq = (lane & 15) >> 2, p = lane & 3;
      LAS const char* Qi = lds + C_COFF + half * 8192;
      v2u a[8][2], bb[8][2];
#pragma unroll
      for (int ks = 0; ks < 8; ++ks)
#pragma unroll
        for (int t = 0; t < 2; ++t) {
          const int k = 16 * ks + 8 * hh + 4 * t + qq, mode = k >> 1, c = k & 1;
          const int off = mode * 128 + (((c * 4 + 2 * blk + (p >> 1)) ^ (mode & 7)) << 4) + (p & 1) * 8;
          a[ks][t] = trread(Qi + 16384 + off);
          bb[ks][t] = trread(Qi + off);
        }
      SCHED_BAR();
      f32x16 acc = {};
#pragma unroll
      for (int ks = 0; ks < 8; ++ks) acc = mfma32h(v4u{a[ks][0].x, a[ks][0].y, a[ks][1].x, a[ks][1].y}, v4u{bb[ks][0].x, bb[ks][0].y, bb[ks][1].x, bb[ks][1].y}, acc);
      LAS float* T = (LAS float*)(lds + C_UOFF) + half;
      const int q = lane & 31, hi = lane >> 5;
      auto put = [&](int idx, float v) { T[(idx + (idx >> 3)) * 2] = v * isc; };
#pragma unroll
      for (int r = 0; r < 4; ++r) put((r + 4 * hi) + 8 * q, acc[r]);
#pragma unroll
      for (int r = 0; r < 4; ++r) put(hi ? 384 + 64 * (r >> 1) + (r & 1) + 2 * q : 256 + r + 4 * q, acc[4 + r]);
      if (hi == 0) {
#pragma unroll
        for (int r = 0; r < 4; ++r) put(512 + 32 * r + q, acc[8 + r]);
      } else if (q == 0) put(640, acc[8]);
    }
    lds_post(lds0, 5 + bt, lane);
    asm volatile("s_waitcnt vmcnt(0)" : "+v"(pre[7]) :: "memory");
    if (bt == 0 && wave == 0) {
#pragma unroll 1
      for (unsigned spin = 0; spin < SPIN_CAP; ++spin) {
        asm volatile("" ::: "memory");
        const v4u f = ld128(rs, (unsigned)CTL_F0 + (wg & (F0_REP - 1)) * 1024 + lane * 16);
        if (wall(f.x == tag && f.y == ~tag && f.z == tag && f.w == ~tag)) break;
        __builtin_amdgcn_s_sleep(POLL_SLEEP);
      }
      __builtin_amdgcn_fence(__ATOMIC_ACQUIRE, "wavefront");
      lds_post(lds0, 8, lane);
    } else lds_wait(lds0, 8, 1);
    xv = ld128(rs, (unsigned)WS_XT + (tid * D + d) * 16);
#pragma unroll
    for (int i = 0; i < 2; ++i) st[i] = ld128(rs, (unsigned)WS_ST1 + (i * 256 + tid) * 16);
    SCHED_BAR();
    STAMP(2);
    lds_wait(lds0, 5 + bt, 8);
    {
      f32x2 tv[8];
#pragma unroll
      for (int j = 0; j < 8; ++j) tv[j] = *(LAS const f32x2*)(lds + C_UOFF + xoff[j]);
      SCHED_BAR();
      float kv[8];
#pragma unroll
      for (int j = 0; j < 8; ++j) kv[j] = tv[j][0] + tv[j][1];
      *(LAS v4u*)(lds + C_KOFF + 64 + tid * 16) = v4u{pkbf(kv[0], kv[1]), pkbf(kv[2], kv[3]), pkbf(kv[4], kv[5]), pkbf(kv[6], kv[7])};
      S[0] = kv[0];
#pragma unroll
      for (int j = 1; j < 8; ++j) S[j] = S[j - 1] + kv[j];
      float inc = S[7];
      inc += dppf<0x111>(inc); inc += dppf<0x112>(inc); inc += dppf<0x114>(inc); inc += dppf<0x118>(inc);
      inc += __builtin_bit_cast(float, __builtin_amdgcn_update_dpp(0, __builtin_bit_cast(int, inc), 0x142, 0xa, 0xf, false));
      inc += __builtin_bit_cast(float, __builtin_amdgcn_update_dpp(0, __builtin_bit_cast(int, inc), 0x143, 0xc, 0xf, false));
      soff = inc - S[7];
      if (lane == 63) ((LAS float*)(lds + C_KOFF + 4160))[wave] = inc;
    }
  }
  lds_post(lds0, 5 + bt, lane);
  lds_wait(lds0, 5 + bt, 12);
  STAMP(3);
  float xf[8], xn[8];
  {
#pragma unroll
    for (int j = 0; j < 4; ++j) { xf[2 * j] = bflo(xv[j]); xf[2 * j + 1] = bfhi(xv[j]); }
    const f32x4 wt = *(LAS const f32x4*)(lds + C_KOFF + 4160);
    if (tid < 128) *(LAS v4u*)(lds + C_UOFF + (tid >> 5) * C_UP + (tid & 31) * 16) = v4u{0u, 0u, 0u, 0u};
#pragma unroll
    for (int j = 0; j < 8; ++j) {
      const unsigned sj = st[j >> 2][j & 3];
      xn[j] = (xf[j] - bflo(sj)) * __builtin_amdgcn_rcpf(bfhi(sj));
    }
#pragma unroll
    for (int e = 0; e < 4; ++e) *(LAS unsigned*)(lds + C_UOFF + e * C_UP + 512 + tid * 4) = pkbf(xn[e], xn[e + 4]);
    soff += (wave > 0 ? wt[0] : 0.f) + (wave > 1 ? wt[1] : 0.f) + (wave > 2 ? wt[2] : 0.f);
  }
  const u16 dskv = A.dskip[d];
  char* mgp = ws + WS_MOD + (size_t)((lane & 3) * 3840 + (lane < 12 ? lane >> 2 : 0) * D + d) * 8;
  const unsigned long long mg = __hip_atomic_load((gu64*)mgp, RLX_AGENT);
  STAMP(5);
  group_sync(lds0, 5 + bt, 16, lane);
  STAMP(6);
  f32x16 y0 = {}, y1 = {};
  {
    const int hh = lane >> 5, blk = (lane >> 4) & 1, qq = (lane & 15) >> 2, p = lane & 3, col = lane & 31;
    LAS const char* ab = lds + C_KOFF + (32 + 16 * blk + 4 * p - 4 * qq + 32 * hh) * 2 - 32;
    LAS const char* bb = lds + C_UOFF + wave * C_UP + (256 + 8 * (col - hh) - 240) * 2;
    struct CF { v2u a0l, a0h, a1l, a1h; v4u b0, b1; };
    CF cf[3];
    auto rdc = [&](int ks, CF& f) {
      f.a0l = trread(ab + ks * 128 + 32); f.a0h = trread(ab + ks * 128);
      f.b0 = *(LAS const v4u*)(bb + (15 - ks) * 32);
      f.b1 = *(LAS const v4u*)(bb + (31 - ks) * 32);
      f.a1l = trread(ab + (ks + 16) * 128 + 32); f.a1h = trread(ab + (ks + 16) * 128);
    };
    rdc(0, cf[0]); rdc(1, cf[1]);
#pragma unroll
    for (int ks = 0; ks < 16; ++ks) {
      if (ks + 2 < 16) rdc(ks + 2, cf[(ks + 2) % 3]);
      SCHED_BAR();
      const CF& f = cf[ks % 3];
      const v4u a0 = {f.a0l.x, f.a0l.y, f.a0h.x, f.a0h.y}, a1 = {f.a1l.x, f.a1l.y, f.a1h.x, f.a1h.y};
      y0 = mfma32(a0, f.b0, y0);
      y1 = mfma32(a0, f.b1, y1);
      y1 = mfma32(a1, f.b0, y1);
      SCHED_BAR();
    }
    asm volatile("" :: "v"(y0), "v"(y1));
    STAMP(7);
    LAS char* cb = lds + C_COFF + (wave * 64 + col) * 144 + hh * 16;
#pragma unroll
    for (int g = 0; g < 4; ++g) {
      *(LAS f32x4*)(cb + g * 32) = f32x4{y0[4 * g], y0[4 * g + 1], y0[4 * g + 2], y0[4 * g + 3]};
      *(LAS f32x4*)(cb + 32 * 144 + g * 32) = f32x4{y1[4 * g], y1[4 * g + 1], y1[4 * g + 2], y1[4 * g + 3]};
    }
  }
  lds_post(lds0, 5 + bt, lane);
  unsigned mv[1] = {(unsigned)mg};
  if (!wall((unsigned)(mg >> 32) == tag)) get_granules<1>(mv, tag, [&](int) { return mgp; });
  float msum = u2f(mv[0]);
  msum += dppf<0xb1>(msum); msum += dppf<0x4e>(msum);
  const unsigned mb = f2u(bfr(msum));
  const float sa = u2f(__builtin_amdgcn_readlane(mb, 0)), c1a = bfr(1.0f + u2f(__builtin_amdgcn_readlane(mb, 4))), ga = u2f(__builtin_amdgcn_readlane(mb, 8)), dsk = bf2f(dskv);
  const float gc = ga * c1a, gs_ = ga * sa, gd = gc * dsk, g0 = gs_ * (soff + dsk);
#pragma unroll
  for (int j = 0; j < 8; ++j) xf[j] = __builtin_fmaf(gd, xn[j], __builtin_fmaf(gs_, S[j], xf[j] + g0));
  STAMP(8);
  lds_wait(lds0, 5 + bt, 20);
  STAMP(9);
  float y[8];
  {
    const int c1 = 2 * tid, c0 = tid ? c1 - 1 : 0;
    const int o0 = (c0 >> 3) * 144 + (c0 & 7) * 16, o1 = (c1 >> 3) * 144 + (c1 & 7) * 16;
    LAS const char* cb = lds + C_COFF;
    f32x4 w0[4], w1[4], w2[4];
#pragma unroll
    for (int e = 0; e < 4; ++e) {
      w1[e] = *(LAS const f32x4*)(cb + e * 64 * 144 + o1); w2[e] = *(LAS const f32x4*)(cb + e * 64 * 144 + o1 + 16);
      if (e) w0[e] = *(LAS const f32x4*)(cb + e * 64 * 144 + o0);
    }
    SCHED_BAR();
#pragma unroll
    for (int e = 0; e < 4; ++e) {
      float w[12];
#pragma unroll
      for (int j = 0; j < 4; ++j) { w[4 + j] = w1[e][j]; w[8 + j] = w2[e][j]; w[j] = (e && tid) ? w0[e][j] : 0.f; }
#pragma unroll
      for (int j = 0; j < 8; ++j) y[j] = e ? y[j] + w[4 + j - e] : w[4 + j];
    }
  }
  float o[8];
#pragma unroll
  for (int j = 0; j < 8; ++j) o[j] = __builtin_fmaf(gc, y[j], xf[j]);
  {
    const unsigned a0 = pkbf(o[0], o[1]), a1 = pkbf(o[2], o[3]), b0 = pkbf(o[4], o[5]), b1 = pkbf(o[6], o[7]);
    const bool odd = tid & 1;
    const unsigned s0 = odd ? a0 : b0, s1 = odd ? a1 : b1;
    const unsigned r0 = __builtin_amdgcn_update_dpp(0, s0, 0xb1, 0xf, 0xf, true), r1 = __builtin_amdgcn_update_dpp(0, s1, 0xb1, 0xf, 0xf, true);
    const v4u ov = odd ? v4u{r0, r1, b0, b1} : v4u{a0, a1, r0, r1};
    st128(rs, (unsigned)WS_X2T + d * (L * 2) + (tid >> 2) * 64 + (tid & 1) * 32 + (tid & 2) * 8, ov);
  }
  STAMP(10);
  if (wave_arrive(lds0, bt, 4, lane)) signal<E1_REP, 8>(ws + CTL_E1 + bt * 65536, wg & 7, lane);
}

__device__ __forceinline__ void aux_group(LAS char* lds, LAS char* lds0, const Args& A, __amdgpu_buffer_rsrc_t rs, unsigned epoch, int u, int tid, int wave, int lane STAMP_ARG) {
  char* ws = A.ws;
  unsigned gs = 0;
  v4u wb[5];
  unsigned tag;
  asm volatile("s_waitcnt vmcnt(1)\n\tv_readfirstlane_b32 %0, %1\n\ts_nop 3" : "=s"(tag) : "v"(epoch) : "memory");
  tag += 1u;
  auto load_wb = [&]() {
    const u16* bp = A.win + (size_t)(160 * (u & 3) + (tid >> 3)) * F + 64 * ((u < 108 ? u : 0) >> 2) + (tid & 7) * 8;
#pragma unroll
    for (int i = 0; i < 5; ++i) wb[i] = NTLOAD((const v4u*)(bp + (size_t)i * 32 * F));
  };
  {
    LAS const char* img = lds + 8192;
    group_sync(lds0, 7, gs += 4, lane);
    STAMP(3);
    const int r = tid >> 4, s = tid & 15;
    v4u v[5];
    {
      const int q = lane >> 4, rr = (lane & 15) >> 2, p = lane & 3, f = (rr | ((q & 1) << 2)) << 1;
      v2u lo[5], hi[5];
#pragma unroll
      for (int j = 0; j < 5; ++j) {
        const int a0 = (2 * (q & 1) * 5 + j) * 1024 + rr * 256 + ((((2 * wave + (q >> 1)) * 2 + (p >> 1)) ^ f) << 4) + (p & 1) * 8;
        lo[j] = trread(img + a0); hi[j] = trread(img + a0 + 5120);
      }
#pragma unroll
      for (int i = 0; i < 5; ++i) v[i] = *(LAS const v4u*)(img + (wave * 5 + i) * 1024 + lane * 16);
      SCHED_BAR();
#pragma unroll
      for (int j = 0; j < 5; ++j)
        st128(rs, (unsigned)WS_XT + ((u * 2 + (q & 1)) * D + (8 * j + 2 * wave + (q >> 1)) * 16 + (lane & 15)) * 16, v4u{lo[j].x, lo[j].y, hi[j].x, hi[j].y});
      SCHED_BAR();
    }
    float s0 = 0.f, s1 = 0.f, q0 = 0.f, q1 = 0.f;
#pragma unroll
    for (int i = 0; i < 5; ++i) {
      s0 = dot2bf(v[i][0], 0x3f803f80u, s0); s1 = dot2bf(v[i][1], 0x3f803f80u, s1); s0 = dot2bf(v[i][2], 0x3f803f80u, s0); s1 = dot2bf(v[i][3], 0x3f803f80u, s1);
      q0 = dot2bf(v[i][0], v[i][0], q0); q1 = dot2bf(v[i][1], v[i][1], q1); q0 = dot2bf(v[i][2], v[i][2], q0); q1 = dot2bf(v[i][3], v[i][3], q1);
    }
    float sum = s0 + s1, sq = q0 + q1;
    sum += dppf<0xb1>(sum); sq += dppf<0xb1>(sq); sum += dppf<0x4e>(sum); sq += dppf<0x4e>(sq);
    sum += dppf<0x141>(sum); sq += dppf<0x141>(sq); sum += dppf<0x140>(sum); sq += dppf<0x140>(sq);
    const float mean = sum * (1.0f / D);
    const int t = u * 16 + r;
    st32(rs, s == 0 ? (unsigned)WS_ST1 + ((((t >> 2) & 1) * 256 + (t >> 3)) * 4 + (t & 3)) * 4 : 0x7ffffff0u, pkbf(mean, ln_std(__builtin_fmaxf(sq * (1.0f / D) - mean * mean, 0.f))));
    STAMP(8);
    if (wave_arrive(lds0, 9, 4, lane) && lane < F0_REP)
      __hip_atomic_store((gu64*)(ws + CTL_F0 + lane * 1024 + u * 8), ((unsigned long long)~tag << 32) | tag, RLX_AGENT);
    STAMP(1);
  }
  if (u < 108) {
    const int slab = u >> 2, r = tid >> 3, s = tid & 7;
    const int half = (r >> 2) & 1, jj = slab < 24 ? slab / 3 : 2 * (slab - 24) + half;
    const int sub = (jj < 6 ? 7 * jj : 42 + 6 * (jj - 6)) + (slab < 24 ? 2 * (slab - 3 * jj) + half : 6);
    const u16* src = A.wout + (size_t)(32 * (sub >> 1) + 8 * (r >> 3) + 4 * (sub & 1) + (r & 3)) * D + (u & 3) * 160;
    v4u v[3];
#pragma unroll
    for (int i = 0; i < 3; ++i) v[i] = NTLOAD((const v4u*)(src + (i < 2 || s < 4 ? s + 8 * i : 0) * 8));
    load_wb();
    SCHED_BAR();
    constexpr int OP = 400;
    LAS char* img = lds + 32768;
#pragma unroll
    for (int i = 0; i < 3; ++i) *(LAS v4u*)(img + r * OP + (s + 8 * i) * 16) = v[i];
    group_sync(lds0, 7, gs += 4, lane);
    const int a0 = (8 * (lane >> 4) + ((lane & 15) >> 2)) * OP + (lane & 3) * 8 + wave * 32;
#pragma unroll
    for (int j = 0; j < 3; ++j) {
      const v2u lo = trread(img + a0 + j * 128), hi = trread(img + a0 + 4 * OP + j * 128);
      st128(rs, wave + 4 * j < 10 ? (unsigned)WS_WOUT + ((((u & 3) * 10 + wave + 4 * j) * 27 + slab) * 64 + lane) * 16 : 0x7ffffff0u, v4u{lo.x, lo.y, hi.x, hi.y});
    }
  }
  if (u < 108) {
    const int cbk = u >> 2, part = u & 3;
    const int c0 = 64 * cbk, r0 = 160 * part, seg = tid & 7, rg = tid >> 3;
    v4u w[5];
#pragma unroll
    for (int i = 0; i < 5; ++i) w[i] = wb[i];
    LAS f32x2* mt2 = (LAS f32x2*)(lds + 28672);
    if (wave < 3) {
      unsigned mv[8];
      const int rw = r0 + (tid < 160 ? tid : 159);
      get_granules<8>(mv, tag, [&](int k) { return ws + WS_MOD + (size_t)((k & 3) * 3840 + (3 + (k >> 2)) * D + rw) * 8; });
      mt2[tid] = f32x2{bfr((u2f(mv[0]) + u2f(mv[1])) + (u2f(mv[2]) + u2f(mv[3]))), bfr(1.0f + bfr((u2f(mv[4]) + u2f(mv[5])) + (u2f(mv[6]) + u2f(mv[7]))))};
    }
    group_sync(lds0, 7, gs += 4, lane);
    STAMP(2);
    float v[16] = {};
    constexpr int WPITCH = 144;
    LAS char* img = lds + 1024;
#pragma unroll
    for (int i = 0; i < 5; ++i) {
      const f32x2 ms = mt2[rg + 32 * i];
      const float c1 = ms.y, sb = ms.x;
#pragma unroll
      for (int j = 0; j < 4; ++j) {
        const float a = bflo(w[i][j]), c = bfhi(w[i][j]);
        const unsigned pk = pkbf(a * c1, c * c1);
        v[8 + 2 * j] += sb * a; v[8 + 2 * j + 1] += sb * c;
        v[2 * j] += bflo(pk); v[2 * j + 1] += bfhi(pk);
        w[i][j] = pk;
      }
      *(LAS v4u*)(img + (rg + 32 * i) * WPITCH + seg * 16) = w[i];
    }
    treduce_xor<8>(v, (lane & 32) != 0, 32);
    treduce_xor<4>(v, (lane & 16) != 0, 16);
    treduce_dpp<2, 0x128>(v, (lane & 8) != 0);
    LAS float* red = (LAS float*)(lds + 24576 + 1024);
    {
      const int which = lane >> 5, j = ((lane >> 4) & 1) * 4 + ((lane >> 3) & 1) * 2;
      *(LAS f32x2*)(red + wave * 128 + which * 64 + seg * 8 + j) = f32x2{v[0], v[1]};
    }
    group_sync(lds0, 7, gs += 4, lane);
    STAMP(3);
    const int q = lane >> 4, rr = (lane & 15) >> 2, p = lane & 3;
    const int colb = (32 * (wave >> 1) + 8 * p + 4 * (wave & 1)) * 2;
#pragma unroll
    for (int i = 0; i < 5; ++i) {
      const int a0 = (32 * i + 8 * q + rr) * WPITCH + colb;
      v2u lo = trread(img + a0), hi = trread(img + a0 + 4 * WPITCH);
      st128(rs, (unsigned)WS_WIN + (((4 * cbk + wave) * 20 + part * 5 + i) * 64 + lane) * 16, v4u{lo.x, lo.y, hi.x, hi.y});
    }
    if (tid < 128) {
      const int which = tid >> 6, o = tid & 63, sub = o >> 4, i = o & 15;
      const int cl = 32 * (sub >> 1) + 8 * (i >> 2) + 4 * (sub & 1) + (i & 3), ix = which * 64 + cl;
      st32(rs, (unsigned)WS_U + ((part * 2 + which) * F + c0 + o) * 4, f2u((red[ix] + red[128 + ix]) + (red[256 + ix] + red[384 + ix])));
    }
  }
  STAMP(8);
  lds_wait(lds0, 8, 1);
  if (u < 108 && wave_arrive(lds0, 2, 4, lane)) signal<E1_REP, 8>(ws + CTL_E1B, u & 7, lane);
}

__device__ __forceinline__ int g1_aoff(int row, int G, int ab, int p) {
  const int f = ((row >> 1) & 1) | (((row >> 3) & 1) << 1);
  return row * 128 + (((4 * G + 2 * ab + (p >> 1)) ^ (f << 1)) << 4) + (p & 1) * 8;
}

template <int NB>
__device__ __forceinline__ void loader_path(LAS char* lds, char* ws, __amdgpu_buffer_rsrc_t rs, const u16* __restrict__ wsp, int wg, int mt, int j, int sub0, int lw, int lane) {
  const unsigned lbase = (unsigned)(size_t)lds, vo = lane * 16;
  {
    unsigned avo[2];
#pragma unroll
    for (int h = 0; h < 2; ++h) {
      const int row = lane >> 3, pos = lane & 7, f = ((row >> 1) & 1) | (h << 1);
      avo[h] = row * (L * 2) + ((pos ^ (f << 1)) << 4);
    }
    const char* asb = ws + WS_X2T + (size_t)(16 * lw) * (L * 2) + mt * 128;
    const char* bsb = ws + WS_WIN + (size_t)((sub0 + (lw >> 1)) * 20 + (lw & 1)) * 1024;
    const unsigned bb = lbase + G1_BOFF;
    auto issueB = [&](int s, unsigned slot) {
      const char* bs = bsb + (s < 10 ? 54 * 20 * 1024 + s * 2048 : (s - 10) * 2048);
#pragma unroll
      for (int i = 0; i < NB; ++i) glds16<true>(bs + i * 2 * 20 * 1024, vo, slot + (lw + 4 * i) * 1024);
    };
    auto issueA = [&](int s0, int s1) {
#pragma unroll 1
      for (int s = s0; s < s1; ++s)
#pragma unroll
        for (int h = 0; h < 2; ++h) glds16<true>(asb + (size_t)(s * 64 + h * 8) * (L * 2), avo[h], lbase + s * 8192 + lw * 2048 + h * 1024);
    };
    {
      const unsigned vb = (unsigned)WS_V + (mt * 4 + lw) * 27 * 1024 + lane * 16;
#pragma unroll
      for (int k = 0; k < 3; ++k) st128(rs, vb + (3 * j + k) * 1024, v4u{~0u, ~0u, ~0u, ~0u});
      if (j < 6) __builtin_amdgcn_raw_buffer_store_b64(v2u{~0u, ~0u}, rs, vb + (24 + (j >> 1)) * 1024 + (j & 1) * 8, 0, 16);
    }
    group_sync(lds, 5, 24, lane);
    if (lw == 0) { wait_count<8>(ws + CTL_E1B, wg & (E1_REP - 1), 108, lane); lds_post(lds, 10, lane); }
    if (lw == 3) { wait_count<8>(ws + CTL_E1, wg & (E1_REP - 1), 256, lane); lds_post(lds, 11, lane); }
    lds_wait(lds, 10, 1);
    STAMP(13);
#pragma unroll
    for (int s = 0; s < 4; ++s) issueB(s, bb + (s + 1) * G1_BST);
    lds_wait(lds, 11, 1);
    lds_wait(lds, 1, 4);
    issueA(0, 4);
    STAMP(12);
    LBAR();
    LSTAMP(1); STAMP(14);
    issueB(4, bb);
    waitv<6 + NB>(); LBAR(); LSTAMP(2);
    waitv<4 + NB>(); LBAR(); issueB(5, bb + G1_BST);
    waitv<2 + 2 * NB>(); LBAR(); LSTAMP(3); issueA(4, 8); issueB(6, bb + 2 * G1_BST);
    waitv<8 + 3 * NB>(); LBAR(); issueB(7, bb + 3 * G1_BST);
    waitv<6 + 2 * NB>(); LBAR(); LSTAMP(4); issueB(8, bb + 4 * G1_BST);
    waitv<4 + 3 * NB>(); LBAR(); LSTAMP(5); issueA(8, 10); issueB(9, bb);
    waitv<4 + 3 * NB>(); LBAR(); issueB(10, bb + G1_BST);
    waitv<4 + 3 * NB>(); LBAR(); issueB(11, bb + 2 * G1_BST);
    waitv<2 + 3 * NB>(); LBAR(); LSTAMP(6); issueB(12, bb + 3 * G1_BST);
    int nxt = 4 * G1_BST;
#pragma unroll 1
    for (int i = 9; i <= 15; ++i) {
      waitv<NB * 3>(); LBAR();
      if (i >= 11) LSTAMP(i - 4);
      issueB(i + 4, bb + nxt);
      nxt += G1_BST; if (nxt == G1_NSLOT * G1_BST) nxt = 0;
    }
    waitv<NB * 3>(); LBAR(); LSTAMP(12);
    waitv<NB * 2>(); LBAR(); LSTAMP(13);
    waitv<NB * 1>(); LBAR();
    waitv<0>(); LBAR(); LSTAMP(14);
    LBAR();
  }
  const char* osb = ws + WS_WOUT + (size_t)j * 135 * 1024;
  if (lw >= 2) {
    unsigned wvo[2];
#pragma unroll
    for (int h = 0; h < 2; ++h) {
      const int i = 8 * h + (lane >> 3);
      wvo[h] = (8 * (i >> 2) + (i & 3)) * (L * 2) + (((lane & 7) ^ (i >> 1)) << 4);
    }
    const char* wsb = (const char*)wsp + (size_t)(mt * 64 + 32 * (lw & 1)) * (L * 2);
    auto issueW = [&](int s, unsigned slot) {
#pragma unroll
      for (int i = 0; i < 4; ++i) glds16<false>(wsb + (i >> 1) * 4 * (L * 2) + s * 128, wvo[i & 1], slot + (lw & 1) * 4096 + i * 1024);
    };
#pragma unroll 1
    for (int s = 0; s < G2_NW; ++s) issueW(s, lbase + s * 8192);
    LBAR();
    waitv<32>(); LBAR();
    int wn = 0;
#pragma unroll 1
    for (int i = 1; i <= 23; ++i) { waitv<28>(); LBAR(); issueW(i + 8, lbase + wn); wn += 8192; if (wn == G2_NW * 8192) wn = 0; }
#pragma unroll 1
    for (int i = 24; i <= 32; ++i) {
      waitv<28>(); LBAR();
#pragma unroll
      for (int k = 0; k < 4; ++k) glds16<true>(osb + wn + (lw & 1) * 4096 + k * 1024, vo, lbase + wn + (lw & 1) * 4096 + k * 1024);
      wn += 8192; if (wn == G2_NW * 8192) wn = 0;
    }
  } else {
    constexpr int NZ = NB == 4 ? 7 : 6;
    const char* zsb = ws + WS_Z2 + (size_t)(sub0 * 64 + lw) * 1024;
    auto issueZ = [&](int s, unsigned slot) {
#pragma unroll
      for (int i = 0; i < NZ; ++i) glds16<true>(zsb + i * 65536 + s * 2048, vo, slot + (lw + 2 * i) * 1024);
    };
    const unsigned zb = lbase + G2_ZOFF;
    LBAR();
#pragma unroll 1
    for (int s = 0; s < G2_NZ; ++s) issueZ(s, zb + s * G2_ZST);
    waitv<NZ * 5>(); LBAR();
    int zn = 0;
#pragma unroll 1
    for (int i = 1; i <= 26; ++i) { waitv<NZ * 4>(); LBAR(); issueZ(i + 5, zb + zn); zn += G2_ZST; if (zn == G2_NZ * G2_ZST) zn = 0; }
    auto issueO = [&](int slot, int n) {
      const int o = G2_ZOFF + slot * G2_ZST + lw * 1024;
#pragma unroll
      for (int k = 0; k < 7; ++k) if (k < n) glds16<true>(osb + o + k * 2048, vo, lbase + o + k * 2048);
    };
    waitv<NZ * 4>(); LBAR(); issueO(2, 7);
    waitv<NZ * 3 + 7>(); LBAR(); issueO(3, 7);
    waitv<NZ * 2 + 14>(); LBAR(); issueO(4, 3);
    waitv<NZ + 17>(); LBAR(); if (lw == 0) glds16<true>(osb + 134 * 1024, vo, lbase + 134 * 1024);
    waitv<17>(); LBAR(); issueO(0, 7);
    LBAR(); issueO(1, 7);
  }
  waitv<0>();
  LBAR();
}

template <int NSUB, bool STATS>
__device__ __forceinline__ void consumer_g12(LAS char* lds, char* ws, __amdgpu_buffer_rsrc_t rs, const u16* __restrict__ bsp, const u16* __restrict__ wsp, int wg, int mt, int j, int sub0, int cnt,
                                             int wave, int tid, int lane, v2u (&x2v)[3] STAMP_ARG) {
  constexpr int NS1 = NSUB > 0 ? NSUB : 1;
  const int G = wave >> 2, ng = G ? 3 - (wave & 3) : (wave & 3);
  const int q = lane >> 4, i16 = lane & 15, rr = i16 >> 2, p = lane & 3, n0 = 2 * ng;
  float bias[2];
#pragma unroll
  for (int m = 0; m < 2; ++m) bias[m] = bf2f(bsp[mt * 64 + 32 * G + 8 * (i16 >> 2) + 4 * m + (i16 & 3)]);
  STAMP(4);
  CBAR();
  v2u z1v[NS1][2];
  {
    const int aoff0 = g1_aoff(8 * q + rr, G, 0, p), aoff1 = g1_aoff(8 * q + rr, G, 1, p);
    const int boff = G1_BOFF + n0 * 2048 + lane * 16;
    struct Frags { v4u a[2][2]; v4u b[2][NS1]; };
    f32x4 acc2[NS1][2], acc1[NS1][2], asum[2], asq[2];
    int acur = 0, bcur = G1_BST;
    auto rd = [&](Frags& f) {
#pragma unroll
      for (int kk = 0; kk < 2; ++kk) {
        const v2u l0 = trread(lds + acur + aoff0 + kk * 4096), h0 = trread(lds + acur + aoff0 + kk * 4096 + 512);
        const v2u l1 = trread(lds + acur + aoff1 + kk * 4096), h1 = trread(lds + acur + aoff1 + kk * 4096 + 512);
        f.a[kk][0] = v4u{l0.x, l0.y, h0.x, h0.y}; f.a[kk][1] = v4u{l1.x, l1.y, h1.x, h1.y};
#pragma unroll
        for (int n = 0; n < NSUB; ++n) f.b[kk][n] = *(LAS const v4u*)(lds + bcur + boff + n * 2048 + kk * 1024);
      }
      acur += 8192; if (acur == 81920) acur = 0;
      bcur += G1_BST; if (bcur == G1_NSLOT * G1_BST) bcur = 0;
    };
    auto pin1 = [&]() {
#pragma unroll
      for (int n = 0; n < NSUB; ++n) asm volatile("" : "+v"(acc1[n][0]), "+v"(acc1[n][1]));
    };
#pragma unroll
    for (int ab = 0; ab < 2; ++ab) {
      asum[ab] = f32x4{0.f, 0.f, 0.f, 0.f}; asq[ab] = f32x4{0.f, 0.f, 0.f, 0.f};
#pragma unroll
      for (int n = 0; n < NS1; ++n) { acc2[n][ab] = f32x4{0.f, 0.f, 0.f, 0.f}; acc1[n][ab] = f32x4{0.f, 0.f, 0.f, 0.f}; }
    }
    const v4u ones = {0x3f803f80u, 0x3f803f80u, 0x3f803f80u, 0x3f803f80u};
    auto mm2 = [&](const Frags& f) {
#pragma unroll
      for (int kk = 0; kk < 2; ++kk)
#pragma unroll
        for (int ab = 0; ab < 2; ++ab) {
#pragma unroll
          for (int n = 0; n < NSUB; ++n) acc2[n][ab] = mfma16(f.a[ABL_KK][ab], f.b[ABL_KK][n], acc2[n][ab]);
          if (STATS) { asum[ab] = mfma16(f.a[kk][ab], ones, asum[ab]); asq[ab] = mfma16(f.a[kk][ab], f.a[kk][ab], asq[ab]); }
        }
      INTERLEAVE(6, 2);
    };
    auto mm1 = [&](const Frags& f) {
#pragma unroll
      for (int kk = 0; kk < 2; ++kk)
#pragma unroll
        for (int ab = 0; ab < 2; ++ab)
#pragma unroll
          for (int n = 0; n < NSUB; ++n) acc1[n][ab] = mfma16(f.b[kk][n], f.a[kk][ab], acc1[n][ab]);
      if (NSUB == 2) INTERLEAVE(6, 2); else INTERLEAVE(4, 3);
    };
    Frags f0, f1;
    CBAR(); rd(f0);
    SCHED_BAR();
    float up[4];
    {
      const int c = tid & 127, which = (tid >> 7) & 1, half = tid >> 8;
#pragma unroll
      for (int k = 0; k < 4; ++k) up[k] = ld32f(rs, c < cnt * 16 ? (unsigned)WS_U + ((k * 2 + which) * F + (sub0 + (half ? 0 : 54)) * 16 + c) * 4 : 0x7ffffff0u);
    }
    SCHED_BAR();
    STAMP(5);
    auto pin2 = [&]() {
#pragma unroll
      for (int n = 0; n < NSUB; ++n) asm volatile("" : "+v"(acc2[n][0]), "+v"(acc2[n][1]));
      asm volatile("" : "+v"(asum[0]), "+v"(asum[1]), "+v"(asq[0]), "+v"(asq[1]));
    };
    if (STATS) {
      char* cb = ws + CTL_E1 + (G + 1) * 65536;
      unsigned pc;
      poll_issue<8>(pc, cb, wg & (E1_REP - 1), lane);
      CBAR(); rd(f1); mm2(f0);
      if (G == 0) poll_finish<8>(pc, cb, wg & (E1_REP - 1), 256, lane);
      CBAR(); pin2(); rd(f0); mm2(f1);
      CBAR(); rd(f1); mm2(f0);
      poll_issue<8>(pc, cb, wg & (E1_REP - 1), lane);
      CBAR(); rd(f0); mm2(f1);
      if (G == 1) poll_finish<8>(pc, cb, wg & (E1_REP - 1), 128, lane);
      asm volatile("" :: "v"(pc));
      pin2();
    }
#pragma unroll 1
    for (int it = STATS ? 2 : 0; it < 5; ++it) {
      CBAR(); rd(f1); mm2(f0);
      CBAR(); rd(f0); mm2(f1);
    }
    ((LAS float*)(lds + G1_T_U))[tid] = (up[0] + up[1]) + (up[2] + up[3]);
    unsigned pv = 0;
    if (!STATS && ng < 2) pv = *(const unsigned*)((const char*)wsp + (size_t)(mt * 64 + (G * 2 + ng) * 16 + (lane >> 2)) * (L * 2) + j * 512 + (lane & 3) * 128);
    LAS f32x2* tab = (LAS f32x2*)(lds + G1_T_LN2);
    if (STATS) {
#pragma unroll
      for (int ab = 0; ab < 2; ++ab) {
        const float a0 = asum[ab][0], a1 = asum[ab][1], a2 = asum[ab][2], a3 = asum[ab][3];
        const float b0 = asq[ab][0], b1 = asq[ab][1], b2 = asq[ab][2], b3 = asq[ab][3];
        const float s1 = (p & 2) ? ((p & 1) ? a3 : a2) : ((p & 1) ? a1 : a0), s2 = (p & 2) ? ((p & 1) ? b3 : b2) : ((p & 1) ? b1 : b0);
        const float mean = s1 * (1.0f / D);
        float mb, rinv;
        ln_scalars(mean, __builtin_fmaxf(s2 * (1.0f / D) - mean * mean, 0.f), mb, rinv);
        if (rr == q) tab[(G * 2 + ab) * 16 + i16] = f32x2{rinv, rinv * mb};
      }
    }
    {
      const int nb = G ? 3 : 0;
#pragma unroll
      for (int n = 0; n < 3; ++n) {
        const int row = 80 * j + 16 * (nb + n < 5 ? nb + n : 4) + 4 * q + rr;
        x2v[n] = trread(lds + g1_aoff(row, (wave >> 1) & 1, wave & 1, p));
      }
    }
    STAMP(6);
    CBAR(); rd(f1); mm1(f0);
    v4u zq[NS1];
    {
      LAS const float* ut = (LAS const float*)(lds + G1_T_U);
      float v[16];
#pragma unroll
      for (int e = 0; e < 16; ++e) v[e] = 0.f;
      if (NSUB > 0) {
        f32x2 rv[8];
#pragma unroll
        for (int e = 0; e < 8; ++e) rv[e] = tab[(G * 2 + (e >> 2)) * 16 + 4 * q + (e & 3)];
#pragma unroll
        for (int n = 0; n < NSUB; ++n) {
          const float c1 = ut[(n0 + n) * 16 + i16], c2 = ut[128 + (n0 + n) * 16 + i16];
          float z[8];
#pragma unroll
          for (int e = 0; e < 8; ++e) z[e] = gelu_f(__builtin_fmaf(rv[e].x, acc2[n][e >> 2][e & 3], __builtin_fmaf(-rv[e].y, c1, c2)));
          zq[n] = v4u{pkbf(z[0], z[1]), pkbf(z[2], z[3]), pkbf(z[4], z[5]), pkbf(z[6], z[7])};
#pragma unroll
          for (int e = 0; e < 4; ++e) {
            const float a = bflo(zq[n][e]), c = bfhi(zq[n][e]);
            v[4 * e] += a; v[4 * e + 1] = __builtin_fmaf(a, a, v[4 * e + 1]);
            v[4 * e + 2] += c; v[4 * e + 3] = __builtin_fmaf(c, c, v[4 * e + 3]);
          }
        }
        treduce_dpp<8, 0x140>(v, (i16 & 8) != 0);
        treduce_dpp<4, 0x141>(v, (i16 & 4) != 0);
        treduce_dpp<2, 0x4e>(v, (i16 & 2) != 0);
        treduce_dpp<1, 0xb1>(v, (i16 & 1) != 0);
      }
      ((LAS float*)(lds + G1_T_P))[ng * 128 + G * 64 + lane] = v[0];
    }
    CBAR(); pin1(); rd(f0); mm1(f1);
    GAS unsigned long long* gr = (GAS unsigned long long*)(ws + CTL_ST3) + (size_t)mt * 1024 + G * 32;
    if (STATS) {
      LAS const float* pp = (LAS const float*)(lds + G1_T_P) + G * 64 + lane;
      const float sv = (pp[0] + pp[128]) + (pp[256] + pp[384]);
      __hip_atomic_store(gr + (j * 2 + (lane & 1)) * 64 + (lane >> 1), (1ull << 32) | f2u(sv), RLX_AGENT);
    }
    CBAR(); pin1(); rd(f1); mm1(f0);
    CBAR(); pin1(); rd(f0); mm1(f1);
    unsigned long long gx[8];
    if (STATS) {
#pragma unroll
      for (int k = 0; k < 8; ++k) gx[k] = __hip_atomic_load(gr + (k * 2 + (lane >> 5)) * 64 + (lane & 31), RLX_AGENT);
    }
    CBAR(); pin1(); rd(f1); mm1(f0);
    if (STATS) {
      float sv;
      for (unsigned spin = 0;; ++spin) {
        bool ok = true;
        sv = 0.f;
#pragma unroll
        for (int k = 0; k < 8; ++k) { ok &= (gx[k] >> 32) == 1ull; sv += u2f((unsigned)gx[k]); }
        if (wall(ok) || spin > SPIN_CAP) break;
        __builtin_amdgcn_s_sleep(1);
#pragma unroll
        for (int k = 0; k < 8; ++k) gx[k] = __hip_atomic_load(gr + (k * 2 + (lane >> 5)) * 64 + (lane & 31), RLX_AGENT);
      }
      const float s2 = shx(sv, 32);
      const float mean = sv * (1.0f / FH);
      float mb, rinv;
      ln_scalars(mean, __builtin_fmaxf(s2 * (1.0f / FH) - mean * mean, 0.f), mb, rinv);
      if (lane < 32) ((LAS f32x2*)(lds + G1_T_Z))[32 * G + lane] = f32x2{-mb * rinv, rinv};
    }
    CBAR(); pin1(); rd(f0); mm1(f1);
    if (NSUB > 0) {
      f32x2 zs[8];
#pragma unroll
      for (int e = 0; e < 8; ++e) zs[e] = ((LAS const f32x2*)(lds + G1_T_Z))[32 * G + 8 * q + e];
#pragma unroll
      for (int n = 0; n < NSUB; ++n) {
        v4u o;
#pragma unroll
        for (int e = 0; e < 4; ++e)
          o[e] = pkbf(__builtin_fmaf(bflo(zq[n][e]), zs[2 * e].y, zs[2 * e].x), __builtin_fmaf(bfhi(zq[n][e]), zs[2 * e + 1].y, zs[2 * e + 1].x));
        st128(rs, (unsigned)WS_Z2 + (((sub0 + n0 + n) * 64 + mt * 2 + G) * 64 + lane) * 16, o);
      }
    }
    CBAR(); pin1(); rd(f1); mm1(f0);
    CBAR(); pin1(); rd(f0); mm1(f1);
    CBAR(); pin1(); rd(f1); mm1(f0);
    if (wave_arrive(lds, 3, 8, lane)) signal<4, 4>(ws + CTL_E2 + j * 4096, mt & 3, lane);
    asm volatile("" :: "v"(pv));
    CBAR(); pin1();
    mm1(f1);
    STAMP(7);
    {
      LAS const float* ut = (LAS const float*)(lds + G1_T_U) + 256;
      f32x2 rv[2];
#pragma unroll
      for (int ab = 0; ab < 2; ++ab) rv[ab] = tab[(G * 2 + ab) * 16 + i16];
#pragma unroll
      for (int n = 0; n < NSUB; ++n) {
        const f32x4 u1 = *(LAS const f32x4*)(ut + (n0 + n) * 16 + 4 * q), u2 = *(LAS const f32x4*)(ut + 128 + (n0 + n) * 16 + 4 * q);
#pragma unroll
        for (int ab = 0; ab < 2; ++ab) {
          float z[4];
#pragma unroll
          for (int e = 0; e < 4; ++e) z[e] = gelu_f(__builtin_fmaf(rv[ab].x, acc1[n][ab][e], __builtin_fmaf(-rv[ab].y, u1[e], u2[e])));
          z1v[n][ab] = v2u{pkbf(z[0], z[1]), pkbf(z[2], z[3])};
          asm volatile("" : "+v"(z1v[n][ab]));
        }
      }
    }
  }
  if (wave == 1 || wave == 2) {
    const int c = tid - 64;
    if (c < 80) {
      const unsigned o = (unsigned)WS_MOD + (5 * D + 80 * j + c) * 8;
      ((LAS float*)(lds + G3_GB))[c] = bfr((ld32f(rs, o) + ld32f(rs, o + 30720)) + (ld32f(rs, o + 61440) + ld32f(rs, o + 92160)));
    }
  }
  if (wave == 3) wait_count<4>(ws + CTL_E2 + j * 4096, (mt >> 2) & 3, 32, lane);
  CBAR();
  STAMP(9);
  {
    f32x4 acc[NS1][2];
#pragma unroll
    for (int n = 0; n < NS1; ++n) { acc[n][0] = f32x4{0.f, 0.f, 0.f, 0.f}; acc[n][1] = f32x4{0.f, 0.f, 0.f, 0.f}; }
    int woff[2];
#pragma unroll
    for (int kk = 0; kk < 2; ++kk) woff[kk] = G * 4096 + i16 * 128 + (((4 * kk + q) ^ (i16 >> 1)) << 4);
    const int zoff = G2_ZOFF + n0 * 2048 + lane * 16;
    struct Frags { v4u w[2][2]; v4u z[2][NS1]; };
    int wc = 0, zc = 0;
    auto rd = [&](Frags& f) {
      if (NSUB > 0) {
#pragma unroll
        for (int kk = 0; kk < 2; ++kk) {
#pragma unroll
          for (int m = 0; m < 2; ++m) f.w[kk][m] = *(LAS const v4u*)(lds + wc + woff[kk] + m * 2048);
#pragma unroll
          for (int n = 0; n < NSUB; ++n) f.z[kk][n] = *(LAS const v4u*)(lds + zc + zoff + n * 2048 + kk * 1024);
        }
        wc += 8192; if (wc == G2_NW * 8192) wc = 0;
        zc += G2_ZST; if (zc == G2_NZ * G2_ZST) zc = 0;
      }
    };
    auto mm = [&](const Frags& f) {
#pragma unroll
      for (int kk = 0; kk < 2; ++kk)
#pragma unroll
        for (int n = 0; n < NSUB; ++n)
#pragma unroll
          for (int m = 0; m < 2; ++m) acc[n][m] = mfma16(f.z[ABL_KK][n], f.w[ABL_KK][m], acc[n][m]);
      INTERLEAVE(4, 2);
    };
    Frags f0, f1;
    CBAR(); rd(f0);
    STAMP(10);
#pragma unroll 1
    for (int it = 0; it < 15; ++it) {
      CBAR(); rd(f1); mm(f0);
      CBAR(); rd(f0); mm(f1);
    }
    CBAR(); rd(f1); mm(f0);
    mm(f1);
    STAMP(11);
#pragma unroll
    for (int m = 0; m < 2; ++m) {
      v2u o[NS1];
#pragma unroll
      for (int n = 0; n < NSUB; ++n) {
        float g[4];
#pragma unroll
        for (int e = 0; e < 4; ++e) g[e] = acc[n][m][e] + bias[m];
        const v2u zz = z1v[n][m];
        o[n] = v2u{pkbf(bflo(zz.x) * g[0], bfhi(zz.x) * g[1]), pkbf(bflo(zz.y) * g[2], bfhi(zz.y) * g[3])};
      }
      const unsigned vb = (unsigned)WS_V + (mt * 4 + G * 2 + m) * 27 * 1024 + lane * 16;
      if (NSUB == 2) st128(rs, vb + (3 * j + ng) * 1024, v4u{o[0].x, o[0].y, o[NS1 - 1].x, o[NS1 - 1].y});
      if (NSUB == 1) __builtin_amdgcn_raw_buffer_store_b64(o[0], rs, vb + (24 + (j >> 1)) * 1024 + (j & 1) * 8, 0, 16);
    }
    CBAR();
  }
}

template <int KH>
__device__ __forceinline__ void consumer_g3(LAS char* lds, char* ws, __amdgpu_buffer_rsrc_t rs, u16* __restrict__ out, int mt, int j, int m, int lane, const v2u (&x2v)[3] STAMP_ARG) {
  constexpr int K0 = KH ? 14 : 0, NK = KH ? 13 : 14, NF = KH ? 2 : 3, N0 = KH ? 3 : 0;
  const int q = lane >> 4, i16 = lane & 15;
  v4u vf[NK];
  __builtin_amdgcn_s_sleep(V_DELAY);
  CBAR();
  STAMP(12);
  const unsigned vo = (unsigned)WS_V + ((mt * 4 + m) * 27 + K0) * 1024 + lane * 16;
  constexpr int VW = V_WIN;
#pragma unroll
  for (int k = 0; k < VW; ++k) { vf[k] = ld128(rs, vo + k * 1024); SCHED_BAR(); }
  f32x4 acc[5];
#pragma unroll
  for (int n = 0; n < 5; ++n) acc[n] = f32x4{0.f, 0.f, 0.f, 0.f};
  LAS const char* wp = lds + K0 * 1024 + lane * 16;
  LAS const char* wp2 = wp + 54 * 1024;
  LAS const char* wp4 = wp + 108 * 1024;
  v4u wf[3][5];
  auto rdw = [&](int k, v4u (&w)[5]) {
    w[0] = *(LAS const v4u*)(wp + k * 1024); w[1] = *(LAS const v4u*)(wp + (27 + k) * 1024);
    w[2] = *(LAS const v4u*)(wp2 + k * 1024); w[3] = *(LAS const v4u*)(wp2 + (27 + k) * 1024);
    w[4] = *(LAS const v4u*)(wp4 + k * 1024);
  };
  rdw(0, wf[0]); rdw(1, wf[1]);
#pragma unroll
  for (int k = 0; k < NK; ++k) {
    if (k + VW < NK) vf[k + VW] = ld128(rs, vo + (k + VW) * 1024);
    if (k + 2 < NK) rdw(k + 2, wf[(k + 2) % 3]);
    SCHED_BAR();
    if (__builtin_expect(!wall(__builtin_elementwise_max(vf[k].x, vf[k].z) != ~0u), 0)) {
      unsigned spin = 0;
      do { asm volatile("" ::: "memory"); vf[k] = ld128(rs, vo + k * 1024); }
      while (!wall(__builtin_elementwise_max(vf[k].x, vf[k].z) != ~0u) && ++spin < SPIN_CAP);
    }
#pragma unroll
    for (int n = 0; n < 5; ++n) acc[n] = mfma16(wf[k % 3][n], vf[k], acc[n]);
    asm volatile("" : "+v"(acc[0]), "+v"(acc[1]), "+v"(acc[2]), "+v"(acc[3]), "+v"(acc[4]));
    SCHED_BAR();
  }
  STAMP(13);
  f32x4 gb[NF];
#pragma unroll
  for (int n = 0; n < NF; ++n) gb[n] = *(LAS const f32x4*)(lds + G3_GB + (16 * (N0 + n) + 4 * q) * 4);
  LAS char* red = lds + G3_RED + m * 5120 + lane * 16;
#pragma unroll
  for (int n = 0; n < 5; ++n) if ((n >= 3) != (KH != 0)) *(LAS f32x4*)(red + n * 1024) = acc[n];
  CBAR();
  const int t = mt * 64 + 32 * (m >> 1) + 8 * (i16 >> 2) + 4 * (m & 1) + (i16 & 3);
#pragma unroll
  for (int n = 0; n < NF; ++n) {
    const f32x4 o = *(LAS const f32x4*)(red + (N0 + n) * 1024);
    const f32x4 a = KH ? o + acc[N0 + n] : acc[N0 + n] + o;
    const float o0 = __builtin_fmaf(gb[n][0], a[0], bflo(x2v[n].x)), o1 = __builtin_fmaf(gb[n][1], a[1], bfhi(x2v[n].x));
    const float o2 = __builtin_fmaf(gb[n][2], a[2], bflo(x2v[n].y)), o3 = __builtin_fmaf(gb[n][3], a[3], bfhi(x2v[n].y));
    *(v2u*)((char*)out + ((size_t)t * D + 80 * j + 16 * (N0 + n) + 4 * q) * 2) = v2u{pkbf(o0, o1), pkbf(o2, o3)};
  }
}

__global__ __launch_bounds__(768, 3) void k_all(Args A) {
  extern __shared__ __attribute__((aligned(16))) char smem[];
  unsigned long long pc;
  asm volatile("s_getpc_b64 %0" : "=s"(pc));
  LAS char* lds = (LAS char*)smem;
  char* ws = A.ws;
  const int tid = __builtin_amdgcn_workitem_id_x(), lane = tid & 63, wave = __builtin_amdgcn_readfirstlane(tid >> 6), wg = __builtin_amdgcn_workgroup_id_x();
  STAMP_DECL; STAMP(0);
  const __amdgpu_buffer_rsrc_t rs = __builtin_amdgcn_make_buffer_rsrc((void*)ws, 0, (int)WS_END, 0x00020000);
  asm volatile("" :: "s"(A.x), "s"(A.Are), "s"(A.Aim), "s"(A.Bre), "s"(A.Bim), "s"(A.Cre), "s"(A.Cim), "s"(A.logdt), "s"(A.dskip), "s"(A.win), "s"(A.wsp),
               "s"(A.bsp), "s"(A.wout), "s"(A.cvec), "s"(A.wmod), "s"(A.ws), "s"(A.out));
  const int grp = wave >> 2, bt = grp == 2 ? 0 : grp + 1, gw = wave & 3, gt = tid & 255;
  const bool isaux = bt == 2 && wg >= 128;
  const int u = wg - 128, d = 256 * bt + (wg & 7) * (bt < 2 ? 32 : 16) + (wg >> 3);
  unsigned epoch;
  asm volatile("global_load_dword %0, %1, %2" : "=v"(epoch) : "v"((unsigned)CTL_EPOCH), "s"(ws) : "memory");
  if (__builtin_expect(isaux, 1)) {
    const int rr = lane >> 4;
    const unsigned vo = rr * (D * 2) + (((lane & 15) ^ ((rr | ((gw >> 1) << 2)) << 1)) << 4);
    const char* xb = (const char*)A.x + (size_t)(u * 16 + gw * 4) * (D * 2);
#pragma unroll
    for (int i = 0; i < 5; ++i) glds16nt(xb + i * 256, vo, (unsigned)(size_t)lds + C_GRP + 8192 + (gw * 5 + i) * 1024);
    const int zc = (u * 4 + gw) * 3 + lane - 32;
    st128(rs, lane < 32 ? (unsigned)CTL_ST3 + ((u * 4 + gw) * 32 + lane) * 16 : (lane < 35 && zc < 1280 ? (unsigned)zc * 256 : 0x7ffffff0u), v4u{0u, 0u, 0u, 0u});
  }
  unsigned pre[10] = {};
  if (__builtin_expect(!isaux, 1)) {
    const unsigned o = (d * NS + (gw & 1) * 64 + lane) * 2;
    LDU16(pre[0], A.Are, o); LDU16(pre[1], A.Aim, o); LDU16(pre[2], A.logdt, d * 2); LDU16(pre[3], A.Bre, o);
    LDU16(pre[4], A.Bim, o); LDU16(pre[5], A.Cre, o); LDU16(pre[6], A.Cim, o);
    const int so = ((wg >> 3) * 4 + gw) * 10 + lane;
    const char* wp = (const char*)(pc & ~255ull) + (size_t)(so < CODE_SECTORS ? so : 0) * 64;
    const size_t po = ((size_t)((lane - 10) & 7) << 21) + wg * 256;
    const char* wq = lane < 18 ? ws + po : (lane < 22 ? (const char*)A.wsp + po : (const char*)A.out + ((lane & 1) << 21) + wg * 256);
    wp = lane >= 10 ? wq : wp;
    wp = bt == 1 && (lane < 10 || (gw == 0 && lane < 24)) ? wp : (const char*)A.logdt;
    asm volatile("global_load_dword %0, %1, off" : "=v"(pre[7]) : "v"(wp) : "memory");
  }
  STAMP(11);
  if (tid < 16) ((LAS unsigned*)(lds + M_CTL))[tid] = 0u;
  unsigned tag = 0;
  CBAR();
  {
    LAS char* gl = lds + (grp == 2 ? G1_TAB - C_GRP : grp * C_GRP);
    if (bt == 0) __builtin_amdgcn_s_setprio(2);
    if (bt == 1) __builtin_amdgcn_s_setprio(1);
    if (isaux) { __builtin_amdgcn_s_setprio(3); aux_group(gl, lds, A, rs, epoch, u, gt, gw, lane STAMP_PASS); }
    else conv_group(gl, lds, bt, A, rs, epoch, tag, pre, wg, d, gt, gw, lane STAMP_PASS);
    __builtin_amdgcn_s_setprio(0);
  }
  const int xcd = wg & 7, idx = wg >> 3;
  const int mt = (xcd >> 1) * 8 + (idx & 7), j = (xcd & 1) * 4 + (idx >> 3);
  const int cnt = j < 6 ? 7 : 6, sub0 = j < 6 ? 7 * j : 42 + 6 * (j - 6);
  if (wave >= 8) {
    STAMP(4);
    const int lw = wave - 8;
    if (lw + 12 < 2 * cnt) loader_path<4>(lds, ws, rs, A.wsp, wg, mt, j, sub0, lw, lane);
    else loader_path<3>(lds, ws, rs, A.wsp, wg, mt, j, sub0, lw, lane);
    if (wg == 0 && tid == 512) *(volatile unsigned*)(ws + CTL_EPOCH) = tag;
    STAMP_FLUSH(1);
    return;
  }
  v2u x2v[3];
  const int ng = wave >> 2 ? 3 - (wave & 3) : (wave & 3);
  if (ng < 3) consumer_g12<2, false>(lds, ws, rs, A.bsp, A.wsp, wg, mt, j, sub0, cnt, wave, tid, lane, x2v STAMP_PASS);
  else if (cnt == 7) consumer_g12<1, true>(lds, ws, rs, A.bsp, A.wsp, wg, mt, j, sub0, cnt, wave, tid, lane, x2v STAMP_PASS);
  else consumer_g12<0, true>(lds, ws, rs, A.bsp, A.wsp, wg, mt, j, sub0, cnt, wave, tid, lane, x2v STAMP_PASS);
  if (wave < 4) consumer_g3<0>(lds, ws, rs, A.out, mt, j, wave & 3, lane, x2v STAMP_PASS);
  else consumer_g3<1>(lds, ws, rs, A.out, mt, j, wave & 3, lane, x2v STAMP_PASS);
  STAMP(14);
  STAMP_FLUSH(1);
}

#if defined(__HIP_DEVICE_COMPILE__) && !defined(KEEP_PK)
#pragma clang attribute pop
#endif

extern "C" void kernel_launch(void* const* d_in, const int* in_sizes, int n_in, void* d_out, int out_size, void* d_ws, size_t ws_size,
                              hipStream_t stream) {
  Args A;
  A.x = (const u16*)d_in[0]; A.Are = (const u16*)d_in[1]; A.Aim = (const u16*)d_in[2]; A.Bre = (const u16*)d_in[3]; A.Bim = (const u16*)d_in[4];
  A.Cre = (const u16*)d_in[5]; A.Cim = (const u16*)d_in[6]; A.logdt = (const u16*)d_in[7]; A.dskip = (const u16*)d_in[8];
  A.win = (const u16*)d_in[10]; A.wsp = (const u16*)d_in[11]; A.bsp = (const u16*)d_in[12]; A.wout = (const u16*)d_in[13];
  A.cvec = (const u16*)d_in[14]; A.wmod = (const u16*)d_in[15];
  A.ws = (char*)d_ws; A.out = (u16*)d_out;
  (void)hipFuncSetAttribute((const void*)k_all, hipFuncAttributeMaxDynamicSharedMemorySize, M_LDS);
  k_all<<<256, 768, M_LDS, stream>>>(A);
}
```

```cpp
#include <hip/hip_runtime.h>
#include <hip/hip_bf16.h>
#include <stdint.h>

#define NT ""
#define NTLOAD(p) (*(p))
#define GAS __attribute__((address_space(1)))
#define LAS __attribute__((address_space(3)))
typedef unsigned v4u __attribute__((ext_vector_type(4)));
typedef unsigned v2u __attribute__((ext_vector_type(2)));
typedef float f32x2 __attribute__((ext_vector_type(2)));
typedef float f32x4 __attribute__((ext_vector_type(4)));
typedef float f32x16 __attribute__((ext_vector_type(16)));
typedef __bf16 bf16x2 __attribute__((ext_vector_type(2)));
typedef __bf16 bf16x8 __attribute__((ext_vector_type(8)));
typedef _Float16 f16x2 __attribute__((ext_vector_type(2)));
typedef _Float16 f16x8 __attribute__((ext_vector_type(8)));
typedef short s16x4 __attribute__((ext_vector_type(4)));
typedef unsigned short u16;

#if defined(__HIP_DEVICE_COMPILE__) && !defined(KEEP_PK)
#pragma clang attribute push(__attribute__((target("no-packed-fp32-ops"))), apply_to = function)
#endif
__device__ __forceinline__ float u2f(unsigned u) { return __builtin_bit_cast(float, u); }
__device__ __forceinline__ unsigned f2u(float f) { return __builtin_bit_cast(unsigned, f); }
__device__ __forceinline__ int lane_id() { return __builtin_amdgcn_mbcnt_hi(~0u, __builtin_amdgcn_mbcnt_lo(~0u, 0u)); }
__device__ __forceinline__ unsigned shx(unsigned v, int m) { return __builtin_amdgcn_ds_bpermute((lane_id() ^ m) << 2, v); }
__device__ __forceinline__ float shx(float v, int m) { return u2f(shx(f2u(v), m)); }
__device__ __forceinline__ bool wall(bool p) { return __builtin_amdgcn_ballot_w64(!p) == 0ull; }
__device__ __forceinline__ float exp_acc(float x) {
  const float hi = x * 1.4426950408889634f;
  const float lo = __builtin_fmaf(x, 1.4426950408889634f, -hi) + x * 1.925963033500414e-8f;
  const float e = __builtin_amdgcn_exp2f(hi);
  return __builtin_fmaf(e, lo * 0.6931471805599453f, e);
}

constexpr int L = 2048, D = 640, NS = 128, F = 1728, FH = 864;

constexpr size_t CTL_E1 = 0;
constexpr size_t CTL_E1B = 196608;
constexpr size_t CTL_E2 = 262144;
constexpr size_t CTL_E3 = 294912;
constexpr size_t CTL_EW = 303104;
constexpr size_t CTL_ST3 = 327680;
constexpr size_t CTL_F0 = CTL_ST3 + 262144;
constexpr size_t CTL_EPOCH = CTL_F0 + 32768;
constexpr size_t WS_MOD = CTL_EPOCH + 4096;
constexpr size_t WS_ST1 = WS_MOD + 131072;
constexpr size_t WS_U = WS_ST1 + 16384;
constexpr size_t WS_XT = WS_U + 65536;
constexpr size_t WS_X2T = WS_XT + (size_t)D * L * 2;
constexpr size_t WS_WIN = WS_X2T + (size_t)D * L * 2;
constexpr size_t WS_WOUT = WS_WIN + (size_t)D * F * 2;
constexpr size_t WS_Z2 = WS_WOUT + (size_t)FH * D * 2;
constexpr size_t WS_V = WS_Z2 + (size_t)L * FH * 2;
constexpr size_t WS_END = WS_V + (size_t)L * FH * 2;

__device__ __forceinline__ float bflo(unsigned u) { return u2f(u << 16); }
__device__ __forceinline__ float bfhi(unsigned u) { return u2f(u & 0xffff0000u); }
__device__ __forceinline__ float bf2f(u16 v) { return u2f((unsigned)v << 16); }
__device__ __forceinline__ unsigned pkbf(float a, float b) {
  f32x2 v = {a, b};
  return __builtin_bit_cast(unsigned, __builtin_convertvector(v, bf16x2));
}
__device__ __forceinline__ unsigned pkh(float a, float b) {
  f32x2 v = {a, b};
  return __builtin_bit_cast(unsigned, __builtin_convertvector(v, f16x2));
}
__device__ __forceinline__ float bfr(float a) { return bflo(pkbf(a, a)); }
__device__ __forceinline__ v2u trread(LAS const char* p) {
  return __builtin_bit_cast(v2u, __builtin_amdgcn_ds_read_tr16_b64_v4i16((LAS s16x4*)p));
}
__device__ __forceinline__ f32x4 mfma16(v4u a, v4u b, f32x4 c) {
  return __builtin_amdgcn_mfma_f32_16x16x32_bf16(__builtin_bit_cast(bf16x8, a), __builtin_bit_cast(bf16x8, b), c, 0, 0, 0);
}
__device__ __forceinline__ f32x16 mfma32(v4u a, v4u b, f32x16 c) {
  return __builtin_amdgcn_mfma_f32_32x32x16_bf16(__builtin_bit_cast(bf16x8, a), __builtin_bit_cast(bf16x8, b), c, 0, 0, 0);
}
__device__ __forceinline__ f32x16 mfma32h(v4u a, v4u b, f32x16 c) {
  return __builtin_amdgcn_mfma_f32_32x32x16_f16(__builtin_bit_cast(f16x8, a), __builtin_bit_cast(f16x8, b), c, 0, 0, 0);
}
template <bool DEV>
__device__ __forceinline__ void glds16(const void* sbase, unsigned voff, unsigned dst) {
  unsigned keep;
  if (DEV) asm volatile(
"s_mov_b32 %0, m0\n\ts_mov_b32 m0, %3\n\ts_nop 0\n\tglobal_load_lds_dwordx4 %1, %2 sc1\n\ts_mov_b32 m0, %0"
                        : "=&s"(keep) : "v"(voff), "s"(sbase), "s"(dst) : "memory");
  else asm volatile("s_mov_b32 %0, m0\n\ts_mov_b32 m0, %3\n\ts_nop 0\n\tglobal_load_lds_dwordx4 %1, %2\n\ts_mov_b32 m0, %0"
                    : "=&s"(keep) : "v"(voff), "s"(sbase), "s"(dst) : "memory");
}
__device__ __forceinline__ void glds16nt(const void* sbase, unsigned voff, unsigned dst) {
  unsigned keep;
  asm volatile("s_mov_b32 %0, m0\n\ts_mov_b32 m0, %3\n\ts_nop 0\n\tglobal_load_lds_dwordx4 %1, %2" NT "\n\ts_mov_b32 m0, %0"
               : "=&s"(keep) : "v"(voff), "s"(sbase), "s"(dst) : "memory");
}
#define GL_(i, s) "global_load_lds_dwordx4 %" #i ", %5 offset:" s
template <int N, bool DEV>
__device__ __forceinline__ void gldsN(const void* sbase, unsigned v0, unsigned v1, unsigned v2, unsigned v3, unsigned dst) {
  unsigned keep;
#define GLN_(body) asm volatile("s_mov_b32 %0, m0\n\ts_mov_b32 m0, %6\n\ts_nop 0\n\t" body "s_mov_b32 m0, %0" : "=&s"(keep) : "v"(v0), "v"(v1), "v"(v2), "v"(v3), "s"(sbase), "s"(dst) : "memory")
  if (DEV) {
    if (N == 1) GLN_(GL_(1, "0 sc1\n\t"));
    if (N == 2) GLN_(GL_(1, "0 sc1\n\t") GL_(2, "1024 sc1\n\t"));
    if (N == 3) GLN_(GL_(1, "0 sc1\n\t") GL_(2, "1024 sc1\n\t") GL_(3, "2048 sc1\n\t"));
    if (N == 4) GLN_(GL_(1, "0 sc1\n\t") GL_(2, "1024 sc1\n\t") GL_(3, "2048 sc1\n\t") GL_(4, "3072 sc1\n\t"));
  } else {
    if (N == 2) GLN_(GL_(1, "0\n\t") GL_(2, "1024\n\t"));
    if (N == 4) GLN_(GL_(1, "0\n\t") GL_(2, "1024\n\t") GL_(3, "2048\n\t") GL_(4, "3072\n\t"));
  }
}
template <int N> __device__ __forceinline__ void gldsL(const void* sbase, unsigned vo, unsigned dst) { gldsN<N, true>(sbase, vo, vo, vo, vo, dst); }
template <int N> __device__ __forceinline__ void waitv() { asm volatile("s_waitcnt vmcnt(%0)" :: "i"(N) : "memory"); }
__device__ __forceinline__ float dot2bf(unsigned a, unsigned b, float c) {
  return __builtin_amdgcn_fdot2_f32_bf16(__builtin_bit_cast(bf16x2, a), __builtin_bit_cast(bf16x2, b), c, false);
}
#define SCHED_BAR() __builtin_amdgcn_sched_barrier(0)
#define STAMP_DECL
#define STAMP(i) do {} while (0)
#define STAMP_FLUSH(kid) do {} while (0)
#define STAMP_ARG
#define STAMP_PASS
#define CBAR() do { SCHED_BAR(); __builtin_amdgcn_s_waitcnt(0xC07F); asm volatile("" ::: "memory"); __builtin_amdgcn_s_barrier(); asm volatile("" ::: "memory"); } while (0)
#define LBAR() __builtin_amdgcn_s_barrier()

template <int CTRL> __device__ __forceinline__ float dppf(float v) {
  return __builtin_bit_cast(float, __builtin_amdgcn_update_dpp(0, __builtin_bit_cast(int, v), CTRL, 0xf, 0xf, true));
}
template <int N, int CTRL> __device__ __forceinline__ void treduce_dpp(float* v, bool up) {
#pragma unroll
  for (int e = 0; e < N; ++e) {
    const float a = v[e], b = v[e + N];
    const float send = up ? a : b, keep = up ? b : a;
    v[e] = keep + dppf<CTRL>(send);
  }
}
template <int N> __device__ __forceinline__ void treduce_xor(float* v, bool up, int mask) {
#pragma unroll
  for (int e = 0; e < N; ++e) {
    const float a = v[e], b = v[e + N];
    const float send = up ? a : b, keep = up ? b : a;
    v[e] = keep + shx(send, mask);
  }
}

__device__ __forceinline__ float ln_std(float var) { return __builtin_amdgcn_sqrtf(bfr(bfr(var) + 1e-5f)); }
__device__ __forceinline__ void ln_scalars(float mean, float var, float& m_b, float& rinv) {
  m_b = bfr(mean);
  rinv = __builtin_amdgcn_rcpf(bfr(ln_std(var)));
}

struct cplx { float r, i; };
__device__ __forceinline__ cplx cmul(cplx a, cplx b) { return {a.r * b.r - a.i * b.i, a.r * b.i + a.i * b.r}; }

typedef GAS unsigned gu32;
#define RLX_AGENT __ATOMIC_RELAXED, __HIP_MEMORY_SCOPE_AGENT
#define F0_REP 8
#define V_DELAY 24
#define V_WIN 5
#define LSTAMP(i) do {} while (0)
#define POLL_SLEEP 6
#define E1_REP 32
#define INTERLEAVE(n, r) do { _Pragma("unroll") for (int i_ = 0; i_ < (n); ++i_) { __builtin_amdgcn_sched_group_barrier(0x008, 1, 0); __builtin_amdgcn_sched_group_barrier(0x100, (r), 0); } \
    __builtin_amdgcn_sched_group_barrier(0x008, 8, 0); SCHED_BAR(); } while (0)
#define ABL_KK kk
#define AUXB_PRIO 2
constexpr unsigned SPIN_CAP = 1u << 16;
constexpr int M_LDS = 163840, M_CTL = M_LDS - 64;
constexpr int C_KOFF = 0, C_UOFF = 4224, C_UP = 1536, C_COFF = C_UOFF + 4 * C_UP, C_GRP = C_COFF + 4 * 64 * 144;
constexpr int G1_BOFF = 81920, G1_BST = 14336, G1_NSLOT = 5, G1_TAB = G1_BOFF + G1_BST * G1_NSLOT;
constexpr int G1_T_LN2 = G1_TAB, G1_T_U = G1_T_LN2 + 512, G1_T_P = G1_T_U + 2048, G1_T_Z = G1_T_P + 2048;
constexpr int G2_NW = 9, G2_NZ = 6, G2_ZOFF = G2_NW * 8192, G2_ZST = 14336;
constexpr int G3_RED = 135 * 1024, G3_GB = 161024;
static_assert(3 * C_GRP <= M_CTL && G1_T_Z + 512 <= M_CTL && G2_ZOFF + G2_NZ * G2_ZST <= M_CTL && G3_GB + 320 <= M_CTL, "lds");

__device__ __forceinline__ void st128(__amdgpu_buffer_rsrc_t rs, unsigned off, v4u v) { __builtin_amdgcn_raw_buffer_store_b128(v, rs, off, 0, 16); }
__device__ __forceinline__ v4u ld128(__amdgpu_buffer_rsrc_t rs, unsigned off) { return __builtin_amdgcn_raw_buffer_load_b128(rs, off, 0, 16); }

__device__ __forceinline__ bool wave_arrive(LAS char* lds, int word, unsigned n, int lane) {
  waitv<0>();
  unsigned old = 0;
  if (lane == 0) old = __hip_atomic_fetch_add((LAS unsigned*)(lds + M_CTL) + word, 1u, __ATOMIC_RELAXED, __HIP_MEMORY_SCOPE_WORKGROUP);
  return (unsigned)__builtin_amdgcn_readfirstlane(old) == n - 1;
}
__device__ __forceinline__ void lds_wait(LAS char* lds0, int word, unsigned target) {
  LAS unsigned* p = (LAS unsigned*)(lds0 + M_CTL) + word;
  while ((unsigned)__builtin_amdgcn_readfirstlane(__hip_atomic_load(p, __ATOMIC_RELAXED, __HIP_MEMORY_SCOPE_WORKGROUP)) < target) __builtin_amdgcn_s_sleep(0);
  asm volatile("" ::: "memory");
}
__device__ __forceinline__ void lds_post(LAS char* lds0, int word, int lane) {
  SCHED_BAR();
  asm volatile("" ::: "memory");
  if (lane == 0) __hip_atomic_fetch_add((LAS unsigned*)(lds0 + M_CTL) + word, 1u, __ATOMIC_RELAXED, __HIP_MEMORY_SCOPE_WORKGROUP);
  asm volatile("" ::: "memory");
}
__device__ __forceinline__ void group_sync(LAS char* lds0, int word, unsigned target, int lane) { lds_post(lds0, word, lane); lds_wait(lds0, word, target); }
template <int NREP, int NSH> __device__ __forceinline__ void signal(char* base, int shard, int lane) {
  if (lane < NREP) __hip_atomic_fetch_add((gu32*)(base + (lane * NSH + shard) * 256), 1u, RLX_AGENT);
}
template <int NSH> __device__ __forceinline__ bool count_ok(unsigned c, unsigned total) {
  if (NSH > 1) c += __builtin_amdgcn_update_dpp(0, c, 0xb1, 0xf, 0xf, true);
  if (NSH > 2) c += __builtin_amdgcn_update_dpp(0, c, 0x4e, 0xf, 0xf, true);
  if (NSH > 4) c += __builtin_amdgcn_update_dpp(0, c, 0x141, 0xf, 0xf, true);
  return (unsigned)__builtin_amdgcn_readfirstlane(c) >= total;
}
template <int NSH> __device__ __forceinline__ void wait_count(char* base, int rep, unsigned total, int lane) {
  gu32* p = (gu32*)(base + (rep * NSH + (lane & (NSH - 1))) * 256);
#pragma unroll 1
  for (unsigned spin = 0; spin < SPIN_CAP; ++spin) {
    if (count_ok<NSH>(__hip_atomic_load(p, RLX_AGENT), total)) break;
    __builtin_amdgcn_s_sleep(POLL_SLEEP);
  }
  __builtin_amdgcn_fence(__ATOMIC_ACQUIRE, "wavefront");
}
template <int NSH> __device__ __forceinline__ void poll_issue(unsigned& c, char* base, int rep, int lane) {
  c = __hip_atomic_load((gu32*)(base + (rep * NSH + (lane & (NSH - 1))) * 256), RLX_AGENT);
}
template <int NSH> __device__ __forceinline__ void poll_finish(unsigned c, char* base, int rep, unsigned total, int lane) {
  if (!count_ok<NSH>(c, total)) wait_count<NSH>(base, rep, total, lane);
  __builtin_amdgcn_fence(__ATOMIC_ACQUIRE, "wavefront");
}

__device__ __forceinline__ float gelu_f(float x) {
  const float p = __builtin_fmaf(x * x, -0.044715f * 2.302208198f, -2.302208198f);
  const float e = __builtin_amdgcn_exp2f(x * p);
  return x * __builtin_amdgcn_rcpf(1.0f + e);
}

__device__ __forceinline__ float ld32f(__amdgpu_buffer_rsrc_t rs, unsigned off) { return u2f(__builtin_amdgcn_raw_buffer_load_b32(rs, off, 0, 16)); }
__device__ __forceinline__ void st32(__amdgpu_buffer_rsrc_t rs, unsigned off, unsigned v) { __builtin_amdgcn_raw_buffer_store_b32(v, rs, off, 0, 16); }

struct Args {
  const u16 *x, *Are, *Aim, *Bre, *Bim, *Cre, *Cim, *logdt, *dskip, *win, *wsp, *bsp, *wout, *cvec, *wmod;
  char* ws; u16* out;
};

typedef GAS unsigned long long gu64;
__device__ __forceinline__ void put_granule(char* p, unsigned v, unsigned tag) { __hip_atomic_store((gu64*)p, ((unsigned long long)tag << 32) | v, RLX_AGENT); }
template <int N, class F> __device__ __forceinline__ void get_granules(unsigned (&v)[N], unsigned tag, F addr) {
#pragma unroll 1
  for (unsigned spin = 0; spin < SPIN_CAP; ++spin) {
    unsigned long long x[N];
#pragma unroll
    for (int k = 0; k < N; ++k) x[k] = __hip_atomic_load((gu64*)addr(k), RLX_AGENT);
    bool ok = true;
#pragma unroll
    for (int k = 0; k < N; ++k) { v[k] = (unsigned)x[k]; ok &= (unsigned)(x[k] >> 32) == tag; }
    if (wall(ok)) break;
    __builtin_amdgcn_s_sleep(3);
  }
}

__device__ __forceinline__ void conv_group(LAS char* lds, LAS char* lds0, int bt, const Args& A, __amdgpu_buffer_rsrc_t rs, unsigned epoch, unsigned& tag, unsigned (&pre)[7],
                                           int wg, int d, int tid, int wave, int lane STAMP_ARG) {
  char* ws = A.ws;
  float S[8], soff;
  v4u xv, st[2];
  {
    const int half = wave & 1;
    int xoff[8];
#pragma unroll
    for (int j = 0; j < 8; ++j) {
      const int l = tid * 8 + j;
      const int idx = l < 128 ? l : (int)(pkbf((float)l, 0.f) & 0xffffu) - 0x4300 + 128;
      xoff[j] = (idx + (idx >> 3)) * 8;
    }
    asm volatile("" : "+v"(xoff[0]), "+v"(xoff[1]), "+v"(xoff[2]), "+v"(xoff[3]), "+v"(xoff[4]), "+v"(xoff[5]), "+v"(xoff[6]), "+v"(xoff[7]));
    waitv<0>();
    asm volatile("v_readfirstlane_b32 %0, %1\n\ts_nop 3" : "=s"(tag) : "v"(epoch) : "memory");
    tag += 1u;
    asm volatile("" : "+v"(pre[0]), "+v"(pre[1]), "+v"(pre[2]), "+v"(pre[3]), "+v"(pre[4]), "+v"(pre[5]), "+v"(pre[6]));
    const unsigned vare = pre[0], vaim = pre[1], vdt = pre[2], vbre = pre[3], vbim = pre[4], vcre = pre[5], vcim = pre[6];
    if (tid < 4) *(LAS v4u*)(lds + C_KOFF + tid * 16) = v4u{0u, 0u, 0u, 0u};
    const float dt = bfr(exp_acc(bflo(vdt)));
    const float ar2 = dt * bflo(vare) * 1.4426950408889634f, ai2 = dt * bflo(vaim) * 0.15915494309189535f;
    auto E = [&](float xx) {
      const float mag = __builtin_amdgcn_exp2f(ar2 * xx), ph = __builtin_amdgcn_fractf(ai2 * xx);
      return cplx{mag * __builtin_amdgcn_cosf(ph), mag * __builtin_amdgcn_sinf(ph)};
    };
    LAS char* img = lds + C_COFF + wave * 8192 + lane * 128;
    const int sw = lane & 7;
    float isc = 0.f;
    if (wave < 2) {
      const cplx e8 = E(8.f), e64 = E(64.f), e128 = E(128.f), e192 = E(192.f);
      cplx q[8];
      q[0] = {1.f, 0.f}; q[1] = e8; q[2] = cmul(e8, e8); q[3] = cmul(q[2], e8); q[4] = cmul(q[2], q[2]);
      q[5] = cmul(q[4], e8); q[6] = cmul(q[4], q[2]); q[7] = cmul(q[4], q[3]);
#pragma unroll
      for (int g = 0; g < 4; ++g) {
        cplx v[8];
#pragma unroll
        for (int j = 0; j < 8; ++j) v[j] = g == 0 ? q[j] : cmul(q[j], g == 1 ? e64 : (g == 2 ? e128 : e192));
        *(LAS v4u*)(img + ((g ^ sw) << 4)) = v4u{pkh(v[0].r, v[1].r), pkh(v[2].r, v[3].r), pkh(v[4].r, v[5].r), pkh(v[6].r, v[7].r)};
        *(LAS v4u*)(img + (((4 + g) ^ sw) << 4)) = v4u{pkh(v[0].i, v[1].i), pkh(v[2].i, v[3].i), pkh(v[4].i, v[5].i), pkh(v[6].i, v[7].i)};
      }
    } else {
      cplx W = cmul(cplx{bflo(vcre), bflo(vcim)}, cplx{dt * bflo(vbre), dt * bflo(vbim)});
      float mx = __builtin_fmaxf(__builtin_fabsf(W.r), __builtin_fabsf(W.i));
      mx = __builtin_fmaxf(mx, dppf<0xb1>(mx)); mx = __builtin_fmaxf(mx, dppf<0x4e>(mx)); mx = __builtin_fmaxf(mx, dppf<0x141>(mx)); mx = __builtin_fmaxf(mx, dppf<0x140>(mx));
      const unsigned mb = f2u(mx);
      const unsigned m01 = __builtin_elementwise_max((unsigned)__builtin_amdgcn_readlane(mb, 0), (unsigned)__builtin_amdgcn_readlane(mb, 16));
      const unsigned m23 = __builtin_elementwise_max((unsigned)__builtin_amdgcn_readlane(mb, 32), (unsigned)__builtin_amdgcn_readlane(mb, 48));
      int ex = (int)((__builtin_elementwise_max(m01, m23) >> 23) & 0xff);
      ex = ex < 20 ? 20 : (ex > 240 ? 240 : ex);
      const float sc = u2f((unsigned)(267 - ex) << 23);
      isc = u2f((unsigned)(ex - 13) << 23);
      W.r *= sc; W.i *= sc;
      const cplx e1 = E(1.f), e2 = cmul(e1, e1), e4 = cmul(e2, e2);
      cplx h[24];
      h[0] = W; h[1] = cmul(W, e1); h[2] = cmul(W, e2); h[3] = cmul(h[2], e1);
      h[4] = cmul(W, e4); h[5] = cmul(h[4], e1); h[6] = cmul(h[4], e2); h[7] = cmul(h[6], e1);
      const cplx g1 = E(256.f), g2 = E(512.f), g3 = E(768.f), g4 = E(1024.f);
      h[8] = cmul(h[0], g1); h[9] = cmul(h[2], g1); h[10] = cmul(h[4], g1); h[11] = cmul(h[6], g1);
      h[12] = cmul(h[0], g2); h[13] = cmul(h[4], g2); h[14] = cmul(h[0], g3); h[15] = cmul(h[4], g3);
      h[16] = cmul(h[0], g4); h[17] = cmul(h[8], g4); h[18] = cmul(h[12], g4); h[19] = cmul(h[14], g4);
      h[20] = cmul(h[16], g4); h[21] = {0.f, 0.f}; h[22] = {0.f, 0.f}; h[23] = {0.f, 0.f};
#pragma unroll
      for (int g = 0; g < 3; ++g) {
        const cplx* v = h + 8 * g;
        *(LAS v4u*)(img + ((g ^ sw) << 4)) = v4u{pkh(v[0].r, v[1].r), pkh(v[2].r, v[3].r), pkh(v[4].r, v[5].r), pkh(v[6].r, v[7].r)};
        *(LAS v4u*)(img + (((4 + g) ^ sw) << 4)) = v4u{pkh(-v[0].i, -v[1].i), pkh(-v[2].i, -v[3].i), pkh(-v[4].i, -v[5].i), pkh(-v[6].i, -v[7].i)};
      }
      *(LAS v4u*)(img + ((3 ^ sw) << 4)) = v4u{0u, 0u, 0u, 0u};
      *(LAS v4u*)(img + ((7 ^ sw) << 4)) = v4u{0u, 0u, 0u, 0u};
    }
    STAMP(1);
    group_sync(lds0, 5 + bt, 4, lane);
    if (wave >= 2) {
      const int hh = lane >> 5, blk = (lane >> 4) & 1, qq = (lane & 15) >> 2, p = lane & 3;
      LAS const char* Qi = lds + C_COFF + half * 8192;
      v2u a[8][2], bb[8][2];
#pragma unroll
      for (int ks = 0; ks < 8; ++ks)
#pragma unroll
        for (int t = 0; t < 2; ++t) {
          const int k = 16 * ks + 8 * hh + 4 * t + qq, mode = k >> 1, c = k & 1;
          const int off = mode * 128 + (((c * 4 + 2 * blk + (p >> 1)) ^ (mode & 7)) << 4) + (p & 1) * 8;
          a[ks][t] = trread(Qi + 16384 + off);
          bb[ks][t] = trread(Qi + off);
        }
      SCHED_BAR();
      f32x16 acc = {};
#pragma unroll
      for (int ks = 0; ks < 8; ++ks) acc = mfma32h(v4u{a[ks][0].x, a[ks][0].y, a[ks][1].x, a[ks][1].y}, v4u{bb[ks][0].x, bb[ks][0].y, bb[ks][1].x, bb[ks][1].y}, acc);
      LAS float* T = (LAS float*)(lds + C_UOFF) + half;
      const int q = lane & 31, hi = lane >> 5;
      auto put = [&](int idx, float v) { T[(idx + (idx >> 3)) * 2] = v * isc; };
#pragma unroll
      for (int r = 0; r < 4; ++r) put((r + 4 * hi) + 8 * q, acc[r]);
#pragma unroll
      for (int r = 0; r < 4; ++r) put(hi ? 384 + 64 * (r >> 1) + (r & 1) + 2 * q : 256 + r + 4 * q, acc[4 + r]);
      if (hi == 0) {
#pragma unroll
        for (int r = 0; r < 4; ++r) put(512 + 32 * r + q, acc[8 + r]);
      } else if (q == 0) put(640, acc[8]);
    }
    lds_post(lds0, 5 + bt, lane);
    auto load_x = [&]() {
      xv = ld128(rs, (unsigned)WS_XT + (tid * D + d) * 16);
#pragma unroll
      for (int i = 0; i < 2; ++i) st[i] = ld128(rs, (unsigned)WS_ST1 + (i * 256 + tid) * 16);
      SCHED_BAR();
    };
    const bool early = __builtin_amdgcn_readfirstlane(__hip_atomic_load((LAS unsigned*)(lds0 + M_CTL) + 8, __ATOMIC_RELAXED, __HIP_MEMORY_SCOPE_WORKGROUP)) != 0;
    asm volatile("" ::: "memory");
    if (early) load_x();
    lds_wait(lds0, 5 + bt, 8);
    {
      f32x2 tv[8];
#pragma unroll
      for (int j = 0; j < 8; ++j) tv[j] = *(LAS const f32x2*)(lds + C_UOFF + xoff[j]);
      SCHED_BAR();
      float kv[8];
#pragma unroll
      for (int j = 0; j < 8; ++j) kv[j] = tv[j][0] + tv[j][1];
      *(LAS v4u*)(lds + C_KOFF + 64 + tid * 16) = v4u{pkbf(kv[0], kv[1]), pkbf(kv[2], kv[3]), pkbf(kv[4], kv[5]), pkbf(kv[6], kv[7])};
      S[0] = kv[0];
#pragma unroll
      for (int j = 1; j < 8; ++j) S[j] = S[j - 1] + kv[j];
      float inc = S[7];
      inc += dppf<0x111>(inc); inc += dppf<0x112>(inc); inc += dppf<0x114>(inc); inc += dppf<0x118>(inc);
      inc += __builtin_bit_cast(float, __builtin_amdgcn_update_dpp(0, __builtin_bit_cast(int, inc), 0x142, 0xa, 0xf, false));
      inc += __builtin_bit_cast(float, __builtin_amdgcn_update_dpp(0, __builtin_bit_cast(int, inc), 0x143, 0xc, 0xf, false));
      soff = inc - S[7];
      if (lane == 63) ((LAS float*)(lds + C_KOFF + 4160))[wave] = inc;
    }
    lds_post(lds0, 5 + bt, lane);
    if (!early) {
      if (bt == 0 && wave == 0) {
#pragma unroll 1
        for (unsigned spin = 0; spin < SPIN_CAP; ++spin) {
          asm volatile("" ::: "memory");
          const v4u f = ld128(rs, (unsigned)CTL_F0 + (wg & (F0_REP - 1)) * 1024 + lane * 16);
          if (wall(f.x == tag && f.y == ~tag && f.z == tag && f.w == ~tag)) break;
          __builtin_amdgcn_s_sleep(2);
        }
        __builtin_amdgcn_fence(__ATOMIC_ACQUIRE, "wavefront");
        lds_post(lds0, 8, lane);
      } else lds_wait(lds0, 8, 1);
      load_x();
    }
    STAMP(2);
  }
  lds_wait(lds0, 5 + bt, 12);
  STAMP(3);
  float xf[8], xn[8];
  {
#pragma unroll
    for (int j = 0; j < 4; ++j) { xf[2 * j] = bflo(xv[j]); xf[2 * j + 1] = bfhi(xv[j]); }
    const f32x4 wt = *(LAS const f32x4*)(lds + C_KOFF + 4160);
    if (tid < 128) *(LAS v4u*)(lds + C_UOFF + (tid >> 5) * C_UP + (tid & 31) * 16) = v4u{0u, 0u, 0u, 0u};
#pragma unroll
    for (int j = 0; j < 8; ++j) {
      const unsigned sj = st[j >> 2][j & 3];
      xn[j] = (xf[j] - bflo(sj)) * __builtin_amdgcn_rcpf(bfhi(sj));
    }
#pragma unroll
    for (int e = 0; e < 4; ++e) *(LAS unsigned*)(lds + C_UOFF + e * C_UP + 512 + tid * 4) = pkbf(xn[e], xn[e + 4]);
    soff += (wave > 0 ? wt[0] : 0.f) + (wave > 1 ? wt[1] : 0.f) + (wave > 2 ? wt[2] : 0.f);
  }
  const u16 dskv = A.dskip[d];
  char* mgp = ws + WS_MOD + (size_t)((lane & 3) * 3840 + (lane < 12 ? lane >> 2 : 0) * D + d) * 8;
  const unsigned long long mg = __hip_atomic_load((gu64*)mgp, RLX_AGENT);
  STAMP(5);
  group_sync(lds0, 5 + bt, 16, lane);
  STAMP(6);
  f32x16 y0 = {}, y1 = {};
  {
    const int hh = lane >> 5, blk = (lane >> 4) & 1, qq = (lane & 15) >> 2, p = lane & 3, col = lane & 31;
    LAS const char* ab = lds + C_KOFF + (32 + 16 * blk + 4 * p - 4 * qq + 32 * hh) * 2 - 32;
    LAS const char* bb = lds + C_UOFF + wave * C_UP + (256 + 8 * (col - hh) - 240) * 2;
    struct CF { v2u a0l, a0h, a1l, a1h; v4u b0, b1; };
    CF cf[3];
    auto rdc = [&](int ks, CF& f) {
      f.a0l = trread(ab + ks * 128 + 32); f.a0h = trread(ab + ks * 128);
      f.b0 = *(LAS const v4u*)(bb + (15 - ks) * 32);
      f.b1 = *(LAS const v4u*)(bb + (31 - ks) * 32);
      f.a1l = trread(ab + (ks + 16) * 128 + 32); f.a1h = trread(ab + (ks + 16) * 128);
    };
    rdc(0, cf[0]); rdc(1, cf[1]);
#pragma unroll
    for (int ks = 0; ks < 16; ++ks) {
      if (ks + 2 < 16) rdc(ks + 2, cf[(ks + 2) % 3]);
      SCHED_BAR();
      const CF& f = cf[ks % 3];
      const v4u a0 = {f.a0l.x, f.a0l.y, f.a0h.x, f.a0h.y}, a1 = {f.a1l.x, f.a1l.y, f.a1h.x, f.a1h.y};
      y0 = mfma32(a0, f.b0, y0);
      y1 = mfma32(a0, f.b1, y1);
      y1 = mfma32(a1, f.b0, y1);
      SCHED_BAR();
    }
    asm volatile("" :: "v"(y0), "v"(y1));
    STAMP(7);
    LAS char* cb = lds + C_COFF + (wave * 64 + col) * 144 + hh * 16;
#pragma unroll
    for (int g = 0; g < 4; ++g) {
      *(LAS f32x4*)(cb + g * 32) = f32x4{y0[4 * g], y0[4 * g + 1], y0[4 * g + 2], y0[4 * g + 3]};
      *(LAS f32x4*)(cb + 32 * 144 + g * 32) = f32x4{y1[4 * g], y1[4 * g + 1], y1[4 * g + 2], y1[4 * g + 3]};
    }
  }
  lds_post(lds0, 5 + bt, lane);
  unsigned mv[1] = {(unsigned)mg};
  if (!wall((unsigned)(mg >> 32) == tag)) get_granules<1>(mv, tag, [&](int) { return mgp; });
  float msum = u2f(mv[0]);
  msum += dppf<0xb1>(msum); msum += dppf<0x4e>(msum);
  const unsigned mb = f2u(bfr(msum));
  const float sa = u2f(__builtin_amdgcn_readlane(mb, 0)), c1a = bfr(1.0f + u2f(__builtin_amdgcn_readlane(mb, 4))), ga = u2f(__builtin_amdgcn_readlane(mb, 8)), dsk = bf2f(dskv);
  const float gc = ga * c1a, gs_ = ga * sa, gd = gc * dsk, g0 = gs_ * (soff + dsk);
#pragma unroll
  for (int j = 0; j < 8; ++j) xf[j] = __builtin_fmaf(gd, xn[j], __builtin_fmaf(gs_, S[j], xf[j] + g0));
  STAMP(8);
  lds_wait(lds0, 5 + bt, 20);
  STAMP(9);
  float y[8];
  {
    const int c1 = 2 * tid, c0 = tid ? c1 - 1 : 0;
    const int o0 = (c0 >> 3) * 144 + (c0 & 7) * 16, o1 = (c1 >> 3) * 144 + (c1 & 7) * 16;
    LAS const char* cb = lds + C_COFF;
    f32x4 w0[4], w1[4], w2[4];
#pragma unroll
    for (int e = 0; e < 4; ++e) {
      w1[e] = *(LAS const f32x4*)(cb + e * 64 * 144 + o1); w2[e] = *(LAS const f32x4*)(cb + e * 64 * 144 + o1 + 16);
      if (e) w0[e] = *(LAS const f32x4*)(cb + e * 64 * 144 + o0);
    }
    SCHED_BAR();
#pragma unroll
    for (int e = 0; e < 4; ++e) {
      float w[12];
#pragma unroll
      for (int j = 0; j < 4; ++j) { w[4 + j] = w1[e][j]; w[8 + j] = w2[e][j]; w[j] = (e && tid) ? w0[e][j] : 0.f; }
#pragma unroll
      for (int j = 0; j < 8; ++j) y[j] = e ? y[j] + w[4 + j - e] : w[4 + j];
    }
  }
  float o[8];
#pragma unroll
  for (int j = 0; j < 8; ++j) o[j] = __builtin_fmaf(gc, y[j], xf[j]);
  {
    const unsigned a0 = pkbf(o[0], o[1]), a1 = pkbf(o[2], o[3]), b0 = pkbf(o[4], o[5]), b1 = pkbf(o[6], o[7]);
    const bool odd = tid & 1;
    const unsigned s0 = odd ? a0 : b0, s1 = odd ? a1 : b1;
    const unsigned r0 = __builtin_amdgcn_update_dpp(0, s0, 0xb1, 0xf, 0xf, true), r1 = __builtin_amdgcn_update_dpp(0, s1, 0xb1, 0xf, 0xf, true);
    const v4u ov = odd ? v4u{r0, r1, b0, b1} : v4u{a0, a1, r0, r1};
    st128(rs, (unsigned)WS_X2T + d * (L * 2) + (tid >> 2) * 64 + (tid & 1) * 32 + (tid & 2) * 8, ov);
  }
  STAMP(10);
  if (wave_arrive(lds0, bt, 4, lane)) signal<E1_REP, 8>(ws + CTL_E1 + bt * 65536, wg & 7, lane);
}

__device__ __forceinline__ void aux_group(LAS char* lds, LAS char* lds0, const Args& A, __amdgpu_buffer_rsrc_t rs, unsigned epoch, int u, int tid, int wave, int lane STAMP_ARG) {
  char* ws = A.ws;
  unsigned gs = 0;
  v4u wb[5], ov[3], mw[10];
  u16 mc;
  const int mpart = u & 3, mcol = (u < 120 ? u >> 2 : 0) * 128;
  unsigned tag;
  asm volatile("s_waitcnt vmcnt(1)\n\tv_readfirstlane_b32 %0, %1\n\ts_nop 3" : "=s"(tag) : "v"(epoch) : "memory");
  tag += 1u;
  auto load_wb = [&]() {
    const u16* bp = A.win + (size_t)(160 * (u & 3) + (tid >> 3)) * F + 64 * ((u < 108 ? u : 0) >> 2) + (tid & 7) * 8;
#pragma unroll
    for (int i = 0; i < 5; ++i) wb[i] = NTLOAD((const v4u*)(bp + (size_t)i * 32 * F));
  };
  {
    LAS const char* img = lds + 8192;
    group_sync(lds0, 7, gs += 4, lane);
    STAMP(3);
    const int r = tid >> 4, s = tid & 15;
    v4u v[5];
    {
      const int q = lane >> 4, rr = (lane & 15) >> 2, p = lane & 3, f = (rr | ((q & 1) << 2)) << 1;
      v2u lo[5], hi[5];
#pragma unroll
      for (int j = 0; j < 5; ++j) {
        const int a0 = (2 * (q & 1) * 5 + j) * 1024 + rr * 256 + ((((2 * wave + (q >> 1)) * 2 + (p >> 1)) ^ f) << 4) + (p & 1) * 8;
        lo[j] = trread(img + a0); hi[j] = trread(img + a0 + 5120);
      }
#pragma unroll
      for (int i = 0; i < 5; ++i) v[i] = *(LAS const v4u*)(img + (wave * 5 + i) * 1024 + lane * 16);
      SCHED_BAR();
#pragma unroll
      for (int j = 0; j < 5; ++j)
        st128(rs, (unsigned)WS_XT + ((u * 2 + (q & 1)) * D + (8 * j + 2 * wave + (q >> 1)) * 16 + (lane & 15)) * 16, v4u{lo[j].x, lo[j].y, hi[j].x, hi[j].y});
      SCHED_BAR();
    }
    float s0 = 0.f, s1 = 0.f, q0 = 0.f, q1 = 0.f;
#pragma unroll
    for (int i = 0; i < 5; ++i) {
      s0 = dot2bf(v[i][0], 0x3f803f80u, s0); s1 = dot2bf(v[i][1], 0x3f803f80u, s1); s0 = dot2bf(v[i][2], 0x3f803f80u, s0); s1 = dot2bf(v[i][3], 0x3f803f80u, s1);
      q0 = dot2bf(v[i][0], v[i][0], q0); q1 = dot2bf(v[i][1], v[i][1], q1); q0 = dot2bf(v[i][2], v[i][2], q0); q1 = dot2bf(v[i][3], v[i][3], q1);
    }
    float sum = s0 + s1, sq = q0 + q1;
    sum += dppf<0xb1>(sum); sq += dppf<0xb1>(sq); sum += dppf<0x4e>(sum); sq += dppf<0x4e>(sq);
    sum += dppf<0x141>(sum); sq += dppf<0x141>(sq); sum += dppf<0x140>(sum); sq += dppf<0x140>(sq);
    const float mean = sum * (1.0f / D);
    const int t = u * 16 + r;
    st32(rs, s == 0 ? (unsigned)WS_ST1 + ((((t >> 2) & 1) * 256 + (t >> 3)) * 4 + (t & 3)) * 4 : 0x7ffffff0u, pkbf(mean, ln_std(__builtin_fmaxf(sq * (1.0f / D) - mean * mean, 0.f))));
    STAMP(8);
    SCHED_BAR();
    mc = A.cvec[mpart * 160 + (tid < 160 ? tid : 0)];
#pragma unroll
    for (int i = 0; i < 10; ++i) mw[i] = NTLOAD((const v4u*)(A.wmod + (size_t)(mpart * 160 + i * 16 + (tid >> 4)) * (6 * D) + mcol + (tid & 15) * 8));
    SCHED_BAR();
    load_wb();
    {
      const int slab = u < 108 ? u >> 2 : 0, r8 = tid >> 3, s8 = tid & 7;
      const int half = (r8 >> 2) & 1, jj = slab < 24 ? slab / 3 : 2 * (slab - 24) + half;
      const int sub = (jj < 6 ? 7 * jj : 42 + 6 * (jj - 6)) + (slab < 24 ? 2 * (slab - 3 * jj) + half : 6);
      const u16* src = A.wout + (size_t)(32 * (sub >> 1) + 8 * (r8 >> 3) + 4 * (sub & 1) + (r8 & 3)) * D + (u & 3) * 160;
#pragma unroll
      for (int i = 0; i < 3; ++i) ov[i] = NTLOAD((const v4u*)(src + (i < 2 || s8 < 4 ? s8 + 8 * i : 0) * 8));
    }
    SCHED_BAR();
    waitv<19>();
    unsigned old = 0;
    if (lane == 0) old = __hip_atomic_fetch_add((LAS unsigned*)(lds0 + M_CTL) + 9, 1u, __ATOMIC_RELAXED, __HIP_MEMORY_SCOPE_WORKGROUP);
    if (__builtin_amdgcn_readfirstlane(old) == 3 && lane < F0_REP)
      __hip_atomic_store((gu64*)(ws + CTL_F0 + lane * 1024 + u * 8), ((unsigned long long)~tag << 32) | tag, RLX_AGENT);
    STAMP(1);
  }
  {
    LAS float* msc = (LAS float*)lds;
    const float c = bf2f(mc);
    msc[tid] = bfr(c * __builtin_amdgcn_rcpf(1.0f + __builtin_amdgcn_exp2f(-1.4426950408889634f * c)));
    group_sync(lds0, 7, gs += 4, lane);
    float sv[10];
#pragma unroll
    for (int i = 0; i < 10; ++i) sv[i] = msc[(tid >> 4) + 16 * i];
    float acc[8] = {};
#pragma unroll
    for (int i = 0; i < 10; ++i)
#pragma unroll
      for (int j = 0; j < 4; ++j) { acc[2 * j] += sv[i] * bflo(mw[i][j]); acc[2 * j + 1] += sv[i] * bfhi(mw[i][j]); }
    treduce_xor<4>(acc, (lane & 32) != 0, 32);
    treduce_xor<2>(acc, (lane & 16) != 0, 16);
    LAS float* red = msc + 256;
    *(LAS f32x2*)(red + wave * 128 + (lane & 15) * 8 + (lane >> 4) * 2) = f32x2{acc[0], acc[1]};
    group_sync(lds0, 7, gs += 4, lane);
    if (tid < 128 && u < 120) put_granule(ws + WS_MOD + (size_t)(mpart * 3840 + mcol + tid) * 8, f2u((red[tid] + red[128 + tid]) + (red[256 + tid] + red[384 + tid])), tag);
  }
  __builtin_amdgcn_s_setprio(AUXB_PRIO);
  if (u < 108) {
    const int cbk = u >> 2, part = u & 3;
    const int c0 = 64 * cbk, r0 = 160 * part, seg = tid & 7, rg = tid >> 3;
    v4u w[5];
#pragma unroll
    for (int i = 0; i < 5; ++i) w[i] = wb[i];
    constexpr int OP = 400;
    LAS char* oimg = lds + 32768;
#pragma unroll
    for (int i = 0; i < 3; ++i) *(LAS v4u*)(oimg + rg * OP + (seg + 8 * i) * 16) = ov[i];
    LAS f32x2* mt2 = (LAS f32x2*)(lds + 28672);
    if (wave < 3) {
      unsigned mv[8];
      const int rw = r0 + (tid < 160 ? tid : 159);
      get_granules<8>(mv, tag, [&](int k) { return ws + WS_MOD + (size_t)((k & 3) * 3840 + (3 + (k >> 2)) * D + rw) * 8; });
      mt2[tid] = f32x2{bfr((u2f(mv[0]) + u2f(mv[1])) + (u2f(mv[2]) + u2f(mv[3]))), bfr(1.0f + bfr((u2f(mv[4]) + u2f(mv[5])) + (u2f(mv[6]) + u2f(mv[7]))))};
    }
    group_sync(lds0, 7, gs += 4, lane);
    STAMP(2);
    float v[16] = {};
    constexpr int WPITCH = 144;
    LAS char* img = lds + 1024;
#pragma unroll
    for (int i = 0; i < 5; ++i) {
      const f32x2 ms = mt2[rg + 32 * i];
      const float c1 = ms.y, sb = ms.x;
#pragma unroll
      for (int j = 0; j < 4; ++j) {
        const float a = bflo(w[i][j]), c = bfhi(w[i][j]);
        const unsigned pk = pkbf(a * c1, c * c1);
        v[8 + 2 * j] += sb * a; v[8 + 2 * j + 1] += sb * c;
        v[2 * j] += bflo(pk); v[2 * j + 1] += bfhi(pk);
        w[i][j] = pk;
      }
      *(LAS v4u*)(img + (rg + 32 * i) * WPITCH + seg * 16) = w[i];
    }
    treduce_xor<8>(v, (lane & 32) != 0, 32);
    treduce_xor<4>(v, (lane & 16) != 0, 16);
    treduce_dpp<2, 0x128>(v, (lane & 8) != 0);
    LAS float* red = (LAS float*)(lds + 24576 + 1024);
    {
      const int which = lane >> 5, j = ((lane >> 4) & 1) * 4 + ((lane >> 3) & 1) * 2;
      *(LAS f32x2*)(red + wave * 128 + which * 64 + seg * 8 + j) = f32x2{v[0], v[1]};
    }
    group_sync(lds0, 7, gs += 4, lane);
    STAMP(3);
    const int q = lane >> 4, rr = (lane & 15) >> 2, p = lane & 3;
    const int colb = (32 * (wave >> 1) + 8 * p + 4 * (wave & 1)) * 2;
#pragma unroll
    for (int i = 0; i < 5; ++i) {
      const int a0 = (32 * i + 8 * q + rr) * WPITCH + colb;
      v2u lo = trread(img + a0), hi = trread(img + a0 + 4 * WPITCH);
      st128(rs, (unsigned)WS_WIN + (((((part * 5 + i) >> 1) * 108 + 4 * cbk + wave) * 2 + ((part * 5 + i) & 1)) * 64 + lane) * 16, v4u{lo.x, lo.y, hi.x, hi.y});
    }
    {
      const int a0 = (8 * q + rr) * OP + p * 8 + wave * 32;
#pragma unroll
      for (int j = 0; j < 3; ++j) {
        const v2u lo = trread(oimg + a0 + j * 128), hi = trread(oimg + a0 + 4 * OP + j * 128);
        st128(rs, wave + 4 * j < 10 ? (unsigned)WS_WOUT + (((part * 10 + wave + 4 * j) * 27 + cbk) * 64 + lane) * 16 : 0x7ffffff0u, v4u{lo.x, lo.y, hi.x, hi.y});
      }
    }
    if (tid < 128) {
      const int which = tid >> 6, o = tid & 63, sub = o >> 4, i = o & 15;
      const int cl = 32 * (sub >> 1) + 8 * (i >> 2) + 4 * (sub & 1) + (i & 3), ix = which * 64 + cl;
      st32(rs, (unsigned)WS_U + ((part * 2 + which) * F + c0 + o) * 4, f2u((red[ix] + red[128 + ix]) + (red[256 + ix] + red[384 + ix])));
    }
  }
  STAMP(8);
  lds_wait(lds0, 8, 1);
  if (wave_arrive(lds0, 2, 4, lane)) signal<E1_REP, 8>(ws + CTL_E1B, u & 7, lane);
}

__device__ __forceinline__ int g1_aoff(int row, int G, int ab, int p) {
  const int f = ((row >> 1) & 1) | (((row >> 3) & 1) << 1);
  return row * 128 + (((4 * G + 2 * ab + (p >> 1)) ^ (f << 1)) << 4) + (p & 1) * 8;
}

template <int NB>
__device__ __forceinline__ void loader_path(LAS char* lds, char* ws, __amdgpu_buffer_rsrc_t rs, const u16* __restrict__ wsp, int wg, int mt, int j, int sub0, int lw, int lane) {
  const unsigned lbase = (unsigned)(size_t)lds, vo = lane * 16;
  const bool CNT7 = j < 6;
  {
    unsigned avo[2];
#pragma unroll
    for (int h = 0; h < 2; ++h) {
      const int row = lane >> 3, pos = lane & 7, f = ((row >> 1) & 1) | (h << 1);
      avo[h] = (row + 8 * h) * (L * 2) + ((pos ^ (f << 1)) << 4) - h * 1024;
    }
    const char* asb = ws + WS_X2T + (size_t)(16 * lw) * (L * 2) + mt * 128;
    const int p0 = (NB == 4 || CNT7) ? (lw < 2 ? 4 * lw : 2 + 3 * lw) : 3 * lw;
    const char* bsb = ws + WS_WIN + (size_t)(sub0 * 2 + p0) * 1024;
    const unsigned bb = lbase + G1_BOFF;
    auto issueB = [&](int s, unsigned slot) {
      gldsL<NB>(bsb + (s < 10 ? 108 * 1024 + s * 216 * 1024 : (s - 10) * 216 * 1024), vo, slot + p0 * 1024);
    };
    auto issueA = [&](int s0, int s1) {
#pragma unroll 1
      for (int s = s0; s < s1; ++s) gldsN<2, true>(asb + (size_t)(s * 64) * (L * 2), avo[0], avo[1], 0, 0, lbase + s * 8192 + lw * 2048);
    };
    {
      const unsigned vb = (unsigned)WS_V + (mt * 4 + lw) * 27 * 1024 + lane * 16;
#pragma unroll
      for (int k = 0; k < 3; ++k) st128(rs, vb + (3 * j + k) * 1024, v4u{~0u, ~0u, ~0u, ~0u});
      if (j < 6) __builtin_amdgcn_raw_buffer_store_b64(v2u{~0u, ~0u}, rs, vb + (24 + (j >> 1)) * 1024 + (j & 1) * 8, 0, 16);
    }
    group_sync(lds, 5, 24, lane);
    if (lw == 0) { wait_count<8>(ws + CTL_E1B, wg & (E1_REP - 1), 128, lane); lds_post(lds, 10, lane); }
    if (lw == 3) { wait_count<8>(ws + CTL_E1, wg & (E1_REP - 1), 256, lane); lds_post(lds, 11, lane); }
    lds_wait(lds, 10, 1);
    STAMP(13);
#pragma unroll
    for (int s = 0; s < 4; ++s) issueB(s, bb + (s + 1) * G1_BST);
    lds_wait(lds, 11, 1);
    lds_wait(lds, 1, 4);
    issueA(0, 4);
    STAMP(12);
    LBAR();
    LSTAMP(1); STAMP(14);
    issueB(4, bb);
    waitv<6 + NB>(); LBAR(); LSTAMP(2);
    waitv<4 + NB>(); LBAR(); issueB(5, bb + G1_BST);
    waitv<2 + 2 * NB>(); LBAR(); LSTAMP(3); issueA(4, 8); issueB(6, bb + 2 * G1_BST);
    waitv<8 + 3 * NB>(); LBAR(); issueB(7, bb + 3 * G1_BST);
    waitv<6 + 2 * NB>(); LBAR(); LSTAMP(4); issueB(8, bb + 4 * G1_BST);
    waitv<4 + 3 * NB>(); LBAR(); LSTAMP(5); issueA(8, 10); issueB(9, bb);
    waitv<4 + 3 * NB>(); LBAR(); issueB(10, bb + G1_BST);
    waitv<4 + 3 * NB>(); LBAR(); issueB(11, bb + 2 * G1_BST);
    waitv<2 + 3 * NB>(); LBAR(); LSTAMP(6); issueB(12, bb + 3 * G1_BST);
    int nxt = 4 * G1_BST;
#pragma unroll 1
    for (int i = 9; i <= 15; ++i) {
      waitv<NB * 3>(); LBAR();
      if (i >= 11) LSTAMP(i - 4);
      issueB(i + 4, bb + nxt);
      nxt += G1_BST; if (nxt == G1_NSLOT * G1_BST) nxt = 0;
    }
    waitv<NB * 3>(); LBAR(); LSTAMP(12);
    waitv<NB * 2>(); LBAR(); LSTAMP(13);
    waitv<NB * 1>(); LBAR();
    waitv<0>(); LBAR(); LSTAMP(14);
    LBAR();
  }
  const char* osb = ws + WS_WOUT + (size_t)j * 135 * 1024;
  if (lw >= 2) {
    unsigned wvo[4];
#pragma unroll
    for (int h = 0; h < 4; ++h) {
      const int i = 8 * (h & 1) + (lane >> 3);
      wvo[h] = (8 * (i >> 2) + (i & 3) + 4 * (h >> 1)) * (L * 2) + (((lane & 7) ^ (i >> 1)) << 4) - h * 1024;
    }
    const char* wsb = (const char*)wsp + (size_t)(mt * 64 + 32 * (lw & 1)) * (L * 2);
    auto issueW = [&](int s, unsigned slot) {
      gldsN<4, false>(wsb + s * 128, wvo[0], wvo[1], wvo[2], wvo[3], slot + (lw & 1) * 4096);
    };
#pragma unroll 1
    for (int s = 0; s < G2_NW; ++s) issueW(s, lbase + s * 8192);
    LBAR();
    waitv<32>(); LBAR();
    int wn = 0;
#pragma unroll 1
    for (int i = 1; i <= 23; ++i) { waitv<28>(); LBAR(); issueW(i + 8, lbase + wn); wn += 8192; if (wn == G2_NW * 8192) wn = 0; }
#pragma unroll 1
    for (int i = 24; i <= 32; ++i) {
      waitv<28>(); LBAR();
      gldsL<4>(osb + wn + (lw & 1) * 4096, vo, lbase + wn + (lw & 1) * 4096);
      wn += 8192; if (wn == G2_NW * 8192) wn = 0;
    }
  } else {
    constexpr int NZ = NB == 4 ? 7 : 6;
    const char* zsb = ws + WS_Z2 + (size_t)(sub0 * 2 + lw * NZ) * 1024;
    auto issueZ = [&](int s, unsigned slot) {
      gldsL<4>(zsb + s * 108 * 1024, vo, slot + lw * NZ * 1024);
      gldsL<NZ - 4>(zsb + s * 108 * 1024 + 4096, vo, slot + lw * NZ * 1024 + 4096);
    };
    const unsigned zb = lbase + G2_ZOFF;
    LBAR();
#pragma unroll 1
    for (int s = 0; s < G2_NZ; ++s) issueZ(s, zb + s * G2_ZST);
    waitv<NZ * 5>(); LBAR();
    int zn = 0;
#pragma unroll 1
    for (int i = 1; i <= 26; ++i) { waitv<NZ * 4>(); LBAR(); issueZ(i + 5, zb + zn); zn += G2_ZST; if (zn == G2_NZ * G2_ZST) zn = 0; }
    auto issueO = [&](int slot, int n) {
      const int o = G2_ZOFF + slot * G2_ZST + lw * n * 1024;
      if (n == 7) { gldsL<4>(osb + o, vo, lbase + o); gldsL<3>(osb + o + 4096, vo, lbase + o + 4096); }
      else gldsL<3>(osb + o, vo, lbase + o);
    };
    waitv<NZ * 4>(); LBAR(); issueO(2, 7);
    waitv<NZ * 3 + 7>(); LBAR(); issueO(3, 7);
    waitv<NZ * 2 + 14>(); LBAR(); issueO(4, 3);
    waitv<NZ + 17>(); LBAR(); if (lw == 0) gldsL<1>(osb + 134 * 1024, vo, lbase + 134 * 1024);
    waitv<17>(); LBAR(); issueO(0, 7);
    LBAR(); issueO(1, 7);
  }
  waitv<0>();
  LBAR();
}

template <int NSUB, bool STATS>
__device__ __forceinline__ void consumer_g12(LAS char* lds, char* ws, __amdgpu_buffer_rsrc_t rs, const u16* __restrict__ bsp, const u16* __restrict__ wsp, int wg, int mt, int j, int sub0, int cnt,
                                             int wave, int tid, int lane, v2u (&x2v)[3] STAMP_ARG) {
  constexpr int NS1 = NSUB > 0 ? NSUB : 1;
  const int G = wave >> 2, ng = G ? 3 - (wave & 3) : (wave & 3);
  const int q = lane >> 4, i16 = lane & 15, rr = i16 >> 2, p = lane & 3, n0 = 2 * ng;
  float bias[2];
#pragma unroll
  for (int m = 0; m < 2; ++m) bias[m] = bf2f(bsp[mt * 64 + 32 * G + 8 * (i16 >> 2) + 4 * m + (i16 & 3)]);
  STAMP(4);
  CBAR();
  v2u z1v[NS1][2];
  {
    const int aoff0 = g1_aoff(8 * q + rr, G, 0, p), aoff1 = g1_aoff(8 * q + rr, G, 1, p);
    const int boff = G1_BOFF + n0 * 2048 + lane * 16;
    struct Frags { v4u a[2][2]; v4u b[2][NS1]; };
    f32x4 acc2[NS1][2], acc1[NS1][2], asum[2], asq[2];
    int acur = 0, bcur = G1_BST;
    auto rd = [&](Frags& f) {
#pragma unroll
      for (int kk = 0; kk < 2; ++kk) {
        const v2u l0 = trread(lds + acur + aoff0 + kk * 4096), h0 = trread(lds + acur + aoff0 + kk * 4096 + 512);
        const v2u l1 = trread(lds + acur + aoff1 + kk * 4096), h1 = trread(lds + acur + aoff1 + kk * 4096 + 512);
        f.a[kk][0] = v4u{l0.x, l0.y, h0.x, h0.y}; f.a[kk][1] = v4u{l1.x, l1.y, h1.x, h1.y};
#pragma unroll
        for (int n = 0; n < NSUB; ++n) f.b[kk][n] = *(LAS const v4u*)(lds + bcur + boff + n * 2048 + kk * 1024);
      }
      acur += 8192; if (acur == 81920) acur = 0;
      bcur += G1_BST; if (bcur == G1_NSLOT * G1_BST) bcur = 0;
    };
    auto pin1 = [&]() {
#pragma unroll
      for (int n = 0; n < NSUB; ++n) asm volatile("" : "+v"(acc1[n][0]), "+v"(acc1[n][1]));
    };
#pragma unroll
    for (int ab = 0; ab < 2; ++ab) {
      asum[ab] = f32x4{0.f, 0.f, 0.f, 0.f}; asq[ab] = f32x4{0.f, 0.f, 0.f, 0.f};
#pragma unroll
      for (int n = 0; n < NS1; ++n) { acc2[n][ab] = f32x4{0.f, 0.f, 0.f, 0.f}; acc1[n][ab] = f32x4{0.f, 0.f, 0.f, 0.f}; }
    }
    const v4u ones = {0x3f803f80u, 0x3f803f80u, 0x3f803f80u, 0x3f803f80u};
    auto mm2 = [&](const Frags& f) {
#pragma unroll
      for (int kk = 0; kk < 2; ++kk)
#pragma unroll
        for (int ab = 0; ab < 2; ++ab) {
#pragma unroll
          for (int n = 0; n < NSUB; ++n) acc2[n][ab] = mfma16(f.a[ABL_KK][ab], f.b[ABL_KK][n], acc2[n][ab]);
          if (STATS) { asum[ab] = mfma16(f.a[kk][ab], ones, asum[ab]); asq[ab] = mfma16(f.a[kk][ab], f.a[kk][ab], asq[ab]); }
        }
      INTERLEAVE(6, 2);
    };
    auto mm1 = [&](const Frags& f) {
#pragma unroll
      for (int kk = 0; kk < 2; ++kk)
#pragma unroll
        for (int ab = 0; ab < 2; ++ab)
#pragma unroll
          for (int n = 0; n < NSUB; ++n) acc1[n][ab] = mfma16(f.b[kk][n], f.a[kk][ab], acc1[n][ab]);
      if (NSUB == 2) INTERLEAVE(6, 2); else INTERLEAVE(4, 3);
    };
    Frags f0, f1;
    CBAR(); rd(f0);
    SCHED_BAR();
    float up[4];
    {
      const int c = tid & 127, which = (tid >> 7) & 1, half = tid >> 8;
#pragma unroll
      for (int k = 0; k < 4; ++k) up[k] = ld32f(rs, c < cnt * 16 ? (unsigned)WS_U + ((k * 2 + which) * F + (sub0 + (half ? 0 : 54)) * 16 + c) * 4 : 0x7ffffff0u);
    }
    SCHED_BAR();
    STAMP(5);
    auto pin2 = [&]() {
#pragma unroll
      for (int n = 0; n < NSUB; ++n) asm volatile("" : "+v"(acc2[n][0]), "+v"(acc2[n][1]));
      asm volatile("" : "+v"(asum[0]), "+v"(asum[1]), "+v"(asq[0]), "+v"(asq[1]));
    };
    if (STATS) {
      char* cb = ws + CTL_E1 + (G + 1) * 65536;
      unsigned pc;
      poll_issue<8>(pc, cb, wg & (E1_REP - 1), lane);
      CBAR(); rd(f1); mm2(f0);
      if (G == 0) poll_finish<8>(pc, cb, wg & (E1_REP - 1), 256, lane);
      CBAR(); pin2(); rd(f0); mm2(f1);
      CBAR(); rd(f1); mm2(f0);
      poll_issue<8>(pc, cb, wg & (E1_REP - 1), lane);
      CBAR(); rd(f0); mm2(f1);
      if (G == 1) poll_finish<8>(pc, cb, wg & (E1_REP - 1), 128, lane);
      asm volatile("" :: "v"(pc));
      pin2();
    }
#pragma unroll 1
    for (int it = STATS ? 2 : 0; it < 5; ++it) {
      CBAR(); rd(f1); mm2(f0);
      CBAR(); rd(f0); mm2(f1);
    }
    ((LAS float*)(lds + G1_T_U))[tid] = (up[0] + up[1]) + (up[2] + up[3]);
    unsigned pv = 0;
    if (!STATS && ng < 2) pv = *(const unsigned*)((const char*)wsp + (size_t)(mt * 64 + (G * 2 + ng) * 16 + (lane >> 2)) * (L * 2) + j * 512 + (lane & 3) * 128);
    LAS f32x2* tab = (LAS f32x2*)(lds + G1_T_LN2);
    if (STATS) {
#pragma unroll
      for (int ab = 0; ab < 2; ++ab) {
        const float a0 = asum[ab][0], a1 = asum[ab][1], a2 = asum[ab][2], a3 = asum[ab][3];
        const float b0 = asq[ab][0], b1 = asq[ab][1], b2 = asq[ab][2], b3 = asq[ab][3];
        const float s1 = (p & 2) ? ((p & 1) ? a3 : a2) : ((p & 1) ? a1 : a0), s2 = (p & 2) ? ((p & 1) ? b3 : b2) : ((p & 1) ? b1 : b0);
        const float mean = s1 * (1.0f / D);
        float mb, rinv;
        ln_scalars(mean, __builtin_fmaxf(s2 * (1.0f / D) - mean * mean, 0.f), mb, rinv);
        if (rr == q) tab[(G * 2 + ab) * 16 + i16] = f32x2{rinv, rinv * mb};
      }
    }
    {
      const int nb = G ? 3 : 0;
#pragma unroll
      for (int n = 0; n < 3; ++n) {
        const int row = 80 * j + 16 * (nb + n < 5 ? nb + n : 4) + 4 * q + rr;
        x2v[n] = trread(lds + g1_aoff(row, (wave >> 1) & 1, wave & 1, p));
      }
    }
    STAMP(6);
    CBAR(); rd(f1); mm1(f0);
    float zq[NS1][8];
    {
      LAS const float* ut = (LAS const float*)(lds + G1_T_U);
      float v[16];
#pragma unroll
      for (int e = 0; e < 16; ++e) v[e] = 0.f;
      if (NSUB > 0) {
        f32x2 rv[8];
#pragma unroll
        for (int e = 0; e < 8; ++e) rv[e] = tab[(G * 2 + (e >> 2)) * 16 + 4 * q + (e & 3)];
#pragma unroll
        for (int n = 0; n < NSUB; ++n) {
          const float c1 = ut[(n0 + n) * 16 + i16], c2 = ut[128 + (n0 + n) * 16 + i16];
#pragma unroll
          for (int e = 0; e < 8; ++e) {
            const float z = gelu_f(__builtin_fmaf(rv[e].x, acc2[n][e >> 2][e & 3], __builtin_fmaf(-rv[e].y, c1, c2)));
            zq[n][e] = z;
            v[2 * e] = n ? v[2 * e] + z : z; v[2 * e + 1] = n ? __builtin_fmaf(z, z, v[2 * e + 1]) : z * z;
          }
        }
        treduce_dpp<8, 0x140>(v, (i16 & 8) != 0);
        treduce_dpp<4, 0x141>(v, (i16 & 4) != 0);
        treduce_dpp<2, 0x4e>(v, (i16 & 2) != 0);
        treduce_dpp<1, 0xb1>(v, (i16 & 1) != 0);
      }
      ((LAS float*)(lds + G1_T_P))[ng * 128 + G * 64 + lane] = v[0];
    }
    CBAR(); pin1();
    GAS unsigned long long* gr = (GAS unsigned long long*)(ws + CTL_ST3) + (size_t)mt * 1024 + G * 32;
    if (STATS) {
      LAS const float* pp = (LAS const float*)(lds + G1_T_P) + G * 64 + lane;
      const float sv = (pp[0] + pp[128]) + (pp[256] + pp[384]);
      __hip_atomic_store(gr + (j * 2 + (lane & 1)) * 64 + (lane >> 1), (1ull << 32) | f2u(sv), RLX_AGENT);
    }
    SCHED_BAR(); rd(f0); mm1(f1);
    CBAR(); pin1(); rd(f1); mm1(f0);
    CBAR(); pin1(); rd(f0); mm1(f1);
    unsigned long long gx[8];
    if (STATS) {
#pragma unroll
      for (int k = 0; k < 8; ++k) gx[k] = __hip_atomic_load(gr + (k * 2 + (lane >> 5)) * 64 + (lane & 31), RLX_AGENT);
    }
    CBAR(); pin1(); rd(f1); mm1(f0);
    if (STATS) {
      float sv;
      for (unsigned spin = 0;; ++spin) {
        bool ok = true;
        sv = 0.f;
#pragma unroll
        for (int k = 0; k < 8; ++k) { ok &= (gx[k] >> 32) == 1ull; sv += u2f((unsigned)gx[k]); }
        if (wall(ok) || spin > SPIN_CAP) break;
        __builtin_amdgcn_s_sleep(1);
#pragma unroll
        for (int k = 0; k < 8; ++k) gx[k] = __hip_atomic_load(gr + (k * 2 + (lane >> 5)) * 64 + (lane & 31), RLX_AGENT);
      }
      const float s2 = shx(sv, 32);
      const float mean = sv * (1.0f / FH);
      float mb, rinv;
      ln_scalars(mean, __builtin_fmaxf(s2 * (1.0f / FH) - mean * mean, 0.f), mb, rinv);
      if (lane < 32) ((LAS f32x2*)(lds + G1_T_Z))[32 * G + lane] = f32x2{-mb * rinv, rinv};
    }
    CBAR(); pin1();
    if (NSUB > 0) {
      f32x2 zs[8];
#pragma unroll
      for (int e = 0; e < 8; ++e) zs[e] = ((LAS const f32x2*)(lds + G1_T_Z))[32 * G + 8 * q + e];
#pragma unroll
      for (int n = 0; n < NSUB; ++n) {
        v4u o;
#pragma unroll
        for (int e = 0; e < 4; ++e)
          o[e] = pkbf(__builtin_fmaf(zq[n][2 * e], zs[2 * e].y, zs[2 * e].x), __builtin_fmaf(zq[n][2 * e + 1], zs[2 * e + 1].y, zs[2 * e + 1].x));
        st128(rs, (unsigned)WS_Z2 + (((mt * 54 + sub0 + n0 + n) * 2 + G) * 64 + lane) * 16, o);
      }
    }
    SCHED_BAR(); rd(f0); mm1(f1);
    CBAR(); pin1(); rd(f1); mm1(f0);
    CBAR(); pin1(); rd(f0); mm1(f1);
    CBAR(); pin1(); rd(f1); mm1(f0);
    if (wave_arrive(lds, 3, 8, lane)) signal<4, 4>(ws + CTL_E2 + j * 4096, mt & 3, lane);
    asm volatile("" :: "v"(pv));
    CBAR(); pin1();
    mm1(f1);
    STAMP(7);
    {
      LAS const float* ut = (LAS const float*)(lds + G1_T_U) + 256;
      f32x2 rv[2];
#pragma unroll
      for (int ab = 0; ab < 2; ++ab) rv[ab] = tab[(G * 2 + ab) * 16 + i16];
#pragma unroll
      for (int n = 0; n < NSUB; ++n) {
        const f32x4 u1 = *(LAS const f32x4*)(ut + (n0 + n) * 16 + 4 * q), u2 = *(LAS const f32x4*)(ut + 128 + (n0 + n) * 16 + 4 * q);
#pragma unroll
        for (int ab = 0; ab < 2; ++ab) {
          float z[4];
#pragma unroll
          for (int e = 0; e < 4; ++e) z[e] = gelu_f(__builtin_fmaf(rv[ab].x, acc1[n][ab][e], __builtin_fmaf(-rv[ab].y, u1[e], u2[e])));
          z1v[n][ab] = v2u{pkbf(z[0], z[1]), pkbf(z[2], z[3])};
          asm volatile("" : "+v"(z1v[n][ab]));
        }
      }
    }
  }
  if (wave == 1 || wave == 2) {
    const int c = tid - 64;
    if (c < 80) {
      const unsigned o = (unsigned)WS_MOD + (5 * D + 80 * j + c) * 8;
      ((LAS float*)(lds + G3_GB))[c] = bfr((ld32f(rs, o) + ld32f(rs, o + 30720)) + (ld32f(rs, o + 61440) + ld32f(rs, o + 92160)));
    }
  }
  if (wave == 3) wait_count<4>(ws + CTL_E2 + j * 4096, (mt >> 2) & 3, 32, lane);
  CBAR();
  STAMP(9);
  {
    f32x4 acc[NS1][2];
#pragma unroll
    for (int n = 0; n < NS1; ++n) { acc[n][0] = f32x4{0.f, 0.f, 0.f, 0.f}; acc[n][1] = f32x4{0.f, 0.f, 0.f, 0.f}; }
    int woff[2];
#pragma unroll
    for (int kk = 0; kk < 2; ++kk) woff[kk] = G * 4096 + i16 * 128 + (((4 * kk + q) ^ (i16 >> 1)) << 4);
    const int zoff = G2_ZOFF + n0 * 2048 + lane * 16;
    struct Frags { v4u w[2][2]; v4u z[2][NS1]; };
    int wc = 0, zc = 0;
    auto rd = [&](Frags& f) {
      if (NSUB > 0) {
#pragma unroll
        for (int kk = 0; kk < 2; ++kk) {
#pragma unroll
          for (int m = 0; m < 2; ++m) f.w[kk][m] = *(LAS const v4u*)(lds + wc + woff[kk] + m * 2048);
#pragma unroll
          for (int n = 0; n < NSUB; ++n) f.z[kk][n] = *(LAS const v4u*)(lds + zc + zoff + n * 2048 + kk * 1024);
        }
        wc += 8192; if (wc == G2_NW * 8192) wc = 0;
        zc += G2_ZST; if (zc == G2_NZ * G2_ZST) zc = 0;
      }
    };
    auto mm = [&](const Frags& f) {
#pragma unroll
      for (int kk = 0; kk < 2; ++kk)
#pragma unroll
        for (int n = 0; n < NSUB; ++n)
#pragma unroll
          for (int m = 0; m < 2; ++m) acc[n][m] = mfma16(f.z[ABL_KK][n], f.w[ABL_KK][m], acc[n][m]);
      INTERLEAVE(4, 2);
    };
    Frags f0, f1;
    CBAR(); rd(f0);
    STAMP(10);
#pragma unroll 1
    for (int it = 0; it < 15; ++it) {
      CBAR(); rd(f1); mm(f0);
      CBAR(); rd(f0); mm(f1);
    }
    CBAR(); rd(f1); mm(f0);
    mm(f1);
    STAMP(11);
#pragma unroll
    for (int m = 0; m < 2; ++m) {
      v2u o[NS1];
#pragma unroll
      for (int n = 0; n < NSUB; ++n) {
        float g[4];
#pragma unroll
        for (int e = 0; e < 4; ++e) g[e] = acc[n][m][e] + bias[m];
        const v2u zz = z1v[n][m];
        o[n] = v2u{pkbf(bflo(zz.x) * g[0], bfhi(zz.x) * g[1]), pkbf(bflo(zz.y) * g[2], bfhi(zz.y) * g[3])};
      }
      const unsigned vb = (unsigned)WS_V + (mt * 4 + G * 2 + m) * 27 * 1024 + lane * 16;
      if (NSUB == 2) st128(rs, vb + (3 * j + ng) * 1024, v4u{o[0].x, o[0].y, o[NS1 - 1].x, o[NS1 - 1].y});
      if (NSUB == 1) __builtin_amdgcn_raw_buffer_store_b64(o[0], rs, vb + (24 + (j >> 1)) * 1024 + (j & 1) * 8, 0, 16);
    }
    CBAR();
  }
}

template <int KH>
__device__ __forceinline__ void consumer_g3(LAS char* lds, char* ws, __amdgpu_buffer_rsrc_t rs, u16* __restrict__ out, int mt, int j, int m, int lane, const v2u (&x2v)[3] STAMP_ARG) {
  constexpr int K0 = KH ? 14 : 0, NK = KH ? 13 : 14, NF = KH ? 2 : 3, N0 = KH ? 3 : 0;
  const int q = lane >> 4, i16 = lane & 15;
  v4u vf[NK];
  __builtin_amdgcn_s_sleep(V_DELAY);
  CBAR();
  STAMP(12);
  const unsigned vo = (unsigned)WS_V + ((mt * 4 + m) * 27 + K0) * 1024 + lane * 16;
  constexpr int VW = V_WIN;
#pragma unroll
  for (int k = 0; k < VW; ++k) { vf[k] = ld128(rs, vo + k * 1024); SCHED_BAR(); }
  f32x4 acc[5];
#pragma unroll
  for (int n = 0; n < 5; ++n) acc[n] = f32x4{0.f, 0.f, 0.f, 0.f};
  LAS const char* wp = lds + K0 * 1024 + lane * 16;
  LAS const char* wp2 = wp + 54 * 1024;
  LAS const char* wp4 = wp + 108 * 1024;
  v4u wf[3][5];
  auto rdw = [&](int k, v4u (&w)[5]) {
    w[0] = *(LAS const v4u*)(wp + k * 1024); w[1] = *(LAS const v4u*)(wp + (27 + k) * 1024);
    w[2] = *(LAS const v4u*)(wp2 + k * 1024); w[3] = *(LAS const v4u*)(wp2 + (27 + k) * 1024);
    w[4] = *(LAS const v4u*)(wp4 + k * 1024);
  };
  rdw(0, wf[0]); rdw(1, wf[1]);
#pragma unroll
  for (int k = 0; k < NK; ++k) {
    if (k + VW < NK) vf[k + VW] = ld128(rs, vo + (k + VW) * 1024);
    if (k + 2 < NK) rdw(k + 2, wf[(k + 2) % 3]);
    SCHED_BAR();
    if (__builtin_expect(!wall(__builtin_elementwise_max(vf[k].x, vf[k].z) != ~0u), 0)) {
      unsigned spin = 0;
      do { asm volatile("" ::: "memory"); vf[k] = ld128(rs, vo + k * 1024); }
      while (!wall(__builtin_elementwise_max(vf[k].x, vf[k].z) != ~0u) && ++spin < SPIN_CAP);
    }
#pragma unroll
    for (int n = 0; n < 5; ++n) acc[n] = mfma16(wf[k % 3][n], vf[k], acc[n]);
    asm volatile("" : "+v"(acc[0]), "+v"(acc[1]), "+v"(acc[2]), "+v"(acc[3]), "+v"(acc[4]));
    SCHED_BAR();
  }
  STAMP(13);
  f32x4 gb[NF];
#pragma unroll
  for (int n = 0; n < NF; ++n) gb[n] = *(LAS const f32x4*)(lds + G3_GB + (16 * (N0 + n) + 4 * q) * 4);
  LAS char* red = lds + G3_RED + m * 5120 + lane * 16;
#pragma unroll
  for (int n = 0; n < 5; ++n) if ((n >= 3) != (KH != 0)) *(LAS f32x4*)(red + n * 1024) = acc[n];
  CBAR();
  const int t = mt * 64 + 32 * (m >> 1) + 8 * (i16 >> 2) + 4 * (m & 1) + (i16 & 3);
#pragma unroll
  for (int n = 0; n < NF; ++n) {
    const f32x4 o = *(LAS const f32x4*)(red + (N0 + n) * 1024);
    const f32x4 a = KH ? o + acc[N0 + n] : acc[N0 + n] + o;
    const float o0 = __builtin_fmaf(gb[n][0], a[0], bflo(x2v[n].x)), o1 = __builtin_fmaf(gb[n][1], a[1], bfhi(x2v[n].x));
    const float o2 = __builtin_fmaf(gb[n][2], a[2], bflo(x2v[n].y)), o3 = __builtin_fmaf(gb[n][3], a[3], bfhi(x2v[n].y));
    *(v2u*)((char*)out + ((size_t)t * D + 80 * j + 16 * (N0 + n) + 4 * q) * 2) = v2u{pkbf(o0, o1), pkbf(o2, o3)};
  }
}

__global__ __launch_bounds__(768, 3) void k_all(Args A) {
  extern __shared__ __attribute__((aligned(16))) char smem[];
  LAS char* lds = (LAS char*)smem;
  char* ws = A.ws;
  const int tid = __builtin_amdgcn_workitem_id_x(), lane = tid & 63, wave = __builtin_amdgcn_readfirstlane(tid >> 6), wg = __builtin_amdgcn_workgroup_id_x();
  STAMP_DECL; STAMP(0);
  const __amdgpu_buffer_rsrc_t rs = __builtin_amdgcn_make_buffer_rsrc((void*)ws, 0, (int)WS_END, 0x00020000);
  asm volatile("" :: "s"(A.x), "s"(A.Are), "s"(A.Aim), "s"(A.Bre), "s"(A.Bim), "s"(A.Cre), "s"(A.Cim), "s"(A.logdt), "s"(A.dskip), "s"(A.win), "s"(A.wsp),
               "s"(A.bsp), "s"(A.wout), "s"(A.cvec), "s"(A.wmod), "s"(A.ws), "s"(A.out));
  const int grp = wave >> 2, bt = grp == 2 ? 0 : grp + 1, gw = wave & 3, gt = tid & 255;
  const bool isaux = bt == 2 && wg >= 128;
  const int u = wg - 128, d = 256 * bt + (wg & 7) * (bt < 2 ? 32 : 16) + (wg >> 3);
  unsigned epoch;
  asm volatile("global_load_dword %0, %1, %2" : "=v"(epoch) : "v"((unsigned)CTL_EPOCH), "s"(ws) : "memory");
  if (__builtin_expect(isaux, 1)) {
    const int rr = lane >> 4;
    const unsigned vo = rr * (D * 2) + (((lane & 15) ^ ((rr | ((gw >> 1) << 2)) << 1)) << 4);
    const char* xb = (const char*)A.x + (size_t)(u * 16 + gw * 4) * (D * 2);
#pragma unroll
    for (int i = 0; i < 5; ++i) glds16nt(xb + i * 256, vo, (unsigned)(size_t)lds + C_GRP + 8192 + (gw * 5 + i) * 1024);
    const int zc = (u * 4 + gw) * 3 + lane - 32;
    st128(rs, lane < 32 ? (unsigned)CTL_ST3 + ((u * 4 + gw) * 32 + lane) * 16 : (lane < 35 && zc < 1280 ? (unsigned)zc * 256 : 0x7ffffff0u), v4u{0u, 0u, 0u, 0u});
  }
  unsigned pre[7] = {};
  if (__builtin_expect(!isaux, 1)) {
    const unsigned o = (d * NS + (gw & 1) * 64 + lane) * 2;
#define LDU16(dst, base, voff) asm volatile("global_load_ushort %0, %1, %2" NT : "=v"(dst) : "v"(voff), "s"(base) : "memory")
    LDU16(pre[0], A.Are, o); LDU16(pre[1], A.Aim, o); LDU16(pre[2], A.logdt, d * 2); LDU16(pre[3], A.Bre, o);
    LDU16(pre[4], A.Bim, o); LDU16(pre[5], A.Cre, o); LDU16(pre[6], A.Cim, o);
  }
  STAMP(11);
  if (tid < 16) ((LAS unsigned*)(lds + M_CTL))[tid] = 0u;
  unsigned tag = 0;
  CBAR();
  {
    LAS char* gl = lds + (grp == 2 ? G1_TAB - C_GRP : grp * C_GRP);
    if (bt == 0) __builtin_amdgcn_s_setprio(2);
    if (bt == 1) __builtin_amdgcn_s_setprio(1);
    if (isaux) { __builtin_amdgcn_s_setprio(3); aux_group(gl, lds, A, rs, epoch, u, gt, gw, lane STAMP_PASS); }
    else conv_group(gl, lds, bt, A, rs, epoch, tag, pre, wg, d, gt, gw, lane STAMP_PASS);
    __builtin_amdgcn_s_setprio(0);
  }
  const int xcd = wg & 7, idx = wg >> 3;
  const int mt = (xcd >> 1) * 8 + (idx & 7), j = (xcd & 1) * 4 + (idx >> 3);
  const int cnt = j < 6 ? 7 : 6, sub0 = j < 6 ? 7 * j : 42 + 6 * (j - 6);
  if (wave >= 8) {
    STAMP(4);
    const int lw = wave - 8;
    if (lw + 12 < 2 * cnt) loader_path<4>(lds, ws, rs, A.wsp, wg, mt, j, sub0, lw, lane);
    else loader_path<3>(lds, ws, rs, A.wsp, wg, mt, j, sub0, lw, lane);
    if (wg == 0 && tid == 512) *(volatile unsigned*)(ws + CTL_EPOCH) = tag;
    STAMP_FLUSH(1);
    return;
  }
  v2u x2v[3];
  const int ng = wave >> 2 ? 3 - (wave & 3) : (wave & 3);
  if (ng < 3) consumer_g12<2, false>(lds, ws, rs, A.bsp, A.wsp, wg, mt, j, sub0, cnt, wave, tid, lane, x2v STAMP_PASS);
  else if (cnt == 7) consumer_g12<1, true>(lds, ws, rs, A.bsp, A.wsp, wg, mt, j, sub0, cnt, wave, tid, lane, x2v STAMP_PASS);
  else consumer_g12<0, true>(lds, ws, rs, A.bsp, A.wsp, wg, mt, j, sub0, cnt, wave, tid, lane, x2v STAMP_PASS);
  if (wave < 4) consumer_g3<0>(lds, ws, rs, A.out, mt, j, wave & 3, lane, x2v STAMP_PASS);
  else consumer_g3<1>(lds, ws, rs, A.out, mt, j, wave & 3, lane, x2v STAMP_PASS);
  STAMP(14);
  STAMP_FLUSH(1);
}

#if defined(__HIP_DEVICE_COMPILE__) && !defined(KEEP_PK)
#pragma clang attribute pop
#endif

extern "C" void kernel_launch(void* const* d_in, const int* in_sizes, int n_in, void* d_out, int out_size, void* d_ws, size_t ws_size,
                              hipStream_t stream) {
  Args A;
  A.x = (const u16*)d_in[0]; A.Are = (const u16*)d_in[1]; A.Aim = (const u16*)d_in[2]; A.Bre = (const u16*)d_in[3]; A.Bim = (const u16*)d_in[4];
  A.Cre = (const u16*)d_in[5]; A.Cim = (const u16*)d_in[6]; A.logdt = (const u16*)d_in[7]; A.dskip = (const u16*)d_in[8];
  A.win = (const u16*)d_in[10]; A.wsp = (const u16*)d_in[11]; A.bsp = (const u16*)d_in[12]; A.wout = (const u16*)d_in[13];
  A.cvec = (const u16*)d_in[14]; A.wmod = (const u16*)d_in[15];
  A.ws = (char*)d_ws; A.out = (u16*)d_out;
  (void)hipFuncSetAttribute((const void*)k_all, hipFuncAttributeMaxDynamicSharedMemorySize, M_LDS);
  k_all<<<256, 768, M_LDS, stream>>>(A);
}
```

```cpp
#include <hip/hip_runtime.h>
#include <hip/hip_bf16.h>
#include <stdint.h>

#define NT ""
#define NTLOAD(p) (*(p))
#define GAS __attribute__((address_space(1)))
#define LAS __attribute__((address_space(3)))
typedef unsigned v4u __attribute__((ext_vector_type(4)));
typedef unsigned v2u __attribute__((ext_vector_type(2)));
typedef float f32x2 __attribute__((ext_vector_type(2)));
typedef float f32x4 __attribute__((ext_vector_type(4)));
typedef float f32x16 __attribute__((ext_vector_type(16)));
typedef __bf16 bf16x2 __attribute__((ext_vector_type(2)));
typedef __bf16 bf16x8 __attribute__((ext_vector_type(8)));
typedef _Float16 f16x2 __attribute__((ext_vector_type(2)));
typedef _Float16 f16x8 __attribute__((ext_vector_type(8)));
typedef short s16x4 __attribute__((ext_vector_type(4)));
typedef unsigned short u16;

#if defined(__HIP_DEVICE_COMPILE__) && !defined(KEEP_PK)
#pragma clang attribute push(__attribute__((target("no-packed-fp32-ops"))), apply_to = function)
#endif
__device__ __forceinline__ float u2f(unsigned u) { return __builtin_bit_cast(float, u); }
__device__ __forceinline__ unsigned f2u(float f) { return __builtin_bit_cast(unsigned, f); }
__device__ __forceinline__ int lane_id() { return __builtin_amdgcn_mbcnt_hi(~0u, __builtin_amdgcn_mbcnt_lo(~0u, 0u)); }
__device__ __forceinline__ unsigned shx(unsigned v, int m) { return __builtin_amdgcn_ds_bpermute((lane_id() ^ m) << 2, v); }
__device__ __forceinline__ float shx(float v, int m) { return u2f(shx(f2u(v), m)); }
__device__ __forceinline__ bool wall(bool p) { return __builtin_amdgcn_ballot_w64(!p) == 0ull; }
__device__ __forceinline__ float exp_acc(float x) {
  const float hi = x * 1.4426950408889634f;
  const float lo = __builtin_fmaf(x, 1.4426950408889634f, -hi) + x * 1.925963033500414e-8f;
  const float e = __builtin_amdgcn_exp2f(hi);
  return __builtin_fmaf(e, lo * 0.6931471805599453f, e);
}

constexpr int L = 2048, D = 640, NS = 128, F = 1728, FH = 864;

constexpr size_t CTL_E1 = 0;
constexpr size_t CTL_E1B = 196608;
constexpr size_t CTL_E2 = 262144;
constexpr size_t CTL_E3 = 294912;
constexpr size_t CTL_EW = 303104;
constexpr size_t CTL_ST3 = 327680;
constexpr size_t CTL_F0 = CTL_ST3 + 262144;
constexpr size_t CTL_EPOCH = CTL_F0 + 32768;
constexpr size_t WS_MOD = CTL_EPOCH + 4096;
constexpr size_t WS_ST1 = WS_MOD + 131072;
constexpr size_t WS_U = WS_ST1 + 16384;
constexpr size_t WS_XT = WS_U + 65536;
constexpr size_t WS_X2T = WS_XT + (size_t)D * L * 2;
constexpr size_t WS_WIN = WS_X2T + (size_t)D * L * 2;
constexpr size_t WS_WOUT = WS_WIN + (size_t)D * F * 2;
constexpr size_t WS_Z2 = WS_WOUT + (size_t)FH * D * 2;
constexpr size_t WS_V = WS_Z2 + (size_t)L * FH * 2;
constexpr size_t WS_END = WS_V + (size_t)L * FH * 2;

__device__ __forceinline__ float bflo(unsigned u) { return u2f(u << 16); }
__device__ __forceinline__ float bfhi(unsigned u) { return u2f(u & 0xffff0000u); }
__device__ __forceinline__ float bf2f(u16 v) { return u2f((unsigned)v << 16); }
__device__ __forceinline__ unsigned pkbf(float a, float b) {
  f32x2 v = {a, b};
  return __builtin_bit_cast(unsigned, __builtin_convertvector(v, bf16x2));
}
__device__ __forceinline__ unsigned pkh(float a, float b) {
  f32x2 v = {a, b};
  return __builtin_bit_cast(unsigned, __builtin_convertvector(v, f16x2));
}
__device__ __forceinline__ float bfr(float a) { return bflo(pkbf(a, a)); }
__device__ __forceinline__ v2u trread(LAS const char* p) {
  return __builtin_bit_cast(v2u, __builtin_amdgcn_ds_read_tr16_b64_v4i16((LAS s16x4*)p));
}
__device__ __forceinline__ f32x4 mfma16(v4u a, v4u b, f32x4 c) {
  return __builtin_amdgcn_mfma_f32_16x16x32_bf16(__builtin_bit_cast(bf16x8, a), __builtin_bit_cast(bf16x8, b), c, 0, 0, 0);
}
__device__ __forceinline__ f32x16 mfma32(v4u a, v4u b, f32x16 c) {
  return __builtin_amdgcn_mfma_f32_32x32x16_bf16(__builtin_bit_cast(bf16x8, a), __builtin_bit_cast(bf16x8, b), c, 0, 0, 0);
}
__device__ __forceinline__ f32x16 mfma32h(v4u a, v4u b, f32x16 c) {
  return __builtin_amdgcn_mfma_f32_32x32x16_f16(__builtin_bit_cast(f16x8, a), __builtin_bit_cast(f16x8, b), c, 0, 0, 0);
}
template <bool DEV>
__device__ __forceinline__ void glds16(const void* sbase, unsigned voff, unsigned dst) {
  unsigned keep;
  if (DEV) asm volatile(
"s_mov_b32 %0, m0\n\ts_mov_b32 m0, %3\n\ts_nop 0\n\tglobal_load_lds_dwordx4 %1, %2 sc1\n\ts_mov_b32 m0, %0"
                        : "=&s"(keep) : "v"(voff), "s"(sbase), "s"(dst) : "memory");
  else asm volatile("s_mov_b32 %0, m0\n\ts_mov_b32 m0, %3\n\ts_nop 0\n\tglobal_load_lds_dwordx4 %1, %2\n\ts_mov_b32 m0, %0"
                    : "=&s"(keep) : "v"(voff), "s"(sbase), "s"(dst) : "memory");
}
__device__ __forceinline__ void glds16nt(const void* sbase, unsigned voff, unsigned dst) {
  unsigned keep;
  asm volatile("s_mov_b32 %0, m0\n\ts_mov_b32 m0, %3\n\ts_nop 0\n\tglobal_load_lds_dwordx4 %1, %2" NT "\n\ts_mov_b32 m0, %0"
               : "=&s"(keep) : "v"(voff), "s"(sbase), "s"(dst) : "memory");
}
#define GL_(i, s) "global_load_lds_dwordx4 %" #i ", %5 offset:" s
template <int N, bool DEV>
__device__ __forceinline__ void gldsN(const void* sbase, unsigned v0, unsigned v1, unsigned v2, unsigned v3, unsigned dst) {
  unsigned keep;
#define GLN_(body) asm volatile("s_mov_b32 %0, m0\n\ts_mov_b32 m0, %6\n\ts_nop 0\n\t" body "s_mov_b32 m0, %0" : "=&s"(keep) : "v"(v0), "v"(v1), "v"(v2), "v"(v3), "s"(sbase), "s"(dst) : "memory")
  if (DEV) {
    if (N == 1) GLN_(GL_(1, "0 sc1\n\t"));
    if (N == 2) GLN_(GL_(1, "0 sc1\n\t") GL_(2, "1024 sc1\n\t"));
    if (N == 3) GLN_(GL_(1, "0 sc1\n\t") GL_(2, "1024 sc1\n\t") GL_(3, "2048 sc1\n\t"));
    if (N == 4) GLN_(GL_(1, "0 sc1\n\t") GL_(2, "1024 sc1\n\t") GL_(3, "2048 sc1\n\t") GL_(4, "3072 sc1\n\t"));
  } else {
    if (N == 2) GLN_(GL_(1, "0\n\t") GL_(2, "1024\n\t"));
    if (N == 4) GLN_(GL_(1, "0\n\t") GL_(2, "1024\n\t") GL_(3, "2048\n\t") GL_(4, "3072\n\t"));
  }
}
template <int N> __device__ __forceinline__ void gldsL(const void* sbase, unsigned vo, unsigned dst) { gldsN<N, true>(sbase, vo, vo, vo, vo, dst); }
template <int N> __device__ __forceinline__ void waitv() { asm volatile("s_waitcnt vmcnt(%0)" :: "i"(N) : "memory"); }
__device__ __forceinline__ float dot2bf(unsigned a, unsigned b, float c) {
  return __builtin_amdgcn_fdot2_f32_bf16(__builtin_bit_cast(bf16x2, a), __builtin_bit_cast(bf16x2, b), c, false);
}
#define SCHED_BAR() __builtin_amdgcn_sched_barrier(0)
#define STAMP_DECL
#define STAMP(i) do {} while (0)
#define STAMP_FLUSH(kid) do {} while (0)
#define STAMP_ARG
#define STAMP_PASS
#define CBAR() do { SCHED_BAR(); __builtin_amdgcn_s_waitcnt(0xC07F); asm volatile("" ::: "memory"); __builtin_amdgcn_s_barrier(); asm volatile("" ::: "memory"); } while (0)
#define LBAR() __builtin_amdgcn_s_barrier()

template <int CTRL> __device__ __forceinline__ float dppf(float v) {
  return __builtin_bit_cast(float, __builtin_amdgcn_update_dpp(0, __builtin_bit_cast(int, v), CTRL, 0xf, 0xf, true));
}
template <int N, int CTRL> __device__ __forceinline__ void treduce_dpp(float* v, bool up) {
#pragma unroll
  for (int e = 0; e < N; ++e) {
    const float a = v[e], b = v[e + N];
    const float send = up ? a : b, keep = up ? b : a;
    v[e] = keep + dppf<CTRL>(send);
  }
}
template <int N> __device__ __forceinline__ void treduce_xor(float* v, bool up, int mask) {
#pragma unroll
  for (int e = 0; e < N; ++e) {
    const float a = v[e], b = v[e + N];
    const float send = up ? a : b, keep = up ? b : a;
    v[e] = keep + shx(send, mask);
  }
}

__device__ __forceinline__ float ln_std(float var) { return __builtin_amdgcn_sqrtf(bfr(bfr(var) + 1e-5f)); }
__device__ __forceinline__ void ln_scalars(float mean, float var, float& m_b, float& rinv) {
  m_b = bfr(mean);
  rinv = __builtin_amdgcn_rcpf(bfr(ln_std(var)));
}

struct cplx { float r, i; };
__device__ __forceinline__ cplx cmul(cplx a, cplx b) { return {a.r * b.r - a.i * b.i, a.r * b.i + a.i * b.r}; }

typedef GAS unsigned gu32;
#define RLX_AGENT __ATOMIC_RELAXED, __HIP_MEMORY_SCOPE_AGENT
#define F0_REP 8
#define V_DELAY 24
#define V_WIN 5
#define LSTAMP(i) do {} while (0)
#define POLL_SLEEP 6
#define E1_REP 32
#define INTERLEAVE(n, r) do { _Pragma("unroll") for (int i_ = 0; i_ < (n); ++i_) { __builtin_amdgcn_sched_group_barrier(0x008, 1, 0); __builtin_amdgcn_sched_group_barrier(0x100, (r), 0); } \
    __builtin_amdgcn_sched_group_barrier(0x008, 8, 0); SCHED_BAR(); } while (0)
#define ABL_KK kk
#define AUXB_PRIO 2
constexpr unsigned SPIN_CAP = 1u << 16;
constexpr int M_LDS = 163840, M_CTL = M_LDS - 64;
constexpr int CODE_LINES = 400;
constexpr int C_KOFF = 0, C_UOFF = 4224, C_UP = 1536, C_COFF = C_UOFF + 4 * C_UP, C_GRP = C_COFF + 4 * 64 * 144;
constexpr int G1_BOFF = 81920, G1_BST = 14336, G1_NSLOT = 5, G1_TAB = G1_BOFF + G1_BST * G1_NSLOT;
constexpr int G1_T_LN2 = G1_TAB, G1_T_U = G1_T_LN2 + 512, G1_T_P = G1_T_U + 2048, G1_T_Z = G1_T_P + 2048;
constexpr int G2_NW = 9, G2_NZ = 6, G2_ZOFF = G2_NW * 8192, G2_ZST = 14336;
constexpr int G3_RED = 135 * 1024, G3_GB = 161024;
static_assert(3 * C_GRP <= M_CTL && G1_T_Z + 512 <= M_CTL && G2_ZOFF + G2_NZ * G2_ZST <= M_CTL && G3_GB + 320 <= M_CTL, "lds");

__device__ __forceinline__ void st128(__amdgpu_buffer_rsrc_t rs, unsigned off, v4u v) { __builtin_amdgcn_raw_buffer_store_b128(v, rs, off, 0, 16); }
__device__ __forceinline__ v4u ld128(__amdgpu_buffer_rsrc_t rs, unsigned off) { return __builtin_amdgcn_raw_buffer_load_b128(rs, off, 0, 16); }

__device__ __forceinline__ bool wave_arrive(LAS char* lds, int word, unsigned n, int lane) {
  waitv<0>();
  unsigned old = 0;
  if (lane == 0) old = __hip_atomic_fetch_add((LAS unsigned*)(lds + M_CTL) + word, 1u, __ATOMIC_RELAXED, __HIP_MEMORY_SCOPE_WORKGROUP);
  return (unsigned)__builtin_amdgcn_readfirstlane(old) == n - 1;
}
__device__ __forceinline__ void lds_wait(LAS char* lds0, int word, unsigned target) {
  LAS unsigned* p = (LAS unsigned*)(lds0 + M_CTL) + word;
  while ((unsigned)__builtin_amdgcn_readfirstlane(__hip_atomic_load(p, __ATOMIC_RELAXED, __HIP_MEMORY_SCOPE_WORKGROUP)) < target) __builtin_amdgcn_s_sleep(0);
  asm volatile("" ::: "memory");
}
__device__ __forceinline__ void lds_post(LAS char* lds0, int word, int lane) {
  SCHED_BAR();
  asm volatile("" ::: "memory");
  if (lane == 0) __hip_atomic_fetch_add((LAS unsigned*)(lds0 + M_CTL) + word, 1u, __ATOMIC_RELAXED, __HIP_MEMORY_SCOPE_WORKGROUP);
  asm volatile("" ::: "memory");
}
__device__ __forceinline__ void group_sync(LAS char* lds0, int word, unsigned target, int lane) { lds_post(lds0, word, lane); lds_wait(lds0, word, target); }
template <int NREP, int NSH> __device__ __forceinline__ void signal(char* base, int shard, int lane) {
  if (lane < NREP) __hip_atomic_fetch_add((gu32*)(base + (lane * NSH + shard) * 256), 1u, RLX_AGENT);
}
template <int NSH> __device__ __forceinline__ bool count_ok(unsigned c, unsigned total) {
  if (NSH > 1) c += __builtin_amdgcn_update_dpp(0, c, 0xb1, 0xf, 0xf, true);
  if (NSH > 2) c += __builtin_amdgcn_update_dpp(0, c, 0x4e, 0xf, 0xf, true);
  if (NSH > 4) c += __builtin_amdgcn_update_dpp(0, c, 0x141, 0xf, 0xf, true);
  return (unsigned)__builtin_amdgcn_readfirstlane(c) >= total;
}
template <int NSH> __device__ __forceinline__ void wait_count(char* base, int rep, unsigned total, int lane) {
  gu32* p = (gu32*)(base + (rep * NSH + (lane & (NSH - 1))) * 256);
#pragma unroll 1
  for (unsigned spin = 0; spin < SPIN_CAP; ++spin) {
    if (count_ok<NSH>(__hip_atomic_load(p, RLX_AGENT), total)) break;
    __builtin_amdgcn_s_sleep(POLL_SLEEP);
  }
  __builtin_amdgcn_fence(__ATOMIC_ACQUIRE, "wavefront");
}
template <int NSH> __device__ __forceinline__ void poll_issue(unsigned& c, char* base, int rep, int lane) {
  c = __hip_atomic_load((gu32*)(base + (rep * NSH + (lane & (NSH - 1))) * 256), RLX_AGENT);
}
template <int NSH> __device__ __forceinline__ void poll_finish(unsigned c, char* base, int rep, unsigned total, int lane) {
  if (!count_ok<NSH>(c, total)) wait_count<NSH>(base, rep, total, lane);
  __builtin_amdgcn_fence(__ATOMIC_ACQUIRE, "wavefront");
}

__device__ __forceinline__ float gelu_f(float x) {
  const float p = __builtin_fmaf(x * x, -0.044715f * 2.302208198f, -2.302208198f);
  const float e = __builtin_amdgcn_exp2f(x * p);
  return x * __builtin_amdgcn_rcpf(1.0f + e);
}

__device__ __forceinline__ float ld32f(__amdgpu_buffer_rsrc_t rs, unsigned off) { return u2f(__builtin_amdgcn_raw_buffer_load_b32(rs, off, 0, 16)); }
__device__ __forceinline__ void st32(__amdgpu_buffer_rsrc_t rs, unsigned off, unsigned v) { __builtin_amdgcn_raw_buffer_store_b32(v, rs, off, 0, 16); }

struct Args {
  const u16 *x, *Are, *Aim, *Bre, *Bim, *Cre, *Cim, *logdt, *dskip, *win, *wsp, *bsp, *wout, *cvec, *wmod;
  char* ws; u16* out;
};

typedef GAS unsigned long long gu64;
__device__ __forceinline__ void put_granule(char* p, unsigned v, unsigned tag) { __hip_atomic_store((gu64*)p, ((unsigned long long)tag << 32) | v, RLX_AGENT); }
template <int N, class F> __device__ __forceinline__ void get_granules(unsigned (&v)[N], unsigned tag, F addr) {
#pragma unroll 1
  for (unsigned spin = 0; spin < SPIN_CAP; ++spin) {
    unsigned long long x[N];
#pragma unroll
    for (int k = 0; k < N; ++k) x[k] = __hip_atomic_load((gu64*)addr(k), RLX_AGENT);
    bool ok = true;
#pragma unroll
    for (int k = 0; k < N; ++k) { v[k] = (unsigned)x[k]; ok &= (unsigned)(x[k] >> 32) == tag; }
    if (wall(ok)) break;
    __builtin_amdgcn_s_sleep(3);
  }
}

__device__ __forceinline__ void conv_group(LAS char* lds, LAS char* lds0, int bt, const Args& A, __amdgpu_buffer_rsrc_t rs, unsigned epoch, unsigned& tag, unsigned (&pre)[7], unsigned long long pc,
                                           int wg, int d, int tid, int wave, int lane STAMP_ARG) {
  char* ws = A.ws;
  float S[8], soff;
  v4u xv, st[2];
  unsigned cpf = 0;
  {
    const int half = wave & 1;
    int xoff[8];
#pragma unroll
    for (int j = 0; j < 8; ++j) {
      const int l = tid * 8 + j;
      const int idx = l < 128 ? l : (int)(pkbf((float)l, 0.f) & 0xffffu) - 0x4300 + 128;
      xoff[j] = (idx + (idx >> 3)) * 8;
    }
    asm volatile("" : "+v"(xoff[0]), "+v"(xoff[1]), "+v"(xoff[2]), "+v"(xoff[3]), "+v"(xoff[4]), "+v"(xoff[5]), "+v"(xoff[6]), "+v"(xoff[7]));
    waitv<0>();
    asm volatile("v_readfirstlane_b32 %0, %1\n\ts_nop 3" : "=s"(tag) : "v"(epoch) : "memory");
    tag += 1u;
    asm volatile("" : "+v"(pre[0]), "+v"(pre[1]), "+v"(pre[2]), "+v"(pre[3]), "+v"(pre[4]), "+v"(pre[5]), "+v"(pre[6]));
    {
      const int so = ((wg >> 3) * 4 + wave) * 4 + (lane & 3);
      cpf = *(const GAS unsigned*)((pc & ~255ull) + (size_t)(bt == 1 && so < CODE_LINES ? so : 0) * 128);
    }
    const unsigned vare = pre[0], vaim = pre[1], vdt = pre[2], vbre = pre[3], vbim = pre[4], vcre = pre[5], vcim = pre[6];
    if (tid < 4) *(LAS v4u*)(lds + C_KOFF + tid * 16) = v4u{0u, 0u, 0u, 0u};
    const float dt = bfr(exp_acc(bflo(vdt)));
    const float ar2 = dt * bflo(vare) * 1.4426950408889634f, ai2 = dt * bflo(vaim) * 0.15915494309189535f;
    auto E = [&](float xx) {
      const float mag = __builtin_amdgcn_exp2f(ar2 * xx), ph = __builtin_amdgcn_fractf(ai2 * xx);
      return cplx{mag * __builtin_amdgcn_cosf(ph), mag * __builtin_amdgcn_sinf(ph)};
    };
    LAS char* img = lds + C_COFF + wave * 8192 + lane * 128;
    const int sw = lane & 7;
    float isc = 0.f;
    if (wave < 2) {
      const cplx e8 = E(8.f), e64 = E(64.f), e128 = E(128.f), e192 = E(192.f);
      cplx q[8];
      q[0] = {1.f, 0.f}; q[1] = e8; q[2] = cmul(e8, e8); q[3] = cmul(q[2], e8); q[4] = cmul(q[2], q[2]);
      q[5] = cmul(q[4], e8); q[6] = cmul(q[4], q[2]); q[7] = cmul(q[4], q[3]);
#pragma unroll
      for (int g = 0; g < 4; ++g) {
        cplx v[8];
#pragma unroll
        for (int j = 0; j < 8; ++j) v[j] = g == 0 ? q[j] : cmul(q[j], g == 1 ? e64 : (g == 2 ? e128 : e192));
        *(LAS v4u*)(img + ((g ^ sw) << 4)) = v4u{pkh(v[0].r, v[1].r), pkh(v[2].r, v[3].r), pkh(v[4].r, v[5].r), pkh(v[6].r, v[7].r)};
        *(LAS v4u*)(img + (((4 + g) ^ sw) << 4)) = v4u{pkh(v[0].i, v[1].i), pkh(v[2].i, v[3].i), pkh(v[4].i, v[5].i), pkh(v[6].i, v[7].i)};
      }
    } else {
      cplx W = cmul(cplx{bflo(vcre), bflo(vcim)}, cplx{dt * bflo(vbre), dt * bflo(vbim)});
      float mx = __builtin_fmaxf(__builtin_fabsf(W.r), __builtin_fabsf(W.i));
      mx = __builtin_fmaxf(mx, dppf<0xb1>(mx)); mx = __builtin_fmaxf(mx, dppf<0x4e>(mx)); mx = __builtin_fmaxf(mx, dppf<0x141>(mx)); mx = __builtin_fmaxf(mx, dppf<0x140>(mx));
      const unsigned mb = f2u(mx);
      const unsigned m01 = __builtin_elementwise_max((unsigned)__builtin_amdgcn_readlane(mb, 0), (unsigned)__builtin_amdgcn_readlane(mb, 16));
      const unsigned m23 = __builtin_elementwise_max((unsigned)__builtin_amdgcn_readlane(mb, 32), (unsigned)__builtin_amdgcn_readlane(mb, 48));
      int ex = (int)((__builtin_elementwise_max(m01, m23) >> 23) & 0xff);
      ex = ex < 20 ? 20 : (ex > 240 ? 240 : ex);
      const float sc = u2f((unsigned)(267 - ex) << 23);
      isc = u2f((unsigned)(ex - 13) << 23);
      W.r *= sc; W.i *= sc;
      const cplx e1 = E(1.f), e2 = cmul(e1, e1), e4 = cmul(e2, e2);
      cplx h[24];
      h[0] = W; h[1] = cmul(W, e1); h[2] = cmul(W, e2); h[3] = cmul(h[2], e1);
      h[4] = cmul(W, e4); h[5] = cmul(h[4], e1); h[6] = cmul(h[4], e2); h[7] = cmul(h[6], e1);
      const cplx g1 = E(256.f), g2 = E(512.f), g3 = E(768.f), g4 = E(1024.f);
      h[8] = cmul(h[0], g1); h[9] = cmul(h[2], g1); h[10] = cmul(h[4], g1); h[11] = cmul(h[6], g1);
      h[12] = cmul(h[0], g2); h[13] = cmul(h[4], g2); h[14] = cmul(h[0], g3); h[15] = cmul(h[4], g3);
      h[16] = cmul(h[0], g4); h[17] = cmul(h[8], g4); h[18] = cmul(h[12], g4); h[19] = cmul(h[14], g4);
      h[20] = cmul(h[16], g4); h[21] = {0.f, 0.f}; h[22] = {0.f, 0.f}; h[23] = {0.f, 0.f};
#pragma unroll
      for (int g = 0; g < 3; ++g) {
        const cplx* v = h + 8 * g;
        *(LAS v4u*)(img + ((g ^ sw) << 4)) = v4u{pkh(v[0].r, v[1].r), pkh(v[2].r, v[3].r), pkh(v[4].r, v[5].r), pkh(v[6].r, v[7].r)};
        *(LAS v4u*)(img + (((4 + g) ^ sw) << 4)) = v4u{pkh(-v[0].i, -v[1].i), pkh(-v[2].i, -v[3].i), pkh(-v[4].i, -v[5].i), pkh(-v[6].i, -v[7].i)};
      }
      *(LAS v4u*)(img + ((3 ^ sw) << 4)) = v4u{0u, 0u, 0u, 0u};
      *(LAS v4u*)(img + ((7 ^ sw) << 4)) = v4u{0u, 0u, 0u, 0u};
    }
    STAMP(1);
    group_sync(lds0, 5 + bt, 4, lane);
    if (wave >= 2) {
      const int hh = lane >> 5, blk = (lane >> 4) & 1, qq = (lane & 15) >> 2, p = lane & 3;
      LAS const char* Qi = lds + C_COFF + half * 8192;
      v2u a[8][2], bb[8][2];
#pragma unroll
      for (int ks = 0; ks < 8; ++ks)
#pragma unroll
        for (int t = 0; t < 2; ++t) {
          const int k = 16 * ks + 8 * hh + 4 * t + qq, mode = k >> 1, c = k & 1;
          const int off = mode * 128 + (((c * 4 + 2 * blk + (p >> 1)) ^ (mode & 7)) << 4) + (p & 1) * 8;
          a[ks][t] = trread(Qi + 16384 + off);
          bb[ks][t] = trread(Qi + off);
        }
      SCHED_BAR();
      f32x16 acc = {};
#pragma unroll
      for (int ks = 0; ks < 8; ++ks) acc = mfma32h(v4u{a[ks][0].x, a[ks][0].y, a[ks][1].x, a[ks][1].y}, v4u{bb[ks][0].x, bb[ks][0].y, bb[ks][1].x, bb[ks][1].y}, acc);
      LAS float* T = (LAS float*)(lds + C_UOFF) + half;
      const int q = lane & 31, hi = lane >> 5;
      auto put = [&](int idx, float v) { T[(idx + (idx >> 3)) * 2] = v * isc; };
#pragma unroll
      for (int r = 0; r < 4; ++r) put((r + 4 * hi) + 8 * q, acc[r]);
#pragma unroll
      for (int r = 0; r < 4; ++r) put(hi ? 384 + 64 * (r >> 1) + (r & 1) + 2 * q : 256 + r + 4 * q, acc[4 + r]);
      if (hi == 0) {
#pragma unroll
        for (int r = 0; r < 4; ++r) put(512 + 32 * r + q, acc[8 + r]);
      } else if (q == 0) put(640, acc[8]);
    }
    lds_post(lds0, 5 + bt, lane);
    auto load_x = [&]() {
      xv = ld128(rs, (unsigned)WS_XT + (tid * D + d) * 16);
#pragma unroll
      for (int i = 0; i < 2; ++i) st[i] = ld128(rs, (unsigned)WS_ST1 + (i * 256 + tid) * 16);
      SCHED_BAR();
    };
    const bool early = __builtin_amdgcn_readfirstlane(__hip_atomic_load((LAS unsigned*)(lds0 + M_CTL) + 8, __ATOMIC_RELAXED, __HIP_MEMORY_SCOPE_WORKGROUP)) != 0;
    asm volatile("" ::: "memory");
    if (early) load_x();
    lds_wait(lds0, 5 + bt, 8);
    {
      f32x2 tv[8];
#pragma unroll
      for (int j = 0; j < 8; ++j) tv[j] = *(LAS const f32x2*)(lds + C_UOFF + xoff[j]);
      SCHED_BAR();
      float kv[8];
#pragma unroll
      for (int j = 0; j < 8; ++j) kv[j] = tv[j][0] + tv[j][1];
      *(LAS v4u*)(lds + C_KOFF + 64 + tid * 16) = v4u{pkbf(kv[0], kv[1]), pkbf(kv[2], kv[3]), pkbf(kv[4], kv[5]), pkbf(kv[6], kv[7])};
      S[0] = kv[0];
#pragma unroll
      for (int j = 1; j < 8; ++j) S[j] = S[j - 1] + kv[j];
      float inc = S[7];
      inc += dppf<0x111>(inc); inc += dppf<0x112>(inc); inc += dppf<0x114>(inc); inc += dppf<0x118>(inc);
      inc += __builtin_bit_cast(float, __builtin_amdgcn_update_dpp(0, __builtin_bit_cast(int, inc), 0x142, 0xa, 0xf, false));
      inc += __builtin_bit_cast(float, __builtin_amdgcn_update_dpp(0, __builtin_bit_cast(int, inc), 0x143, 0xc, 0xf, false));
      soff = inc - S[7];
      if (lane == 63) ((LAS float*)(lds + C_KOFF + 4160))[wave] = inc;
    }
    lds_post(lds0, 5 + bt, lane);
    if (!early) {
      if (bt == 0 && wave == 0) {
#pragma unroll 1
        for (unsigned spin = 0; spin < SPIN_CAP; ++spin) {
          asm volatile("" ::: "memory");
          const v4u f = ld128(rs, (unsigned)CTL_F0 + (wg & (F0_REP - 1)) * 1024 + lane * 16);
          if (wall(f.x == tag && f.y == ~tag && f.z == tag && f.w == ~tag)) break;
          __builtin_amdgcn_s_sleep(2);
        }
        __builtin_amdgcn_fence(__ATOMIC_ACQUIRE, "wavefront");
        lds_post(lds0, 8, lane);
      } else lds_wait(lds0, 8, 1);
      load_x();
    }
    STAMP(2);
  }
  lds_wait(lds0, 5 + bt, 12);
  STAMP(3);
  float xf[8], xn[8];
  {
#pragma unroll
    for (int j = 0; j < 4; ++j) { xf[2 * j] = bflo(xv[j]); xf[2 * j + 1] = bfhi(xv[j]); }
    const f32x4 wt = *(LAS const f32x4*)(lds + C_KOFF + 4160);
    if (tid < 128) *(LAS v4u*)(lds + C_UOFF + (tid >> 5) * C_UP + (tid & 31) * 16) = v4u{0u, 0u, 0u, 0u};
#pragma unroll
    for (int j = 0; j < 8; ++j) {
      const unsigned sj = st[j >> 2][j & 3];
      xn[j] = (xf[j] - bflo(sj)) * __builtin_amdgcn_rcpf(bfhi(sj));
    }
#pragma unroll
    for (int e = 0; e < 4; ++e) *(LAS unsigned*)(lds + C_UOFF + e * C_UP + 512 + tid * 4) = pkbf(xn[e], xn[e + 4]);
    soff += (wave > 0 ? wt[0] : 0.f) + (wave > 1 ? wt[1] : 0.f) + (wave > 2 ? wt[2] : 0.f);
  }
  const u16 dskv = A.dskip[d];
  char* mgp = ws + WS_MOD + (size_t)((lane & 3) * 3840 + (lane < 12 ? lane >> 2 : 0) * D + d) * 8;
  const unsigned long long mg = __hip_atomic_load((gu64*)mgp, RLX_AGENT);
  STAMP(5);
  group_sync(lds0, 5 + bt, 16, lane);
  STAMP(6);
  f32x16 y0 = {}, y1 = {};
  {
    const int hh = lane >> 5, blk = (lane >> 4) & 1, qq = (lane & 15) >> 2, p = lane & 3, col = lane & 31;
    LAS const char* ab = lds + C_KOFF + (32 + 16 * blk + 4 * p - 4 * qq + 32 * hh) * 2 - 32;
    LAS const char* bb = lds + C_UOFF + wave * C_UP + (256 + 8 * (col - hh) - 240) * 2;
    struct CF { v2u a0l, a0h, a1l, a1h; v4u b0, b1; };
    CF cf[3];
    auto rdc = [&](int ks, CF& f) {
      f.a0l = trread(ab + ks * 128 + 32); f.a0h = trread(ab + ks * 128);
      f.b0 = *(LAS const v4u*)(bb + (15 - ks) * 32);
      f.b1 = *(LAS const v4u*)(bb + (31 - ks) * 32);
      f.a1l = trread(ab + (ks + 16) * 128 + 32); f.a1h = trread(ab + (ks + 16) * 128);
    };
    rdc(0, cf[0]); rdc(1, cf[1]);
#pragma unroll
    for (int ks = 0; ks < 16; ++ks) {
      if (ks + 2 < 16) rdc(ks + 2, cf[(ks + 2) % 3]);
      SCHED_BAR();
      const CF& f = cf[ks % 3];
      const v4u a0 = {f.a0l.x, f.a0l.y, f.a0h.x, f.a0h.y}, a1 = {f.a1l.x, f.a1l.y, f.a1h.x, f.a1h.y};
      y0 = mfma32(a0, f.b0, y0);
      y1 = mfma32(a0, f.b1, y1);
      y1 = mfma32(a1, f.b0, y1);
      SCHED_BAR();
    }
    asm volatile("" :: "v"(y0), "v"(y1));
    STAMP(7);
    LAS char* cb = lds + C_COFF + (wave * 64 + col) * 144 + hh * 16;
#pragma unroll
    for (int g = 0; g < 4; ++g) {
      *(LAS f32x4*)(cb + g * 32) = f32x4{y0[4 * g], y0[4 * g + 1], y0[4 * g + 2], y0[4 * g + 3]};
      *(LAS f32x4*)(cb + 32 * 144 + g * 32) = f32x4{y1[4 * g], y1[4 * g + 1], y1[4 * g + 2], y1[4 * g + 3]};
    }
  }
  lds_post(lds0, 5 + bt, lane);
  unsigned mv[1] = {(unsigned)mg};
  if (!wall((unsigned)(mg >> 32) == tag)) get_granules<1>(mv, tag, [&](int) { return mgp; });
  float msum = u2f(mv[0]);
  msum += dppf<0xb1>(msum); msum += dppf<0x4e>(msum);
  const unsigned mb = f2u(bfr(msum));
  const float sa = u2f(__builtin_amdgcn_readlane(mb, 0)), c1a = bfr(1.0f + u2f(__builtin_amdgcn_readlane(mb, 4))), ga = u2f(__builtin_amdgcn_readlane(mb, 8)), dsk = bf2f(dskv);
  const float gc = ga * c1a, gs_ = ga * sa, gd = gc * dsk, g0 = gs_ * (soff + dsk);
#pragma unroll
  for (int j = 0; j < 8; ++j) xf[j] = __builtin_fmaf(gd, xn[j], __builtin_fmaf(gs_, S[j], xf[j] + g0));
  STAMP(8);
  lds_wait(lds0, 5 + bt, 20);
  STAMP(9);
  float y[8];
  {
    const int c1 = 2 * tid, c0 = tid ? c1 - 1 : 0;
    const int o0 = (c0 >> 3) * 144 + (c0 & 7) * 16, o1 = (c1 >> 3) * 144 + (c1 & 7) * 16;
    LAS const char* cb = lds + C_COFF;
    f32x4 w0[4], w1[4], w2[4];
#pragma unroll
    for (int e = 0; e < 4; ++e) {
      w1[e] = *(LAS const f32x4*)(cb + e * 64 * 144 + o1); w2[e] = *(LAS const f32x4*)(cb + e * 64 * 144 + o1 + 16);
      if (e) w0[e] = *(LAS const f32x4*)(cb + e * 64 * 144 + o0);
    }
    SCHED_BAR();
#pragma unroll
    for (int e = 0; e < 4; ++e) {
      float w[12];
#pragma unroll
      for (int j = 0; j < 4; ++j) { w[4 + j] = w1[e][j]; w[8 + j] = w2[e][j]; w[j] = (e && tid) ? w0[e][j] : 0.f; }
#pragma unroll
      for (int j = 0; j < 8; ++j) y[j] = e ? y[j] + w[4 + j - e] : w[4 + j];
    }
  }
  float o[8];
#pragma unroll
  for (int j = 0; j < 8; ++j) o[j] = __builtin_fmaf(gc, y[j], xf[j]);
  {
    const unsigned a0 = pkbf(o[0], o[1]), a1 = pkbf(o[2], o[3]), b0 = pkbf(o[4], o[5]), b1 = pkbf(o[6], o[7]);
    const bool odd = tid & 1;
    const unsigned s0 = odd ? a0 : b0, s1 = odd ? a1 : b1;
    const unsigned r0 = __builtin_amdgcn_update_dpp(0, s0, 0xb1, 0xf, 0xf, true), r1 = __builtin_amdgcn_update_dpp(0, s1, 0xb1, 0xf, 0xf, true);
    const v4u ov = odd ? v4u{r0, r1, b0, b1} : v4u{a0, a1, r0, r1};
    st128(rs, (unsigned)WS_X2T + d * (L * 2) + (tid >> 2) * 64 + (tid & 1) * 32 + (tid & 2) * 8, ov);
  }
  STAMP(10);
  asm volatile("" :: "v"(cpf));
  if (wave_arrive(lds0, bt, 4, lane)) signal<E1_REP, 8>(ws + CTL_E1 + bt * 65536, wg & 7, lane);
}

__device__ __forceinline__ void aux_group(LAS char* lds, LAS char* lds0, const Args& A, __amdgpu_buffer_rsrc_t rs, unsigned epoch, int u, int tid, int wave, int lane STAMP_ARG) {
  char* ws = A.ws;
  unsigned gs = 0;
  v4u wb[5], ov[3], mw[10];
  u16 mc;
  const int mpart = u & 3, mcol = (u < 120 ? u >> 2 : 0) * 128;
  unsigned tag;
  asm volatile("s_waitcnt vmcnt(1)\n\tv_readfirstlane_b32 %0, %1\n\ts_nop 3" : "=s"(tag) : "v"(epoch) : "memory");
  tag += 1u;
  auto load_wb = [&]() {
    const u16* bp = A.win + (size_t)(160 * (u & 3) + (tid >> 3)) * F + 64 * ((u < 108 ? u : 0) >> 2) + (tid & 7) * 8;
#pragma unroll
    for (int i = 0; i < 5; ++i) wb[i] = NTLOAD((const v4u*)(bp + (size_t)i * 32 * F));
  };
  {
    LAS const char* img = lds + 8192;
    group_sync(lds0, 7, gs += 4, lane);
    STAMP(3);
    const int r = tid >> 4, s = tid & 15;
    v4u v[5];
    {
      const int q = lane >> 4, rr = (lane & 15) >> 2, p = lane & 3, f = (rr | ((q & 1) << 2)) << 1;
      v2u lo[5], hi[5];
#pragma unroll
      for (int j = 0; j < 5; ++j) {
        const int a0 = (2 * (q & 1) * 5 + j) * 1024 + rr * 256 + ((((2 * wave + (q >> 1)) * 2 + (p >> 1)) ^ f) << 4) + (p & 1) * 8;
        lo[j] = trread(img + a0); hi[j] = trread(img + a0 + 5120);
      }
#pragma unroll
      for (int i = 0; i < 5; ++i) v[i] = *(LAS const v4u*)(img + (wave * 5 + i) * 1024 + lane * 16);
      SCHED_BAR();
#pragma unroll
      for (int j = 0; j < 5; ++j)
        st128(rs, (unsigned)WS_XT + ((u * 2 + (q & 1)) * D + (8 * j + 2 * wave + (q >> 1)) * 16 + (lane & 15)) * 16, v4u{lo[j].x, lo[j].y, hi[j].x, hi[j].y});
      SCHED_BAR();
    }
    float s0 = 0.f, s1 = 0.f, q0 = 0.f, q1 = 0.f;
#pragma unroll
    for (int i = 0; i < 5; ++i) {
      s0 = dot2bf(v[i][0], 0x3f803f80u, s0); s1 = dot2bf(v[i][1], 0x3f803f80u, s1); s0 = dot2bf(v[i][2], 0x3f803f80u, s0); s1 = dot2bf(v[i][3], 0x3f803f80u, s1);
      q0 = dot2bf(v[i][0], v[i][0], q0); q1 = dot2bf(v[i][1], v[i][1], q1); q0 = dot2bf(v[i][2], v[i][2], q0); q1 = dot2bf(v[i][3], v[i][3], q1);
    }
    float sum = s0 + s1, sq = q0 + q1;
    sum += dppf<0xb1>(sum); sq += dppf<0xb1>(sq); sum += dppf<0x4e>(sum); sq += dppf<0x4e>(sq);
    sum += dppf<0x141>(sum); sq += dppf<0x141>(sq); sum += dppf<0x140>(sum); sq += dppf<0x140>(sq);
    const float mean = sum * (1.0f / D);
    const int t = u * 16 + r;
    st32(rs, s == 0 ? (unsigned)WS_ST1 + ((((t >> 2) & 1) * 256 + (t >> 3)) * 4 + (t & 3)) * 4 : 0x7ffffff0u, pkbf(mean, ln_std(__builtin_fmaxf(sq * (1.0f / D) - mean * mean, 0.f))));
    STAMP(8);
    SCHED_BAR();
    mc = A.cvec[mpart * 160 + (tid < 160 ? tid : 0)];
#pragma unroll
    for (int i = 0; i < 10; ++i) mw[i] = NTLOAD((const v4u*)(A.wmod + (size_t)(mpart * 160 + i * 16 + (tid >> 4)) * (6 * D) + mcol + (tid & 15) * 8));
    SCHED_BAR();
    load_wb();
    {
      const int slab = u < 108 ? u >> 2 : 0, r8 = tid >> 3, s8 = tid & 7;
      const int half = (r8 >> 2) & 1, jj = slab < 24 ? slab / 3 : 2 * (slab - 24) + half;
      const int sub = (jj < 6 ? 7 * jj : 42 + 6 * (jj - 6)) + (slab < 24 ? 2 * (slab - 3 * jj) + half : 6);
      const u16* src = A.wout + (size_t)(32 * (sub >> 1) + 8 * (r8 >> 3) + 4 * (sub & 1) + (r8 & 3)) * D + (u & 3) * 160;
#pragma unroll
      for (int i = 0; i < 3; ++i) ov[i] = NTLOAD((const v4u*)(src + (i < 2 || s8 < 4 ? s8 + 8 * i : 0) * 8));
    }
    SCHED_BAR();
    waitv<19>();
    unsigned old = 0;
    if (lane == 0) old = __hip_atomic_fetch_add((LAS unsigned*)(lds0 + M_CTL) + 9, 1u, __ATOMIC_RELAXED, __HIP_MEMORY_SCOPE_WORKGROUP);
    if (__builtin_amdgcn_readfirstlane(old) == 3 && lane < F0_REP)
      __hip_atomic_store((gu64*)(ws + CTL_F0 + lane * 1024 + u * 8), ((unsigned long long)~tag << 32) | tag, RLX_AGENT);
    STAMP(1);
  }
  {
    LAS float* msc = (LAS float*)lds;
    const float c = bf2f(mc);
    msc[tid] = bfr(c * __builtin_amdgcn_rcpf(1.0f + __builtin_amdgcn_exp2f(-1.4426950408889634f * c)));
    group_sync(lds0, 7, gs += 4, lane);
    float sv[10];
#pragma unroll
    for (int i = 0; i < 10; ++i) sv[i] = msc[(tid >> 4) + 16 * i];
    float acc[8] = {};
#pragma unroll
    for (int i = 0; i < 10; ++i)
#pragma unroll
      for (int j = 0; j < 4; ++j) { acc[2 * j] += sv[i] * bflo(mw[i][j]); acc[2 * j + 1] += sv[i] * bfhi(mw[i][j]); }
    treduce_xor<4>(acc, (lane & 32) != 0, 32);
    treduce_xor<2>(acc, (lane & 16) != 0, 16);
    LAS float* red = msc + 256;
    *(LAS f32x2*)(red + wave * 128 + (lane & 15) * 8 + (lane >> 4) * 2) = f32x2{acc[0], acc[1]};
    group_sync(lds0, 7, gs += 4, lane);
    if (tid < 128 && u < 120) put_granule(ws + WS_MOD + (size_t)(mpart * 3840 + mcol + tid) * 8, f2u((red[tid] + red[128 + tid]) + (red[256 + tid] + red[384 + tid])), tag);
  }
  __builtin_amdgcn_s_setprio(AUXB_PRIO);
  if (u < 108) {
    const int cbk = u >> 2, part = u & 3;
    const int c0 = 64 * cbk, r0 = 160 * part, seg = tid & 7, rg = tid >> 3;
    v4u w[5];
#pragma unroll
    for (int i = 0; i < 5; ++i) w[i] = wb[i];
    constexpr int OP = 400;
    LAS char* oimg = lds + 32768;
#pragma unroll
    for (int i = 0; i < 3; ++i) *(LAS v4u*)(oimg + rg * OP + (seg + 8 * i) * 16) = ov[i];
    LAS f32x2* mt2 = (LAS f32x2*)(lds + 28672);
    if (wave < 3) {
      unsigned mv[8];
      const int rw = r0 + (tid < 160 ? tid : 159);
      get_granules<8>(mv, tag, [&](int k) { return ws + WS_MOD + (size_t)((k & 3) * 3840 + (3 + (k >> 2)) * D + rw) * 8; });
      mt2[tid] = f32x2{bfr((u2f(mv[0]) + u2f(mv[1])) + (u2f(mv[2]) + u2f(mv[3]))), bfr(1.0f + bfr((u2f(mv[4]) + u2f(mv[5])) + (u2f(mv[6]) + u2f(mv[7]))))};
    }
    group_sync(lds0, 7, gs += 4, lane);
    STAMP(2);
    float v[16] = {};
    constexpr int WPITCH = 144;
    LAS char* img = lds + 1024;
#pragma unroll
    for (int i = 0; i < 5; ++i) {
      const f32x2 ms = mt2[rg + 32 * i];
      const float c1 = ms.y, sb = ms.x;
#pragma unroll
      for (int j = 0; j < 4; ++j) {
        const float a = bflo(w[i][j]), c = bfhi(w[i][j]);
        const unsigned pk = pkbf(a * c1, c * c1);
        v[8 + 2 * j] += sb * a; v[8 + 2 * j + 1] += sb * c;
        v[2 * j] += bflo(pk); v[2 * j + 1] += bfhi(pk);
        w[i][j] = pk;
      }
      *(LAS v4u*)(img + (rg + 32 * i) * WPITCH + seg * 16) = w[i];
    }
    treduce_xor<8>(v, (lane & 32) != 0, 32);
    treduce_xor<4>(v, (lane & 16) != 0, 16);
    treduce_dpp<2, 0x128>(v, (lane & 8) != 0);
    LAS float* red = (LAS float*)(lds + 24576 + 1024);
    {
      const int which = lane >> 5, j = ((lane >> 4) & 1) * 4 + ((lane >> 3) & 1) * 2;
      *(LAS f32x2*)(red + wave * 128 + which * 64 + seg * 8 + j) = f32x2{v[0], v[1]};
    }
    group_sync(lds0, 7, gs += 4, lane);
    STAMP(3);
    const int q = lane >> 4, rr = (lane & 15) >> 2, p = lane & 3;
    const int colb = (32 * (wave >> 1) + 8 * p + 4 * (wave & 1)) * 2;
#pragma unroll
    for (int i = 0; i < 5; ++i) {
      const int a0 = (32 * i + 8 * q + rr) * WPITCH + colb;
      v2u lo = trread(img + a0), hi = trread(img + a0 + 4 * WPITCH);
      st128(rs, (unsigned)WS_WIN + (((((part * 5 + i) >> 1) * 108 + 4 * cbk + wave) * 2 + ((part * 5 + i) & 1)) * 64 + lane) * 16, v4u{lo.x, lo.y, hi.x, hi.y});
    }
    {
      const int a0 = (8 * q + rr) * OP + p * 8 + wave * 32;
#pragma unroll
      for (int j = 0; j < 3; ++j) {
        const v2u lo = trread(oimg + a0 + j * 128), hi = trread(oimg + a0 + 4 * OP + j * 128);
        st128(rs, wave + 4 * j < 10 ? (unsigned)WS_WOUT + (((part * 10 + wave + 4 * j) * 27 + cbk) * 64 + lane) * 16 : 0x7ffffff0u, v4u{lo.x, lo.y, hi.x, hi.y});
      }
    }
    if (tid < 128) {
      const int which = tid >> 6, o = tid & 63, sub = o >> 4, i = o & 15;
      const int cl = 32 * (sub >> 1) + 8 * (i >> 2) + 4 * (sub & 1) + (i & 3), ix = which * 64 + cl;
      st32(rs, (unsigned)WS_U + ((part * 2 + which) * F + c0 + o) * 4, f2u((red[ix] + red[128 + ix]) + (red[256 + ix] + red[384 + ix])));
    }
  }
  STAMP(8);
  lds_wait(lds0, 8, 1);
  if (wave_arrive(lds0, 2, 4, lane)) signal<E1_REP, 8>(ws + CTL_E1B, u & 7, lane);
}

__device__ __forceinline__ int g1_aoff(int row, int G, int ab, int p) {
  const int f = ((row >> 1) & 1) | (((row >> 3) & 1) << 1);
  return row * 128 + (((4 * G + 2 * ab + (p >> 1)) ^ (f << 1)) << 4) + (p & 1) * 8;
}

template <int NB>
__device__ __forceinline__ void loader_path(LAS char* lds, char* ws, __amdgpu_buffer_rsrc_t rs, const u16* __restrict__ wsp, int wg, int mt, int j, int sub0, int lw, int lane) {
  const unsigned lbase = (unsigned)(size_t)lds, vo = lane * 16;
  const bool CNT7 = j < 6;
  {
    unsigned avo[2];
#pragma unroll
    for (int h = 0; h < 2; ++h) {
      const int row = lane >> 3, pos = lane & 7, f = ((row >> 1) & 1) | (h << 1);
      avo[h] = (row + 8 * h) * (L * 2) + ((pos ^ (f << 1)) << 4) - h * 1024;
    }
    const char* asb = ws + WS_X2T + (size_t)(16 * lw) * (L * 2) + mt * 128;
    const int p0 = (NB == 4 || CNT7) ? (lw < 2 ? 4 * lw : 2 + 3 * lw) : 3 * lw;
    const char* bsb = ws + WS_WIN + (size_t)(sub0 * 2 + p0) * 1024;
    const unsigned bb = lbase + G1_BOFF;
    auto issueB = [&](int s, unsigned slot) {
      gldsL<NB>(bsb + (s < 10 ? 108 * 1024 + s * 216 * 1024 : (s - 10) * 216 * 1024), vo, slot + p0 * 1024);
    };
    auto issueA = [&](int s0, int s1) {
#pragma unroll 1
      for (int s = s0; s < s1; ++s) gldsN<2, true>(asb + (size_t)(s * 64) * (L * 2), avo[0], avo[1], 0, 0, lbase + s * 8192 + lw * 2048);
    };
    {
      const unsigned vb = (unsigned)WS_V + (mt * 4 + lw) * 27 * 1024 + lane * 16;
#pragma unroll
      for (int k = 0; k < 3; ++k) st128(rs, vb + (3 * j + k) * 1024, v4u{~0u, ~0u, ~0u, ~0u});
      if (j < 6) __builtin_amdgcn_raw_buffer_store_b64(v2u{~0u, ~0u}, rs, vb + (24 + (j >> 1)) * 1024 + (j & 1) * 8, 0, 16);
    }
    group_sync(lds, 5, 24, lane);
    if (lw == 0) { wait_count<8>(ws + CTL_E1B, wg & (E1_REP - 1), 128, lane); lds_post(lds, 10, lane); }
    if (lw == 3) { wait_count<8>(ws + CTL_E1, wg & (E1_REP - 1), 256, lane); lds_post(lds, 11, lane); }
    lds_wait(lds, 10, 1);
    STAMP(13);
#pragma unroll
    for (int s = 0; s < 4; ++s) issueB(s, bb + (s + 1) * G1_BST);
    lds_wait(lds, 11, 1);
    lds_wait(lds, 1, 4);
    issueA(0, 4);
    STAMP(12);
    LBAR();
    LSTAMP(1); STAMP(14);
    issueB(4, bb);
    waitv<6 + NB>(); LBAR(); LSTAMP(2);
    waitv<4 + NB>(); LBAR(); issueB(5, bb + G1_BST);
    waitv<2 + 2 * NB>(); LBAR(); LSTAMP(3); issueA(4, 8); issueB(6, bb + 2 * G1_BST);
    waitv<8 + 3 * NB>(); LBAR(); issueB(7, bb + 3 * G1_BST);
    waitv<6 + 2 * NB>(); LBAR(); LSTAMP(4); issueB(8, bb + 4 * G1_BST);
    waitv<4 + 3 * NB>(); LBAR(); LSTAMP(5); issueA(8, 10); issueB(9, bb);
    waitv<4 + 3 * NB>(); LBAR(); issueB(10, bb + G1_BST);
    waitv<4 + 3 * NB>(); LBAR(); issueB(11, bb + 2 * G1_BST);
    waitv<2 + 3 * NB>(); LBAR(); LSTAMP(6); issueB(12, bb + 3 * G1_BST);
    int nxt = 4 * G1_BST;
#pragma unroll 1
    for (int i = 9; i <= 15; ++i) {
      waitv<NB * 3>(); LBAR();
      if (i >= 11) LSTAMP(i - 4);
      issueB(i + 4, bb + nxt);
      nxt += G1_BST; if (nxt == G1_NSLOT * G1_BST) nxt = 0;
    }
    waitv<NB * 3>(); LBAR(); LSTAMP(12);
    waitv<NB * 2>(); LBAR(); LSTAMP(13);
    waitv<NB * 1>(); LBAR();
    waitv<0>(); LBAR(); LSTAMP(14);
    LBAR();
  }
  const char* osb = ws + WS_WOUT + (size_t)j * 135 * 1024;
  if (lw >= 2) {
    unsigned wvo[4];
#pragma unroll
    for (int h = 0; h < 4; ++h) {
      const int i = 8 * (h & 1) + (lane >> 3);
      wvo[h] = (8 * (i >> 2) + (i & 3) + 4 * (h >> 1)) * (L * 2) + (((lane & 7) ^ (i >> 1)) << 4) - h * 1024;
    }
    const char* wsb = (const char*)wsp + (size_t)(mt * 64 + 32 * (lw & 1)) * (L * 2);
    auto issueW = [&](int s, unsigned slot) {
      gldsN<4, false>(wsb + s * 128, wvo[0], wvo[1], wvo[2], wvo[3], slot + (lw & 1) * 4096);
    };
#pragma unroll 1
    for (int s = 0; s < G2_NW; ++s) issueW(s, lbase + s * 8192);
    LBAR();
    waitv<32>(); LBAR();
    int wn = 0;
#pragma unroll 1
    for (int i = 1; i <= 23; ++i) { waitv<28>(); LBAR(); issueW(i + 8, lbase + wn); wn += 8192; if (wn == G2_NW * 8192) wn = 0; }
#pragma unroll 1
    for (int i = 24; i <= 32; ++i) {
      waitv<28>(); LBAR();
      gldsL<4>(osb + wn + (lw & 1) * 4096, vo, lbase + wn + (lw & 1) * 4096);
      wn += 8192; if (wn == G2_NW * 8192) wn = 0;
    }
  } else {
    constexpr int NZ = NB == 4 ? 7 : 6;
    const char* zsb = ws + WS_Z2 + (size_t)(sub0 * 2 + lw * NZ) * 1024;
    auto issueZ = [&](int s, unsigned slot) {
      gldsL<4>(zsb + s * 108 * 1024, vo, slot + lw * NZ * 1024);
      gldsL<NZ - 4>(zsb + s * 108 * 1024 + 4096, vo, slot + lw * NZ * 1024 + 4096);
    };
    const unsigned zb = lbase + G2_ZOFF;
    LBAR();
#pragma unroll 1
    for (int s = 0; s < G2_NZ; ++s) issueZ(s, zb + s * G2_ZST);
    waitv<NZ * 5>(); LBAR();
    int zn = 0;
#pragma unroll 1
    for (int i = 1; i <= 26; ++i) { waitv<NZ * 4>(); LBAR(); issueZ(i + 5, zb + zn); zn += G2_ZST; if (zn == G2_NZ * G2_ZST) zn = 0; }
    auto issueO = [&](int slot, int n) {
      const int o = G2_ZOFF + slot * G2_ZST + lw * n * 1024;
      if (n == 7) { gldsL<4>(osb + o, vo, lbase + o); gldsL<3>(osb + o + 4096, vo, lbase + o + 4096); }
      else gldsL<3>(osb + o, vo, lbase + o);
    };
    waitv<NZ * 4>(); LBAR(); issueO(2, 7);
    waitv<NZ * 3 + 7>(); LBAR(); issueO(3, 7);
    waitv<NZ * 2 + 14>(); LBAR(); issueO(4, 3);
    waitv<NZ + 17>(); LBAR(); if (lw == 0) gldsL<1>(osb + 134 * 1024, vo, lbase + 134 * 1024);
    waitv<17>(); LBAR(); issueO(0, 7);
    LBAR(); issueO(1, 7);
  }
  waitv<0>();
  LBAR();
}

template <int NSUB, bool STATS>
__device__ __forceinline__ void consumer_g12(LAS char* lds, char* ws, __amdgpu_buffer_rsrc_t rs, const u16* __restrict__ bsp, const u16* __restrict__ wsp, int wg, int mt, int j, int sub0, int cnt,
                                             int wave, int tid, int lane, v2u (&x2v)[3] STAMP_ARG) {
  constexpr int NS1 = NSUB > 0 ? NSUB : 1;
  const int G = wave >> 2, ng = G ? 3 - (wave & 3) : (wave & 3);
  const int q = lane >> 4, i16 = lane & 15, rr = i16 >> 2, p = lane & 3, n0 = 2 * ng;
  float bias[2];
#pragma unroll
  for (int m = 0; m < 2; ++m) bias[m] = bf2f(bsp[mt * 64 + 32 * G + 8 * (i16 >> 2) + 4 * m + (i16 & 3)]);
  STAMP(4);
  CBAR();
  v2u z1v[NS1][2];
  {
    const int aoff0 = g1_aoff(8 * q + rr, G, 0, p), aoff1 = g1_aoff(8 * q + rr, G, 1, p);
    const int boff = G1_BOFF + n0 * 2048 + lane * 16;
    struct Frags { v4u a[2][2]; v4u b[2][NS1]; };
    f32x4 acc2[NS1][2], acc1[NS1][2], asum[2], asq[2];
    int acur = 0, bcur = G1_BST;
    auto rd = [&](Frags& f) {
#pragma unroll
      for (int kk = 0; kk < 2; ++kk) {
        const v2u l0 = trread(lds + acur + aoff0 + kk * 4096), h0 = trread(lds + acur + aoff0 + kk * 4096 + 512);
        const v2u l1 = trread(lds + acur + aoff1 + kk * 4096), h1 = trread(lds + acur + aoff1 + kk * 4096 + 512);
        f.a[kk][0] = v4u{l0.x, l0.y, h0.x, h0.y}; f.a[kk][1] = v4u{l1.x, l1.y, h1.x, h1.y};
#pragma unroll
        for (int n = 0; n < NSUB; ++n) f.b[kk][n] = *(LAS const v4u*)(lds + bcur + boff + n * 2048 + kk * 1024);
      }
      acur += 8192; if (acur == 81920) acur = 0;
      bcur += G1_BST; if (bcur == G1_NSLOT * G1_BST) bcur = 0;
    };
    auto pin1 = [&]() {
#pragma unroll
      for (int n = 0; n < NSUB; ++n) asm volatile("" : "+v"(acc1[n][0]), "+v"(acc1[n][1]));
    };
#pragma unroll
    for (int ab = 0; ab < 2; ++ab) {
      asum[ab] = f32x4{0.f, 0.f, 0.f, 0.f}; asq[ab] = f32x4{0.f, 0.f, 0.f, 0.f};
#pragma unroll
      for (int n = 0; n < NS1; ++n) { acc2[n][ab] = f32x4{0.f, 0.f, 0.f, 0.f}; acc1[n][ab] = f32x4{0.f, 0.f, 0.f, 0.f}; }
    }
    const v4u ones = {0x3f803f80u, 0x3f803f80u, 0x3f803f80u, 0x3f803f80u};
    auto mm2 = [&](const Frags& f) {
#pragma unroll
      for (int kk = 0; kk < 2; ++kk)
#pragma unroll
        for (int ab = 0; ab < 2; ++ab) {
#pragma unroll
          for (int n = 0; n < NSUB; ++n) acc2[n][ab] = mfma16(f.a[ABL_KK][ab], f.b[ABL_KK][n], acc2[n][ab]);
          if (STATS) { asum[ab] = mfma16(f.a[kk][ab], ones, asum[ab]); asq[ab] = mfma16(f.a[kk][ab], f.a[kk][ab], asq[ab]); }
        }
      INTERLEAVE(6, 2);
    };
    auto mm1 = [&](const Frags& f) {
#pragma unroll
      for (int kk = 0; kk < 2; ++kk)
#pragma unroll
        for (int ab = 0; ab < 2; ++ab)
#pragma unroll
          for (int n = 0; n < NSUB; ++n) acc1[n][ab] = mfma16(f.b[kk][n], f.a[kk][ab], acc1[n][ab]);
      if (NSUB == 2) INTERLEAVE(6, 2); else INTERLEAVE(4, 3);
    };
    Frags f0, f1;
    CBAR(); rd(f0);
    SCHED_BAR();
    float up[4];
    {
      const int c = tid & 127, which = (tid >> 7) & 1, half = tid >> 8;
#pragma unroll
      for (int k = 0; k < 4; ++k) up[k] = ld32f(rs, c < cnt * 16 ? (unsigned)WS_U + ((k * 2 + which) * F + (sub0 + (half ? 0 : 54)) * 16 + c) * 4 : 0x7ffffff0u);
    }
    SCHED_BAR();
    STAMP(5);
    auto pin2 = [&]() {
#pragma unroll
      for (int n = 0; n < NSUB; ++n) asm volatile("" : "+v"(acc2[n][0]), "+v"(acc2[n][1]));
      asm volatile("" : "+v"(asum[0]), "+v"(asum[1]), "+v"(asq[0]), "+v"(asq[1]));
    };
    if (STATS) {
      char* cb = ws + CTL_E1 + (G + 1) * 65536;
      unsigned pc;
      poll_issue<8>(pc, cb, wg & (E1_REP - 1), lane);
      CBAR(); rd(f1); mm2(f0);
      if (G == 0) poll_finish<8>(pc, cb, wg & (E1_REP - 1), 256, lane);
      CBAR(); pin2(); rd(f0); mm2(f1);
      CBAR(); rd(f1); mm2(f0);
      poll_issue<8>(pc, cb, wg & (E1_REP - 1), lane);
      CBAR(); rd(f0); mm2(f1);
      if (G == 1) poll_finish<8>(pc, cb, wg & (E1_REP - 1), 128, lane);
      asm volatile("" :: "v"(pc));
      pin2();
    }
#pragma unroll 1
    for (int it = STATS ? 2 : 0; it < 5; ++it) {
      CBAR(); rd(f1); mm2(f0);
      CBAR(); rd(f0); mm2(f1);
    }
    ((LAS float*)(lds + G1_T_U))[tid] = (up[0] + up[1]) + (up[2] + up[3]);
    unsigned pv = 0;
    if (!STATS && ng < 2) pv = *(const unsigned*)((const char*)wsp + (size_t)(mt * 64 + (G * 2 + ng) * 16 + (lane >> 2)) * (L * 2) + j * 512 + (lane & 3) * 128);
    LAS f32x2* tab = (LAS f32x2*)(lds + G1_T_LN2);
    if (STATS) {
#pragma unroll
      for (int ab = 0; ab < 2; ++ab) {
        const float a0 = asum[ab][0], a1 = asum[ab][1], a2 = asum[ab][2], a3 = asum[ab][3];
        const float b0 = asq[ab][0], b1 = asq[ab][1], b2 = asq[ab][2], b3 = asq[ab][3];
        const float s1 = (p & 2) ? ((p & 1) ? a3 : a2) : ((p & 1) ? a1 : a0), s2 = (p & 2) ? ((p & 1) ? b3 : b2) : ((p & 1) ? b1 : b0);
        const float mean = s1 * (1.0f / D);
        float mb, rinv;
        ln_scalars(mean, __builtin_fmaxf(s2 * (1.0f / D) - mean * mean, 0.f), mb, rinv);
        if (rr == q) tab[(G * 2 + ab) * 16 + i16] = f32x2{rinv, rinv * mb};
      }
    }
    {
      const int nb = G ? 3 : 0;
#pragma unroll
      for (int n = 0; n < 3; ++n) {
        const int row = 80 * j + 16 * (nb + n < 5 ? nb + n : 4) + 4 * q + rr;
        x2v[n] = trread(lds + g1_aoff(row, (wave >> 1) & 1, wave & 1, p));
      }
    }
    STAMP(6);
    CBAR(); rd(f1); mm1(f0);
    float zq[NS1][8];
    {
      LAS const float* ut = (LAS const float*)(lds + G1_T_U);
      float v[16];
#pragma unroll
      for (int e = 0; e < 16; ++e) v[e] = 0.f;
      if (NSUB > 0) {
        f32x2 rv[8];
#pragma unroll
        for (int e = 0; e < 8; ++e) rv[e] = tab[(G * 2 + (e >> 2)) * 16 + 4 * q + (e & 3)];
#pragma unroll
        for (int n = 0; n < NSUB; ++n) {
          const float c1 = ut[(n0 + n) * 16 + i16], c2 = ut[128 + (n0 + n) * 16 + i16];
#pragma unroll
          for (int e = 0; e < 8; ++e) {
            const float z = gelu_f(__builtin_fmaf(rv[e].x, acc2[n][e >> 2][e & 3], __builtin_fmaf(-rv[e].y, c1, c2)));
            zq[n][e] = z;
            v[2 * e] = n ? v[2 * e] + z : z; v[2 * e + 1] = n ? __builtin_fmaf(z, z, v[2 * e + 1]) : z * z;
          }
        }
        treduce_dpp<8, 0x140>(v, (i16 & 8) != 0);
        treduce_dpp<4, 0x141>(v, (i16 & 4) != 0);
        treduce_dpp<2, 0x4e>(v, (i16 & 2) != 0);
        treduce_dpp<1, 0xb1>(v, (i16 & 1) != 0);
      }
      ((LAS float*)(lds + G1_T_P))[ng * 128 + G * 64 + lane] = v[0];
    }
    CBAR(); pin1();
    GAS unsigned long long* gr = (GAS unsigned long long*)(ws + CTL_ST3) + (size_t)mt * 1024 + G * 32;
    if (STATS) {
      LAS const float* pp = (LAS const float*)(lds + G1_T_P) + G * 64 + lane;
      const float sv = (pp[0] + pp[128]) + (pp[256] + pp[384]);
      __hip_atomic_store(gr + (j * 2 + (lane & 1)) * 64 + (lane >> 1), (1ull << 32) | f2u(sv), RLX_AGENT);
    }
    SCHED_BAR(); rd(f0); mm1(f1);
    CBAR(); pin1(); rd(f1); mm1(f0);
    CBAR(); pin1(); rd(f0); mm1(f1);
    unsigned long long gx[8];
    if (STATS) {
#pragma unroll
      for (int k = 0; k < 8; ++k) gx[k] = __hip_atomic_load(gr + (k * 2 + (lane >> 5)) * 64 + (lane & 31), RLX_AGENT);
    }
    CBAR(); pin1(); rd(f1); mm1(f0);
    if (STATS) {
      float sv;
      for (unsigned spin = 0;; ++spin) {
        bool ok = true;
        sv = 0.f;
#pragma unroll
        for (int k = 0; k < 8; ++k) { ok &= (gx[k] >> 32) == 1ull; sv += u2f((unsigned)gx[k]); }
        if (wall(ok) || spin > SPIN_CAP) break;
        __builtin_amdgcn_s_sleep(1);
#pragma unroll
        for (int k = 0; k < 8; ++k) gx[k] = __hip_atomic_load(gr + (k * 2 + (lane >> 5)) * 64 + (lane & 31), RLX_AGENT);
      }
      const float s2 = shx(sv, 32);
      const float mean = sv * (1.0f / FH);
      float mb, rinv;
      ln_scalars(mean, __builtin_fmaxf(s2 * (1.0f / FH) - mean * mean, 0.f), mb, rinv);
      if (lane < 32) ((LAS f32x2*)(lds + G1_T_Z))[32 * G + lane] = f32x2{-mb * rinv, rinv};
    }
    CBAR(); pin1();
    if (NSUB > 0) {
      f32x2 zs[8];
#pragma unroll
      for (int e = 0; e < 8; ++e) zs[e] = ((LAS const f32x2*)(lds + G1_T_Z))[32 * G + 8 * q + e];
#pragma unroll
      for (int n = 0; n < NSUB; ++n) {
        v4u o;
#pragma unroll
        for (int e = 0; e < 4; ++e)
          o[e] = pkbf(__builtin_fmaf(zq[n][2 * e], zs[2 * e].y, zs[2 * e].x), __builtin_fmaf(zq[n][2 * e + 1], zs[2 * e + 1].y, zs[2 * e + 1].x));
        st128(rs, (unsigned)WS_Z2 + (((mt * 54 + sub0 + n0 + n) * 2 + G) * 64 + lane) * 16, o);
      }
    }
    SCHED_BAR(); rd(f0); mm1(f1);
    CBAR(); pin1(); rd(f1); mm1(f0);
    CBAR(); pin1(); rd(f0); mm1(f1);
    CBAR(); pin1(); rd(f1); mm1(f0);
    if (wave_arrive(lds, 3, 8, lane)) signal<4, 4>(ws + CTL_E2 + j * 4096, mt & 3, lane);
    asm volatile("" :: "v"(pv));
    CBAR(); pin1();
    mm1(f1);
    STAMP(7);
    {
      LAS const float* ut = (LAS const float*)(lds + G1_T_U) + 256;
      f32x2 rv[2];
#pragma unroll
      for (int ab = 0; ab < 2; ++ab) rv[ab] = tab[(G * 2 + ab) * 16 + i16];
#pragma unroll
      for (int n = 0; n < NSUB; ++n) {
        const f32x4 u1 = *(LAS const f32x4*)(ut + (n0 + n) * 16 + 4 * q), u2 = *(LAS const f32x4*)(ut + 128 + (n0 + n) * 16 + 4 * q);
#pragma unroll
        for (int ab = 0; ab < 2; ++ab) {
          float z[4];
#pragma unroll
          for (int e = 0; e < 4; ++e) z[e] = gelu_f(__builtin_fmaf(rv[ab].x, acc1[n][ab][e], __builtin_fmaf(-rv[ab].y, u1[e], u2[e])));
          z1v[n][ab] = v2u{pkbf(z[0], z[1]), pkbf(z[2], z[3])};
          asm volatile("" : "+v"(z1v[n][ab]));
        }
      }
    }
  }
  if (wave == 1 || wave == 2) {
    const int c = tid - 64;
    if (c < 80) {
      const unsigned o = (unsigned)WS_MOD + (5 * D + 80 * j + c) * 8;
      ((LAS float*)(lds + G3_GB))[c] = bfr((ld32f(rs, o) + ld32f(rs, o + 30720)) + (ld32f(rs, o + 61440) + ld32f(rs, o + 92160)));
    }
  }
  if (wave == 3) wait_count<4>(ws + CTL_E2 + j * 4096, (mt >> 2) & 3, 32, lane);
  CBAR();
  STAMP(9);
  {
    f32x4 acc[NS1][2];
#pragma unroll
    for (int n = 0; n < NS1; ++n) { acc[n][0] = f32x4{0.f, 0.f, 0.f, 0.f}; acc[n][1] = f32x4{0.f, 0.f, 0.f, 0.f}; }
    int woff[2];
#pragma unroll
    for (int kk = 0; kk < 2; ++kk) woff[kk] = G * 4096 + i16 * 128 + (((4 * kk + q) ^ (i16 >> 1)) << 4);
    const int zoff = G2_ZOFF + n0 * 2048 + lane * 16;
    struct Frags { v4u w[2][2]; v4u z[2][NS1]; };
    int wc = 0, zc = 0;
    auto rd = [&](Frags& f) {
      if (NSUB > 0) {
#pragma unroll
        for (int kk = 0; kk < 2; ++kk) {
#pragma unroll
          for (int m = 0; m < 2; ++m) f.w[kk][m] = *(LAS const v4u*)(lds + wc + woff[kk] + m * 2048);
#pragma unroll
          for (int n = 0; n < NSUB; ++n) f.z[kk][n] = *(LAS const v4u*)(lds + zc + zoff + n * 2048 + kk * 1024);
        }
        wc += 8192; if (wc == G2_NW * 8192) wc = 0;
        zc += G2_ZST; if (zc == G2_NZ * G2_ZST) zc = 0;
      }
    };
    auto mm = [&](const Frags& f) {
#pragma unroll
      for (int kk = 0; kk < 2; ++kk)
#pragma unroll
        for (int n = 0; n < NSUB; ++n)
#pragma unroll
          for (int m = 0; m < 2; ++m) acc[n][m] = mfma16(f.z[ABL_KK][n], f.w[ABL_KK][m], acc[n][m]);
      INTERLEAVE(4, 2);
    };
    Frags f0, f1;
    CBAR(); rd(f0);
    STAMP(10);
#pragma unroll 1
    for (int it = 0; it < 15; ++it) {
      CBAR(); rd(f1); mm(f0);
      CBAR(); rd(f0); mm(f1);
    }
    CBAR(); rd(f1); mm(f0);
    mm(f1);
    STAMP(11);
#pragma unroll
    for (int m = 0; m < 2; ++m) {
      v2u o[NS1];
#pragma unroll
      for (int n = 0; n < NSUB; ++n) {
        float g[4];
#pragma unroll
        for (int e = 0; e < 4; ++e) g[e] = acc[n][m][e] + bias[m];
        const v2u zz = z1v[n][m];
        o[n] = v2u{pkbf(bflo(zz.x) * g[0], bfhi(zz.x) * g[1]), pkbf(bflo(zz.y) * g[2], bfhi(zz.y) * g[3])};
      }
      const unsigned vb = (unsigned)WS_V + (mt * 4 + G * 2 + m) * 27 * 1024 + lane * 16;
      if (NSUB == 2) st128(rs, vb + (3 * j + ng) * 1024, v4u{o[0].x, o[0].y, o[NS1 - 1].x, o[NS1 - 1].y});
      if (NSUB == 1) __builtin_amdgcn_raw_buffer_store_b64(o[0], rs, vb + (24 + (j >> 1)) * 1024 + (j & 1) * 8, 0, 16);
    }
    CBAR();
  }
}

template <int KH>
__device__ __forceinline__ void consumer_g3(LAS char* lds, char* ws, __amdgpu_buffer_rsrc_t rs, u16* __restrict__ out, int mt, int j, int m, int lane, const v2u (&x2v)[3] STAMP_ARG) {
  constexpr int K0 = KH ? 14 : 0, NK = KH ? 13 : 14, NF = KH ? 2 : 3, N0 = KH ? 3 : 0;
  const int q = lane >> 4, i16 = lane & 15;
  v4u vf[NK];
  __builtin_amdgcn_s_sleep(V_DELAY);
  CBAR();
  STAMP(12);
  const unsigned vo = (unsigned)WS_V + ((mt * 4 + m) * 27 + K0) * 1024 + lane * 16;
  constexpr int VW = V_WIN;
#pragma unroll
  for (int k = 0; k < VW; ++k) { vf[k] = ld128(rs, vo + k * 1024); SCHED_BAR(); }
  f32x4 acc[5];
#pragma unroll
  for (int n = 0; n < 5; ++n) acc[n] = f32x4{0.f, 0.f, 0.f, 0.f};
  LAS const char* wp = lds + K0 * 1024 + lane * 16;
  LAS const char* wp2 = wp + 54 * 1024;
  LAS const char* wp4 = wp + 108 * 1024;
  v4u wf[3][5];
  auto rdw = [&](int k, v4u (&w)[5]) {
    w[0] = *(LAS const v4u*)(wp + k * 1024); w[1] = *(LAS const v4u*)(wp + (27 + k) * 1024);
    w[2] = *(LAS const v4u*)(wp2 + k * 1024); w[3] = *(LAS const v4u*)(wp2 + (27 + k) * 1024);
    w[4] = *(LAS const v4u*)(wp4 + k * 1024);
  };
  rdw(0, wf[0]); rdw(1, wf[1]);
#pragma unroll
  for (int k = 0; k < NK; ++k) {
    if (k + VW < NK) vf[k + VW] = ld128(rs, vo + (k + VW) * 1024);
    if (k + 2 < NK) rdw(k + 2, wf[(k + 2) % 3]);
    SCHED_BAR();
    if (__builtin_expect(!wall(__builtin_elementwise_max(vf[k].x, vf[k].z) != ~0u), 0)) {
      unsigned spin = 0;
      do { asm volatile("" ::: "memory"); vf[k] = ld128(rs, vo + k * 1024); }
      while (!wall(__builtin_elementwise_max(vf[k].x, vf[k].z) != ~0u) && ++spin < SPIN_CAP);
    }
#pragma unroll
    for (int n = 0; n < 5; ++n) acc[n] = mfma16(wf[k % 3][n], vf[k], acc[n]);
    asm volatile("" : "+v"(acc[0]), "+v"(acc[1]), "+v"(acc[2]), "+v"(acc[3]), "+v"(acc[4]));
    SCHED_BAR();
  }
  STAMP(13);
  f32x4 gb[NF];
#pragma unroll
  for (int n = 0; n < NF; ++n) gb[n] = *(LAS const f32x4*)(lds + G3_GB + (16 * (N0 + n) + 4 * q) * 4);
  LAS char* red = lds + G3_RED + m * 5120 + lane * 16;
#pragma unroll
  for (int n = 0; n < 5; ++n) if ((n >= 3) != (KH != 0)) *(LAS f32x4*)(red + n * 1024) = acc[n];
  CBAR();
  const int t = mt * 64 + 32 * (m >> 1) + 8 * (i16 >> 2) + 4 * (m & 1) + (i16 & 3);
#pragma unroll
  for (int n = 0; n < NF; ++n) {
    const f32x4 o = *(LAS const f32x4*)(red + (N0 + n) * 1024);
    const f32x4 a = KH ? o + acc[N0 + n] : acc[N0 + n] + o;
    const float o0 = __builtin_fmaf(gb[n][0], a[0], bflo(x2v[n].x)), o1 = __builtin_fmaf(gb[n][1], a[1], bfhi(x2v[n].x));
    const float o2 = __builtin_fmaf(gb[n][2], a[2], bflo(x2v[n].y)), o3 = __builtin_fmaf(gb[n][3], a[3], bfhi(x2v[n].y));
    *(v2u*)((char*)out + ((size_t)t * D + 80 * j + 16 * (N0 + n) + 4 * q) * 2) = v2u{pkbf(o0, o1), pkbf(o2, o3)};
  }
}

__global__ __launch_bounds__(768, 3) void k_all(Args A) {
  extern __shared__ __attribute__((aligned(16))) char smem[];
  unsigned long long pc;
  asm volatile("s_getpc_b64 %0" : "=s"(pc));
  LAS char* lds = (LAS char*)smem;
  char* ws = A.ws;
  const int tid = __builtin_amdgcn_workitem_id_x(), lane = tid & 63, wave = __builtin_amdgcn_readfirstlane(tid >> 6), wg = __builtin_amdgcn_workgroup_id_x();
  STAMP_DECL; STAMP(0);
  const __amdgpu_buffer_rsrc_t rs = __builtin_amdgcn_make_buffer_rsrc((void*)ws, 0, (int)WS_END, 0x00020000);
  asm volatile("" :: "s"(A.x), "s"(A.Are), "s"(A.Aim), "s"(A.Bre), "s"(A.Bim), "s"(A.Cre), "s"(A.Cim), "s"(A.logdt), "s"(A.dskip), "s"(A.win), "s"(A.wsp),
               "s"(A.bsp), "s"(A.wout), "s"(A.cvec), "s"(A.wmod), "s"(A.ws), "s"(A.out));
  const int grp = wave >> 2, bt = grp == 2 ? 0 : grp + 1, gw = wave & 3, gt = tid & 255;
  const bool isaux = bt == 2 && wg >= 128;
  const int u = wg - 128, d = 256 * bt + (wg & 7) * (bt < 2 ? 32 : 16) + (wg >> 3);
  unsigned epoch;
  asm volatile("global_load_dword %0, %1, %2" : "=v"(epoch) : "v"((unsigned)CTL_EPOCH), "s"(ws) : "memory");
  if (__builtin_expect(isaux, 1)) {
    const int rr = lane >> 4;
    const unsigned vo = rr * (D * 2) + (((lane & 15) ^ ((rr | ((gw >> 1) << 2)) << 1)) << 4);
    const char* xb = (const char*)A.x + (size_t)(u * 16 + gw * 4) * (D * 2);
#pragma unroll
    for (int i = 0; i < 5; ++i) glds16nt(xb + i * 256, vo, (unsigned)(size_t)lds + C_GRP + 8192 + (gw * 5 + i) * 1024);
    const int zc = (u * 4 + gw) * 3 + lane - 32;
    st128(rs, lane < 32 ? (unsigned)CTL_ST3 + ((u * 4 + gw) * 32 + lane) * 16 : (lane < 35 && zc < 1280 ? (unsigned)zc * 256 : 0x7ffffff0u), v4u{0u, 0u, 0u, 0u});
  }
  unsigned pre[7] = {};
  if (__builtin_expect(!isaux, 1)) {
    const unsigned o = (d * NS + (gw & 1) * 64 + lane) * 2;
#define LDU16(dst, base, voff) asm volatile("global_load_ushort %0, %1, %2" NT : "=v"(dst) : "v"(voff), "s"(base) : "memory")
    LDU16(pre[0], A.Are, o); LDU16(pre[1], A.Aim, o); LDU16(pre[2], A.logdt, d * 2); LDU16(pre[3], A.Bre, o);
    LDU16(pre[4], A.Bim, o); LDU16(pre[5], A.Cre, o); LDU16(pre[6], A.Cim, o);
  }
  STAMP(11);
  if (tid < 16) ((LAS unsigned*)(lds + M_CTL))[tid] = 0u;
  unsigned tag = 0;
  CBAR();
  {
    LAS char* gl = lds + (grp == 2 ? G1_TAB - C_GRP : grp * C_GRP);
    if (bt == 0) __builtin_amdgcn_s_setprio(2);
    if (bt == 1) __builtin_amdgcn_s_setprio(1);
    if (isaux) { __builtin_amdgcn_s_setprio(3); aux_group(gl, lds, A, rs, epoch, u, gt, gw, lane STAMP_PASS); }
    else conv_group(gl, lds, bt, A, rs, epoch, tag, pre, pc, wg, d, gt, gw, lane STAMP_PASS);
    __builtin_amdgcn_s_setprio(0);
  }
  const int xcd = wg & 7, idx = wg >> 3;
  const int mt = (xcd >> 1) * 8 + (idx & 7), j = (xcd & 1) * 4 + (idx >> 3);
  const int cnt = j < 6 ? 7 : 6, sub0 = j < 6 ? 7 * j : 42 + 6 * (j - 6);
  if (wave >= 8) {
    STAMP(4);
    const int lw = wave - 8;
    if (lw + 12 < 2 * cnt) loader_path<4>(lds, ws, rs, A.wsp, wg, mt, j, sub0, lw, lane);
    else loader_path<3>(lds, ws, rs, A.wsp, wg, mt, j, sub0, lw, lane);
    if (wg == 0 && tid == 512) *(volatile unsigned*)(ws + CTL_EPOCH) = tag;
    STAMP_FLUSH(1);
    return;
  }
  v2u x2v[3];
  const int ng = wave >> 2 ? 3 - (wave & 3) : (wave & 3);
  if (ng < 3) consumer_g12<2, false>(lds, ws, rs, A.bsp, A.wsp, wg, mt, j, sub0, cnt, wave, tid, lane, x2v STAMP_PASS);
  else if (cnt == 7) consumer_g12<1, true>(lds, ws, rs, A.bsp, A.wsp, wg, mt, j, sub0, cnt, wave, tid, lane, x2v STAMP_PASS);
  else consumer_g12<0, true>(lds, ws, rs, A.bsp, A.wsp, wg, mt, j, sub0, cnt, wave, tid, lane, x2v STAMP_PASS);
  if (wave < 4) consumer_g3<0>(lds, ws, rs, A.out, mt, j, wave & 3, lane, x2v STAMP_PASS);
  else consumer_g3<1>(lds, ws, rs, A.out, mt, j, wave & 3, lane, x2v STAMP_PASS);
  STAMP(14);
  STAMP_FLUSH(1);
}

#if defined(__HIP_DEVICE_COMPILE__) && !defined(KEEP_PK)
#pragma clang attribute pop
#endif

extern "C" void kernel_launch(void* const* d_in, const int* in_sizes, int n_in, void* d_out, int out_size, void* d_ws, size_t ws_size,
                              hipStream_t stream) {
  Args A;
  A.x = (const u16*)d_in[0]; A.Are = (const u16*)d_in[1]; A.Aim = (const u16*)d_in[2]; A.Bre = (const u16*)d_in[3]; A.Bim = (const u16*)d_in[4];
  A.Cre = (const u16*)d_in[5]; A.Cim = (const u16*)d_in[6]; A.logdt = (const u16*)d_in[7]; A.dskip = (const u16*)d_in[8];
  A.win = (const u16*)d_in[10]; A.wsp = (const u16*)d_in[11]; A.bsp = (const u16*)d_in[12]; A.wout = (const u16*)d_in[13];
  A.cvec = (const u16*)d_in[14]; A.wmod = (const u16*)d_in[15];
  A.ws = (char*)d_ws; A.out = (u16*)d_out;
  (void)hipFuncSetAttribute((const void*)k_all, hipFuncAttributeMaxDynamicSharedMemorySize, M_LDS);
  k_all<<<256, 768, M_LDS, stream>>>(A);
}
```

```cpp
#include <hip/hip_runtime.h>
#include <hip/hip_bf16.h>
#include <stdint.h>

#define NT ""
#define NTLOAD(p) (*(p))
#define GAS __attribute__((address_space(1)))
#define LAS __attribute__((address_space(3)))
typedef unsigned v4u __attribute__((ext_vector_type(4)));
typedef unsigned v2u __attribute__((ext_vector_type(2)));
typedef float f32x2 __attribute__((ext_vector_type(2)));
typedef float f32x4 __attribute__((ext_vector_type(4)));
typedef float f32x16 __attribute__((ext_vector_type(16)));
typedef __bf16 bf16x2 __attribute__((ext_vector_type(2)));
typedef __bf16 bf16x8 __attribute__((ext_vector_type(8)));
typedef _Float16 f16x2 __attribute__((ext_vector_type(2)));
typedef _Float16 f16x8 __attribute__((ext_vector_type(8)));
typedef short s16x4 __attribute__((ext_vector_type(4)));
typedef unsigned short u16;

#if defined(__HIP_DEVICE_COMPILE__) && !defined(KEEP_PK)
#pragma clang attribute push(__attribute__((target("no-packed-fp32-ops"))), apply_to = function)
#endif
__device__ __forceinline__ float u2f(unsigned u) { return __builtin_bit_cast(float, u); }
__device__ __forceinline__ unsigned f2u(float f) { return __builtin_bit_cast(unsigned, f); }
__device__ __forceinline__ int lane_id() { return __builtin_amdgcn_mbcnt_hi(~0u, __builtin_amdgcn_mbcnt_lo(~0u, 0u)); }
__device__ __forceinline__ unsigned shx(unsigned v, int m) { return __builtin_amdgcn_ds_bpermute((lane_id() ^ m) << 2, v); }
__device__ __forceinline__ float shx(float v, int m) { return u2f(shx(f2u(v), m)); }
__device__ __forceinline__ bool wall(bool p) { return __builtin_amdgcn_ballot_w64(!p) == 0ull; }
__device__ __forceinline__ float exp_acc(float x) {
  const float hi = x * 1.4426950408889634f;
  const float lo = __builtin_fmaf(x, 1.4426950408889634f, -hi) + x * 1.925963033500414e-8f;
  const float e = __builtin_amdgcn_exp2f(hi);
  return __builtin_fmaf(e, lo * 0.6931471805599453f, e);
}

constexpr int L = 2048, D = 640, NS = 128, F = 1728, FH = 864;

constexpr size_t CTL_E1 = 0;
constexpr size_t CTL_E1B = 196608;
constexpr size_t CTL_E2 = 262144;
constexpr size_t CTL_E3 = 294912;
constexpr size_t CTL_EW = 303104;
constexpr size_t CTL_ST3 = 327680;
constexpr size_t CTL_F0 = CTL_ST3 + 262144;
constexpr size_t CTL_EPOCH = CTL_F0 + 32768;
constexpr size_t WS_MOD = CTL_EPOCH + 4096;
constexpr size_t WS_ST1 = WS_MOD + 131072;
constexpr size_t WS_U = WS_ST1 + 16384;
constexpr size_t WS_XT = WS_U + 65536;
constexpr size_t WS_X2T = WS_XT + (size_t)D * L * 2;
constexpr size_t WS_WIN = WS_X2T + (size_t)D * L * 2;
constexpr size_t WS_WOUT = WS_WIN + (size_t)D * F * 2;
constexpr size_t WS_Z2 = WS_WOUT + (size_t)FH * D * 2;
constexpr size_t WS_V = WS_Z2 + (size_t)L * FH * 2;
constexpr size_t WS_END = WS_V + (size_t)L * FH * 2;

__device__ __forceinline__ float bflo(unsigned u) { return u2f(u << 16); }
__device__ __forceinline__ float bfhi(unsigned u) { return u2f(u & 0xffff0000u); }
__device__ __forceinline__ float bf2f(u16 v) { return u2f((unsigned)v << 16); }
__device__ __forceinline__ unsigned pkbf(float a, float b) {
  f32x2 v = {a, b};
  return __builtin_bit_cast(unsigned, __builtin_convertvector(v, bf16x2));
}
__device__ __forceinline__ unsigned pkh(float a, float b) {
  f32x2 v = {a, b};
  return __builtin_bit_cast(unsigned, __builtin_convertvector(v, f16x2));
}
__device__ __forceinline__ float bfr(float a) { return bflo(pkbf(a, a)); }
__device__ __forceinline__ v2u trread(LAS const char* p) {
  return __builtin_bit_cast(v2u, __builtin_amdgcn_ds_read_tr16_b64_v4i16((LAS s16x4*)p));
}
__device__ __forceinline__ f32x4 mfma16(v4u a, v4u b, f32x4 c) {
  return __builtin_amdgcn_mfma_f32_16x16x32_bf16(__builtin_bit_cast(bf16x8, a), __builtin_bit_cast(bf16x8, b), c, 0, 0, 0);
}
__device__ __forceinline__ f32x16 mfma32(v4u a, v4u b, f32x16 c) {
  return __builtin_amdgcn_mfma_f32_32x32x16_bf16(__builtin_bit_cast(bf16x8, a), __builtin_bit_cast(bf16x8, b), c, 0, 0, 0);
}
__device__ __forceinline__ f32x16 mfma32h(v4u a, v4u b, f32x16 c) {
  return __builtin_amdgcn_mfma_f32_32x32x16_f16(__builtin_bit_cast(f16x8, a), __builtin_bit_cast(f16x8, b), c, 0, 0, 0);
}
template <bool DEV>
__device__ __forceinline__ void glds16(const void* sbase, unsigned voff, unsigned dst) {
  unsigned keep;
  if (DEV) asm volatile(
"s_mov_b32 %0, m0\n\ts_mov_b32 m0, %3\n\ts_nop 0\n\tglobal_load_lds_dwordx4 %1, %2 sc1\n\ts_mov_b32 m0, %0"
                        : "=&s"(keep) : "v"(voff), "s"(sbase), "s"(dst) : "memory");
  else asm volatile("s_mov_b32 %0, m0\n\ts_mov_b32 m0, %3\n\ts_nop 0\n\tglobal_load_lds_dwordx4 %1, %2\n\ts_mov_b32 m0, %0"
                    : "=&s"(keep) : "v"(voff), "s"(sbase), "s"(dst) : "memory");
}
__device__ __forceinline__ void glds16nt(const void* sbase, unsigned voff, unsigned dst) {
  unsigned keep;
  asm volatile("s_mov_b32 %0, m0\n\ts_mov_b32 m0, %3\n\ts_nop 0\n\tglobal_load_lds_dwordx4 %1, %2" NT "\n\ts_mov_b32 m0, %0"
               : "=&s"(keep) : "v"(voff), "s"(sbase), "s"(dst) : "memory");
}
#define GL_(i, s) "global_load_lds_dwordx4 %" #i ", %5 offset:" s
template <int N, bool DEV>
__device__ __forceinline__ void gldsN(const void* sbase, unsigned v0, unsigned v1, unsigned v2, unsigned v3, unsigned dst) {
  unsigned keep;
#define GLN_(body) asm volatile("s_mov_b32 %0, m0\n\ts_mov_b32 m0, %6\n\ts_nop 0\n\t" body "s_mov_b32 m0, %0" : "=&s"(keep) : "v"(v0), "v"(v1), "v"(v2), "v"(v3), "s"(sbase), "s"(dst) : "memory")
  if (DEV) {
    if (N == 1) GLN_(GL_(1, "0 sc1\n\t"));
    if (N == 2) GLN_(GL_(1, "0 sc1\n\t") GL_(2, "1024 sc1\n\t"));
    if (N == 3) GLN_(GL_(1, "0 sc1\n\t") GL_(2, "1024 sc1\n\t") GL_(3, "2048 sc1\n\t"));
    if (N == 4) GLN_(GL_(1, "0 sc1\n\t") GL_(2, "1024 sc1\n\t") GL_(3, "2048 sc1\n\t") GL_(4, "3072 sc1\n\t"));
  } else {
    if (N == 2) GLN_(GL_(1, "0\n\t") GL_(2, "1024\n\t"));
    if (N == 4) GLN_(GL_(1, "0\n\t") GL_(2, "1024\n\t") GL_(3, "2048\n\t") GL_(4, "3072\n\t"));
  }
}
template <int N> __device__ __forceinline__ void gldsL(const void* sbase, unsigned vo, unsigned dst) { gldsN<N, true>(sbase, vo, vo, vo, vo, dst); }
template <int N> __device__ __forceinline__ void waitv() { asm volatile("s_waitcnt vmcnt(%0)" :: "i"(N) : "memory"); }
__device__ __forceinline__ float dot2bf(unsigned a, unsigned b, float c) {
  return __builtin_amdgcn_fdot2_f32_bf16(__builtin_bit_cast(bf16x2, a), __builtin_bit_cast(bf16x2, b), c, false);
}
#define SCHED_BAR() __builtin_amdgcn_sched_barrier(0)
#define STAMP_DECL
#define STAMP(i) do {} while (0)
#define STAMP_FLUSH(kid) do {} while (0)
#define STAMP_ARG
#define STAMP_PASS
#define CBAR() do { SCHED_BAR(); __builtin_amdgcn_s_waitcnt(0xC07F); asm volatile("" ::: "memory"); __builtin_amdgcn_s_barrier(); asm volatile("" ::: "memory"); } while (0)
#define LBAR() __builtin_amdgcn_s_barrier()

template <int CTRL> __device__ __forceinline__ float dppf(float v) {
  return __builtin_bit_cast(float, __builtin_amdgcn_update_dpp(0, __builtin_bit_cast(int, v), CTRL, 0xf, 0xf, true));
}
template <int N, int CTRL> __device__ __forceinline__ void treduce_dpp(float* v, bool up) {
#pragma unroll
  for (int e = 0; e < N; ++e) {
    const float a = v[e], b = v[e + N];
    const float send = up ? a : b, keep = up ? b : a;
    v[e] = keep + dppf<CTRL>(send);
  }
}
template <int N> __device__ __forceinline__ void treduce_xor(float* v, bool up, int mask) {
#pragma unroll
  for (int e = 0; e < N; ++e) {
    const float a = v[e], b = v[e + N];
    const float send = up ? a : b, keep = up ? b : a;
    v[e] = keep + shx(send, mask);
  }
}

__device__ __forceinline__ float ln_std(float var) { return __builtin_amdgcn_sqrtf(bfr(bfr(var) + 1e-5f)); }
__device__ __forceinline__ void ln_scalars(float mean, float var, float& m_b, float& rinv) {
  m_b = bfr(mean);
  rinv = __builtin_amdgcn_rcpf(bfr(ln_std(var)));
}

struct cplx { float r, i; };
__device__ __forceinline__ cplx cmul(cplx a, cplx b) { return {a.r * b.r - a.i * b.i, a.r * b.i + a.i * b.r}; }

typedef GAS unsigned gu32;
#define RLX_AGENT __ATOMIC_RELAXED, __HIP_MEMORY_SCOPE_AGENT
#define F0_REP 8
#define V_DELAY 24
#define V_WIN 5
#define LSTAMP(i) do {} while (0)
#define POLL_SLEEP 6
#define E1_REP 32
#define INTERLEAVE(n, r) do { _Pragma("unroll") for (int i_ = 0; i_ < (n); ++i_) { __builtin_amdgcn_sched_group_barrier(0x008, 1, 0); __builtin_amdgcn_sched_group_barrier(0x100, (r), 0); } \
    __builtin_amdgcn_sched_group_barrier(0x008, 8, 0); SCHED_BAR(); } while (0)
#define ABL_KK kk
#define AUXB_PRIO 2
constexpr unsigned SPIN_CAP = 1u << 16;
constexpr int M_LDS = 163840, M_CTL = M_LDS - 64;
constexpr int CODE_LINES = 400;
constexpr int C_KOFF = 0, C_UOFF = 4224, C_UP = 1536, C_COFF = C_UOFF + 4 * C_UP, C_GRP = C_COFF + 4 * 64 * 144;
constexpr int G1_BOFF = 81920, G1_BST = 14336, G1_NSLOT = 5, G1_TAB = G1_BOFF + G1_BST * G1_NSLOT;
constexpr int G1_T_LN2 = G1_TAB, G1_T_U = G1_T_LN2 + 512, G1_T_P = G1_T_U + 2048, G1_T_Z = G1_T_P + 2048;
constexpr int G2_NW = 9, G2_NZ = 6, G2_ZOFF = G2_NW * 8192, G2_ZST = 14336;
constexpr int G3_RED = 135 * 1024, G3_GB = 161024;
static_assert(3 * C_GRP <= M_CTL && G1_T_Z + 512 <= M_CTL && G2_ZOFF + G2_NZ * G2_ZST <= M_CTL && G3_GB + 320 <= M_CTL, "lds");

__device__ __forceinline__ void st128(__amdgpu_buffer_rsrc_t rs, unsigned off, v4u v) { __builtin_amdgcn_raw_buffer_store_b128(v, rs, off, 0, 16); }
__device__ __forceinline__ v4u ld128(__amdgpu_buffer_rsrc_t rs, unsigned off) { return __builtin_amdgcn_raw_buffer_load_b128(rs, off, 0, 16); }

__device__ __forceinline__ bool wave_arrive(LAS char* lds, int word, unsigned n, int lane) {
  waitv<0>();
  unsigned old = 0;
  if (lane == 0) old = __hip_atomic_fetch_add((LAS unsigned*)(lds + M_CTL) + word, 1u, __ATOMIC_RELAXED, __HIP_MEMORY_SCOPE_WORKGROUP);
  return (unsigned)__builtin_amdgcn_readfirstlane(old) == n - 1;
}
__device__ __forceinline__ void lds_wait(LAS char* lds0, int word, unsigned target) {
  LAS unsigned* p = (LAS unsigned*)(lds0 + M_CTL) + word;
  while ((unsigned)__builtin_amdgcn_readfirstlane(__hip_atomic_load(p, __ATOMIC_RELAXED, __HIP_MEMORY_SCOPE_WORKGROUP)) < target) __builtin_amdgcn_s_sleep(0);
  asm volatile("" ::: "memory");
}
__device__ __forceinline__ void lds_post(LAS char* lds0, int word, int lane) {
  SCHED_BAR();
  asm volatile("" ::: "memory");
  if (lane == 0) __hip_atomic_fetch_add((LAS unsigned*)(lds0 + M_CTL) + word, 1u, __ATOMIC_RELAXED, __HIP_MEMORY_SCOPE_WORKGROUP);
  asm volatile("" ::: "memory");
}
__device__ __forceinline__ void group_sync(LAS char* lds0, int word, unsigned target, int lane) { lds_post(lds0, word, lane); lds_wait(lds0, word, target); }
template <int NREP, int NSH> __device__ __forceinline__ void signal(char* base, int shard, int lane) {
  if (lane < NREP) __hip_atomic_fetch_add((gu32*)(base + (lane * NSH + shard) * 256), 1u, RLX_AGENT);
}
template <int NSH> __device__ __forceinline__ bool count_ok(unsigned c, unsigned total) {
  if (NSH > 1) c += __builtin_amdgcn_update_dpp(0, c, 0xb1, 0xf, 0xf, true);
  if (NSH > 2) c += __builtin_amdgcn_update_dpp(0, c, 0x4e, 0xf, 0xf, true);
  if (NSH > 4) c += __builtin_amdgcn_update_dpp(0, c, 0x141, 0xf, 0xf, true);
  return (unsigned)__builtin_amdgcn_readfirstlane(c) >= total;
}
template <int NSH> __device__ __forceinline__ void wait_count(char* base, int rep, unsigned total, int lane) {
  gu32* p = (gu32*)(base + (rep * NSH + (lane & (NSH - 1))) * 256);
#pragma unroll 1
  for (unsigned spin = 0; spin < SPIN_CAP; ++spin) {
    if (count_ok<NSH>(__hip_atomic_load(p, RLX_AGENT), total)) break;
    __builtin_amdgcn_s_sleep(POLL_SLEEP);
  }
  __builtin_amdgcn_fence(__ATOMIC_ACQUIRE, "wavefront");
}
template <int NSH> __device__ __forceinline__ void poll_issue(unsigned& c, char* base, int rep, int lane) {
  c = __hip_atomic_load((gu32*)(base + (rep * NSH + (lane & (NSH - 1))) * 256), RLX_AGENT);
}
template <int NSH> __device__ __forceinline__ void poll_finish(unsigned c, char* base, int rep, unsigned total, int lane) {
  if (!count_ok<NSH>(c, total)) wait_count<NSH>(base, rep, total, lane);
  __builtin_amdgcn_fence(__ATOMIC_ACQUIRE, "wavefront");
}

__device__ __forceinline__ float gelu_f(float x) {
  const float p = __builtin_fmaf(x * x, -0.044715f * 2.302208198f, -2.302208198f);
  const float e = __builtin_amdgcn_exp2f(x * p);
  return x * __builtin_amdgcn_rcpf(1.0f + e);
}

__device__ __forceinline__ float ld32f(__amdgpu_buffer_rsrc_t rs, unsigned off) { return u2f(__builtin_amdgcn_raw_buffer_load_b32(rs, off, 0, 16)); }
__device__ __forceinline__ void st32(__amdgpu_buffer_rsrc_t rs, unsigned off, unsigned v) { __builtin_amdgcn_raw_buffer_store_b32(v, rs, off, 0, 16); }

struct Args {
  const u16 *x, *Are, *Aim, *Bre, *Bim, *Cre, *Cim, *logdt, *dskip, *win, *wsp, *bsp, *wout, *cvec, *wmod;
  char* ws; u16* out;
};

typedef GAS unsigned long long gu64;
__device__ __forceinline__ void put_granule(char* p, unsigned v, unsigned tag) { __hip_atomic_store((gu64*)p, ((unsigned long long)tag << 32) | v, RLX_AGENT); }
template <int N, class F> __device__ __forceinline__ void get_granules(unsigned (&v)[N], unsigned tag, F addr) {
#pragma unroll 1
  for (unsigned spin = 0; spin < SPIN_CAP; ++spin) {
    unsigned long long x[N];
#pragma unroll
    for (int k = 0; k < N; ++k) x[k] = __hip_atomic_load((gu64*)addr(k), RLX_AGENT);
    bool ok = true;
#pragma unroll
    for (int k = 0; k < N; ++k) { v[k] = (unsigned)x[k]; ok &= (unsigned)(x[k] >> 32) == tag; }
    if (wall(ok)) break;
    __builtin_amdgcn_s_sleep(3);
  }
}

__device__ __forceinline__ void conv_group(LAS char* lds, LAS char* lds0, int bt, const Args& A, __amdgpu_buffer_rsrc_t rs, unsigned epoch, unsigned& tag, unsigned (&pre)[7], unsigned long long pc,
                                           int wg, int d, int tid, int wave, int lane STAMP_ARG) {
  char* ws = A.ws;
  float S[8], soff;
  v4u xv, st[2];
  unsigned cpf = 0;
  {
    const int half = wave & 1;
    int xoff[8];
#pragma unroll
    for (int j = 0; j < 8; ++j) {
      const int l = tid * 8 + j;
      const int idx = l < 128 ? l : (int)(pkbf((float)l, 0.f) & 0xffffu) - 0x4300 + 128;
      xoff[j] = (idx + (idx >> 3)) * 8;
    }
    asm volatile("" : "+v"(xoff[0]), "+v"(xoff[1]), "+v"(xoff[2]), "+v"(xoff[3]), "+v"(xoff[4]), "+v"(xoff[5]), "+v"(xoff[6]), "+v"(xoff[7]));
    waitv<0>();
    asm volatile("v_readfirstlane_b32 %0, %1\n\ts_nop 3" : "=s"(tag) : "v"(epoch) : "memory");
    tag += 1u;
    asm volatile("" : "+v"(pre[0]), "+v"(pre[1]), "+v"(pre[2]), "+v"(pre[3]), "+v"(pre[4]), "+v"(pre[5]), "+v"(pre[6]));
    {
      const int so = ((wg >> 3) * 4 + wave) * 4 + (lane & 3);
      cpf = *(const GAS unsigned*)((pc & ~255ull) + (size_t)(bt == 1 && so < CODE_LINES ? so : 0) * 128);
    }
    const unsigned vare = pre[0], vaim = pre[1], vdt = pre[2], vbre = pre[3], vbim = pre[4], vcre = pre[5], vcim = pre[6];
    if (tid < 4) *(LAS v4u*)(lds + C_KOFF + tid * 16) = v4u{0u, 0u, 0u, 0u};
    const float dt = bfr(exp_acc(bflo(vdt)));
    const float ar2 = dt * bflo(vare) * 1.4426950408889634f, ai2 = dt * bflo(vaim) * 0.15915494309189535f;
    auto E = [&](float xx) {
      const float mag = __builtin_amdgcn_exp2f(ar2 * xx), ph = __builtin_amdgcn_fractf(ai2 * xx);
      return cplx{mag * __builtin_amdgcn_cosf(ph), mag * __builtin_amdgcn_sinf(ph)};
    };
    LAS char* img = lds + C_COFF + wave * 8192 + lane * 128;
    const int sw = lane & 7;
    float isc = 0.f;
    if (wave < 2) {
      const cplx e8 = E(8.f), e64 = E(64.f), e128 = E(128.f), e192 = E(192.f);
      cplx q[8];
      q[0] = {1.f, 0.f}; q[1] = e8; q[2] = cmul(e8, e8); q[3] = cmul(q[2], e8); q[4] = cmul(q[2], q[2]);
      q[5] = cmul(q[4], e8); q[6] = cmul(q[4], q[2]); q[7] = cmul(q[4], q[3]);
#pragma unroll
      for (int g = 0; g < 4; ++g) {
        cplx v[8];
#pragma unroll
        for (int j = 0; j < 8; ++j) v[j] = g == 0 ? q[j] : cmul(q[j], g == 1 ? e64 : (g == 2 ? e128 : e192));
        *(LAS v4u*)(img + ((g ^ sw) << 4)) = v4u{pkh(v[0].r, v[1].r), pkh(v[2].r, v[3].r), pkh(v[4].r, v[5].r), pkh(v[6].r, v[7].r)};
        *(LAS v4u*)(img + (((4 + g) ^ sw) << 4)) = v4u{pkh(v[0].i, v[1].i), pkh(v[2].i, v[3].i), pkh(v[4].i, v[5].i), pkh(v[6].i, v[7].i)};
      }
    } else {
      cplx W = cmul(cplx{bflo(vcre), bflo(vcim)}, cplx{dt * bflo(vbre), dt * bflo(vbim)});
      float mx = __builtin_fmaxf(__builtin_fabsf(W.r), __builtin_fabsf(W.i));
      mx = __builtin_fmaxf(mx, dppf<0xb1>(mx)); mx = __builtin_fmaxf(mx, dppf<0x4e>(mx)); mx = __builtin_fmaxf(mx, dppf<0x141>(mx)); mx = __builtin_fmaxf(mx, dppf<0x140>(mx));
      const unsigned mb = f2u(mx);
      const unsigned m01 = __builtin_elementwise_max((unsigned)__builtin_amdgcn_readlane(mb, 0), (unsigned)__builtin_amdgcn_readlane(mb, 16));
      const unsigned m23 = __builtin_elementwise_max((unsigned)__builtin_amdgcn_readlane(mb, 32), (unsigned)__builtin_amdgcn_readlane(mb, 48));
      int ex = (int)((__builtin_elementwise_max(m01, m23) >> 23) & 0xff);
      ex = ex < 20 ? 20 : (ex > 240 ? 240 : ex);
      const float sc = u2f((unsigned)(267 - ex) << 23);
      isc = u2f((unsigned)(ex - 13) << 23);
      W.r *= sc; W.i *= sc;
      const cplx e1 = E(1.f), e2 = cmul(e1, e1), e4 = cmul(e2, e2);
      cplx h[24];
      h[0] = W; h[1] = cmul(W, e1); h[2] = cmul(W, e2); h[3] = cmul(h[2], e1);
      h[4] = cmul(W, e4); h[5] = cmul(h[4], e1); h[6] = cmul(h[4], e2); h[7] = cmul(h[6], e1);
      const cplx g1 = E(256.f), g2 = E(512.f), g3 = E(768.f), g4 = E(1024.f);
      h[8] = cmul(h[0], g1); h[9] = cmul(h[2], g1); h[10] = cmul(h[4], g1); h[11] = cmul(h[6], g1);
      h[12] = cmul(h[0], g2); h[13] = cmul(h[4], g2); h[14] = cmul(h[0], g3); h[15] = cmul(h[4], g3);
      h[16] = cmul(h[0], g4); h[17] = cmul(h[8], g4); h[18] = cmul(h[12], g4); h[19] = cmul(h[14], g4);
      h[20] = cmul(h[16], g4); h[21] = {0.f, 0.f}; h[22] = {0.f, 0.f}; h[23] = {0.f, 0.f};
#pragma unroll
      for (int g = 0; g < 3; ++g) {
        const cplx* v = h + 8 * g;
        *(LAS v4u*)(img + ((g ^ sw) << 4)) = v4u{pkh(v[0].r, v[1].r), pkh(v[2].r, v[3].r), pkh(v[4].r, v[5].r), pkh(v[6].r, v[7].r)};
        *(LAS v4u*)(img + (((4 + g) ^ sw) << 4)) = v4u{pkh(-v[0].i, -v[1].i), pkh(-v[2].i, -v[3].i), pkh(-v[4].i, -v[5].i), pkh(-v[6].i, -v[7].i)};
      }
      *(LAS v4u*)(img + ((3 ^ sw) << 4)) = v4u{0u, 0u, 0u, 0u};
      *(LAS v4u*)(img + ((7 ^ sw) << 4)) = v4u{0u, 0u, 0u, 0u};
    }
    STAMP(1);
    group_sync(lds0, 5 + bt, 4, lane);
    if (wave >= 2) {
      const int hh = lane >> 5, blk = (lane >> 4) & 1, qq = (lane & 15) >> 2, p = lane & 3;
      LAS const char* Qi = lds + C_COFF + half * 8192;
      v2u a[8][2], bb[8][2];
#pragma unroll
      for (int ks = 0; ks < 8; ++ks)
#pragma unroll
        for (int t = 0; t < 2; ++t) {
          const int k = 16 * ks + 8 * hh + 4 * t + qq, mode = k >> 1, c = k & 1;
          const int off = mode * 128 + (((c * 4 + 2 * blk + (p >> 1)) ^ (mode & 7)) << 4) + (p & 1) * 8;
          a[ks][t] = trread(Qi + 16384 + off);
          bb[ks][t] = trread(Qi + off);
        }
      SCHED_BAR();
      f32x16 acc = {};
#pragma unroll
      for (int ks = 0; ks < 8; ++ks) acc = mfma32h(v4u{a[ks][0].x, a[ks][0].y, a[ks][1].x, a[ks][1].y}, v4u{bb[ks][0].x, bb[ks][0].y, bb[ks][1].x, bb[ks][1].y}, acc);
      LAS float* T = (LAS float*)(lds + C_UOFF) + half;
      const int q = lane & 31, hi = lane >> 5;
      auto put = [&](int idx, float v) { T[(idx + (idx >> 3)) * 2] = v * isc; };
#pragma unroll
      for (int r = 0; r < 4; ++r) put((r + 4 * hi) + 8 * q, acc[r]);
#pragma unroll
      for (int r = 0; r < 4; ++r) put(hi ? 384 + 64 * (r >> 1) + (r & 1) + 2 * q : 256 + r + 4 * q, acc[4 + r]);
      if (hi == 0) {
#pragma unroll
        for (int r = 0; r < 4; ++r) put(512 + 32 * r + q, acc[8 + r]);
      } else if (q == 0) put(640, acc[8]);
    }
    lds_post(lds0, 5 + bt, lane);
    auto load_x = [&]() {
      xv = ld128(rs, (unsigned)WS_XT + (tid * D + d) * 16);
#pragma unroll
      for (int i = 0; i < 2; ++i) st[i] = ld128(rs, (unsigned)WS_ST1 + (i * 256 + tid) * 16);
      SCHED_BAR();
    };
    const bool early = __builtin_amdgcn_readfirstlane(__hip_atomic_load((LAS unsigned*)(lds0 + M_CTL) + 8, __ATOMIC_RELAXED, __HIP_MEMORY_SCOPE_WORKGROUP)) != 0;
    asm volatile("" ::: "memory");
    if (early) load_x();
    lds_wait(lds0, 5 + bt, 8);
    {
      f32x2 tv[8];
#pragma unroll
      for (int j = 0; j < 8; ++j) tv[j] = *(LAS const f32x2*)(lds + C_UOFF + xoff[j]);
      SCHED_BAR();
      float kv[8];
#pragma unroll
      for (int j = 0; j < 8; ++j) kv[j] = tv[j][0] + tv[j][1];
      *(LAS v4u*)(lds + C_KOFF + 64 + tid * 16) = v4u{pkbf(kv[0], kv[1]), pkbf(kv[2], kv[3]), pkbf(kv[4], kv[5]), pkbf(kv[6], kv[7])};
      S[0] = kv[0];
#pragma unroll
      for (int j = 1; j < 8; ++j) S[j] = S[j - 1] + kv[j];
      float inc = S[7];
      inc += dppf<0x111>(inc); inc += dppf<0x112>(inc); inc += dppf<0x114>(inc); inc += dppf<0x118>(inc);
      inc += __builtin_bit_cast(float, __builtin_amdgcn_update_dpp(0, __builtin_bit_cast(int, inc), 0x142, 0xa, 0xf, false));
      inc += __builtin_bit_cast(float, __builtin_amdgcn_update_dpp(0, __builtin_bit_cast(int, inc), 0x143, 0xc, 0xf, false));
      soff = inc - S[7];
      if (lane == 63) ((LAS float*)(lds + C_KOFF + 4160))[wave] = inc;
    }
    lds_post(lds0, 5 + bt, lane);
    if (!early) {
      if (bt == 0 && wave == 0) {
#pragma unroll 1
        for (unsigned spin = 0; spin < SPIN_CAP; ++spin) {
          asm volatile("" ::: "memory");
          const v4u f = ld128(rs, (unsigned)CTL_F0 + (wg & (F0_REP - 1)) * 1024 + lane * 16);
          if (wall(f.x == tag && f.y == ~tag && f.z == tag && f.w == ~tag)) break;
          __builtin_amdgcn_s_sleep(2);
        }
        __builtin_amdgcn_fence(__ATOMIC_ACQUIRE, "wavefront");
        lds_post(lds0, 8, lane);
      } else lds_wait(lds0, 8, 1);
      load_x();
    }
    STAMP(2);
  }
  lds_wait(lds0, 5 + bt, 12);
  STAMP(3);
  float xf[8], xn[8];
  {
#pragma unroll
    for (int j = 0; j < 4; ++j) { xf[2 * j] = bflo(xv[j]); xf[2 * j + 1] = bfhi(xv[j]); }
    const f32x4 wt = *(LAS const f32x4*)(lds + C_KOFF + 4160);
    if (tid < 128) *(LAS v4u*)(lds + C_UOFF + (tid >> 5) * C_UP + (tid & 31) * 16) = v4u{0u, 0u, 0u, 0u};
#pragma unroll
    for (int j = 0; j < 8; ++j) {
      const unsigned sj = st[j >> 2][j & 3];
      xn[j] = (xf[j] - bflo(sj)) * __builtin_amdgcn_rcpf(bfhi(sj));
    }
#pragma unroll
    for (int e = 0; e < 4; ++e) *(LAS unsigned*)(lds + C_UOFF + e * C_UP + 512 + tid * 4) = pkbf(xn[e], xn[e + 4]);
    soff += (wave > 0 ? wt[0] : 0.f) + (wave > 1 ? wt[1] : 0.f) + (wave > 2 ? wt[2] : 0.f);
  }
  const u16 dskv = A.dskip[d];
  char* mgp = ws + WS_MOD + (size_t)((lane & 3) * 3840 + (lane < 12 ? lane >> 2 : 0) * D + d) * 8;
  const unsigned long long mg = __hip_atomic_load((gu64*)mgp, RLX_AGENT);
  STAMP(5);
  group_sync(lds0, 5 + bt, 16, lane);
  STAMP(6);
  f32x16 y0 = {}, y1 = {};
  {
    const int hh = lane >> 5, blk = (lane >> 4) & 1, qq = (lane & 15) >> 2, p = lane & 3, col = lane & 31;
    LAS const char* ab = lds + C_KOFF + (32 + 16 * blk + 4 * p - 4 * qq + 32 * hh) * 2 - 32;
    LAS const char* bb = lds + C_UOFF + wave * C_UP + (256 + 8 * (col - hh) - 240) * 2;
    struct CF { v2u a0l, a0h, a1l, a1h; v4u b0, b1; };
    CF cf[3];
    auto rdc = [&](int ks, CF& f) {
      f.a0l = trread(ab + ks * 128 + 32); f.a0h = trread(ab + ks * 128);
      f.b0 = *(LAS const v4u*)(bb + (15 - ks) * 32);
      f.b1 = *(LAS const v4u*)(bb + (31 - ks) * 32);
      f.a1l = trread(ab + (ks + 16) * 128 + 32); f.a1h = trread(ab + (ks + 16) * 128);
    };
    rdc(0, cf[0]); rdc(1, cf[1]);
#pragma unroll
    for (int ks = 0; ks < 16; ++ks) {
      if (ks + 2 < 16) rdc(ks + 2, cf[(ks + 2) % 3]);
      SCHED_BAR();
      const CF& f = cf[ks % 3];
      const v4u a0 = {f.a0l.x, f.a0l.y, f.a0h.x, f.a0h.y}, a1 = {f.a1l.x, f.a1l.y, f.a1h.x, f.a1h.y};
      y0 = mfma32(a0, f.b0, y0);
      y1 = mfma32(a0, f.b1, y1);
      y1 = mfma32(a1, f.b0, y1);
      SCHED_BAR();
    }
    asm volatile("" :: "v"(y0), "v"(y1));
    STAMP(7);
    LAS char* cb = lds + C_COFF + (wave * 64 + col) * 144 + hh * 16;
#pragma unroll
    for (int g = 0; g < 4; ++g) {
      *(LAS f32x4*)(cb + g * 32) = f32x4{y0[4 * g], y0[4 * g + 1], y0[4 * g + 2], y0[4 * g + 3]};
      *(LAS f32x4*)(cb + 32 * 144 + g * 32) = f32x4{y1[4 * g], y1[4 * g + 1], y1[4 * g + 2], y1[4 * g + 3]};
    }
  }
  lds_post(lds0, 5 + bt, lane);
  unsigned mv[1] = {(unsigned)mg};
  if (!wall((unsigned)(mg >> 32) == tag)) get_granules<1>(mv, tag, [&](int) { return mgp; });
  float msum = u2f(mv[0]);
  msum += dppf<0xb1>(msum); msum += dppf<0x4e>(msum);
  const unsigned mb = f2u(bfr(msum));
  const float sa = u2f(__builtin_amdgcn_readlane(mb, 0)), c1a = bfr(1.0f + u2f(__builtin_amdgcn_readlane(mb, 4))), ga = u2f(__builtin_amdgcn_readlane(mb, 8)), dsk = bf2f(dskv);
  const float gc = ga * c1a, gs_ = ga * sa, gd = gc * dsk, g0 = gs_ * (soff + dsk);
#pragma unroll
  for (int j = 0; j < 8; ++j) xf[j] = __builtin_fmaf(gd, xn[j], __builtin_fmaf(gs_, S[j], xf[j] + g0));
  STAMP(8);
  lds_wait(lds0, 5 + bt, 20);
  STAMP(9);
  float y[8];
  {
    const int c1 = 2 * tid, c0 = tid ? c1 - 1 : 0;
    const int o0 = (c0 >> 3) * 144 + (c0 & 7) * 16, o1 = (c1 >> 3) * 144 + (c1 & 7) * 16;
    LAS const char* cb = lds + C_COFF;
    f32x4 w0[4], w1[4], w2[4];
#pragma unroll
    for (int e = 0; e < 4; ++e) {
      w1[e] = *(LAS const f32x4*)(cb + e * 64 * 144 + o1); w2[e] = *(LAS const f32x4*)(cb + e * 64 * 144 + o1 + 16);
      if (e) w0[e] = *(LAS const f32x4*)(cb + e * 64 * 144 + o0);
    }
    SCHED_BAR();
#pragma unroll
    for (int e = 0; e < 4; ++e) {
      float w[12];
#pragma unroll
      for (int j = 0; j < 4; ++j) { w[4 + j] = w1[e][j]; w[8 + j] = w2[e][j]; w[j] = (e && tid) ? w0[e][j] : 0.f; }
#pragma unroll
      for (int j = 0; j < 8; ++j) y[j] = e ? y[j] + w[4 + j - e] : w[4 + j];
    }
  }
  float o[8];
#pragma unroll
  for (int j = 0; j < 8; ++j) o[j] = __builtin_fmaf(gc, y[j], xf[j]);
  {
    const unsigned a0 = pkbf(o[0], o[1]), a1 = pkbf(o[2], o[3]), b0 = pkbf(o[4], o[5]), b1 = pkbf(o[6], o[7]);
    const bool odd = tid & 1;
    const unsigned s0 = odd ? a0 : b0, s1 = odd ? a1 : b1;
    const unsigned r0 = __builtin_amdgcn_update_dpp(0, s0, 0xb1, 0xf, 0xf, true), r1 = __builtin_amdgcn_update_dpp(0, s1, 0xb1, 0xf, 0xf, true);
    const v4u ov = odd ? v4u{r0, r1, b0, b1} : v4u{a0, a1, r0, r1};
    st128(rs, (unsigned)WS_X2T + d * (L * 2) + (tid >> 2) * 64 + (tid & 1) * 32 + (tid & 2) * 8, ov);
  }
  STAMP(10);
  asm volatile("" :: "v"(cpf));
  if (wave_arrive(lds0, bt, 4, lane)) signal<E1_REP, 8>(ws + CTL_E1 + bt * 65536, wg & 7, lane);
}

__device__ __forceinline__ void aux_group(LAS char* lds, LAS char* lds0, const Args& A, __amdgpu_buffer_rsrc_t rs, unsigned epoch, int u, int tid, int wave, int lane STAMP_ARG) {
  char* ws = A.ws;
  unsigned gs = 0;
  v4u wb[5], ov[3], mw[10];
  u16 mc;
  const int mpart = u & 3, mcol = (u < 120 ? u >> 2 : 0) * 128;
  unsigned tag;
  asm volatile("s_waitcnt vmcnt(0)\n\tv_readfirstlane_b32 %0, %1\n\ts_nop 3" : "=s"(tag) : "v"(epoch) : "memory");
  tag += 1u;
  auto load_wb = [&]() {
    const u16* bp = A.win + (size_t)(160 * (u & 3) + (tid >> 3)) * F + 64 * ((u < 108 ? u : 0) >> 2) + (tid & 7) * 8;
#pragma unroll
    for (int i = 0; i < 5; ++i) wb[i] = NTLOAD((const v4u*)(bp + (size_t)i * 32 * F));
  };
  {
    LAS const char* img = lds + 8192;
    group_sync(lds0, 7, gs += 4, lane);
    STAMP(3);
    const int r = tid >> 4, s = tid & 15;
    v4u v[5];
    {
      const int q = lane >> 4, rr = (lane & 15) >> 2, p = lane & 3, f = (rr | ((q & 1) << 2)) << 1;
      v2u lo[5], hi[5];
#pragma unroll
      for (int j = 0; j < 5; ++j) {
        const int a0 = (2 * (q & 1) * 5 + j) * 1024 + rr * 256 + ((((2 * wave + (q >> 1)) * 2 + (p >> 1)) ^ f) << 4) + (p & 1) * 8;
        lo[j] = trread(img + a0); hi[j] = trread(img + a0 + 5120);
      }
#pragma unroll
      for (int i = 0; i < 5; ++i) v[i] = *(LAS const v4u*)(img + (wave * 5 + i) * 1024 + lane * 16);
      SCHED_BAR();
#pragma unroll
      for (int j = 0; j < 5; ++j)
        st128(rs, (unsigned)WS_XT + ((u * 2 + (q & 1)) * D + (8 * j + 2 * wave + (q >> 1)) * 16 + (lane & 15)) * 16, v4u{lo[j].x, lo[j].y, hi[j].x, hi[j].y});
      SCHED_BAR();
    }
    {
      const int zc = (u * 4 + wave) * 3 + lane - 32;
      st128(rs, lane < 32 ? (unsigned)CTL_ST3 + ((u * 4 + wave) * 32 + lane) * 16 : (lane < 35 && zc < 1280 ? (unsigned)zc * 256 : 0x7ffffff0u), v4u{0u, 0u, 0u, 0u});
    }
    float s0 = 0.f, s1 = 0.f, q0 = 0.f, q1 = 0.f;
#pragma unroll
    for (int i = 0; i < 5; ++i) {
      s0 = dot2bf(v[i][0], 0x3f803f80u, s0); s1 = dot2bf(v[i][1], 0x3f803f80u, s1); s0 = dot2bf(v[i][2], 0x3f803f80u, s0); s1 = dot2bf(v[i][3], 0x3f803f80u, s1);
      q0 = dot2bf(v[i][0], v[i][0], q0); q1 = dot2bf(v[i][1], v[i][1], q1); q0 = dot2bf(v[i][2], v[i][2], q0); q1 = dot2bf(v[i][3], v[i][3], q1);
    }
    float sum = s0 + s1, sq = q0 + q1;
    sum += dppf<0xb1>(sum); sq += dppf<0xb1>(sq); sum += dppf<0x4e>(sum); sq += dppf<0x4e>(sq);
    sum += dppf<0x141>(sum); sq += dppf<0x141>(sq); sum += dppf<0x140>(sum); sq += dppf<0x140>(sq);
    const float mean = sum * (1.0f / D);
    const int t = u * 16 + r;
    st32(rs, s == 0 ? (unsigned)WS_ST1 + ((((t >> 2) & 1) * 256 + (t >> 3)) * 4 + (t & 3)) * 4 : 0x7ffffff0u, pkbf(mean, ln_std(__builtin_fmaxf(sq * (1.0f / D) - mean * mean, 0.f))));
    STAMP(8);
    if (wave_arrive(lds0, 9, 4, lane) && lane < F0_REP)
      __hip_atomic_store((gu64*)(ws + CTL_F0 + lane * 1024 + u * 8), ((unsigned long long)~tag << 32) | tag, RLX_AGENT);
    SCHED_BAR();
    mc = A.cvec[mpart * 160 + (tid < 160 ? tid : 0)];
#pragma unroll
    for (int i = 0; i < 10; ++i) mw[i] = NTLOAD((const v4u*)(A.wmod + (size_t)(mpart * 160 + i * 16 + (tid >> 4)) * (6 * D) + mcol + (tid & 15) * 8));
    SCHED_BAR();
    load_wb();
    {
      const int slab = u < 108 ? u >> 2 : 0, r8 = tid >> 3, s8 = tid & 7;
      const int half = (r8 >> 2) & 1, jj = slab < 24 ? slab / 3 : 2 * (slab - 24) + half;
      const int sub = (jj < 6 ? 7 * jj : 42 + 6 * (jj - 6)) + (slab < 24 ? 2 * (slab - 3 * jj) + half : 6);
      const u16* src = A.wout + (size_t)(32 * (sub >> 1) + 8 * (r8 >> 3) + 4 * (sub & 1) + (r8 & 3)) * D + (u & 3) * 160;
#pragma unroll
      for (int i = 0; i < 3; ++i) ov[i] = NTLOAD((const v4u*)(src + (i < 2 || s8 < 4 ? s8 + 8 * i : 0) * 8));
    }
    SCHED_BAR();
    STAMP(1);
  }
  {
    LAS float* msc = (LAS float*)lds;
    const float c = bf2f(mc);
    msc[tid] = bfr(c * __builtin_amdgcn_rcpf(1.0f + __builtin_amdgcn_exp2f(-1.4426950408889634f * c)));
    group_sync(lds0, 7, gs += 4, lane);
    float sv[10];
#pragma unroll
    for (int i = 0; i < 10; ++i) sv[i] = msc[(tid >> 4) + 16 * i];
    float acc[8] = {};
#pragma unroll
    for (int i = 0; i < 10; ++i)
#pragma unroll
      for (int j = 0; j < 4; ++j) { acc[2 * j] += sv[i] * bflo(mw[i][j]); acc[2 * j + 1] += sv[i] * bfhi(mw[i][j]); }
    treduce_xor<4>(acc, (lane & 32) != 0, 32);
    treduce_xor<2>(acc, (lane & 16) != 0, 16);
    LAS float* red = msc + 256;
    *(LAS f32x2*)(red + wave * 128 + (lane & 15) * 8 + (lane >> 4) * 2) = f32x2{acc[0], acc[1]};
    group_sync(lds0, 7, gs += 4, lane);
    if (tid < 128 && u < 120) put_granule(ws + WS_MOD + (size_t)(mpart * 3840 + mcol + tid) * 8, f2u((red[tid] + red[128 + tid]) + (red[256 + tid] + red[384 + tid])), tag);
  }
  __builtin_amdgcn_s_setprio(AUXB_PRIO);
  if (u < 108) {
    const int cbk = u >> 2, part = u & 3;
    const int c0 = 64 * cbk, r0 = 160 * part, seg = tid & 7, rg = tid >> 3;
    v4u w[5];
#pragma unroll
    for (int i = 0; i < 5; ++i) w[i] = wb[i];
    constexpr int OP = 400;
    LAS char* oimg = lds + 32768;
#pragma unroll
    for (int i = 0; i < 3; ++i) *(LAS v4u*)(oimg + rg * OP + (seg + 8 * i) * 16) = ov[i];
    LAS f32x2* mt2 = (LAS f32x2*)(lds + 28672);
    if (wave < 3) {
      unsigned mv[8];
      const int rw = r0 + (tid < 160 ? tid : 159);
      get_granules<8>(mv, tag, [&](int k) { return ws + WS_MOD + (size_t)((k & 3) * 3840 + (3 + (k >> 2)) * D + rw) * 8; });
      mt2[tid] = f32x2{bfr((u2f(mv[0]) + u2f(mv[1])) + (u2f(mv[2]) + u2f(mv[3]))), bfr(1.0f + bfr((u2f(mv[4]) + u2f(mv[5])) + (u2f(mv[6]) + u2f(mv[7]))))};
    }
    group_sync(lds0, 7, gs += 4, lane);
    STAMP(2);
    float v[16] = {};
    constexpr int WPITCH = 144;
    LAS char* img = lds + 1024;
#pragma unroll
    for (int i = 0; i < 5; ++i) {
      const f32x2 ms = mt2[rg + 32 * i];
      const float c1 = ms.y, sb = ms.x;
#pragma unroll
      for (int j = 0; j < 4; ++j) {
        const float a = bflo(w[i][j]), c = bfhi(w[i][j]);
        const unsigned pk = pkbf(a * c1, c * c1);
        v[8 + 2 * j] += sb * a; v[8 + 2 * j + 1] += sb * c;
        v[2 * j] += bflo(pk); v[2 * j + 1] += bfhi(pk);
        w[i][j] = pk;
      }
      *(LAS v4u*)(img + (rg + 32 * i) * WPITCH + seg * 16) = w[i];
    }
    treduce_xor<8>(v, (lane & 32) != 0, 32);
    treduce_xor<4>(v, (lane & 16) != 0, 16);
    treduce_dpp<2, 0x128>(v, (lane & 8) != 0);
    LAS float* red = (LAS float*)(lds + 24576 + 1024);
    {
      const int which = lane >> 5, j = ((lane >> 4) & 1) * 4 + ((lane >> 3) & 1) * 2;
      *(LAS f32x2*)(red + wave * 128 + which * 64 + seg * 8 + j) = f32x2{v[0], v[1]};
    }
    group_sync(lds0, 7, gs += 4, lane);
    STAMP(3);
    const int q = lane >> 4, rr = (lane & 15) >> 2, p = lane & 3;
    const int colb = (32 * (wave >> 1) + 8 * p + 4 * (wave & 1)) * 2;
#pragma unroll
    for (int i = 0; i < 5; ++i) {
      const int a0 = (32 * i + 8 * q + rr) * WPITCH + colb;
      v2u lo = trread(img + a0), hi = trread(img + a0 + 4 * WPITCH);
      st128(rs, (unsigned)WS_WIN + (((((part * 5 + i) >> 1) * 108 + 4 * cbk + wave) * 2 + ((part * 5 + i) & 1)) * 64 + lane) * 16, v4u{lo.x, lo.y, hi.x, hi.y});
    }
    {
      const int a0 = (8 * q + rr) * OP + p * 8 + wave * 32;
#pragma unroll
      for (int j = 0; j < 3; ++j) {
        const v2u lo = trread(oimg + a0 + j * 128), hi = trread(oimg + a0 + 4 * OP + j * 128);
        st128(rs, wave + 4 * j < 10 ? (unsigned)WS_WOUT + (((part * 10 + wave + 4 * j) * 27 + cbk) * 64 + lane) * 16 : 0x7ffffff0u, v4u{lo.x, lo.y, hi.x, hi.y});
      }
    }
    if (tid < 128) {
      const int which = tid >> 6, o = tid & 63, sub = o >> 4, i = o & 15;
      const int cl = 32 * (sub >> 1) + 8 * (i >> 2) + 4 * (sub & 1) + (i & 3), ix = which * 64 + cl;
      st32(rs, (unsigned)WS_U + ((part * 2 + which) * F + c0 + o) * 4, f2u((red[ix] + red[128 + ix]) + (red[256 + ix] + red[384 + ix])));
    }
  }
  STAMP(8);
  lds_wait(lds0, 8, 1);
  if (wave_arrive(lds0, 2, 4, lane)) signal<E1_REP, 8>(ws + CTL_E1B, u & 7, lane);
}

__device__ __forceinline__ int g1_aoff(int row, int G, int ab, int p) {
  const int f = ((row >> 1) & 1) | (((row >> 3) & 1) << 1);
  return row * 128 + (((4 * G + 2 * ab + (p >> 1)) ^ (f << 1)) << 4) + (p & 1) * 8;
}

template <int NB>
__device__ __forceinline__ void loader_path(LAS char* lds, char* ws, __amdgpu_buffer_rsrc_t rs, const u16* __restrict__ wsp, int wg, int mt, int j, int sub0, int lw, int lane) {
  const unsigned lbase = (unsigned)(size_t)lds, vo = lane * 16;
  const bool CNT7 = j < 6;
  {
    unsigned avo[2];
#pragma unroll
    for (int h = 0; h < 2; ++h) {
      const int row = lane >> 3, pos = lane & 7, f = ((row >> 1) & 1) | (h << 1);
      avo[h] = (row + 8 * h) * (L * 2) + ((pos ^ (f << 1)) << 4) - h * 1024;
    }
    const char* asb = ws + WS_X2T + (size_t)(16 * lw) * (L * 2) + mt * 128;
    const int p0 = (NB == 4 || CNT7) ? (lw < 2 ? 4 * lw : 2 + 3 * lw) : 3 * lw;
    const char* bsb = ws + WS_WIN + (size_t)(sub0 * 2 + p0) * 1024;
    const unsigned bb = lbase + G1_BOFF;
    auto issueB = [&](int s, unsigned slot) {
      gldsL<NB>(bsb + (s < 10 ? 108 * 1024 + s * 216 * 1024 : (s - 10) * 216 * 1024), vo, slot + p0 * 1024);
    };
    auto issueA = [&](int s0, int s1) {
#pragma unroll 1
      for (int s = s0; s < s1; ++s) gldsN<2, true>(asb + (size_t)(s * 64) * (L * 2), avo[0], avo[1], 0, 0, lbase + s * 8192 + lw * 2048);
    };
    {
      const unsigned vb = (unsigned)WS_V + (mt * 4 + lw) * 27 * 1024 + lane * 16;
#pragma unroll
      for (int k = 0; k < 3; ++k) st128(rs, vb + (3 * j + k) * 1024, v4u{~0u, ~0u, ~0u, ~0u});
      if (j < 6) __builtin_amdgcn_raw_buffer_store_b64(v2u{~0u, ~0u}, rs, vb + (24 + (j >> 1)) * 1024 + (j & 1) * 8, 0, 16);
    }
    waitv<0>();
    group_sync(lds, 5, 24, lane);
    if (lw == 0) { wait_count<8>(ws + CTL_E1B, wg & (E1_REP - 1), 128, lane); lds_post(lds, 10, lane); }
    if (lw == 3) { wait_count<8>(ws + CTL_E1, wg & (E1_REP - 1), 256, lane); lds_post(lds, 11, lane); }
    lds_wait(lds, 10, 1);
    STAMP(13);
#pragma unroll
    for (int s = 0; s < 4; ++s) issueB(s, bb + (s + 1) * G1_BST);
    lds_wait(lds, 11, 1);
    lds_wait(lds, 1, 4);
    issueA(0, 4);
    STAMP(12);
    LBAR();
    LSTAMP(1); STAMP(14);
    issueB(4, bb);
    waitv<6 + NB>(); LBAR(); LSTAMP(2);
    waitv<4 + NB>(); LBAR(); issueB(5, bb + G1_BST);
    waitv<2 + 2 * NB>(); LBAR(); LSTAMP(3); issueA(4, 8); issueB(6, bb + 2 * G1_BST);
    waitv<8 + 3 * NB>(); LBAR(); issueB(7, bb + 3 * G1_BST);
    waitv<6 + 2 * NB>(); LBAR(); LSTAMP(4); issueB(8, bb + 4 * G1_BST);
    waitv<4 + 3 * NB>(); LBAR(); LSTAMP(5); issueA(8, 10); issueB(9, bb);
    waitv<4 + 3 * NB>(); LBAR(); issueB(10, bb + G1_BST);
    waitv<4 + 3 * NB>(); LBAR(); issueB(11, bb + 2 * G1_BST);
    waitv<2 + 3 * NB>(); LBAR(); LSTAMP(6); issueB(12, bb + 3 * G1_BST);
    int nxt = 4 * G1_BST;
#pragma unroll 1
    for (int i = 9; i <= 15; ++i) {
      waitv<NB * 3>(); LBAR();
      if (i >= 11) LSTAMP(i - 4);
      issueB(i + 4, bb + nxt);
      nxt += G1_BST; if (nxt == G1_NSLOT * G1_BST) nxt = 0;
    }
    waitv<NB * 3>(); LBAR(); LSTAMP(12);
    waitv<NB * 2>(); LBAR(); LSTAMP(13);
    waitv<NB * 1>(); LBAR();
    waitv<0>(); LBAR(); LSTAMP(14);
    LBAR();
  }
  const char* osb = ws + WS_WOUT + (size_t)j * 135 * 1024;
  if (lw >= 2) {
    unsigned wvo[4];
#pragma unroll
    for (int h = 0; h < 4; ++h) {
      const int i = 8 * (h & 1) + (lane >> 3);
      wvo[h] = (8 * (i >> 2) + (i & 3) + 4 * (h >> 1)) * (L * 2) + (((lane & 7) ^ (i >> 1)) << 4) - h * 1024;
    }
    const char* wsb = (const char*)wsp + (size_t)(mt * 64 + 32 * (lw & 1)) * (L * 2);
    auto issueW = [&](int s, unsigned slot) {
      gldsN<4, false>(wsb + s * 128, wvo[0], wvo[1], wvo[2], wvo[3], slot + (lw & 1) * 4096);
    };
#pragma unroll 1
    for (int s = 0; s < G2_NW; ++s) issueW(s, lbase + s * 8192);
    LBAR();
    waitv<32>(); LBAR();
    int wn = 0;
#pragma unroll 1
    for (int i = 1; i <= 23; ++i) { waitv<28>(); LBAR(); issueW(i + 8, lbase + wn); wn += 8192; if (wn == G2_NW * 8192) wn = 0; }
#pragma unroll 1
    for (int i = 24; i <= 32; ++i) {
      waitv<28>(); LBAR();
      gldsL<4>(osb + wn + (lw & 1) * 4096, vo, lbase + wn + (lw & 1) * 4096);
      wn += 8192; if (wn == G2_NW * 8192) wn = 0;
    }
  } else {
    constexpr int NZ = NB == 4 ? 7 : 6;
    const char* zsb = ws + WS_Z2 + (size_t)(sub0 * 2 + lw * NZ) * 1024;
    auto issueZ = [&](int s, unsigned slot) {
      gldsL<4>(zsb + s * 108 * 1024, vo, slot + lw * NZ * 1024);
      gldsL<NZ - 4>(zsb + s * 108 * 1024 + 4096, vo, slot + lw * NZ * 1024 + 4096);
    };
    const unsigned zb = lbase + G2_ZOFF;
    LBAR();
#pragma unroll 1
    for (int s = 0; s < G2_NZ; ++s) issueZ(s, zb + s * G2_ZST);
    waitv<NZ * 5>(); LBAR();
    int zn = 0;
#pragma unroll 1
    for (int i = 1; i <= 26; ++i) { waitv<NZ * 4>(); LBAR(); issueZ(i + 5, zb + zn); zn += G2_ZST; if (zn == G2_NZ * G2_ZST) zn = 0; }
    auto issueO = [&](int slot, int n) {
      const int o = G2_ZOFF + slot * G2_ZST + lw * n * 1024;
      if (n == 7) { gldsL<4>(osb + o, vo, lbase + o); gldsL<3>(osb + o + 4096, vo, lbase + o + 4096); }
      else gldsL<3>(osb + o, vo, lbase + o);
    };
    waitv<NZ * 4>(); LBAR(); issueO(2, 7);
    waitv<NZ * 3 + 7>(); LBAR(); issueO(3, 7);
    waitv<NZ * 2 + 14>(); LBAR(); issueO(4, 3);
    waitv<NZ + 17>(); LBAR(); if (lw == 0) gldsL<1>(osb + 134 * 1024, vo, lbase + 134 * 1024);
    waitv<17>(); LBAR(); issueO(0, 7);
    LBAR(); issueO(1, 7);
  }
  waitv<0>();
  LBAR();
}

template <int NSUB, bool STATS>
__device__ __forceinline__ void consumer_g12(LAS char* lds, char* ws, __amdgpu_buffer_rsrc_t rs, const u16* __restrict__ bsp, const u16* __restrict__ wsp, int wg, int mt, int j, int sub0, int cnt,
                                             int wave, int tid, int lane, v2u (&x2v)[3] STAMP_ARG) {
  constexpr int NS1 = NSUB > 0 ? NSUB : 1;
  const int G = wave >> 2, ng = G ? 3 - (wave & 3) : (wave & 3);
  const int q = lane >> 4, i16 = lane & 15, rr = i16 >> 2, p = lane & 3, n0 = 2 * ng;
  float bias[2];
#pragma unroll
  for (int m = 0; m < 2; ++m) bias[m] = bf2f(bsp[mt * 64 + 32 * G + 8 * (i16 >> 2) + 4 * m + (i16 & 3)]);
  STAMP(4);
  CBAR();
  v2u z1v[NS1][2];
  {
    const int aoff0 = g1_aoff(8 * q + rr, G, 0, p), aoff1 = g1_aoff(8 * q + rr, G, 1, p);
    const int boff = G1_BOFF + n0 * 2048 + lane * 16;
    struct Frags { v4u a[2][2]; v4u b[2][NS1]; };
    f32x4 acc2[NS1][2], acc1[NS1][2], asum[2], asq[2];
    int acur = 0, bcur = G1_BST;
    auto rd = [&](Frags& f) {
#pragma unroll
      for (int kk = 0; kk < 2; ++kk) {
        const v2u l0 = trread(lds + acur + aoff0 + kk * 4096), h0 = trread(lds + acur + aoff0 + kk * 4096 + 512);
        const v2u l1 = trread(lds + acur + aoff1 + kk * 4096), h1 = trread(lds + acur + aoff1 + kk * 4096 + 512);
        f.a[kk][0] = v4u{l0.x, l0.y, h0.x, h0.y}; f.a[kk][1] = v4u{l1.x, l1.y, h1.x, h1.y};
#pragma unroll
        for (int n = 0; n < NSUB; ++n) f.b[kk][n] = *(LAS const v4u*)(lds + bcur + boff + n * 2048 + kk * 1024);
      }
      acur += 8192; if (acur == 81920) acur = 0;
      bcur += G1_BST; if (bcur == G1_NSLOT * G1_BST) bcur = 0;
    };
    auto pin1 = [&]() {
#pragma unroll
      for (int n = 0; n < NSUB; ++n) asm volatile("" : "+v"(acc1[n][0]), "+v"(acc1[n][1]));
    };
#pragma unroll
    for (int ab = 0; ab < 2; ++ab) {
      asum[ab] = f32x4{0.f, 0.f, 0.f, 0.f}; asq[ab] = f32x4{0.f, 0.f, 0.f, 0.f};
#pragma unroll
      for (int n = 0; n < NS1; ++n) { acc2[n][ab] = f32x4{0.f, 0.f, 0.f, 0.f}; acc1[n][ab] = f32x4{0.f, 0.f, 0.f, 0.f}; }
    }
    const v4u ones = {0x3f803f80u, 0x3f803f80u, 0x3f803f80u, 0x3f803f80u};
    auto mm2 = [&](const Frags& f) {
#pragma unroll
      for (int kk = 0; kk < 2; ++kk)
#pragma unroll
        for (int ab = 0; ab < 2; ++ab) {
#pragma unroll
          for (int n = 0; n < NSUB; ++n) acc2[n][ab] = mfma16(f.a[ABL_KK][ab], f.b[ABL_KK][n], acc2[n][ab]);
          if (STATS) { asum[ab] = mfma16(f.a[kk][ab], ones, asum[ab]); asq[ab] = mfma16(f.a[kk][ab], f.a[kk][ab], asq[ab]); }
        }
      INTERLEAVE(6, 2);
    };
    auto mm1 = [&](const Frags& f) {
#pragma unroll
      for (int kk = 0; kk < 2; ++kk)
#pragma unroll
        for (int ab = 0; ab < 2; ++ab)
#pragma unroll
          for (int n = 0; n < NSUB; ++n) acc1[n][ab] = mfma16(f.b[kk][n], f.a[kk][ab], acc1[n][ab]);
      if (NSUB == 2) INTERLEAVE(6, 2); else INTERLEAVE(4, 3);
    };
    Frags f0, f1;
    CBAR(); rd(f0);
    SCHED_BAR();
    float up[4];
    {
      const int c = tid & 127, which = (tid >> 7) & 1, half = tid >> 8;
#pragma unroll
      for (int k = 0; k < 4; ++k) up[k] = ld32f(rs, c < cnt * 16 ? (unsigned)WS_U + ((k * 2 + which) * F + (sub0 + (half ? 0 : 54)) * 16 + c) * 4 : 0x7ffffff0u);
    }
    SCHED_BAR();
    STAMP(5);
    auto pin2 = [&]() {
#pragma unroll
      for (int n = 0; n < NSUB; ++n) asm volatile("" : "+v"(acc2[n][0]), "+v"(acc2[n][1]));
      asm volatile("" : "+v"(asum[0]), "+v"(asum[1]), "+v"(asq[0]), "+v"(asq[1]));
    };
    if (STATS) {
      char* cb = ws + CTL_E1 + (G + 1) * 65536;
      unsigned pc;
      poll_issue<8>(pc, cb, wg & (E1_REP - 1), lane);
      CBAR(); rd(f1); mm2(f0);
      if (G == 0) poll_finish<8>(pc, cb, wg & (E1_REP - 1), 256, lane);
      CBAR(); pin2(); rd(f0); mm2(f1);
      CBAR(); rd(f1); mm2(f0);
      poll_issue<8>(pc, cb, wg & (E1_REP - 1), lane);
      CBAR(); rd(f0); mm2(f1);
      if (G == 1) poll_finish<8>(pc, cb, wg & (E1_REP - 1), 128, lane);
      asm volatile("" :: "v"(pc));
      pin2();
    }
#pragma unroll 1
    for (int it = STATS ? 2 : 0; it < 5; ++it) {
      CBAR(); rd(f1); mm2(f0);
      CBAR(); rd(f0); mm2(f1);
    }
    ((LAS float*)(lds + G1_T_U))[tid] = (up[0] + up[1]) + (up[2] + up[3]);
    unsigned pv = 0;
    if (!STATS && ng < 2) pv = *(const unsigned*)((const char*)wsp + (size_t)(mt * 64 + (G * 2 + ng) * 16 + (lane >> 2)) * (L * 2) + j * 512 + (lane & 3) * 128);
    LAS f32x2* tab = (LAS f32x2*)(lds + G1_T_LN2);
    if (STATS) {
#pragma unroll
      for (int ab = 0; ab < 2; ++ab) {
        const float a0 = asum[ab][0], a1 = asum[ab][1], a2 = asum[ab][2], a3 = asum[ab][3];
        const float b0 = asq[ab][0], b1 = asq[ab][1], b2 = asq[ab][2], b3 = asq[ab][3];
        const float s1 = (p & 2) ? ((p & 1) ? a3 : a2) : ((p & 1) ? a1 : a0), s2 = (p & 2) ? ((p & 1) ? b3 : b2) : ((p & 1) ? b1 : b0);
        const float mean = s1 * (1.0f / D);
        float mb, rinv;
        ln_scalars(mean, __builtin_fmaxf(s2 * (1.0f / D) - mean * mean, 0.f), mb, rinv);
        if (rr == q) tab[(G * 2 + ab) * 16 + i16] = f32x2{rinv, rinv * mb};
      }
    }
    {
      const int nb = G ? 3 : 0;
#pragma unroll
      for (int n = 0; n < 3; ++n) {
        const int row = 80 * j + 16 * (nb + n < 5 ? nb + n : 4) + 4 * q + rr;
        x2v[n] = trread(lds + g1_aoff(row, (wave >> 1) & 1, wave & 1, p));
      }
    }
    STAMP(6);
    CBAR(); rd(f1); mm1(f0);
    float zq[NS1][8];
    {
      LAS const float* ut = (LAS const float*)(lds + G1_T_U);
      float v[16];
#pragma unroll
      for (int e = 0; e < 16; ++e) v[e] = 0.f;
      if (NSUB > 0) {
        f32x2 rv[8];
#pragma unroll
        for (int e = 0; e < 8; ++e) rv[e] = tab[(G * 2 + (e >> 2)) * 16 + 4 * q + (e & 3)];
#pragma unroll
        for (int n = 0; n < NSUB; ++n) {
          const float c1 = ut[(n0 + n) * 16 + i16], c2 = ut[128 + (n0 + n) * 16 + i16];
#pragma unroll
          for (int e = 0; e < 8; ++e) {
            const float z = gelu_f(__builtin_fmaf(rv[e].x, acc2[n][e >> 2][e & 3], __builtin_fmaf(-rv[e].y, c1, c2)));
            zq[n][e] = z;
            v[2 * e] = n ? v[2 * e] + z : z; v[2 * e + 1] = n ? __builtin_fmaf(z, z, v[2 * e + 1]) : z * z;
          }
        }
        treduce_dpp<8, 0x140>(v, (i16 & 8) != 0);
        treduce_dpp<4, 0x141>(v, (i16 & 4) != 0);
        treduce_dpp<2, 0x4e>(v, (i16 & 2) != 0);
        treduce_dpp<1, 0xb1>(v, (i16 & 1) != 0);
      }
      ((LAS float*)(lds + G1_T_P))[ng * 128 + G * 64 + lane] = v[0];
    }
    CBAR(); pin1();
    GAS unsigned long long* gr = (GAS unsigned long long*)(ws + CTL_ST3) + (size_t)mt * 1024 + G * 32;
    if (STATS) {
      LAS const float* pp = (LAS const float*)(lds + G1_T_P) + G * 64 + lane;
      const float sv = (pp[0] + pp[128]) + (pp[256] + pp[384]);
      __hip_atomic_store(gr + (j * 2 + (lane & 1)) * 64 + (lane >> 1), (1ull << 32) | f2u(sv), RLX_AGENT);
    }
    SCHED_BAR(); rd(f0); mm1(f1);
    CBAR(); pin1(); rd(f1); mm1(f0);
    CBAR(); pin1(); rd(f0); mm1(f1);
    unsigned long long gx[8];
    if (STATS) {
#pragma unroll
      for (int k = 0; k < 8; ++k) gx[k] = __hip_atomic_load(gr + (k * 2 + (lane >> 5)) * 64 + (lane & 31), RLX_AGENT);
    }
    CBAR(); pin1(); rd(f1); mm1(f0);
    if (STATS) {
      float sv;
      for (unsigned spin = 0;; ++spin) {
        bool ok = true;
        sv = 0.f;
#pragma unroll
        for (int k = 0; k < 8; ++k) { ok &= (gx[k] >> 32) == 1ull; sv += u2f((unsigned)gx[k]); }
        if (wall(ok) || spin > SPIN_CAP) break;
        __builtin_amdgcn_s_sleep(1);
#pragma unroll
        for (int k = 0; k < 8; ++k) gx[k] = __hip_atomic_load(gr + (k * 2 + (lane >> 5)) * 64 + (lane & 31), RLX_AGENT);
      }
      const float s2 = shx(sv, 32);
      const float mean = sv * (1.0f / FH);
      float mb, rinv;
      ln_scalars(mean, __builtin_fmaxf(s2 * (1.0f / FH) - mean * mean, 0.f), mb, rinv);
      if (lane < 32) ((LAS f32x2*)(lds + G1_T_Z))[32 * G + lane] = f32x2{-mb * rinv, rinv};
    }
    CBAR(); pin1();
    if (NSUB > 0) {
      f32x2 zs[8];
#pragma unroll
      for (int e = 0; e < 8; ++e) zs[e] = ((LAS const f32x2*)(lds + G1_T_Z))[32 * G + 8 * q + e];
#pragma unroll
      for (int n = 0; n < NSUB; ++n) {
        v4u o;
#pragma unroll
        for (int e = 0; e < 4; ++e)
          o[e] = pkbf(__builtin_fmaf(zq[n][2 * e], zs[2 * e].y, zs[2 * e].x), __builtin_fmaf(zq[n][2 * e + 1], zs[2 * e + 1].y, zs[2 * e + 1].x));
        st128(rs, (unsigned)WS_Z2 + (((mt * 54 + sub0 + n0 + n) * 2 + G) * 64 + lane) * 16, o);
      }
    }
    SCHED_BAR(); rd(f0); mm1(f1);
    CBAR(); pin1(); rd(f1); mm1(f0);
    CBAR(); pin1(); rd(f0); mm1(f1);
    CBAR(); pin1(); rd(f1); mm1(f0);
    if (wave_arrive(lds, 3, 8, lane)) signal<4, 4>(ws + CTL_E2 + j * 4096, mt & 3, lane);
    asm volatile("" :: "v"(pv));
    CBAR(); pin1();
    mm1(f1);
    STAMP(7);
    {
      LAS const float* ut = (LAS const float*)(lds + G1_T_U) + 256;
      f32x2 rv[2];
#pragma unroll
      for (int ab = 0; ab < 2; ++ab) rv[ab] = tab[(G * 2 + ab) * 16 + i16];
#pragma unroll
      for (int n = 0; n < NSUB; ++n) {
        const f32x4 u1 = *(LAS const f32x4*)(ut + (n0 + n) * 16 + 4 * q), u2 = *(LAS const f32x4*)(ut + 128 + (n0 + n) * 16 + 4 * q);
#pragma unroll
        for (int ab = 0; ab < 2; ++ab) {
          float z[4];
#pragma unroll
          for (int e = 0; e < 4; ++e) z[e] = gelu_f(__builtin_fmaf(rv[ab].x, acc1[n][ab][e], __builtin_fmaf(-rv[ab].y, u1[e], u2[e])));
          z1v[n][ab] = v2u{pkbf(z[0], z[1]), pkbf(z[2], z[3])};
          asm volatile("" : "+v"(z1v[n][ab]));
        }
      }
    }
  }
  if (wave == 1 || wave == 2) {
    const int c = tid - 64;
    if (c < 80) {
      const unsigned o = (unsigned)WS_MOD + (5 * D + 80 * j + c) * 8;
      ((LAS float*)(lds + G3_GB))[c] = bfr((ld32f(rs, o) + ld32f(rs, o + 30720)) + (ld32f(rs, o + 61440) + ld32f(rs, o + 92160)));
    }
  }
  if (wave == 3) wait_count<4>(ws + CTL_E2 + j * 4096, (mt >> 2) & 3, 32, lane);
  CBAR();
  STAMP(9);
  {
    f32x4 acc[NS1][2];
#pragma unroll
    for (int n = 0; n < NS1; ++n) { acc[n][0] = f32x4{0.f, 0.f, 0.f, 0.f}; acc[n][1] = f32x4{0.f, 0.f, 0.f, 0.f}; }
    int woff[2];
#pragma unroll
    for (int kk = 0; kk < 2; ++kk) woff[kk] = G * 4096 + i16 * 128 + (((4 * kk + q) ^ (i16 >> 1)) << 4);
    const int zoff = G2_ZOFF + n0 * 2048 + lane * 16;
    struct Frags { v4u w[2][2]; v4u z[2][NS1]; };
    int wc = 0, zc = 0;
    auto rd = [&](Frags& f) {
      if (NSUB > 0) {
#pragma unroll
        for (int kk = 0; kk < 2; ++kk) {
#pragma unroll
          for (int m = 0; m < 2; ++m) f.w[kk][m] = *(LAS const v4u*)(lds + wc + woff[kk] + m * 2048);
#pragma unroll
          for (int n = 0; n < NSUB; ++n) f.z[kk][n] = *(LAS const v4u*)(lds + zc + zoff + n * 2048 + kk * 1024);
        }
        wc += 8192; if (wc == G2_NW * 8192) wc = 0;
        zc += G2_ZST; if (zc == G2_NZ * G2_ZST) zc = 0;
      }
    };
    auto mm = [&](const Frags& f) {
#pragma unroll
      for (int kk = 0; kk < 2; ++kk)
#pragma unroll
        for (int n = 0; n < NSUB; ++n)
#pragma unroll
          for (int m = 0; m < 2; ++m) acc[n][m] = mfma16(f.z[ABL_KK][n], f.w[ABL_KK][m], acc[n][m]);
      INTERLEAVE(4, 2);
    };
    Frags f0, f1;
    CBAR(); rd(f0);
    STAMP(10);
#pragma unroll 1
    for (int it = 0; it < 15; ++it) {
      CBAR(); rd(f1); mm(f0);
      CBAR(); rd(f0); mm(f1);
    }
    CBAR(); rd(f1); mm(f0);
    mm(f1);
    STAMP(11);
#pragma unroll
    for (int m = 0; m < 2; ++m) {
      v2u o[NS1];
#pragma unroll
      for (int n = 0; n < NSUB; ++n) {
        float g[4];
#pragma unroll
        for (int e = 0; e < 4; ++e) g[e] = acc[n][m][e] + bias[m];
        const v2u zz = z1v[n][m];
        o[n] = v2u{pkbf(bflo(zz.x) * g[0], bfhi(zz.x) * g[1]), pkbf(bflo(zz.y) * g[2], bfhi(zz.y) * g[3])};
      }
      const unsigned vb = (unsigned)WS_V + (mt * 4 + G * 2 + m) * 27 * 1024 + lane * 16;
      if (NSUB == 2) st128(rs, vb + (3 * j + ng) * 1024, v4u{o[0].x, o[0].y, o[NS1 - 1].x, o[NS1 - 1].y});
      if (NSUB == 1) __builtin_amdgcn_raw_buffer_store_b64(o[0], rs, vb + (24 + (j >> 1)) * 1024 + (j & 1) * 8, 0, 16);
    }
    CBAR();
  }
}

template <int KH>
__device__ __forceinline__ void consumer_g3(LAS char* lds, char* ws, __amdgpu_buffer_rsrc_t rs, u16* __restrict__ out, int mt, int j, int m, int lane, const v2u (&x2v)[3] STAMP_ARG) {
  constexpr int K0 = KH ? 14 : 0, NK = KH ? 13 : 14, NF = KH ? 2 : 3, N0 = KH ? 3 : 0;
  const int q = lane >> 4, i16 = lane & 15;
  v4u vf[NK];
  __builtin_amdgcn_s_sleep(V_DELAY);
  CBAR();
  STAMP(12);
  const unsigned vo = (unsigned)WS_V + ((mt * 4 + m) * 27 + K0) * 1024 + lane * 16;
  constexpr int VW = V_WIN;
#pragma unroll
  for (int k = 0; k < VW; ++k) { vf[k] = ld128(rs, vo + k * 1024); SCHED_BAR(); }
  f32x4 acc[5];
#pragma unroll
  for (int n = 0; n < 5; ++n) acc[n] = f32x4{0.f, 0.f, 0.f, 0.f};
  LAS const char* wp = lds + K0 * 1024 + lane * 16;
  LAS const char* wp2 = wp + 54 * 1024;
  LAS const char* wp4 = wp + 108 * 1024;
  v4u wf[3][5];
  auto rdw = [&](int k, v4u (&w)[5]) {
    w[0] = *(LAS const v4u*)(wp + k * 1024); w[1] = *(LAS const v4u*)(wp + (27 + k) * 1024);
    w[2] = *(LAS const v4u*)(wp2 + k * 1024); w[3] = *(LAS const v4u*)(wp2 + (27 + k) * 1024);
    w[4] = *(LAS const v4u*)(wp4 + k * 1024);
  };
  rdw(0, wf[0]); rdw(1, wf[1]);
#pragma unroll
  for (int k = 0; k < NK; ++k) {
    if (k + VW < NK) vf[k + VW] = ld128(rs, vo + (k + VW) * 1024);
    if (k + 2 < NK) rdw(k + 2, wf[(k + 2) % 3]);
    SCHED_BAR();
    if (__builtin_expect(!wall(__builtin_elementwise_max(vf[k].x, vf[k].z) != ~0u), 0)) {
      unsigned spin = 0;
      do { asm volatile("" ::: "memory"); vf[k] = ld128(rs, vo + k * 1024); }
      while (!wall(__builtin_elementwise_max(vf[k].x, vf[k].z) != ~0u) && ++spin < SPIN_CAP);
    }
#pragma unroll
    for (int n = 0; n < 5; ++n) acc[n] = mfma16(wf[k % 3][n], vf[k], acc[n]);
    asm volatile("" : "+v"(acc[0]), "+v"(acc[1]), "+v"(acc[2]), "+v"(acc[3]), "+v"(acc[4]));
    SCHED_BAR();
  }
  STAMP(13);
  f32x4 gb[NF];
#pragma unroll
  for (int n = 0; n < NF; ++n) gb[n] = *(LAS const f32x4*)(lds + G3_GB + (16 * (N0 + n) + 4 * q) * 4);
  LAS char* red = lds + G3_RED + m * 5120 + lane * 16;
#pragma unroll
  for (int n = 0; n < 5; ++n) if ((n >= 3) != (KH != 0)) *(LAS f32x4*)(red + n * 1024) = acc[n];
  CBAR();
  const int t = mt * 64 + 32 * (m >> 1) + 8 * (i16 >> 2) + 4 * (m & 1) + (i16 & 3);
#pragma unroll
  for (int n = 0; n < NF; ++n) {
    const f32x4 o = *(LAS const f32x4*)(red + (N0 + n) * 1024);
    const f32x4 a = KH ? o + acc[N0 + n] : acc[N0 + n] + o;
    const float o0 = __builtin_fmaf(gb[n][0], a[0], bflo(x2v[n].x)), o1 = __builtin_fmaf(gb[n][1], a[1], bfhi(x2v[n].x));
    const float o2 = __builtin_fmaf(gb[n][2], a[2], bflo(x2v[n].y)), o3 = __builtin_fmaf(gb[n][3], a[3], bfhi(x2v[n].y));
    *(v2u*)((char*)out + ((size_t)t * D + 80 * j + 16 * (N0 + n) + 4 * q) * 2) = v2u{pkbf(o0, o1), pkbf(o2, o3)};
  }
}

__global__ __launch_bounds__(768, 3) void k_all(Args A) {
  extern __shared__ __attribute__((aligned(16))) char smem[];
  unsigned long long pc;
  asm volatile("s_getpc_b64 %0" : "=s"(pc));
  LAS char* lds = (LAS char*)smem;
  char* ws = A.ws;
  const int tid = __builtin_amdgcn_workitem_id_x(), lane = tid & 63, wave = __builtin_amdgcn_readfirstlane(tid >> 6), wg = __builtin_amdgcn_workgroup_id_x();
  STAMP_DECL; STAMP(0);
  const __amdgpu_buffer_rsrc_t rs = __builtin_amdgcn_make_buffer_rsrc((void*)ws, 0, (int)WS_END, 0x00020000);
  asm volatile("" :: "s"(A.x), "s"(A.Are), "s"(A.Aim), "s"(A.Bre), "s"(A.Bim), "s"(A.Cre), "s"(A.Cim), "s"(A.logdt), "s"(A.dskip), "s"(A.win), "s"(A.wsp),
               "s"(A.bsp), "s"(A.wout), "s"(A.cvec), "s"(A.wmod), "s"(A.ws), "s"(A.out));
  const int grp = wave >> 2, bt = grp == 2 ? 0 : grp + 1, gw = wave & 3, gt = tid & 255;
  const bool isaux = bt == 2 && wg >= 128;
  const int u = wg - 128, d = 256 * bt + (wg & 7) * (bt < 2 ? 32 : 16) + (wg >> 3);
  unsigned epoch;
  asm volatile("global_load_dword %0, %1, %2" : "=v"(epoch) : "v"((unsigned)CTL_EPOCH), "s"(ws) : "memory");
  if (__builtin_expect(isaux, 1)) {
    const int rr = lane >> 4;
    const unsigned vo = rr * (D * 2) + (((lane & 15) ^ ((rr | ((gw >> 1) << 2)) << 1)) << 4);
    const char* xb = (const char*)A.x + (size_t)(u * 16 + gw * 4) * (D * 2);
#pragma unroll
    for (int i = 0; i < 5; ++i) glds16nt(xb + i * 256, vo, (unsigned)(size_t)lds + C_GRP + 8192 + (gw * 5 + i) * 1024);
  }
  unsigned pre[7] = {};
  if (__builtin_expect(!isaux, 1)) {
    const unsigned o = (d * NS + (gw & 1) * 64 + lane) * 2;
#define LDU16(dst, base, voff) asm volatile("global_load_ushort %0, %1, %2" NT : "=v"(dst) : "v"(voff), "s"(base) : "memory")
    LDU16(pre[0], A.Are, o); LDU16(pre[1], A.Aim, o); LDU16(pre[2], A.logdt, d * 2); LDU16(pre[3], A.Bre, o);
    LDU16(pre[4], A.Bim, o); LDU16(pre[5], A.Cre, o); LDU16(pre[6], A.Cim, o);
  }
  STAMP(11);
  if (tid < 16) ((LAS unsigned*)(lds + M_CTL))[tid] = 0u;
  unsigned tag = 0;
  CBAR();
  {
    LAS char* gl = lds + (grp == 2 ? G1_TAB - C_GRP : grp * C_GRP);
    if (bt == 0) __builtin_amdgcn_s_setprio(2);
    if (bt == 1) __builtin_amdgcn_s_setprio(1);
    if (isaux) { __builtin_amdgcn_s_setprio(3); aux_group(gl, lds, A, rs, epoch, u, gt, gw, lane STAMP_PASS); }
    else conv_group(gl, lds, bt, A, rs, epoch, tag, pre, pc, wg, d, gt, gw, lane STAMP_PASS);
    __builtin_amdgcn_s_setprio(0);
  }
  const int xcd = wg & 7, idx = wg >> 3;
  const int mt = (xcd >> 1) * 8 + (idx & 7), j = (xcd & 1) * 4 + (idx >> 3);
  const int cnt = j < 6 ? 7 : 6, sub0 = j < 6 ? 7 * j : 42 + 6 * (j - 6);
  if (wave >= 8) {
    STAMP(4);
    const int lw = wave - 8;
    if (lw + 12 < 2 * cnt) loader_path<4>(lds, ws, rs, A.wsp, wg, mt, j, sub0, lw, lane);
    else loader_path<3>(lds, ws, rs, A.wsp, wg, mt, j, sub0, lw, lane);
    if (wg == 0 && tid == 512) *(volatile unsigned*)(ws + CTL_EPOCH) = tag;
    STAMP_FLUSH(1);
    return;
  }
  v2u x2v[3];
  const int ng = wave >> 2 ? 3 - (wave & 3) : (wave & 3);
  if (ng < 3) consumer_g12<2, false>(lds, ws, rs, A.bsp, A.wsp, wg, mt, j, sub0, cnt, wave, tid, lane, x2v STAMP_PASS);
  else if (cnt == 7) consumer_g12<1, true>(lds, ws, rs, A.bsp, A.wsp, wg, mt, j, sub0, cnt, wave, tid, lane, x2v STAMP_PASS);
  else consumer_g12<0, true>(lds, ws, rs, A.bsp, A.wsp, wg, mt, j, sub0, cnt, wave, tid, lane, x2v STAMP_PASS);
  if (wave < 4) consumer_g3<0>(lds, ws, rs, A.out, mt, j, wave & 3, lane, x2v STAMP_PASS);
  else consumer_g3<1>(lds, ws, rs, A.out, mt, j, wave & 3, lane, x2v STAMP_PASS);
  STAMP(14);
  STAMP_FLUSH(1);
}

#if defined(__HIP_DEVICE_COMPILE__) && !defined(KEEP_PK)
#pragma clang attribute pop
#endif

extern "C" void kernel_launch(void* const* d_in, const int* in_sizes, int n_in, void* d_out, int out_size, void* d_ws, size_t ws_size,
                              hipStream_t stream) {
  Args A;
  A.x = (const u16*)d_in[0]; A.Are = (const u16*)d_in[1]; A.Aim = (const u16*)d_in[2]; A.Bre = (const u16*)d_in[3]; A.Bim = (const u16*)d_in[4];
  A.Cre = (const u16*)d_in[5]; A.Cim = (const u16*)d_in[6]; A.logdt = (const u16*)d_in[7]; A.dskip = (const u16*)d_in[8];
  A.win = (const u16*)d_in[10]; A.wsp = (const u16*)d_in[11]; A.bsp = (const u16*)d_in[12]; A.wout = (const u16*)d_in[13];
  A.cvec = (const u16*)d_in[14]; A.wmod = (const u16*)d_in[15];
  A.ws = (char*)d_ws; A.out = (u16*)d_out;
  (void)hipFuncSetAttribute((const void*)k_all, hipFuncAttributeMaxDynamicSharedMemorySize, M_LDS);
  k_all<<<256, 768, M_LDS, stream>>>(A);
}
```

```cpp
#include <hip/hip_runtime.h>
#include <hip/hip_bf16.h>
#include <stdint.h>

#define NT ""
#define NTLOAD(p) (*(p))
#define GAS __attribute__((address_space(1)))
#define LAS __attribute__((address_space(3)))
typedef unsigned v4u __attribute__((ext_vector_type(4)));
typedef unsigned v2u __attribute__((ext_vector_type(2)));
typedef float f32x2 __attribute__((ext_vector_type(2)));
typedef float f32x4 __attribute__((ext_vector_type(4)));
typedef float f32x16 __attribute__((ext_vector_type(16)));
typedef __bf16 bf16x2 __attribute__((ext_vector_type(2)));
typedef __bf16 bf16x8 __attribute__((ext_vector_type(8)));
typedef _Float16 f16x2 __attribute__((ext_vector_type(2)));
typedef _Float16 f16x8 __attribute__((ext_vector_type(8)));
typedef short s16x4 __attribute__((ext_vector_type(4)));
typedef unsigned short u16;

#if defined(__HIP_DEVICE_COMPILE__) && !defined(KEEP_PK)
#pragma clang attribute push(__attribute__((target("no-packed-fp32-ops"))), apply_to = function)
#endif
__device__ __forceinline__ float u2f(unsigned u) { return __builtin_bit_cast(float, u); }
__device__ __forceinline__ unsigned f2u(float f) { return __builtin_bit_cast(unsigned, f); }
__device__ __forceinline__ int lane_id() { return __builtin_amdgcn_mbcnt_hi(~0u, __builtin_amdgcn_mbcnt_lo(~0u, 0u)); }
__device__ __forceinline__ unsigned shx(unsigned v, int m) { return __builtin_amdgcn_ds_bpermute((lane_id() ^ m) << 2, v); }
__device__ __forceinline__ float shx(float v, int m) { return u2f(shx(f2u(v), m)); }
__device__ __forceinline__ bool wall(bool p) { return __builtin_amdgcn_ballot_w64(!p) == 0ull; }
__device__ __forceinline__ float exp_acc(float x) {
  const float hi = x * 1.4426950408889634f;
  const float lo = __builtin_fmaf(x, 1.4426950408889634f, -hi) + x * 1.925963033500414e-8f;
  const float e = __builtin_amdgcn_exp2f(hi);
  return __builtin_fmaf(e, lo * 0.6931471805599453f, e);
}

constexpr int L = 2048, D = 640, NS = 128, F = 1728, FH = 864;

constexpr size_t CTL_E1 = 0;
constexpr size_t CTL_E1B = 196608;
constexpr size_t CTL_E2 = 262144;
constexpr size_t CTL_E3 = 294912;
constexpr size_t CTL_EW = 303104;
constexpr size_t CTL_ST3 = 327680;
constexpr size_t CTL_F0 = CTL_ST3 + 262144;
constexpr size_t CTL_EPOCH = CTL_F0 + 32768;
constexpr size_t WS_MOD = CTL_EPOCH + 4096;
constexpr size_t WS_ST1 = WS_MOD + 131072;
constexpr size_t WS_U = WS_ST1 + 16384;
constexpr size_t WS_XT = WS_U + 65536;
constexpr size_t WS_X2T = WS_XT + (size_t)D * L * 2;
constexpr size_t WS_WIN = WS_X2T + (size_t)D * L * 2;
constexpr size_t WS_WOUT = WS_WIN + (size_t)D * F * 2;
constexpr size_t WS_Z2 = WS_WOUT + (size_t)FH * D * 2;
constexpr size_t WS_V = WS_Z2 + (size_t)L * FH * 2;
constexpr size_t WS_END = WS_V + (size_t)L * FH * 2;

__device__ __forceinline__ float bflo(unsigned u) { return u2f(u << 16); }
__device__ __forceinline__ float bfhi(unsigned u) { return u2f(u & 0xffff0000u); }
__device__ __forceinline__ float bf2f(u16 v) { return u2f((unsigned)v << 16); }
__device__ __forceinline__ unsigned pkbf(float a, float b) {
  f32x2 v = {a, b};
  return __builtin_bit_cast(unsigned, __builtin_convertvector(v, bf16x2));
}
__device__ __forceinline__ unsigned pkh(float a, float b) {
  f32x2 v = {a, b};
  return __builtin_bit_cast(unsigned, __builtin_convertvector(v, f16x2));
}
__device__ __forceinline__ float bfr(float a) { return bflo(pkbf(a, a)); }
__device__ __forceinline__ v2u trread(LAS const char* p) {
  return __builtin_bit_cast(v2u, __builtin_amdgcn_ds_read_tr16_b64_v4i16((LAS s16x4*)p));
}
__device__ __forceinline__ f32x4 mfma16(v4u a, v4u b, f32x4 c) {
  return __builtin_amdgcn_mfma_f32_16x16x32_bf16(__builtin_bit_cast(bf16x8, a), __builtin_bit_cast(bf16x8, b), c, 0, 0, 0);
}
__device__ __forceinline__ f32x16 mfma32(v4u a, v4u b, f32x16 c) {
  return __builtin_amdgcn_mfma_f32_32x32x16_bf16(__builtin_bit_cast(bf16x8, a), __builtin_bit_cast(bf16x8, b), c, 0, 0, 0);
}
__device__ __forceinline__ f32x16 mfma32h(v4u a, v4u b, f32x16 c) {
  return __builtin_amdgcn_mfma_f32_32x32x16_f16(__builtin_bit_cast(f16x8, a), __builtin_bit_cast(f16x8, b), c, 0, 0, 0);
}
template <bool DEV>
__device__ __forceinline__ void glds16(const void* sbase, unsigned voff, unsigned dst) {
  unsigned keep;
  if (DEV) asm volatile(
"s_mov_b32 %0, m0\n\ts_mov_b32 m0, %3\n\ts_nop 0\n\tglobal_load_lds_dwordx4 %1, %2 sc1\n\ts_mov_b32 m0, %0"
                        : "=&s"(keep) : "v"(voff), "s"(sbase), "s"(dst) : "memory");
  else asm volatile("s_mov_b32 %0, m0\n\ts_mov_b32 m0, %3\n\ts_nop 0\n\tglobal_load_lds_dwordx4 %1, %2\n\ts_mov_b32 m0, %0"
                    : "=&s"(keep) : "v"(voff), "s"(sbase), "s"(dst) : "memory");
}
__device__ __forceinline__ void glds16nt(const void* sbase, unsigned voff, unsigned dst) {
  unsigned keep;
  asm volatile("s_mov_b32 %0, m0\n\ts_mov_b32 m0, %3\n\ts_nop 0\n\tglobal_load_lds_dwordx4 %1, %2" NT "\n\ts_mov_b32 m0, %0"
               : "=&s"(keep) : "v"(voff), "s"(sbase), "s"(dst) : "memory");
}
#define GL_(i, s) "global_load_lds_dwordx4 %" #i ", %5 offset:" s
template <int N, bool DEV>
__device__ __forceinline__ void gldsN(const void* sbase, unsigned v0, unsigned v1, unsigned v2, unsigned v3, unsigned dst) {
  unsigned keep;
#define GLN_(body) asm volatile("s_mov_b32 %0, m0\n\ts_mov_b32 m0, %6\n\ts_nop 0\n\t" body "s_mov_b32 m0, %0" : "=&s"(keep) : "v"(v0), "v"(v1), "v"(v2), "v"(v3), "s"(sbase), "s"(dst) : "memory")
  if (DEV) {
    if (N == 1) GLN_(GL_(1, "0 sc1\n\t"));
    if (N == 2) GLN_(GL_(1, "0 sc1\n\t") GL_(2, "1024 sc1\n\t"));
    if (N == 3) GLN_(GL_(1, "0 sc1\n\t") GL_(2, "1024 sc1\n\t") GL_(3, "2048 sc1\n\t"));
    if (N == 4) GLN_(GL_(1, "0 sc1\n\t") GL_(2, "1024 sc1\n\t") GL_(3, "2048 sc1\n\t") GL_(4, "3072 sc1\n\t"));
  } else {
    if (N == 2) GLN_(GL_(1, "0\n\t") GL_(2, "1024\n\t"));
    if (N == 4) GLN_(GL_(1, "0\n\t") GL_(2, "1024\n\t") GL_(3, "2048\n\t") GL_(4, "3072\n\t"));
  }
}
template <int N> __device__ __forceinline__ void gldsL(const void* sbase, unsigned vo, unsigned dst) { gldsN<N, true>(sbase, vo, vo, vo, vo, dst); }
template <int N> __device__ __forceinline__ void waitv() { asm volatile("s_waitcnt vmcnt(%0)" :: "i"(N) : "memory"); }
__device__ __forceinline__ float dot2bf(unsigned a, unsigned b, float c) {
  return __builtin_amdgcn_fdot2_f32_bf16(__builtin_bit_cast(bf16x2, a), __builtin_bit_cast(bf16x2, b), c, false);
}
#define SCHED_BAR() __builtin_amdgcn_sched_barrier(0)
#define STAMP_DECL
#define STAMP(i) do {} while (0)
#define STAMP_FLUSH(kid) do {} while (0)
#define STAMP_ARG
#define STAMP_PASS
#define CBAR() do { SCHED_BAR(); __builtin_amdgcn_s_waitcnt(0xC07F); asm volatile("" ::: "memory"); __builtin_amdgcn_s_barrier(); asm volatile("" ::: "memory"); } while (0)
#define LBAR() __builtin_amdgcn_s_barrier()

template <int CTRL> __device__ __forceinline__ float dppf(float v) {
  return __builtin_bit_cast(float, __builtin_amdgcn_update_dpp(0, __builtin_bit_cast(int, v), CTRL, 0xf, 0xf, true));
}
template <int N, int CTRL> __device__ __forceinline__ void treduce_dpp(float* v, bool up) {
#pragma unroll
  for (int e = 0; e < N; ++e) {
    const float a = v[e], b = v[e + N];
    const float send = up ? a : b, keep = up ? b : a;
    v[e] = keep + dppf<CTRL>(send);
  }
}
template <int N> __device__ __forceinline__ void treduce_xor(float* v, bool up, int mask) {
#pragma unroll
  for (int e = 0; e < N; ++e) {
    const float a = v[e], b = v[e + N];
    const float send = up ? a : b, keep = up ? b : a;
    v[e] = keep + shx(send, mask);
  }
}

__device__ __forceinline__ float ln_std(float var) { return __builtin_amdgcn_sqrtf(bfr(bfr(var) + 1e-5f)); }
__device__ __forceinline__ void ln_scalars(float mean, float var, float& m_b, float& rinv) {
  m_b = bfr(mean);
  rinv = __builtin_amdgcn_rcpf(bfr(ln_std(var)));
}

struct cplx { float r, i; };
__device__ __forceinline__ cplx cmul(cplx a, cplx b) { return {a.r * b.r - a.i * b.i, a.r * b.i + a.i * b.r}; }

typedef GAS unsigned gu32;
#define RLX_AGENT __ATOMIC_RELAXED, __HIP_MEMORY_SCOPE_AGENT
#define F0_REP 8
#define V_DELAY 24
#define V_WIN 5
#define LSTAMP(i) do {} while (0)
#define POLL_SLEEP 6
#define E1_REP 32
#define INTERLEAVE(n, r) do { _Pragma("unroll") for (int i_ = 0; i_ < (n); ++i_) { __builtin_amdgcn_sched_group_barrier(0x008, 1, 0); __builtin_amdgcn_sched_group_barrier(0x100, (r), 0); } \
    __builtin_amdgcn_sched_group_barrier(0x008, 8, 0); SCHED_BAR(); } while (0)
#define ABL_KK kk
#define AUXB_PRIO 2
#define XGATE 16
constexpr unsigned SPIN_CAP = 1u << 16;
constexpr int M_LDS = 163840, M_CTL = M_LDS - 64;
constexpr int CODE_LINES = 400;
constexpr int C_KOFF = 0, C_UOFF = 4224, C_UP = 1536, C_COFF = C_UOFF + 4 * C_UP, C_GRP = C_COFF + 4 * 64 * 144;
constexpr int G1_BOFF = 81920, G1_BST = 14336, G1_NSLOT = 5, G1_TAB = G1_BOFF + G1_BST * G1_NSLOT;
constexpr int G1_T_LN2 = G1_TAB, G1_T_U = G1_T_LN2 + 512, G1_T_P = G1_T_U + 2048, G1_T_Z = G1_T_P + 2048;
constexpr int G2_NW = 9, G2_NZ = 6, G2_ZOFF = G2_NW * 8192, G2_ZST = 14336;
constexpr int G3_RED = 135 * 1024, G3_GB = 161024;
static_assert(3 * C_GRP <= M_CTL && G1_T_Z + 512 <= M_CTL && G2_ZOFF + G2_NZ * G2_ZST <= M_CTL && G3_GB + 320 <= M_CTL, "lds");

__device__ __forceinline__ void st128(__amdgpu_buffer_rsrc_t rs, unsigned off, v4u v) { __builtin_amdgcn_raw_buffer_store_b128(v, rs, off, 0, 16); }
__device__ __forceinline__ v4u ld128(__amdgpu_buffer_rsrc_t rs, unsigned off) { return __builtin_amdgcn_raw_buffer_load_b128(rs, off, 0, 16); }

__device__ __forceinline__ bool wave_arrive(LAS char* lds, int word, unsigned n, int lane) {
  waitv<0>();
  unsigned old = 0;
  if (lane == 0) old = __hip_atomic_fetch_add((LAS unsigned*)(lds + M_CTL) + word, 1u, __ATOMIC_RELAXED, __HIP_MEMORY_SCOPE_WORKGROUP);
  return (unsigned)__builtin_amdgcn_readfirstlane(old) == n - 1;
}
__device__ __forceinline__ void lds_wait(LAS char* lds0, int word, unsigned target) {
  LAS unsigned* p = (LAS unsigned*)(lds0 + M_CTL) + word;
  while ((unsigned)__builtin_amdgcn_readfirstlane(__hip_atomic_load(p, __ATOMIC_RELAXED, __HIP_MEMORY_SCOPE_WORKGROUP)) < target) __builtin_amdgcn_s_sleep(0);
  asm volatile("" ::: "memory");
}
__device__ __forceinline__ void lds_post(LAS char* lds0, int word, int lane) {
  SCHED_BAR();
  asm volatile("" ::: "memory");
  if (lane == 0) __hip_atomic_fetch_add((LAS unsigned*)(lds0 + M_CTL) + word, 1u, __ATOMIC_RELAXED, __HIP_MEMORY_SCOPE_WORKGROUP);
  asm volatile("" ::: "memory");
}
__device__ __forceinline__ void group_sync(LAS char* lds0, int word, unsigned target, int lane) { lds_post(lds0, word, lane); lds_wait(lds0, word, target); }
template <int NREP, int NSH> __device__ __forceinline__ void signal(char* base, int shard, int lane) {
  if (lane < NREP) __hip_atomic_fetch_add((gu32*)(base + (lane * NSH + shard) * 256), 1u, RLX_AGENT);
}
template <int NSH> __device__ __forceinline__ bool count_ok(unsigned c, unsigned total) {
  if (NSH > 1) c += __builtin_amdgcn_update_dpp(0, c, 0xb1, 0xf, 0xf, true);
  if (NSH > 2) c += __builtin_amdgcn_update_dpp(0, c, 0x4e, 0xf, 0xf, true);
  if (NSH > 4) c += __builtin_amdgcn_update_dpp(0, c, 0x141, 0xf, 0xf, true);
  return (unsigned)__builtin_amdgcn_readfirstlane(c) >= total;
}
template <int NSH> __device__ __forceinline__ void wait_count(char* base, int rep, unsigned total, int lane) {
  gu32* p = (gu32*)(base + (rep * NSH + (lane & (NSH - 1))) * 256);
#pragma unroll 1
  for (unsigned spin = 0; spin < SPIN_CAP; ++spin) {
    if (count_ok<NSH>(__hip_atomic_load(p, RLX_AGENT), total)) break;
    __builtin_amdgcn_s_sleep(POLL_SLEEP);
  }
  __builtin_amdgcn_fence(__ATOMIC_ACQUIRE, "wavefront");
}
template <int NSH> __device__ __forceinline__ void poll_issue(unsigned& c, char* base, int rep, int lane) {
  c = __hip_atomic_load((gu32*)(base + (rep * NSH + (lane & (NSH - 1))) * 256), RLX_AGENT);
}
template <int NSH> __device__ __forceinline__ void poll_finish(unsigned c, char* base, int rep, unsigned total, int lane) {
  if (!count_ok<NSH>(c, total)) wait_count<NSH>(base, rep, total, lane);
  __builtin_amdgcn_fence(__ATOMIC_ACQUIRE, "wavefront");
}

__device__ __forceinline__ float gelu_f(float x) {
  const float p = __builtin_fmaf(x * x, -0.044715f * 2.302208198f, -2.302208198f);
  const float e = __builtin_amdgcn_exp2f(x * p);
  return x * __builtin_amdgcn_rcpf(1.0f + e);
}

__device__ __forceinline__ float ld32f(__amdgpu_buffer_rsrc_t rs, unsigned off) { return u2f(__builtin_amdgcn_raw_buffer_load_b32(rs, off, 0, 16)); }
__device__ __forceinline__ void st32(__amdgpu_buffer_rsrc_t rs, unsigned off, unsigned v) { __builtin_amdgcn_raw_buffer_store_b32(v, rs, off, 0, 16); }

struct Args {
  const u16 *x, *Are, *Aim, *Bre, *Bim, *Cre, *Cim, *logdt, *dskip, *win, *wsp, *bsp, *wout, *cvec, *wmod;
  char* ws; u16* out;
};

typedef GAS unsigned long long gu64;
__device__ __forceinline__ void put_granule(char* p, unsigned v, unsigned tag) { __hip_atomic_store((gu64*)p, ((unsigned long long)tag << 32) | v, RLX_AGENT); }
template <int N, class F> __device__ __forceinline__ void get_granules(unsigned (&v)[N], unsigned tag, F addr) {
#pragma unroll 1
  for (unsigned spin = 0; spin < SPIN_CAP; ++spin) {
    unsigned long long x[N];
#pragma unroll
    for (int k = 0; k < N; ++k) x[k] = __hip_atomic_load((gu64*)addr(k), RLX_AGENT);
    bool ok = true;
#pragma unroll
    for (int k = 0; k < N; ++k) { v[k] = (unsigned)x[k]; ok &= (unsigned)(x[k] >> 32) == tag; }
    if (wall(ok)) break;
    __builtin_amdgcn_s_sleep(3);
  }
}

__device__ __forceinline__ void conv_group(LAS char* lds, LAS char* lds0, int bt, const Args& A, __amdgpu_buffer_rsrc_t rs, unsigned epoch, unsigned& tag, unsigned (&pre)[7], unsigned long long pc,
                                           int wg, int d, int tid, int wave, int lane STAMP_ARG) {
  char* ws = A.ws;
  float S[8], soff;
  v4u xv, st[2];
  unsigned cpf = 0;
  {
    const int half = wave & 1;
    int xoff[8];
#pragma unroll
    for (int j = 0; j < 8; ++j) {
      const int l = tid * 8 + j;
      const int idx = l < 128 ? l : (int)(pkbf((float)l, 0.f) & 0xffffu) - 0x4300 + 128;
      xoff[j] = (idx + (idx >> 3)) * 8;
    }
    asm volatile("" : "+v"(xoff[0]), "+v"(xoff[1]), "+v"(xoff[2]), "+v"(xoff[3]), "+v"(xoff[4]), "+v"(xoff[5]), "+v"(xoff[6]), "+v"(xoff[7]));
    waitv<0>();
    asm volatile("v_readfirstlane_b32 %0, %1\n\ts_nop 3" : "=s"(tag) : "v"(epoch) : "memory");
    tag += 1u;
    asm volatile("" : "+v"(pre[0]), "+v"(pre[1]), "+v"(pre[2]), "+v"(pre[3]), "+v"(pre[4]), "+v"(pre[5]), "+v"(pre[6]));
    {
      const int so = ((wg >> 3) * 4 + wave) * 4 + (lane & 3);
      cpf = *(const GAS unsigned*)((pc & ~255ull) + (size_t)(bt == 1 && so < CODE_LINES ? so : 0) * 128);
    }
    const unsigned vare = pre[0], vaim = pre[1], vdt = pre[2], vbre = pre[3], vbim = pre[4], vcre = pre[5], vcim = pre[6];
    if (tid < 4) *(LAS v4u*)(lds + C_KOFF + tid * 16) = v4u{0u, 0u, 0u, 0u};
    const float dt = bfr(exp_acc(bflo(vdt)));
    const float ar2 = dt * bflo(vare) * 1.4426950408889634f, ai2 = dt * bflo(vaim) * 0.15915494309189535f;
    auto E = [&](float xx) {
      const float mag = __builtin_amdgcn_exp2f(ar2 * xx), ph = __builtin_amdgcn_fractf(ai2 * xx);
      return cplx{mag * __builtin_amdgcn_cosf(ph), mag * __builtin_amdgcn_sinf(ph)};
    };
    LAS char* img = lds + C_COFF + wave * 8192 + lane * 128;
    const int sw = lane & 7;
    float isc = 0.f;
    if (wave < 2) {
      const cplx e8 = E(8.f), e64 = E(64.f), e128 = E(128.f), e192 = E(192.f);
      cplx q[8];
      q[0] = {1.f, 0.f}; q[1] = e8; q[2] = cmul(e8, e8); q[3] = cmul(q[2], e8); q[4] = cmul(q[2], q[2]);
      q[5] = cmul(q[4], e8); q[6] = cmul(q[4], q[2]); q[7] = cmul(q[4], q[3]);
#pragma unroll
      for (int g = 0; g < 4; ++g) {
        cplx v[8];
#pragma unroll
        for (int j = 0; j < 8; ++j) v[j] = g == 0 ? q[j] : cmul(q[j], g == 1 ? e64 : (g == 2 ? e128 : e192));
        *(LAS v4u*)(img + ((g ^ sw) << 4)) = v4u{pkh(v[0].r, v[1].r), pkh(v[2].r, v[3].r), pkh(v[4].r, v[5].r), pkh(v[6].r, v[7].r)};
        *(LAS v4u*)(img + (((4 + g) ^ sw) << 4)) = v4u{pkh(v[0].i, v[1].i), pkh(v[2].i, v[3].i), pkh(v[4].i, v[5].i), pkh(v[6].i, v[7].i)};
      }
    } else {
      cplx W = cmul(cplx{bflo(vcre), bflo(vcim)}, cplx{dt * bflo(vbre), dt * bflo(vbim)});
      float mx = __builtin_fmaxf(__builtin_fabsf(W.r), __builtin_fabsf(W.i));
      mx = __builtin_fmaxf(mx, dppf<0xb1>(mx)); mx = __builtin_fmaxf(mx, dppf<0x4e>(mx)); mx = __builtin_fmaxf(mx, dppf<0x141>(mx)); mx = __builtin_fmaxf(mx, dppf<0x140>(mx));
      const unsigned mb = f2u(mx);
      const unsigned m01 = __builtin_elementwise_max((unsigned)__builtin_amdgcn_readlane(mb, 0), (unsigned)__builtin_amdgcn_readlane(mb, 16));
      const unsigned m23 = __builtin_elementwise_max((unsigned)__builtin_amdgcn_readlane(mb, 32), (unsigned)__builtin_amdgcn_readlane(mb, 48));
      int ex = (int)((__builtin_elementwise_max(m01, m23) >> 23) & 0xff);
      ex = ex < 20 ? 20 : (ex > 240 ? 240 : ex);
      const float sc = u2f((unsigned)(267 - ex) << 23);
      isc = u2f((unsigned)(ex - 13) << 23);
      W.r *= sc; W.i *= sc;
      const cplx e1 = E(1.f), e2 = cmul(e1, e1), e4 = cmul(e2, e2);
      cplx h[24];
      h[0] = W; h[1] = cmul(W, e1); h[2] = cmul(W, e2); h[3] = cmul(h[2], e1);
      h[4] = cmul(W, e4); h[5] = cmul(h[4], e1); h[6] = cmul(h[4], e2); h[7] = cmul(h[6], e1);
      const cplx g1 = E(256.f), g2 = E(512.f), g3 = E(768.f), g4 = E(1024.f);
      h[8] = cmul(h[0], g1); h[9] = cmul(h[2], g1); h[10] = cmul(h[4], g1); h[11] = cmul(h[6], g1);
      h[12] = cmul(h[0], g2); h[13] = cmul(h[4], g2); h[14] = cmul(h[0], g3); h[15] = cmul(h[4], g3);
      h[16] = cmul(h[0], g4); h[17] = cmul(h[8], g4); h[18] = cmul(h[12], g4); h[19] = cmul(h[14], g4);
      h[20] = cmul(h[16], g4); h[21] = {0.f, 0.f}; h[22] = {0.f, 0.f}; h[23] = {0.f, 0.f};
#pragma unroll
      for (int g = 0; g < 3; ++g) {
        const cplx* v = h + 8 * g;
        *(LAS v4u*)(img + ((g ^ sw) << 4)) = v4u{pkh(v[0].r, v[1].r), pkh(v[2].r, v[3].r), pkh(v[4].r, v[5].r), pkh(v[6].r, v[7].r)};
        *(LAS v4u*)(img + (((4 + g) ^ sw) << 4)) = v4u{pkh(-v[0].i, -v[1].i), pkh(-v[2].i, -v[3].i), pkh(-v[4].i, -v[5].i), pkh(-v[6].i, -v[7].i)};
      }
      *(LAS v4u*)(img + ((3 ^ sw) << 4)) = v4u{0u, 0u, 0u, 0u};
      *(LAS v4u*)(img + ((7 ^ sw) << 4)) = v4u{0u, 0u, 0u, 0u};
    }
    STAMP(1);
    group_sync(lds0, 5 + bt, 4, lane);
    if (wave >= 2) {
      const int hh = lane >> 5, blk = (lane >> 4) & 1, qq = (lane & 15) >> 2, p = lane & 3;
      LAS const char* Qi = lds + C_COFF + half * 8192;
      v2u a[8][2], bb[8][2];
#pragma unroll
      for (int ks = 0; ks < 8; ++ks)
#pragma unroll
        for (int t = 0; t < 2; ++t) {
          const int k = 16 * ks + 8 * hh + 4 * t + qq, mode = k >> 1, c = k & 1;
          const int off = mode * 128 + (((c * 4 + 2 * blk + (p >> 1)) ^ (mode & 7)) << 4) + (p & 1) * 8;
          a[ks][t] = trread(Qi + 16384 + off);
          bb[ks][t] = trread(Qi + off);
        }
      SCHED_BAR();
      f32x16 acc = {};
#pragma unroll
      for (int ks = 0; ks < 8; ++ks) acc = mfma32h(v4u{a[ks][0].x, a[ks][0].y, a[ks][1].x, a[ks][1].y}, v4u{bb[ks][0].x, bb[ks][0].y, bb[ks][1].x, bb[ks][1].y}, acc);
      LAS float* T = (LAS float*)(lds + C_UOFF) + half;
      const int q = lane & 31, hi = lane >> 5;
      auto put = [&](int idx, float v) { T[(idx + (idx >> 3)) * 2] = v * isc; };
#pragma unroll
      for (int r = 0; r < 4; ++r) put((r + 4 * hi) + 8 * q, acc[r]);
#pragma unroll
      for (int r = 0; r < 4; ++r) put(hi ? 384 + 64 * (r >> 1) + (r & 1) + 2 * q : 256 + r + 4 * q, acc[4 + r]);
      if (hi == 0) {
#pragma unroll
        for (int r = 0; r < 4; ++r) put(512 + 32 * r + q, acc[8 + r]);
      } else if (q == 0) put(640, acc[8]);
    }
    lds_post(lds0, 5 + bt, lane);
    auto load_x = [&]() {
      xv = ld128(rs, (unsigned)WS_XT + (tid * D + d) * 16);
#pragma unroll
      for (int i = 0; i < 2; ++i) st[i] = ld128(rs, (unsigned)WS_ST1 + (i * 256 + tid) * 16);
      SCHED_BAR();
    };
    const bool early = __builtin_amdgcn_readfirstlane(__hip_atomic_load((LAS unsigned*)(lds0 + M_CTL) + (bt ? 5 : 8), __ATOMIC_RELAXED, __HIP_MEMORY_SCOPE_WORKGROUP)) >= (bt ? XGATE : 1);
    asm volatile("" ::: "memory");
    if (early) load_x();
    lds_wait(lds0, 5 + bt, 8);
    {
      f32x2 tv[8];
#pragma unroll
      for (int j = 0; j < 8; ++j) tv[j] = *(LAS const f32x2*)(lds + C_UOFF + xoff[j]);
      SCHED_BAR();
      float kv[8];
#pragma unroll
      for (int j = 0; j < 8; ++j) kv[j] = tv[j][0] + tv[j][1];
      *(LAS v4u*)(lds + C_KOFF + 64 + tid * 16) = v4u{pkbf(kv[0], kv[1]), pkbf(kv[2], kv[3]), pkbf(kv[4], kv[5]), pkbf(kv[6], kv[7])};
      S[0] = kv[0];
#pragma unroll
      for (int j = 1; j < 8; ++j) S[j] = S[j - 1] + kv[j];
      float inc = S[7];
      inc += dppf<0x111>(inc); inc += dppf<0x112>(inc); inc += dppf<0x114>(inc); inc += dppf<0x118>(inc);
      inc += __builtin_bit_cast(float, __builtin_amdgcn_update_dpp(0, __builtin_bit_cast(int, inc), 0x142, 0xa, 0xf, false));
      inc += __builtin_bit_cast(float, __builtin_amdgcn_update_dpp(0, __builtin_bit_cast(int, inc), 0x143, 0xc, 0xf, false));
      soff = inc - S[7];
      if (lane == 63) ((LAS float*)(lds + C_KOFF + 4160))[wave] = inc;
    }
    lds_post(lds0, 5 + bt, lane);
    if (!early) {
      if (bt == 0 && wave == 0) {
#pragma unroll 1
        for (unsigned spin = 0; spin < SPIN_CAP; ++spin) {
          asm volatile("" ::: "memory");
          const v4u f = ld128(rs, (unsigned)CTL_F0 + (wg & (F0_REP - 1)) * 1024 + lane * 16);
          if (wall(f.x == tag && f.y == ~tag && f.z == tag && f.w == ~tag)) break;
          __builtin_amdgcn_s_sleep(2);
        }
        __builtin_amdgcn_fence(__ATOMIC_ACQUIRE, "wavefront");
        lds_post(lds0, 8, lane);
      } else if (bt == 0) lds_wait(lds0, 8, 1);
      else lds_wait(lds0, 5, XGATE);
      load_x();
    }
    STAMP(2);
  }
  lds_wait(lds0, 5 + bt, 12);
  STAMP(3);
  float xf[8], xn[8];
  {
#pragma unroll
    for (int j = 0; j < 4; ++j) { xf[2 * j] = bflo(xv[j]); xf[2 * j + 1] = bfhi(xv[j]); }
    const f32x4 wt = *(LAS const f32x4*)(lds + C_KOFF + 4160);
    if (tid < 128) *(LAS v4u*)(lds + C_UOFF + (tid >> 5) * C_UP + (tid & 31) * 16) = v4u{0u, 0u, 0u, 0u};
#pragma unroll
    for (int j = 0; j < 8; ++j) {
      const unsigned sj = st[j >> 2][j & 3];
      xn[j] = (xf[j] - bflo(sj)) * __builtin_amdgcn_rcpf(bfhi(sj));
    }
#pragma unroll
    for (int e = 0; e < 4; ++e) *(LAS unsigned*)(lds + C_UOFF + e * C_UP + 512 + tid * 4) = pkbf(xn[e], xn[e + 4]);
    soff += (wave > 0 ? wt[0] : 0.f) + (wave > 1 ? wt[1] : 0.f) + (wave > 2 ? wt[2] : 0.f);
  }
  const u16 dskv = A.dskip[d];
  char* mgp = ws + WS_MOD + (size_t)((lane & 3) * 3840 + (lane < 12 ? lane >> 2 : 0) * D + d) * 8;
  const unsigned long long mg = __hip_atomic_load((gu64*)mgp, RLX_AGENT);
  STAMP(5);
  group_sync(lds0, 5 + bt, 16, lane);
  STAMP(6);
  f32x16 y0 = {}, y1 = {};
  {
    const int hh = lane >> 5, blk = (lane >> 4) & 1, qq = (lane & 15) >> 2, p = lane & 3, col = lane & 31;
    LAS const char* ab = lds + C_KOFF + (32 + 16 * blk + 4 * p - 4 * qq + 32 * hh) * 2 - 32;
    LAS const char* bb = lds + C_UOFF + wave * C_UP + (256 + 8 * (col - hh) - 240) * 2;
    struct CF { v2u a0l, a0h, a1l, a1h; v4u b0, b1; };
    CF cf[3];
    auto rdc = [&](int ks, CF& f) {
      f.a0l = trread(ab + ks * 128 + 32); f.a0h = trread(ab + ks * 128);
      f.b0 = *(LAS const v4u*)(bb + (15 - ks) * 32);
      f.b1 = *(LAS const v4u*)(bb + (31 - ks) * 32);
      f.a1l = trread(ab + (ks + 16) * 128 + 32); f.a1h = trread(ab + (ks + 16) * 128);
    };
    rdc(0, cf[0]); rdc(1, cf[1]);
    SCHED_BAR();
#pragma unroll
    for (int ks = 0; ks < 16; ++ks) {
      if (ks + 2 < 16) rdc(ks + 2, cf[(ks + 2) % 3]);
      const CF& f = cf[ks % 3];
      const v4u a0 = {f.a0l.x, f.a0l.y, f.a0h.x, f.a0h.y}, a1 = {f.a1l.x, f.a1l.y, f.a1h.x, f.a1h.y};
      y0 = mfma32(a0, f.b0, y0);
      y1 = mfma32(a0, f.b1, y1);
      y1 = mfma32(a1, f.b0, y1);
      INTERLEAVE(3, 2);
    }
    asm volatile("" :: "v"(y0), "v"(y1));
    STAMP(7);
    LAS char* cb = lds + C_COFF + (wave * 64 + col) * 144 + hh * 16;
#pragma unroll
    for (int g = 0; g < 4; ++g) {
      *(LAS f32x4*)(cb + g * 32) = f32x4{y0[4 * g], y0[4 * g + 1], y0[4 * g + 2], y0[4 * g + 3]};
      *(LAS f32x4*)(cb + 32 * 144 + g * 32) = f32x4{y1[4 * g], y1[4 * g + 1], y1[4 * g + 2], y1[4 * g + 3]};
    }
  }
  lds_post(lds0, 5 + bt, lane);
  unsigned mv[1] = {(unsigned)mg};
  if (!wall((unsigned)(mg >> 32) == tag)) get_granules<1>(mv, tag, [&](int) { return mgp; });
  float msum = u2f(mv[0]);
  msum += dppf<0xb1>(msum); msum += dppf<0x4e>(msum);
  const unsigned mb = f2u(bfr(msum));
  const float sa = u2f(__builtin_amdgcn_readlane(mb, 0)), c1a = bfr(1.0f + u2f(__builtin_amdgcn_readlane(mb, 4))), ga = u2f(__builtin_amdgcn_readlane(mb, 8)), dsk = bf2f(dskv);
  const float gc = ga * c1a, gs_ = ga * sa, gd = gc * dsk, g0 = gs_ * (soff + dsk);
#pragma unroll
  for (int j = 0; j < 8; ++j) xf[j] = __builtin_fmaf(gd, xn[j], __builtin_fmaf(gs_, S[j], xf[j] + g0));
  STAMP(8);
  lds_wait(lds0, 5 + bt, 20);
  STAMP(9);
  float y[8];
  {
    const int c1 = 2 * tid, c0 = tid ? c1 - 1 : 0;
    const int o0 = (c0 >> 3) * 144 + (c0 & 7) * 16, o1 = (c1 >> 3) * 144 + (c1 & 7) * 16;
    LAS const char* cb = lds + C_COFF;
    f32x4 w0[4], w1[4], w2[4];
#pragma unroll
    for (int e = 0; e < 4; ++e) {
      w1[e] = *(LAS const f32x4*)(cb + e * 64 * 144 + o1); w2[e] = *(LAS const f32x4*)(cb + e * 64 * 144 + o1 + 16);
      if (e) w0[e] = *(LAS const f32x4*)(cb + e * 64 * 144 + o0);
    }
    SCHED_BAR();
#pragma unroll
    for (int e = 0; e < 4; ++e) {
      float w[12];
#pragma unroll
      for (int j = 0; j < 4; ++j) { w[4 + j] = w1[e][j]; w[8 + j] = w2[e][j]; w[j] = (e && tid) ? w0[e][j] : 0.f; }
#pragma unroll
      for (int j = 0; j < 8; ++j) y[j] = e ? y[j] + w[4 + j - e] : w[4 + j];
    }
  }
  float o[8];
#pragma unroll
  for (int j = 0; j < 8; ++j) o[j] = __builtin_fmaf(gc, y[j], xf[j]);
  {
    const unsigned a0 = pkbf(o[0], o[1]), a1 = pkbf(o[2], o[3]), b0 = pkbf(o[4], o[5]), b1 = pkbf(o[6], o[7]);
    const bool odd = tid & 1;
    const unsigned s0 = odd ? a0 : b0, s1 = odd ? a1 : b1;
    const unsigned r0 = __builtin_amdgcn_update_dpp(0, s0, 0xb1, 0xf, 0xf, true), r1 = __builtin_amdgcn_update_dpp(0, s1, 0xb1, 0xf, 0xf, true);
    const v4u ov = odd ? v4u{r0, r1, b0, b1} : v4u{a0, a1, r0, r1};
    st128(rs, (unsigned)WS_X2T + d * (L * 2) + (tid >> 2) * 64 + (tid & 1) * 32 + (tid & 2) * 8, ov);
  }
  STAMP(10);
  asm volatile("" :: "v"(cpf));
  if (wave_arrive(lds0, bt, 4, lane)) signal<E1_REP, 8>(ws + CTL_E1 + bt * 65536, wg & 7, lane);
}

__device__ __forceinline__ void aux_group(LAS char* lds, LAS char* lds0, const Args& A, __amdgpu_buffer_rsrc_t rs, unsigned epoch, int u, int tid, int wave, int lane STAMP_ARG) {
  char* ws = A.ws;
  unsigned gs = 0;
  v4u wb[5], ov[3], mw[10];
  u16 mc;
  const int mpart = u & 3, mcol = (u < 120 ? u >> 2 : 0) * 128;
  unsigned tag;
  {
    const int r = tid >> 4, s = tid & 15, q = lane >> 4;
    v4u v[5];
    {
      const u16* xp = A.x + (size_t)(u * 16 + r) * D + (s ^ ((q | ((wave >> 1) << 2)) << 1)) * 8;
#pragma unroll
      for (int i = 0; i < 5; ++i) v[i] = *(const v4u*)(xp + i * 128);
    }
    SCHED_BAR();
    mc = A.cvec[mpart * 160 + (tid < 160 ? tid : 0)];
#pragma unroll
    for (int i = 0; i < 10; ++i) mw[i] = *(const v4u*)(A.wmod + (size_t)(mpart * 160 + i * 16 + (tid >> 4)) * (6 * D) + mcol + (tid & 15) * 8);
    SCHED_BAR();
    {
      const int zc = (u * 4 + wave) * 3 + lane - 32;
      st128(rs, lane < 32 ? (unsigned)CTL_ST3 + ((u * 4 + wave) * 32 + lane) * 16 : (lane < 35 && zc < 1280 ? (unsigned)zc * 256 : 0x7ffffff0u), v4u{0u, 0u, 0u, 0u});
    }
    LAS char* img = lds + 8192;
#pragma unroll
    for (int i = 0; i < 5; ++i) *(LAS v4u*)(img + (wave * 5 + i) * 1024 + lane * 16) = v[i];
    lds_post(lds0, 7, lane); gs += 4;
    STAMP(3);
    float s0 = 0.f, s1 = 0.f, q0 = 0.f, q1 = 0.f;
#pragma unroll
    for (int i = 0; i < 5; ++i) {
      s0 = dot2bf(v[i][0], 0x3f803f80u, s0); s1 = dot2bf(v[i][1], 0x3f803f80u, s1); s0 = dot2bf(v[i][2], 0x3f803f80u, s0); s1 = dot2bf(v[i][3], 0x3f803f80u, s1);
      q0 = dot2bf(v[i][0], v[i][0], q0); q1 = dot2bf(v[i][1], v[i][1], q1); q0 = dot2bf(v[i][2], v[i][2], q0); q1 = dot2bf(v[i][3], v[i][3], q1);
    }
    SCHED_BAR();
    lds_wait(lds0, 7, gs);
    const int rr = (lane & 15) >> 2, p = lane & 3, f = (rr | ((q & 1) << 2)) << 1;
    v2u lo[5], hi[5];
#pragma unroll
    for (int j = 0; j < 5; ++j) {
      const int a0 = (2 * (q & 1) * 5 + j) * 1024 + rr * 256 + ((((2 * wave + (q >> 1)) * 2 + (p >> 1)) ^ f) << 4) + (p & 1) * 8;
      lo[j] = trread(img + a0); hi[j] = trread(img + a0 + 5120);
    }
    float sum = s0 + s1, sq = q0 + q1;
    sum += dppf<0xb1>(sum); sq += dppf<0xb1>(sq); sum += dppf<0x4e>(sum); sq += dppf<0x4e>(sq);
    sum += dppf<0x141>(sum); sq += dppf<0x141>(sq); sum += dppf<0x140>(sum); sq += dppf<0x140>(sq);
#pragma unroll
    for (int j = 0; j < 5; ++j)
      st128(rs, (unsigned)WS_XT + ((u * 2 + (q & 1)) * D + (8 * j + 2 * wave + (q >> 1)) * 16 + (lane & 15)) * 16, v4u{lo[j].x, lo[j].y, hi[j].x, hi[j].y});
    const float mean = sum * (1.0f / D);
    const int t = u * 16 + r;
    st32(rs, s == 0 ? (unsigned)WS_ST1 + ((((t >> 2) & 1) * 256 + (t >> 3)) * 4 + (t & 3)) * 4 : 0x7ffffff0u, pkbf(mean, ln_std(__builtin_fmaxf(sq * (1.0f / D) - mean * mean, 0.f))));
    STAMP(8);
    SCHED_BAR();
    {
      const float c = bf2f(mc);
      ((LAS float*)lds)[tid] = bfr(c * __builtin_amdgcn_rcpf(1.0f + __builtin_amdgcn_exp2f(-1.4426950408889634f * c)));
    }
    lds_post(lds0, 7, lane); gs += 4;
    asm volatile("s_waitcnt vmcnt(0)\n\tv_readfirstlane_b32 %0, %1\n\ts_nop 3" : "=s"(tag) : "v"(epoch) : "memory");
    tag += 1u;
    if (wave_arrive(lds0, 9, 4, lane) && lane < F0_REP)
      __hip_atomic_store((gu64*)(ws + CTL_F0 + lane * 1024 + u * 8), ((unsigned long long)~tag << 32) | tag, RLX_AGENT);
    SCHED_BAR();
    {
      const int r8 = tid >> 3, s8 = tid & 7;
      const u16* bp = A.win + (size_t)(160 * (u & 3) + r8) * F + 64 * ((u < 108 ? u : 0) >> 2) + s8 * 8;
#pragma unroll
      for (int i = 0; i < 5; ++i) wb[i] = *(const v4u*)(bp + (size_t)i * 32 * F);
      const int slab = u < 108 ? u >> 2 : 0;
      const int half = (r8 >> 2) & 1, jj = slab < 24 ? slab / 3 : 2 * (slab - 24) + half;
      const int sub = (jj < 6 ? 7 * jj : 42 + 6 * (jj - 6)) + (slab < 24 ? 2 * (slab - 3 * jj) + half : 6);
      const u16* src = A.wout + (size_t)(32 * (sub >> 1) + 8 * (r8 >> 3) + 4 * (sub & 1) + (r8 & 3)) * D + (u & 3) * 160;
#pragma unroll
      for (int i = 0; i < 3; ++i) ov[i] = *(const v4u*)(src + (i < 2 || s8 < 4 ? s8 + 8 * i : 0) * 8);
    }
    SCHED_BAR();
    STAMP(1);
  }
  {
    LAS float* msc = (LAS float*)lds;
    lds_wait(lds0, 7, gs);
    float sv[10];
#pragma unroll
    for (int i = 0; i < 10; ++i) sv[i] = msc[(tid >> 4) + 16 * i];
    float acc[8] = {};
#pragma unroll
    for (int i = 0; i < 10; ++i)
#pragma unroll
      for (int j = 0; j < 4; ++j) { acc[2 * j] += sv[i] * bflo(mw[i][j]); acc[2 * j + 1] += sv[i] * bfhi(mw[i][j]); }
    treduce_xor<4>(acc, (lane & 32) != 0, 32);
    treduce_xor<2>(acc, (lane & 16) != 0, 16);
    LAS float* red = msc + 256;
    *(LAS f32x2*)(red + wave * 128 + (lane & 15) * 8 + (lane >> 4) * 2) = f32x2{acc[0], acc[1]};
    group_sync(lds0, 7, gs += 4, lane);
    if (tid < 128 && u < 120) put_granule(ws + WS_MOD + (size_t)(mpart * 3840 + mcol + tid) * 8, f2u((red[tid] + red[128 + tid]) + (red[256 + tid] + red[384 + tid])), tag);
  }
  __builtin_amdgcn_s_setprio(AUXB_PRIO);
  if (u < 108) {
    const int cbk = u >> 2, part = u & 3;
    const int c0 = 64 * cbk, r0 = 160 * part, seg = tid & 7, rg = tid >> 3;
    constexpr int OP = 400, WPITCH = 144;
    LAS char* oimg = lds + 32768;
    LAS char* img = lds + 4096;
#pragma unroll
    for (int i = 0; i < 5; ++i) *(LAS v4u*)(img + (rg + 32 * i) * WPITCH + seg * 16) = wb[i];
#pragma unroll
    for (int i = 0; i < 3; ++i) *(LAS v4u*)(oimg + rg * OP + (seg + 8 * i) * 16) = ov[i];
    group_sync(lds0, 7, gs += 4, lane);
    const int q = lane >> 4, rr = (lane & 15) >> 2, p = lane & 3;
    v4u wf[5];
    {
      const int colb = (32 * (wave >> 1) + 8 * p + 4 * (wave & 1)) * 2;
#pragma unroll
      for (int i = 0; i < 5; ++i) {
        const int a0 = (32 * i + 8 * q + rr) * WPITCH + colb;
        const v2u lo = trread(img + a0), hi = trread(img + a0 + 4 * WPITCH);
        wf[i] = v4u{lo.x, lo.y, hi.x, hi.y};
      }
      const int a0 = (8 * q + rr) * OP + p * 8 + wave * 32;
#pragma unroll
      for (int j = 0; j < 3; ++j) {
        const v2u lo = trread(oimg + a0 + j * 128), hi = trread(oimg + a0 + 4 * OP + j * 128);
        st128(rs, wave + 4 * j < 10 ? (unsigned)WS_WOUT + (((part * 10 + wave + 4 * j) * 27 + cbk) * 64 + lane) * 16 : 0x7ffffff0u, v4u{lo.x, lo.y, hi.x, hi.y});
      }
    }
    LAS float* tc = (LAS float*)(lds + 28672);
    LAS u16* ts = (LAS u16*)(lds + 29696);
    if (wave < 3) {
      unsigned mv[8];
      const int rw = r0 + (tid < 160 ? tid : 159);
      get_granules<8>(mv, tag, [&](int k) { return ws + WS_MOD + (size_t)((k & 3) * 3840 + (3 + (k >> 2)) * D + rw) * 8; });
      ts[tid] = (u16)pkbf((u2f(mv[0]) + u2f(mv[1])) + (u2f(mv[2]) + u2f(mv[3])), 0.f);
      tc[tid] = bfr(1.0f + bfr((u2f(mv[4]) + u2f(mv[5])) + (u2f(mv[6]) + u2f(mv[7]))));
    }
    group_sync(lds0, 7, gs += 4, lane);
    STAMP(2);
    f32x4 u1 = {0.f, 0.f, 0.f, 0.f}, u2 = {0.f, 0.f, 0.f, 0.f};
    const v4u ones = {0x3f803f80u, 0x3f803f80u, 0x3f803f80u, 0x3f803f80u};
#pragma unroll
    for (int i = 0; i < 5; ++i) {
      const f32x4 ca = *(LAS const f32x4*)(tc + 32 * i + 8 * q), cb = *(LAS const f32x4*)(tc + 32 * i + 8 * q + 4);
      const v4u w = wf[i];
      const v4u o = {pkbf(bflo(w.x) * ca[0], bfhi(w.x) * ca[1]), pkbf(bflo(w.y) * ca[2], bfhi(w.y) * ca[3]),
                     pkbf(bflo(w.z) * cb[0], bfhi(w.z) * cb[1]), pkbf(bflo(w.w) * cb[2], bfhi(w.w) * cb[3])};
      st128(rs, (unsigned)WS_WIN + (((((part * 5 + i) >> 1) * 108 + 4 * cbk + wave) * 2 + ((part * 5 + i) & 1)) * 64 + lane) * 16, o);
      u1 = mfma16(ones, o, u1);
      u2 = mfma16(*(LAS const v4u*)(ts + 32 * i + 8 * q), w, u2);
    }
    STAMP(3);
    st32(rs, lane < 32 ? (unsigned)WS_U + ((part * 2 + q) * F + c0 + wave * 16 + (lane & 15)) * 4 : 0x7ffffff0u, f2u(q ? u2[0] : u1[0]));
  }
  STAMP(8);
  lds_wait(lds0, 8, 1);
  if (wave_arrive(lds0, 2, 4, lane)) signal<E1_REP, 8>(ws + CTL_E1B, u & 7, lane);
}

__device__ __forceinline__ int g1_aoff(int row, int G, int ab, int p) {
  const int f = ((row >> 1) & 1) | (((row >> 3) & 1) << 1);
  return row * 128 + (((4 * G + 2 * ab + (p >> 1)) ^ (f << 1)) << 4) + (p & 1) * 8;
}

template <int NB>
__device__ __forceinline__ void loader_path(LAS char* lds, char* ws, __amdgpu_buffer_rsrc_t rs, const u16* __restrict__ wsp, int wg, int mt, int j, int sub0, int lw, int lane) {
  const unsigned lbase = (unsigned)(size_t)lds, vo = lane * 16;
  const bool CNT7 = j < 6;
  {
    unsigned avo[2];
#pragma unroll
    for (int h = 0; h < 2; ++h) {
      const int row = lane >> 3, pos = lane & 7, f = ((row >> 1) & 1) | (h << 1);
      avo[h] = (row + 8 * h) * (L * 2) + ((pos ^ (f << 1)) << 4) - h * 1024;
    }
    const char* asb = ws + WS_X2T + (size_t)(16 * lw) * (L * 2) + mt * 128;
    const int p0 = (NB == 4 || CNT7) ? (lw < 2 ? 4 * lw : 2 + 3 * lw) : 3 * lw;
    const char* bsb = ws + WS_WIN + (size_t)(sub0 * 2 + p0) * 1024;
    const unsigned bb = lbase + G1_BOFF;
    auto issueB = [&](int s, unsigned slot) {
      gldsL<NB>(bsb + (s < 10 ? 108 * 1024 + s * 216 * 1024 : (s - 10) * 216 * 1024), vo, slot + p0 * 1024);
    };
    auto issueA = [&](int s0, int s1) {
#pragma unroll 1
      for (int s = s0; s < s1; ++s) gldsN<2, true>(asb + (size_t)(s * 64) * (L * 2), avo[0], avo[1], 0, 0, lbase + s * 8192 + lw * 2048);
    };
    {
      const unsigned vb = (unsigned)WS_V + (mt * 4 + lw) * 27 * 1024 + lane * 16;
#pragma unroll
      for (int k = 0; k < 3; ++k) st128(rs, vb + (3 * j + k) * 1024, v4u{~0u, ~0u, ~0u, ~0u});
      if (j < 6) __builtin_amdgcn_raw_buffer_store_b64(v2u{~0u, ~0u}, rs, vb + (24 + (j >> 1)) * 1024 + (j & 1) * 8, 0, 16);
    }
    waitv<0>();
    group_sync(lds, 5, 24, lane);
    if (lw == 0) { wait_count<8>(ws + CTL_E1B, wg & (E1_REP - 1), 128, lane); lds_post(lds, 10, lane); }
    if (lw == 3) { wait_count<8>(ws + CTL_E1, wg & (E1_REP - 1), 256, lane); lds_post(lds, 11, lane); }
    lds_wait(lds, 10, 1);
    STAMP(13);
#pragma unroll
    for (int s = 0; s < 4; ++s) issueB(s, bb + (s + 1) * G1_BST);
    lds_wait(lds, 11, 1);
    lds_wait(lds, 1, 4);
    issueA(0, 4);
    STAMP(12);
    LBAR();
    LSTAMP(1); STAMP(14);
    issueB(4, bb);
    waitv<6 + NB>(); LBAR(); LSTAMP(2);
    waitv<4 + NB>(); LBAR(); issueB(5, bb + G1_BST);
    waitv<2 + 2 * NB>(); LBAR(); LSTAMP(3); issueA(4, 8); issueB(6, bb + 2 * G1_BST);
    waitv<8 + 3 * NB>(); LBAR(); issueB(7, bb + 3 * G1_BST);
    waitv<6 + 2 * NB>(); LBAR(); LSTAMP(4); issueB(8, bb + 4 * G1_BST);
    waitv<4 + 3 * NB>(); LBAR(); LSTAMP(5); issueA(8, 10); issueB(9, bb);
    waitv<4 + 3 * NB>(); LBAR(); issueB(10, bb + G1_BST);
    waitv<4 + 3 * NB>(); LBAR(); issueB(11, bb + 2 * G1_BST);
    waitv<2 + 3 * NB>(); LBAR(); LSTAMP(6); issueB(12, bb + 3 * G1_BST);
    int nxt = 4 * G1_BST;
#pragma unroll 1
    for (int i = 9; i <= 15; ++i) {
      waitv<NB * 3>(); LBAR();
      if (i >= 11) LSTAMP(i - 4);
      issueB(i + 4, bb + nxt);
      nxt += G1_BST; if (nxt == G1_NSLOT * G1_BST) nxt = 0;
    }
    waitv<NB * 3>(); LBAR(); LSTAMP(12);
    waitv<NB * 2>(); LBAR(); LSTAMP(13);
    waitv<NB * 1>(); LBAR();
    waitv<0>(); LBAR(); LSTAMP(14);
    LBAR();
  }
  const char* osb = ws + WS_WOUT + (size_t)j * 135 * 1024;
  if (lw >= 2) {
    unsigned wvo[4];
#pragma unroll
    for (int h = 0; h < 4; ++h) {
      const int i = 8 * (h & 1) + (lane >> 3);
      wvo[h] = (8 * (i >> 2) + (i & 3) + 4 * (h >> 1)) * (L * 2) + (((lane & 7) ^ (i >> 1)) << 4) - h * 1024;
    }
    const char* wsb = (const char*)wsp + (size_t)(mt * 64 + 32 * (lw & 1)) * (L * 2);
    auto issueW = [&](int s, unsigned slot) {
      gldsN<4, false>(wsb + s * 128, wvo[0], wvo[1], wvo[2], wvo[3], slot + (lw & 1) * 4096);
    };
#pragma unroll 1
    for (int s = 0; s < G2_NW; ++s) issueW(s, lbase + s * 8192);
    LBAR();
    waitv<32>(); LBAR();
    int wn = 0;
#pragma unroll 1
    for (int i = 1; i <= 23; ++i) { waitv<28>(); LBAR(); issueW(i + 8, lbase + wn); wn += 8192; if (wn == G2_NW * 8192) wn = 0; }
#pragma unroll 1
    for (int i = 24; i <= 32; ++i) {
      waitv<28>(); LBAR();
      gldsL<4>(osb + wn + (lw & 1) * 4096, vo, lbase + wn + (lw & 1) * 4096);
      wn += 8192; if (wn == G2_NW * 8192) wn = 0;
    }
  } else {
    constexpr int NZ = NB == 4 ? 7 : 6;
    const char* zsb = ws + WS_Z2 + (size_t)(sub0 * 2 + lw * NZ) * 1024;
    auto issueZ = [&](int s, unsigned slot) {
      gldsL<4>(zsb + s * 108 * 1024, vo, slot + lw * NZ * 1024);
      gldsL<NZ - 4>(zsb + s * 108 * 1024 + 4096, vo, slot + lw * NZ * 1024 + 4096);
    };
    const unsigned zb = lbase + G2_ZOFF;
    LBAR();
#pragma unroll 1
    for (int s = 0; s < G2_NZ; ++s) issueZ(s, zb + s * G2_ZST);
    waitv<NZ * 5>(); LBAR();
    int zn = 0;
#pragma unroll 1
    for (int i = 1; i <= 26; ++i) { waitv<NZ * 4>(); LBAR(); issueZ(i + 5, zb + zn); zn += G2_ZST; if (zn == G2_NZ * G2_ZST) zn = 0; }
    auto issueO = [&](int slot, int n) {
      const int o = G2_ZOFF + slot * G2_ZST + lw * n * 1024;
      if (n == 7) { gldsL<4>(osb + o, vo, lbase + o); gldsL<3>(osb + o + 4096, vo, lbase + o + 4096); }
      else gldsL<3>(osb + o, vo, lbase + o);
    };
    waitv<NZ * 4>(); LBAR(); issueO(2, 7);
    waitv<NZ * 3 + 7>(); LBAR(); issueO(3, 7);
    waitv<NZ * 2 + 14>(); LBAR(); issueO(4, 3);
    waitv<NZ + 17>(); LBAR(); if (lw == 0) gldsL<1>(osb + 134 * 1024, vo, lbase + 134 * 1024);
    waitv<17>(); LBAR(); issueO(0, 7);
    LBAR(); issueO(1, 7);
  }
  waitv<0>();
  LBAR();
}

template <int NSUB, bool STATS>
__device__ __forceinline__ void consumer_g12(LAS char* lds, char* ws, __amdgpu_buffer_rsrc_t rs, const u16* __restrict__ bsp, const u16* __restrict__ wsp, int wg, int mt, int j, int sub0, int cnt,
                                             int wave, int tid, int lane, v2u (&x2v)[3] STAMP_ARG) {
  constexpr int NS1 = NSUB > 0 ? NSUB : 1;
  const int G = wave >> 2, ng = G ? 3 - (wave & 3) : (wave & 3);
  const int q = lane >> 4, i16 = lane & 15, rr = i16 >> 2, p = lane & 3, n0 = 2 * ng;
  float bias[2];
#pragma unroll
  for (int m = 0; m < 2; ++m) bias[m] = bf2f(bsp[mt * 64 + 32 * G + 8 * (i16 >> 2) + 4 * m + (i16 & 3)]);
  STAMP(4);
  CBAR();
  v2u z1v[NS1][2];
  {
    const int aoff0 = g1_aoff(8 * q + rr, G, 0, p), aoff1 = g1_aoff(8 * q + rr, G, 1, p);
    const int boff = G1_BOFF + n0 * 2048 + lane * 16;
    struct Frags { v4u a[2][2]; v4u b[2][NS1]; };
    f32x4 acc2[NS1][2], acc1[NS1][2], asum[2], asq[2];
    int acur = 0, bcur = G1_BST;
    auto rd = [&](Frags& f) {
#pragma unroll
      for (int kk = 0; kk < 2; ++kk) {
        const v2u l0 = trread(lds + acur + aoff0 + kk * 4096), h0 = trread(lds + acur + aoff0 + kk * 4096 + 512);
        const v2u l1 = trread(lds + acur + aoff1 + kk * 4096), h1 = trread(lds + acur + aoff1 + kk * 4096 + 512);
        f.a[kk][0] = v4u{l0.x, l0.y, h0.x, h0.y}; f.a[kk][1] = v4u{l1.x, l1.y, h1.x, h1.y};
#pragma unroll
        for (int n = 0; n < NSUB; ++n) f.b[kk][n] = *(LAS const v4u*)(lds + bcur + boff + n * 2048 + kk * 1024);
      }
      acur += 8192; if (acur == 81920) acur = 0;
      bcur += G1_BST; if (bcur == G1_NSLOT * G1_BST) bcur = 0;
    };
    auto pin1 = [&]() {
#pragma unroll
      for (int n = 0; n < NSUB; ++n) asm volatile("" : "+v"(acc1[n][0]), "+v"(acc1[n][1]));
    };
#pragma unroll
    for (int ab = 0; ab < 2; ++ab) {
      asum[ab] = f32x4{0.f, 0.f, 0.f, 0.f}; asq[ab] = f32x4{0.f, 0.f, 0.f, 0.f};
#pragma unroll
      for (int n = 0; n < NS1; ++n) { acc2[n][ab] = f32x4{0.f, 0.f, 0.f, 0.f}; acc1[n][ab] = f32x4{0.f, 0.f, 0.f, 0.f}; }
    }
    const v4u ones = {0x3f803f80u, 0x3f803f80u, 0x3f803f80u, 0x3f803f80u};
    auto mm2 = [&](const Frags& f) {
#pragma unroll
      for (int kk = 0; kk < 2; ++kk)
#pragma unroll
        for (int ab = 0; ab < 2; ++ab) {
#pragma unroll
          for (int n = 0; n < NSUB; ++n) acc2[n][ab] = mfma16(f.a[ABL_KK][ab], f.b[ABL_KK][n], acc2[n][ab]);
          if (STATS) { asum[ab] = mfma16(f.a[kk][ab], ones, asum[ab]); asq[ab] = mfma16(f.a[kk][ab], f.a[kk][ab], asq[ab]); }
        }
      INTERLEAVE(6, 2);
    };
    auto mm1 = [&](const Frags& f) {
#pragma unroll
      for (int kk = 0; kk < 2; ++kk)
#pragma unroll
        for (int ab = 0; ab < 2; ++ab)
#pragma unroll
          for (int n = 0; n < NSUB; ++n) acc1[n][ab] = mfma16(f.b[kk][n], f.a[kk][ab], acc1[n][ab]);
      if (NSUB == 2) INTERLEAVE(6, 2); else INTERLEAVE(4, 3);
    };
    Frags f0, f1;
    CBAR(); rd(f0);
    SCHED_BAR();
    float up[4];
    {
      const int c = tid & 127, which = (tid >> 7) & 1, half = tid >> 8;
#pragma unroll
      for (int k = 0; k < 4; ++k) up[k] = ld32f(rs, c < cnt * 16 ? (unsigned)WS_U + ((k * 2 + which) * F + (sub0 + (half ? 0 : 54)) * 16 + c) * 4 : 0x7ffffff0u);
    }
    SCHED_BAR();
    STAMP(5);
    auto pin2 = [&]() {
#pragma unroll
      for (int n = 0; n < NSUB; ++n) asm volatile("" : "+v"(acc2[n][0]), "+v"(acc2[n][1]));
      asm volatile("" : "+v"(asum[0]), "+v"(asum[1]), "+v"(asq[0]), "+v"(asq[1]));
    };
    if (STATS) {
      char* cb = ws + CTL_E1 + (G + 1) * 65536;
      unsigned pc;
      poll_issue<8>(pc, cb, wg & (E1_REP - 1), lane);
      CBAR(); rd(f1); mm2(f0);
      if (G == 0) poll_finish<8>(pc, cb, wg & (E1_REP - 1), 256, lane);
      CBAR(); pin2(); rd(f0); mm2(f1);
      CBAR(); rd(f1); mm2(f0);
      poll_issue<8>(pc, cb, wg & (E1_REP - 1), lane);
      CBAR(); rd(f0); mm2(f1);
      if (G == 1) poll_finish<8>(pc, cb, wg & (E1_REP - 1), 128, lane);
      asm volatile("" :: "v"(pc));
      pin2();
    }
#pragma unroll 1
    for (int it = STATS ? 2 : 0; it < 5; ++it) {
      CBAR(); rd(f1); mm2(f0);
      CBAR(); rd(f0); mm2(f1);
    }
    ((LAS float*)(lds + G1_T_U))[tid] = (up[0] + up[1]) + (up[2] + up[3]);
    unsigned pv = 0;
    if (!STATS && ng < 2) pv = *(const unsigned*)((const char*)wsp + (size_t)(mt * 64 + (G * 2 + ng) * 16 + (lane >> 2)) * (L * 2) + j * 512 + (lane & 3) * 128);
    LAS f32x2* tab = (LAS f32x2*)(lds + G1_T_LN2);
    if (STATS) {
#pragma unroll
      for (int ab = 0; ab < 2; ++ab) {
        const float a0 = asum[ab][0], a1 = asum[ab][1], a2 = asum[ab][2], a3 = asum[ab][3];
        const float b0 = asq[ab][0], b1 = asq[ab][1], b2 = asq[ab][2], b3 = asq[ab][3];
        const float s1 = (p & 2) ? ((p & 1) ? a3 : a2) : ((p & 1) ? a1 : a0), s2 = (p & 2) ? ((p & 1) ? b3 : b2) : ((p & 1) ? b1 : b0);
        const float mean = s1 * (1.0f / D);
        float mb, rinv;
        ln_scalars(mean, __builtin_fmaxf(s2 * (1.0f / D) - mean * mean, 0.f), mb, rinv);
        if (rr == q) tab[(G * 2 + ab) * 16 + i16] = f32x2{rinv, rinv * mb};
      }
    }
    {
      const int nb = G ? 3 : 0;
#pragma unroll
      for (int n = 0; n < 3; ++n) {
        const int row = 80 * j + 16 * (nb + n < 5 ? nb + n : 4) + 4 * q + rr;
        x2v[n] = trread(lds + g1_aoff(row, (wave >> 1) & 1, wave & 1, p));
      }
    }
    STAMP(6);
    CBAR(); rd(f1); mm1(f0);
    float zq[NS1][8];
    {
      LAS const float* ut = (LAS const float*)(lds + G1_T_U);
      float v[16];
#pragma unroll
      for (int e = 0; e < 16; ++e) v[e] = 0.f;
      if (NSUB > 0) {
        f32x2 rv[8];
#pragma unroll
        for (int e = 0; e < 8; ++e) rv[e] = tab[(G * 2 + (e >> 2)) * 16 + 4 * q + (e & 3)];
#pragma unroll
        for (int n = 0; n < NSUB; ++n) {
          const float c1 = ut[(n0 + n) * 16 + i16], c2 = ut[128 + (n0 + n) * 16 + i16];
#pragma unroll
          for (int e = 0; e < 8; ++e) {
            const float z = gelu_f(__builtin_fmaf(rv[e].x, acc2[n][e >> 2][e & 3], __builtin_fmaf(-rv[e].y, c1, c2)));
            zq[n][e] = z;
            v[2 * e] = n ? v[2 * e] + z : z; v[2 * e + 1] = n ? __builtin_fmaf(z, z, v[2 * e + 1]) : z * z;
          }
        }
        treduce_dpp<8, 0x140>(v, (i16 & 8) != 0);
        treduce_dpp<4, 0x141>(v, (i16 & 4) != 0);
        treduce_dpp<2, 0x4e>(v, (i16 & 2) != 0);
        treduce_dpp<1, 0xb1>(v, (i16 & 1) != 0);
      }
      ((LAS float*)(lds + G1_T_P))[ng * 128 + G * 64 + lane] = v[0];
    }
    CBAR(); pin1();
    GAS unsigned long long* gr = (GAS unsigned long long*)(ws + CTL_ST3) + (size_t)mt * 1024 + G * 32;
    if (STATS) {
      LAS const float* pp = (LAS const float*)(lds + G1_T_P) + G * 64 + lane;
      const float sv = (pp[0] + pp[128]) + (pp[256] + pp[384]);
      __hip_atomic_store(gr + (j * 2 + (lane & 1)) * 64 + (lane >> 1), (1ull << 32) | f2u(sv), RLX_AGENT);
    }
    SCHED_BAR(); rd(f0); mm1(f1);
    CBAR(); pin1(); rd(f1); mm1(f0);
    CBAR(); pin1(); rd(f0); mm1(f1);
    unsigned long long gx[8];
    if (STATS) {
#pragma unroll
      for (int k = 0; k < 8; ++k) gx[k] = __hip_atomic_load(gr + (k * 2 + (lane >> 5)) * 64 + (lane & 31), RLX_AGENT);
    }
    CBAR(); pin1(); rd(f1); mm1(f0);
    if (STATS) {
      float sv;
      for (unsigned spin = 0;; ++spin) {
        bool ok = true;
        sv = 0.f;
#pragma unroll
        for (int k = 0; k < 8; ++k) { ok &= (gx[k] >> 32) == 1ull; sv += u2f((unsigned)gx[k]); }
        if (wall(ok) || spin > SPIN_CAP) break;
        __builtin_amdgcn_s_sleep(1);
#pragma unroll
        for (int k = 0; k < 8; ++k) gx[k] = __hip_atomic_load(gr + (k * 2 + (lane >> 5)) * 64 + (lane & 31), RLX_AGENT);
      }
      const float s2 = shx(sv, 32);
      const float mean = sv * (1.0f / FH);
      float mb, rinv;
      ln_scalars(mean, __builtin_fmaxf(s2 * (1.0f / FH) - mean * mean, 0.f), mb, rinv);
      if (lane < 32) ((LAS f32x2*)(lds + G1_T_Z))[32 * G + lane] = f32x2{-mb * rinv, rinv};
    }
    CBAR(); pin1();
    if (NSUB > 0) {
      f32x2 zs[8];
#pragma unroll
      for (int e = 0; e < 8; ++e) zs[e] = ((LAS const f32x2*)(lds + G1_T_Z))[32 * G + 8 * q + e];
#pragma unroll
      for (int n = 0; n < NSUB; ++n) {
        v4u o;
#pragma unroll
        for (int e = 0; e < 4; ++e)
          o[e] = pkbf(__builtin_fmaf(zq[n][2 * e], zs[2 * e].y, zs[2 * e].x), __builtin_fmaf(zq[n][2 * e + 1], zs[2 * e + 1].y, zs[2 * e + 1].x));
        st128(rs, (unsigned)WS_Z2 + (((mt * 54 + sub0 + n0 + n) * 2 + G) * 64 + lane) * 16, o);
      }
    }
    SCHED_BAR(); rd(f0); mm1(f1);
    CBAR(); pin1(); rd(f1); mm1(f0);
    CBAR(); pin1(); rd(f0); mm1(f1);
    CBAR(); pin1(); rd(f1); mm1(f0);
    if (wave_arrive(lds, 3, 8, lane)) signal<4, 4>(ws + CTL_E2 + j * 4096, mt & 3, lane);
    asm volatile("" :: "v"(pv));
    CBAR(); pin1();
    mm1(f1);
    STAMP(7);
    {
      LAS const float* ut = (LAS const float*)(lds + G1_T_U) + 256;
      f32x2 rv[2];
#pragma unroll
      for (int ab = 0; ab < 2; ++ab) rv[ab] = tab[(G * 2 + ab) * 16 + i16];
#pragma unroll
      for (int n = 0; n < NSUB; ++n) {
        const f32x4 u1 = *(LAS const f32x4*)(ut + (n0 + n) * 16 + 4 * q), u2 = *(LAS const f32x4*)(ut + 128 + (n0 + n) * 16 + 4 * q);
#pragma unroll
        for (int ab = 0; ab < 2; ++ab) {
          float z[4];
#pragma unroll
          for (int e = 0; e < 4; ++e) z[e] = gelu_f(__builtin_fmaf(rv[ab].x, acc1[n][ab][e], __builtin_fmaf(-rv[ab].y, u1[e], u2[e])));
          z1v[n][ab] = v2u{pkbf(z[0], z[1]), pkbf(z[2], z[3])};
          asm volatile("" : "+v"(z1v[n][ab]));
        }
      }
    }
  }
  if (wave == 1 || wave == 2) {
    const int c = tid - 64;
    if (c < 80) {
      const unsigned o = (unsigned)WS_MOD + (5 * D + 80 * j + c) * 8;
      ((LAS float*)(lds + G3_GB))[c] = bfr((ld32f(rs, o) + ld32f(rs, o + 30720)) + (ld32f(rs, o + 61440) + ld32f(rs, o + 92160)));
    }
  }
  if (wave == 3) wait_count<4>(ws + CTL_E2 + j * 4096, (mt >> 2) & 3, 32, lane);
  CBAR();
  STAMP(9);
  {
    f32x4 acc[NS1][2];
#pragma unroll
    for (int n = 0; n < NS1; ++n) { acc[n][0] = f32x4{0.f, 0.f, 0.f, 0.f}; acc[n][1] = f32x4{0.f, 0.f, 0.f, 0.f}; }
    int woff[2];
#pragma unroll
    for (int kk = 0; kk < 2; ++kk) woff[kk] = G * 4096 + i16 * 128 + (((4 * kk + q) ^ (i16 >> 1)) << 4);
    const int zoff = G2_ZOFF + n0 * 2048 + lane * 16;
    struct Frags { v4u w[2][2]; v4u z[2][NS1]; };
    int wc = 0, zc = 0;
    auto rd = [&](Frags& f) {
      if (NSUB > 0) {
#pragma unroll
        for (int kk = 0; kk < 2; ++kk) {
#pragma unroll
          for (int m = 0; m < 2; ++m) f.w[kk][m] = *(LAS const v4u*)(lds + wc + woff[kk] + m * 2048);
#pragma unroll
          for (int n = 0; n < NSUB; ++n) f.z[kk][n] = *(LAS const v4u*)(lds + zc + zoff + n * 2048 + kk * 1024);
        }
        wc += 8192; if (wc == G2_NW * 8192) wc = 0;
        zc += G2_ZST; if (zc == G2_NZ * G2_ZST) zc = 0;
      }
    };
    auto mm = [&](const Frags& f) {
#pragma unroll
      for (int kk = 0; kk < 2; ++kk)
#pragma unroll
        for (int n = 0; n < NSUB; ++n)
#pragma unroll
          for (int m = 0; m < 2; ++m) acc[n][m] = mfma16(f.z[ABL_KK][n], f.w[ABL_KK][m], acc[n][m]);
      INTERLEAVE(4, 2);
    };
    Frags f0, f1;
    CBAR(); rd(f0);
    STAMP(10);
#pragma unroll 1
    for (int it = 0; it < 15; ++it) {
      CBAR(); rd(f1); mm(f0);
      CBAR(); rd(f0); mm(f1);
    }
    CBAR(); rd(f1); mm(f0);
    mm(f1);
    STAMP(11);
#pragma unroll
    for (int m = 0; m < 2; ++m) {
      v2u o[NS1];
#pragma unroll
      for (int n = 0; n < NSUB; ++n) {
        float g[4];
#pragma unroll
        for (int e = 0; e < 4; ++e) g[e] = acc[n][m][e] + bias[m];
        const v2u zz = z1v[n][m];
        o[n] = v2u{pkbf(bflo(zz.x) * g[0], bfhi(zz.x) * g[1]), pkbf(bflo(zz.y) * g[2], bfhi(zz.y) * g[3])};
      }
      const unsigned vb = (unsigned)WS_V + (mt * 4 + G * 2 + m) * 27 * 1024 + lane * 16;
      if (NSUB == 2) st128(rs, vb + (3 * j + ng) * 1024, v4u{o[0].x, o[0].y, o[NS1 - 1].x, o[NS1 - 1].y});
      if (NSUB == 1) __builtin_amdgcn_raw_buffer_store_b64(o[0], rs, vb + (24 + (j >> 1)) * 1024 + (j & 1) * 8, 0, 16);
    }
    CBAR();
  }
}

template <int KH>
__device__ __forceinline__ void consumer_g3(LAS char* lds, char* ws, __amdgpu_buffer_rsrc_t rs, u16* __restrict__ out, int mt, int j, int m, int lane, const v2u (&x2v)[3] STAMP_ARG) {
  constexpr int K0 = KH ? 14 : 0, NK = KH ? 13 : 14, NF = KH ? 2 : 3, N0 = KH ? 3 : 0;
  const int q = lane >> 4, i16 = lane & 15;
  v4u vf[NK];
  __builtin_amdgcn_s_sleep(V_DELAY);
  CBAR();
  STAMP(12);
  const unsigned vo = (unsigned)WS_V + ((mt * 4 + m) * 27 + K0) * 1024 + lane * 16;
  constexpr int VW = V_WIN;
#pragma unroll
  for (int k = 0; k < VW; ++k) { vf[k] = ld128(rs, vo + k * 1024); SCHED_BAR(); }
  f32x4 acc[5];
#pragma unroll
  for (int n = 0; n < 5; ++n) acc[n] = f32x4{0.f, 0.f, 0.f, 0.f};
  LAS const char* wp = lds + K0 * 1024 + lane * 16;
  LAS const char* wp2 = wp + 54 * 1024;
  LAS const char* wp4 = wp + 108 * 1024;
  v4u wf[3][5];
  auto rdw = [&](int k, v4u (&w)[5]) {
    w[0] = *(LAS const v4u*)(wp + k * 1024); w[1] = *(LAS const v4u*)(wp + (27 + k) * 1024);
    w[2] = *(LAS const v4u*)(wp2 + k * 1024); w[3] = *(LAS const v4u*)(wp2 + (27 + k) * 1024);
    w[4] = *(LAS const v4u*)(wp4 + k * 1024);
  };
  rdw(0, wf[0]); rdw(1, wf[1]);
#pragma unroll
  for (int k = 0; k < NK; ++k) {
    if (k + VW < NK) vf[k + VW] = ld128(rs, vo + (k + VW) * 1024);
    if (k + 2 < NK) rdw(k + 2, wf[(k + 2) % 3]);
    SCHED_BAR();
    if (__builtin_expect(!wall(__builtin_elementwise_max(vf[k].x, vf[k].z) != ~0u), 0)) {
      unsigned spin = 0;
      do { asm volatile("" ::: "memory"); vf[k] = ld128(rs, vo + k * 1024); }
      while (!wall(__builtin_elementwise_max(vf[k].x, vf[k].z) != ~0u) && ++spin < SPIN_CAP);
    }
#pragma unroll
    for (int n = 0; n < 5; ++n) acc[n] = mfma16(wf[k % 3][n], vf[k], acc[n]);
    asm volatile("" : "+v"(acc[0]), "+v"(acc[1]), "+v"(acc[2]), "+v"(acc[3]), "+v"(acc[4]));
    SCHED_BAR();
  }
  STAMP(13);
  f32x4 gb[NF];
#pragma unroll
  for (int n = 0; n < NF; ++n) gb[n] = *(LAS const f32x4*)(lds + G3_GB + (16 * (N0 + n) + 4 * q) * 4);
  LAS char* red = lds + G3_RED + m * 5120 + lane * 16;
#pragma unroll
  for (int n = 0; n < 5; ++n) if ((n >= 3) != (KH != 0)) *(LAS f32x4*)(red + n * 1024) = acc[n];
  CBAR();
  const int t = mt * 64 + 32 * (m >> 1) + 8 * (i16 >> 2) + 4 * (m & 1) + (i16 & 3);
#pragma unroll
  for (int n = 0; n < NF; ++n) {
    const f32x4 o = *(LAS const f32x4*)(red + (N0 + n) * 1024);
    const f32x4 a = KH ? o + acc[N0 + n] : acc[N0 + n] + o;
    const float o0 = __builtin_fmaf(gb[n][0], a[0], bflo(x2v[n].x)), o1 = __builtin_fmaf(gb[n][1], a[1], bfhi(x2v[n].x));
    const float o2 = __builtin_fmaf(gb[n][2], a[2], bflo(x2v[n].y)), o3 = __builtin_fmaf(gb[n][3], a[3], bfhi(x2v[n].y));
    *(v2u*)((char*)out + ((size_t)t * D + 80 * j + 16 * (N0 + n) + 4 * q) * 2) = v2u{pkbf(o0, o1), pkbf(o2, o3)};
  }
}

__global__ __launch_bounds__(768, 3) void k_all(Args A) {
  extern __shared__ __attribute__((aligned(16))) char smem[];
  unsigned long long pc;
  asm volatile("s_getpc_b64 %0" : "=s"(pc));
  LAS char* lds = (LAS char*)smem;
  char* ws = A.ws;
  const int tid = __builtin_amdgcn_workitem_id_x(), lane = tid & 63, wave = __builtin_amdgcn_readfirstlane(tid >> 6), wg = __builtin_amdgcn_workgroup_id_x();
  STAMP_DECL; STAMP(0);
  const __amdgpu_buffer_rsrc_t rs = __builtin_amdgcn_make_buffer_rsrc((void*)ws, 0, (int)WS_END, 0x00020000);
  asm volatile("" :: "s"(A.x), "s"(A.Are), "s"(A.Aim), "s"(A.Bre), "s"(A.Bim), "s"(A.Cre), "s"(A.Cim), "s"(A.logdt), "s"(A.dskip), "s"(A.win), "s"(A.wsp),
               "s"(A.bsp), "s"(A.wout), "s"(A.cvec), "s"(A.wmod), "s"(A.ws), "s"(A.out));
  const int grp = wave >> 2, bt = grp == 2 ? 0 : grp + 1, gw = wave & 3, gt = tid & 255;
  const bool isaux = bt == 2 && wg >= 128;
  const int u = wg - 128, d = 256 * bt + (wg & 7) * (bt < 2 ? 32 : 16) + (wg >> 3);
  unsigned epoch;
  asm volatile("global_load_dword %0, %1, %2" : "=v"(epoch) : "v"((unsigned)CTL_EPOCH), "s"(ws) : "memory");
  unsigned pre[7] = {};
  if (__builtin_expect(!isaux, 1)) {
    const unsigned o = (d * NS + (gw & 1) * 64 + lane) * 2;
#define LDU16(dst, base, voff) asm volatile("global_load_ushort %0, %1, %2" NT : "=v"(dst) : "v"(voff), "s"(base) : "memory")
    LDU16(pre[0], A.Are, o); LDU16(pre[1], A.Aim, o); LDU16(pre[2], A.logdt, d * 2); LDU16(pre[3], A.Bre, o);
    LDU16(pre[4], A.Bim, o); LDU16(pre[5], A.Cre, o); LDU16(pre[6], A.Cim, o);
  }
  STAMP(11);
  if (tid < 16) ((LAS unsigned*)(lds + M_CTL))[tid] = 0u;
  unsigned tag = 0;
  CBAR();
  {
    LAS char* gl = lds + (grp == 2 ? G1_TAB - C_GRP : grp * C_GRP);
    if (bt == 0) __builtin_amdgcn_s_setprio(2);
    if (bt == 1) __builtin_amdgcn_s_setprio(1);
    if (isaux) { __builtin_amdgcn_s_setprio(3); aux_group(gl, lds, A, rs, epoch, u, gt, gw, lane STAMP_PASS); }
    else conv_group(gl, lds, bt, A, rs, epoch, tag, pre, pc, wg, d, gt, gw, lane STAMP_PASS);
    __builtin_amdgcn_s_setprio(0);
  }
  const int xcd = wg & 7, idx = wg >> 3;
  const int mt = (xcd >> 1) * 8 + (idx & 7), j = (xcd & 1) * 4 + (idx >> 3);
  const int cnt = j < 6 ? 7 : 6, sub0 = j < 6 ? 7 * j : 42 + 6 * (j - 6);
  if (wave >= 8) {
    STAMP(4);
    const int lw = wave - 8;
    if (lw + 12 < 2 * cnt) loader_path<4>(lds, ws, rs, A.wsp, wg, mt, j, sub0, lw, lane);
    else loader_path<3>(lds, ws, rs, A.wsp, wg, mt, j, sub0, lw, lane);
    if (wg == 0 && tid == 512) *(volatile unsigned*)(ws + CTL_EPOCH) = tag;
    STAMP_FLUSH(1);
    return;
  }
  v2u x2v[3];
  const int ng = wave >> 2 ? 3 - (wave & 3) : (wave & 3);
  if (ng < 3) consumer_g12<2, false>(lds, ws, rs, A.bsp, A.wsp, wg, mt, j, sub0, cnt, wave, tid, lane, x2v STAMP_PASS);
  else if (cnt == 7) consumer_g12<1, true>(lds, ws, rs, A.bsp, A.wsp, wg, mt, j, sub0, cnt, wave, tid, lane, x2v STAMP_PASS);
  else consumer_g12<0, true>(lds, ws, rs, A.bsp, A.wsp, wg, mt, j, sub0, cnt, wave, tid, lane, x2v STAMP_PASS);
  if (wave < 4) consumer_g3<0>(lds, ws, rs, A.out, mt, j, wave & 3, lane, x2v STAMP_PASS);
  else consumer_g3<1>(lds, ws, rs, A.out, mt, j, wave & 3, lane, x2v STAMP_PASS);
  STAMP(14);
  STAMP_FLUSH(1);
}

#if defined(__HIP_DEVICE_COMPILE__) && !defined(KEEP_PK)
#pragma clang attribute pop
#endif

extern "C" void kernel_launch(void* const* d_in, const int* in_sizes, int n_in, void* d_out, int out_size, void* d_ws, size_t ws_size,
                              hipStream_t stream) {
  Args A;
  A.x = (const u16*)d_in[0]; A.Are = (const u16*)d_in[1]; A.Aim = (const u16*)d_in[2]; A.Bre = (const u16*)d_in[3]; A.Bim = (const u16*)d_in[4];
  A.Cre = (const u16*)d_in[5]; A.Cim = (const u16*)d_in[6]; A.logdt = (const u16*)d_in[7]; A.dskip = (const u16*)d_in[8];
  A.win = (const u16*)d_in[10]; A.wsp = (const u16*)d_in[11]; A.bsp = (const u16*)d_in[12]; A.wout = (const u16*)d_in[13];
  A.cvec = (const u16*)d_in[14]; A.wmod = (const u16*)d_in[15];
  A.ws = (char*)d_ws; A.out = (u16*)d_out;
  (void)hipFuncSetAttribute((const void*)k_all, hipFuncAttributeMaxDynamicSharedMemorySize, M_LDS);
  k_all<<<256, 768, M_LDS, stream>>>(A);
}
```

```cpp
#include <hip/hip_runtime.h>
#include <hip/hip_bf16.h>
#include <stdint.h>

#define NT ""
#define NTLOAD(p) (*(p))
#define GAS __attribute__((address_space(1)))
#define LAS __attribute__((address_space(3)))
typedef unsigned v4u __attribute__((ext_vector_type(4)));
typedef unsigned v2u __attribute__((ext_vector_type(2)));
typedef float f32x2 __attribute__((ext_vector_type(2)));
typedef float f32x4 __attribute__((ext_vector_type(4)));
typedef float f32x16 __attribute__((ext_vector_type(16)));
typedef __bf16 bf16x2 __attribute__((ext_vector_type(2)));
typedef __bf16 bf16x8 __attribute__((ext_vector_type(8)));
typedef _Float16 f16x2 __attribute__((ext_vector_type(2)));
typedef _Float16 f16x8 __attribute__((ext_vector_type(8)));
typedef short s16x4 __attribute__((ext_vector_type(4)));
typedef unsigned short u16;

#if defined(__HIP_DEVICE_COMPILE__) && !defined(KEEP_PK)
#pragma clang attribute push(__attribute__((target("no-packed-fp32-ops"))), apply_to = function)
#endif
__device__ __forceinline__ float u2f(unsigned u) { return __builtin_bit_cast(float, u); }
__device__ __forceinline__ unsigned f2u(float f) { return __builtin_bit_cast(unsigned, f); }
__device__ __forceinline__ int lane_id() { return __builtin_amdgcn_mbcnt_hi(~0u, __builtin_amdgcn_mbcnt_lo(~0u, 0u)); }
__device__ __forceinline__ unsigned shx(unsigned v, int m) { return __builtin_amdgcn_ds_bpermute((lane_id() ^ m) << 2, v); }
__device__ __forceinline__ float shx(float v, int m) { return u2f(shx(f2u(v), m)); }
__device__ __forceinline__ bool wall(bool p) { return __builtin_amdgcn_ballot_w64(!p) == 0ull; }
__device__ __forceinline__ float exp_acc(float x) {
  const float hi = x * 1.4426950408889634f;
  const float lo = __builtin_fmaf(x, 1.4426950408889634f, -hi) + x * 1.925963033500414e-8f;
  const float e = __builtin_amdgcn_exp2f(hi);
  return __builtin_fmaf(e, lo * 0.6931471805599453f, e);
}

constexpr int L = 2048, D = 640, NS = 128, F = 1728, FH = 864;

constexpr size_t CTL_E1 = 0;
constexpr size_t CTL_E1B = 196608;
constexpr size_t CTL_E2 = 262144;
constexpr size_t CTL_E3 = 294912;
constexpr size_t CTL_EW = 303104;
constexpr size_t CTL_ST3 = 327680;
constexpr size_t CTL_F0 = CTL_ST3 + 262144;
constexpr size_t CTL_EPOCH = CTL_F0 + 32768;
constexpr size_t WS_MOD = CTL_EPOCH + 4096;
constexpr size_t WS_ST1 = WS_MOD + 131072;
constexpr size_t WS_U = WS_ST1 + 16384;
constexpr size_t WS_XT = WS_U + 65536;
constexpr size_t WS_X2T = WS_XT + (size_t)D * L * 2;
constexpr size_t WS_WIN = WS_X2T + (size_t)D * L * 2;
constexpr size_t WS_WOUT = WS_WIN + (size_t)D * F * 2;
constexpr size_t WS_Z2 = WS_WOUT + (size_t)FH * D * 2;
constexpr size_t WS_V = WS_Z2 + (size_t)L * FH * 2;
constexpr size_t WS_END = WS_V + (size_t)L * FH * 2;

__device__ __forceinline__ float bflo(unsigned u) { return u2f(u << 16); }
__device__ __forceinline__ float bfhi(unsigned u) { return u2f(u & 0xffff0000u); }
__device__ __forceinline__ float bf2f(u16 v) { return u2f((unsigned)v << 16); }
__device__ __forceinline__ unsigned pkbf(float a, float b) {
  f32x2 v = {a, b};
  return __builtin_bit_cast(unsigned, __builtin_convertvector(v, bf16x2));
}
__device__ __forceinline__ unsigned pkh(float a, float b) {
  f32x2 v = {a, b};
  return __builtin_bit_cast(unsigned, __builtin_convertvector(v, f16x2));
}
__device__ __forceinline__ float bfr(float a) { return bflo(pkbf(a, a)); }
__device__ __forceinline__ v2u trread(LAS const char* p) {
  return __builtin_bit_cast(v2u, __builtin_amdgcn_ds_read_tr16_b64_v4i16((LAS s16x4*)p));
}
__device__ __forceinline__ f32x4 mfma16(v4u a, v4u b, f32x4 c) {
  return __builtin_amdgcn_mfma_f32_16x16x32_bf16(__builtin_bit_cast(bf16x8, a), __builtin_bit_cast(bf16x8, b), c, 0, 0, 0);
}
__device__ __forceinline__ f32x16 mfma32(v4u a, v4u b, f32x16 c) {
  return __builtin_amdgcn_mfma_f32_32x32x16_bf16(__builtin_bit_cast(bf16x8, a), __builtin_bit_cast(bf16x8, b), c, 0, 0, 0);
}
__device__ __forceinline__ f32x16 mfma32h(v4u a, v4u b, f32x16 c) {
  return __builtin_amdgcn_mfma_f32_32x32x16_f16(__builtin_bit_cast(f16x8, a), __builtin_bit_cast(f16x8, b), c, 0, 0, 0);
}
template <bool DEV>
__device__ __forceinline__ void glds16(const void* sbase, unsigned voff, unsigned dst) {
  unsigned keep;
  if (DEV) asm volatile(
"s_mov_b32 %0, m0\n\ts_mov_b32 m0, %3\n\ts_nop 0\n\tglobal_load_lds_dwordx4 %1, %2 sc1\n\ts_mov_b32 m0, %0"
                        : "=&s"(keep) : "v"(voff), "s"(sbase), "s"(dst) : "memory");
  else asm volatile("s_mov_b32 %0, m0\n\ts_mov_b32 m0, %3\n\ts_nop 0\n\tglobal_load_lds_dwordx4 %1, %2\n\ts_mov_b32 m0, %0"
                    : "=&s"(keep) : "v"(voff), "s"(sbase), "s"(dst) : "memory");
}
__device__ __forceinline__ void glds16nt(const void* sbase, unsigned voff, unsigned dst) {
  unsigned keep;
  asm volatile("s_mov_b32 %0, m0\n\ts_mov_b32 m0, %3\n\ts_nop 0\n\tglobal_load_lds_dwordx4 %1, %2" NT "\n\ts_mov_b32 m0, %0"
               : "=&s"(keep) : "v"(voff), "s"(sbase), "s"(dst) : "memory");
}
#define GL_(i, s) "global_load_lds_dwordx4 %" #i ", %5 offset:" s
template <int N, bool DEV>
__device__ __forceinline__ void gldsN(const void* sbase, unsigned v0, unsigned v1, unsigned v2, unsigned v3, unsigned dst) {
  unsigned keep;
#define GLN_(body) asm volatile("s_mov_b32 %0, m0\n\ts_mov_b32 m0, %6\n\ts_nop 0\n\t" body "s_mov_b32 m0, %0" : "=&s"(keep) : "v"(v0), "v"(v1), "v"(v2), "v"(v3), "s"(sbase), "s"(dst) : "memory")
  if (DEV) {
    if (N == 1) GLN_(GL_(1, "0 sc1\n\t"));
    if (N == 2) GLN_(GL_(1, "0 sc1\n\t") GL_(2, "1024 sc1\n\t"));
    if (N == 3) GLN_(GL_(1, "0 sc1\n\t") GL_(2, "1024 sc1\n\t") GL_(3, "2048 sc1\n\t"));
    if (N == 4) GLN_(GL_(1, "0 sc1\n\t") GL_(2, "1024 sc1\n\t") GL_(3, "2048 sc1\n\t") GL_(4, "3072 sc1\n\t"));
  } else {
    if (N == 2) GLN_(GL_(1, "0\n\t") GL_(2, "1024\n\t"));
    if (N == 4) GLN_(GL_(1, "0\n\t") GL_(2, "1024\n\t") GL_(3, "2048\n\t") GL_(4, "3072\n\t"));
  }
}
template <int N> __device__ __forceinline__ void gldsL(const void* sbase, unsigned vo, unsigned dst) { gldsN<N, true>(sbase, vo, vo, vo, vo, dst); }
template <int N> __device__ __forceinline__ void waitv() { asm volatile("s_waitcnt vmcnt(%0)" :: "i"(N) : "memory"); }
__device__ __forceinline__ float dot2bf(unsigned a, unsigned b, float c) {
  return __builtin_amdgcn_fdot2_f32_bf16(__builtin_bit_cast(bf16x2, a), __builtin_bit_cast(bf16x2, b), c, false);
}
#define SCHED_BAR() __builtin_amdgcn_sched_barrier(0)
#define STAMP_DECL
#define STAMP(i) do {} while (0)
#define STAMP_FLUSH(kid) do {} while (0)
#define STAMP_ARG
#define STAMP_PASS
#define CBAR() do { SCHED_BAR(); __builtin_amdgcn_s_waitcnt(0xC07F); asm volatile("" ::: "memory"); __builtin_amdgcn_s_barrier(); asm volatile("" ::: "memory"); } while (0)
#define LBAR() __builtin_amdgcn_s_barrier()

template <int CTRL> __device__ __forceinline__ float dppf(float v) {
  return __builtin_bit_cast(float, __builtin_amdgcn_update_dpp(0, __builtin_bit_cast(int, v), CTRL, 0xf, 0xf, true));
}
template <int N, int CTRL> __device__ __forceinline__ void treduce_dpp(float* v, bool up) {
#pragma unroll
  for (int e = 0; e < N; ++e) {
    const float a = v[e], b = v[e + N];
    const float send = up ? a : b, keep = up ? b : a;
    v[e] = keep + dppf<CTRL>(send);
  }
}
template <int N> __device__ __forceinline__ void treduce_xor(float* v, bool up, int mask) {
#pragma unroll
  for (int e = 0; e < N; ++e) {
    const float a = v[e], b = v[e + N];
    const float send = up ? a : b, keep = up ? b : a;
    v[e] = keep + shx(send, mask);
  }
}

__device__ __forceinline__ float ln_std(float var) { return __builtin_amdgcn_sqrtf(bfr(bfr(var) + 1e-5f)); }
__device__ __forceinline__ void ln_scalars(float mean, float var, float& m_b, float& rinv) {
  m_b = bfr(mean);
  rinv = __builtin_amdgcn_rcpf(bfr(ln_std(var)));
}

struct cplx { float r, i; };
__device__ __forceinline__ cplx cmul(cplx a, cplx b) { return {a.r * b.r - a.i * b.i, a.r * b.i + a.i * b.r}; }

typedef GAS unsigned gu32;
#define RLX_AGENT __ATOMIC_RELAXED, __HIP_MEMORY_SCOPE_AGENT
#define F0_REP 8
#define V_DELAY 24
#define V_WIN 5
#define LSTAMP(i) do {} while (0)
#define POLL_SLEEP 6
#define E1_REP 32
#define INTERLEAVE(n, r) do { _Pragma("unroll") for (int i_ = 0; i_ < (n); ++i_) { __builtin_amdgcn_sched_group_barrier(0x008, 1, 0); __builtin_amdgcn_sched_group_barrier(0x100, (r), 0); } \
    __builtin_amdgcn_sched_group_barrier(0x008, 8, 0); SCHED_BAR(); } while (0)
#define ABL_KK kk
#define AUXB_PRIO 2
#define XGATE 16
#define PV_POS 0
constexpr unsigned SPIN_CAP = 1u << 16;
constexpr int M_LDS = 163840, M_CTL = M_LDS - 64;
constexpr int CODE_LINES = 400;
constexpr int C_KOFF = 0, C_UOFF = 4224, C_UP = 1536, C_COFF = C_UOFF + 4 * C_UP, C_GRP = C_COFF + 4 * 64 * 144;
constexpr int G1_BOFF = 81920, G1_BST = 14336, G1_NSLOT = 5, G1_TAB = G1_BOFF + G1_BST * G1_NSLOT;
constexpr int G1_T_LN2 = G1_TAB, G1_T_U = G1_T_LN2 + 512, G1_T_P = G1_T_U + 2048, G1_T_Z = G1_T_P + 2048;
constexpr int G2_NW = 9, G2_NZ = 6, G2_ZOFF = G2_NW * 8192, G2_ZST = 14336;
constexpr int G3_RED = 135 * 1024, G3_GB = 161024;
static_assert(3 * C_GRP <= M_CTL && G1_T_Z + 512 <= M_CTL && G2_ZOFF + G2_NZ * G2_ZST <= M_CTL && G3_GB + 320 <= M_CTL, "lds");

__device__ __forceinline__ void st128(__amdgpu_buffer_rsrc_t rs, unsigned off, v4u v) { __builtin_amdgcn_raw_buffer_store_b128(v, rs, off, 0, 16); }
__device__ __forceinline__ v4u ld128(__amdgpu_buffer_rsrc_t rs, unsigned off) { return __builtin_amdgcn_raw_buffer_load_b128(rs, off, 0, 16); }

__device__ __forceinline__ bool wave_arrive(LAS char* lds, int word, unsigned n, int lane) {
  waitv<0>();
  unsigned old = 0;
  if (lane == 0) old = __hip_atomic_fetch_add((LAS unsigned*)(lds + M_CTL) + word, 1u, __ATOMIC_RELAXED, __HIP_MEMORY_SCOPE_WORKGROUP);
  return (unsigned)__builtin_amdgcn_readfirstlane(old) == n - 1;
}
__device__ __forceinline__ void lds_wait(LAS char* lds0, int word, unsigned target) {
  LAS unsigned* p = (LAS unsigned*)(lds0 + M_CTL) + word;
  while ((unsigned)__builtin_amdgcn_readfirstlane(__hip_atomic_load(p, __ATOMIC_RELAXED, __HIP_MEMORY_SCOPE_WORKGROUP)) < target) __builtin_amdgcn_s_sleep(0);
  asm volatile("" ::: "memory");
}
__device__ __forceinline__ void lds_post(LAS char* lds0, int word, int lane) {
  SCHED_BAR();
  asm volatile("" ::: "memory");
  if (lane == 0) __hip_atomic_fetch_add((LAS unsigned*)(lds0 + M_CTL) + word, 1u, __ATOMIC_RELAXED, __HIP_MEMORY_SCOPE_WORKGROUP);
  asm volatile("" ::: "memory");
}
__device__ __forceinline__ void group_sync(LAS char* lds0, int word, unsigned target, int lane) { lds_post(lds0, word, lane); lds_wait(lds0, word, target); }
template <int NREP, int NSH> __device__ __forceinline__ void signal(char* base, int shard, int lane) {
  if (lane < NREP) __hip_atomic_fetch_add((gu32*)(base + (lane * NSH + shard) * 256), 1u, RLX_AGENT);
}
template <int NSH> __device__ __forceinline__ bool count_ok(unsigned c, unsigned total) {
  if (NSH > 1) c += __builtin_amdgcn_update_dpp(0, c, 0xb1, 0xf, 0xf, true);
  if (NSH > 2) c += __builtin_amdgcn_update_dpp(0, c, 0x4e, 0xf, 0xf, true);
  if (NSH > 4) c += __builtin_amdgcn_update_dpp(0, c, 0x141, 0xf, 0xf, true);
  return (unsigned)__builtin_amdgcn_readfirstlane(c) >= total;
}
template <int NSH> __device__ __forceinline__ void wait_count(char* base, int rep, unsigned total, int lane) {
  gu32* p = (gu32*)(base + (rep * NSH + (lane & (NSH - 1))) * 256);
#pragma unroll 1
  for (unsigned spin = 0; spin < SPIN_CAP; ++spin) {
    if (count_ok<NSH>(__hip_atomic_load(p, RLX_AGENT), total)) break;
    __builtin_amdgcn_s_sleep(POLL_SLEEP);
  }
  __builtin_amdgcn_fence(__ATOMIC_ACQUIRE, "wavefront");
}
template <int NSH> __device__ __forceinline__ void poll_issue(unsigned& c, char* base, int rep, int lane) {
  c = __hip_atomic_load((gu32*)(base + (rep * NSH + (lane & (NSH - 1))) * 256), RLX_AGENT);
}
template <int NSH> __device__ __forceinline__ void poll_finish(unsigned c, char* base, int rep, unsigned total, int lane) {
  if (!count_ok<NSH>(c, total)) wait_count<NSH>(base, rep, total, lane);
  __builtin_amdgcn_fence(__ATOMIC_ACQUIRE, "wavefront");
}

__device__ __forceinline__ float gelu_f(float x) {
  const float p = __builtin_fmaf(x * x, -0.044715f * 2.302208198f, -2.302208198f);
  const float e = __builtin_amdgcn_exp2f(x * p);
  return x * __builtin_amdgcn_rcpf(1.0f + e);
}

__device__ __forceinline__ float ld32f(__amdgpu_buffer_rsrc_t rs, unsigned off) { return u2f(__builtin_amdgcn_raw_buffer_load_b32(rs, off, 0, 16)); }
__device__ __forceinline__ void st32(__amdgpu_buffer_rsrc_t rs, unsigned off, unsigned v) { __builtin_amdgcn_raw_buffer_store_b32(v, rs, off, 0, 16); }

struct Args {
  const u16 *x, *Are, *Aim, *Bre, *Bim, *Cre, *Cim, *logdt, *dskip, *win, *wsp, *bsp, *wout, *cvec, *wmod;
  char* ws; u16* out;
};

typedef GAS unsigned long long gu64;
__device__ __forceinline__ void put_granule(char* p, unsigned v, unsigned tag) { __hip_atomic_store((gu64*)p, ((unsigned long long)tag << 32) | v, RLX_AGENT); }
template <int N, class F> __device__ __forceinline__ void get_granules(unsigned (&v)[N], unsigned tag, F addr) {
#pragma unroll 1
  for (unsigned spin = 0; spin < SPIN_CAP; ++spin) {
    unsigned long long x[N];
#pragma unroll
    for (int k = 0; k < N; ++k) x[k] = __hip_atomic_load((gu64*)addr(k), RLX_AGENT);
    bool ok = true;
#pragma unroll
    for (int k = 0; k < N; ++k) { v[k] = (unsigned)x[k]; ok &= (unsigned)(x[k] >> 32) == tag; }
    if (wall(ok)) break;
    __builtin_amdgcn_s_sleep(3);
  }
}

__device__ __forceinline__ void conv_group(LAS char* lds, LAS char* lds0, int bt, const Args& A, __amdgpu_buffer_rsrc_t rs, unsigned epoch, unsigned& tag, unsigned (&pre)[7], unsigned long long pc,
                                           int wg, int d, int tid, int wave, int lane STAMP_ARG) {
  char* ws = A.ws;
  float S[8], soff;
  v4u xv, st[2];
  unsigned cpf = 0, pv3 = 0;
  {
    const int half = wave & 1;
    int xoff[8];
#pragma unroll
    for (int j = 0; j < 8; ++j) {
      const int l = tid * 8 + j;
      const int idx = l < 128 ? l : (int)(pkbf((float)l, 0.f) & 0xffffu) - 0x4300 + 128;
      xoff[j] = (idx + (idx >> 3)) * 8;
    }
    asm volatile("" : "+v"(xoff[0]), "+v"(xoff[1]), "+v"(xoff[2]), "+v"(xoff[3]), "+v"(xoff[4]), "+v"(xoff[5]), "+v"(xoff[6]), "+v"(xoff[7]));
    waitv<0>();
    asm volatile("v_readfirstlane_b32 %0, %1\n\ts_nop 3" : "=s"(tag) : "v"(epoch) : "memory");
    tag += 1u;
    asm volatile("" : "+v"(pre[0]), "+v"(pre[1]), "+v"(pre[2]), "+v"(pre[3]), "+v"(pre[4]), "+v"(pre[5]), "+v"(pre[6]));
    {
      const int so = ((wg >> 3) * 4 + wave) * 4 + (lane & 3);
      cpf = *(const GAS unsigned*)((pc & ~255ull) + (size_t)(bt == 1 && so < CODE_LINES ? so : 0) * 128);
    }
    const unsigned vare = pre[0], vaim = pre[1], vdt = pre[2], vbre = pre[3], vbim = pre[4], vcre = pre[5], vcim = pre[6];
    if (tid < 4) *(LAS v4u*)(lds + C_KOFF + tid * 16) = v4u{0u, 0u, 0u, 0u};
    const float dt = bfr(exp_acc(bflo(vdt)));
    const float ar2 = dt * bflo(vare) * 1.4426950408889634f, ai2 = dt * bflo(vaim) * 0.15915494309189535f;
    auto E = [&](float xx) {
      const float mag = __builtin_amdgcn_exp2f(ar2 * xx), ph = __builtin_amdgcn_fractf(ai2 * xx);
      return cplx{mag * __builtin_amdgcn_cosf(ph), mag * __builtin_amdgcn_sinf(ph)};
    };
    LAS char* img = lds + C_COFF + wave * 8192 + lane * 128;
    const int sw = lane & 7;
    float isc = 0.f;
    if (wave < 2) {
      const cplx e8 = E(8.f), e64 = E(64.f), e128 = E(128.f), e192 = E(192.f);
      cplx q[8];
      q[0] = {1.f, 0.f}; q[1] = e8; q[2] = cmul(e8, e8); q[3] = cmul(q[2], e8); q[4] = cmul(q[2], q[2]);
      q[5] = cmul(q[4], e8); q[6] = cmul(q[4], q[2]); q[7] = cmul(q[4], q[3]);
#pragma unroll
      for (int g = 0; g < 4; ++g) {
        cplx v[8];
#pragma unroll
        for (int j = 0; j < 8; ++j) v[j] = g == 0 ? q[j] : cmul(q[j], g == 1 ? e64 : (g == 2 ? e128 : e192));
        *(LAS v4u*)(img + ((g ^ sw) << 4)) = v4u{pkh(v[0].r, v[1].r), pkh(v[2].r, v[3].r), pkh(v[4].r, v[5].r), pkh(v[6].r, v[7].r)};
        *(LAS v4u*)(img + (((4 + g) ^ sw) << 4)) = v4u{pkh(v[0].i, v[1].i), pkh(v[2].i, v[3].i), pkh(v[4].i, v[5].i), pkh(v[6].i, v[7].i)};
      }
    } else {
      cplx W = cmul(cplx{bflo(vcre), bflo(vcim)}, cplx{dt * bflo(vbre), dt * bflo(vbim)});
      float mx = __builtin_fmaxf(__builtin_fabsf(W.r), __builtin_fabsf(W.i));
      mx = __builtin_fmaxf(mx, dppf<0xb1>(mx)); mx = __builtin_fmaxf(mx, dppf<0x4e>(mx)); mx = __builtin_fmaxf(mx, dppf<0x141>(mx)); mx = __builtin_fmaxf(mx, dppf<0x140>(mx));
      const unsigned mb = f2u(mx);
      const unsigned m01 = __builtin_elementwise_max((unsigned)__builtin_amdgcn_readlane(mb, 0), (unsigned)__builtin_amdgcn_readlane(mb, 16));
      const unsigned m23 = __builtin_elementwise_max((unsigned)__builtin_amdgcn_readlane(mb, 32), (unsigned)__builtin_amdgcn_readlane(mb, 48));
      int ex = (int)((__builtin_elementwise_max(m01, m23) >> 23) & 0xff);
      ex = ex < 20 ? 20 : (ex > 240 ? 240 : ex);
      const float sc = u2f((unsigned)(267 - ex) << 23);
      isc = u2f((unsigned)(ex - 13) << 23);
      W.r *= sc; W.i *= sc;
      const cplx e1 = E(1.f), e2 = cmul(e1, e1), e4 = cmul(e2, e2);
      cplx h[24];
      h[0] = W; h[1] = cmul(W, e1); h[2] = cmul(W, e2); h[3] = cmul(h[2], e1);
      h[4] = cmul(W, e4); h[5] = cmul(h[4], e1); h[6] = cmul(h[4], e2); h[7] = cmul(h[6], e1);
      const cplx g1 = E(256.f), g2 = E(512.f), g3 = E(768.f), g4 = E(1024.f);
      h[8] = cmul(h[0], g1); h[9] = cmul(h[2], g1); h[10] = cmul(h[4], g1); h[11] = cmul(h[6], g1);
      h[12] = cmul(h[0], g2); h[13] = cmul(h[4], g2); h[14] = cmul(h[0], g3); h[15] = cmul(h[4], g3);
      h[16] = cmul(h[0], g4); h[17] = cmul(h[8], g4); h[18] = cmul(h[12], g4); h[19] = cmul(h[14], g4);
      h[20] = cmul(h[16], g4); h[21] = {0.f, 0.f}; h[22] = {0.f, 0.f}; h[23] = {0.f, 0.f};
#pragma unroll
      for (int g = 0; g < 3; ++g) {
        const cplx* v = h + 8 * g;
        *(LAS v4u*)(img + ((g ^ sw) << 4)) = v4u{pkh(v[0].r, v[1].r), pkh(v[2].r, v[3].r), pkh(v[4].r, v[5].r), pkh(v[6].r, v[7].r)};
        *(LAS v4u*)(img + (((4 + g) ^ sw) << 4)) = v4u{pkh(-v[0].i, -v[1].i), pkh(-v[2].i, -v[3].i), pkh(-v[4].i, -v[5].i), pkh(-v[6].i, -v[7].i)};
      }
      *(LAS v4u*)(img + ((3 ^ sw) << 4)) = v4u{0u, 0u, 0u, 0u};
      *(LAS v4u*)(img + ((7 ^ sw) << 4)) = v4u{0u, 0u, 0u, 0u};
    }
    STAMP(1);
    group_sync(lds0, 5 + bt, 4, lane);
    if (wave >= 2) {
      const int hh = lane >> 5, blk = (lane >> 4) & 1, qq = (lane & 15) >> 2, p = lane & 3;
      LAS const char* Qi = lds + C_COFF + half * 8192;
      v2u a[8][2], bb[8][2];
#pragma unroll
      for (int ks = 0; ks < 8; ++ks)
#pragma unroll
        for (int t = 0; t < 2; ++t) {
          const int k = 16 * ks + 8 * hh + 4 * t + qq, mode = k >> 1, c = k & 1;
          const int off = mode * 128 + (((c * 4 + 2 * blk + (p >> 1)) ^ (mode & 7)) << 4) + (p & 1) * 8;
          a[ks][t] = trread(Qi + 16384 + off);
          bb[ks][t] = trread(Qi + off);
        }
      SCHED_BAR();
      f32x16 acc = {};
#pragma unroll
      for (int ks = 0; ks < 8; ++ks) acc = mfma32h(v4u{a[ks][0].x, a[ks][0].y, a[ks][1].x, a[ks][1].y}, v4u{bb[ks][0].x, bb[ks][0].y, bb[ks][1].x, bb[ks][1].y}, acc);
      LAS float* T = (LAS float*)(lds + C_UOFF) + half;
      const int q = lane & 31, hi = lane >> 5;
      auto put = [&](int idx, float v) { T[(idx + (idx >> 3)) * 2] = v * isc; };
#pragma unroll
      for (int r = 0; r < 4; ++r) put((r + 4 * hi) + 8 * q, acc[r]);
#pragma unroll
      for (int r = 0; r < 4; ++r) put(hi ? 384 + 64 * (r >> 1) + (r & 1) + 2 * q : 256 + r + 4 * q, acc[4 + r]);
      if (hi == 0) {
#pragma unroll
        for (int r = 0; r < 4; ++r) put(512 + 32 * r + q, acc[8 + r]);
      } else if (q == 0) put(640, acc[8]);
    }
    lds_post(lds0, 5 + bt, lane);
    auto load_x = [&]() {
      xv = ld128(rs, (unsigned)WS_XT + (tid * D + d) * 16);
#pragma unroll
      for (int i = 0; i < 2; ++i) st[i] = ld128(rs, (unsigned)WS_ST1 + (i * 256 + tid) * 16);
      SCHED_BAR();
    };
    const bool early = __builtin_amdgcn_readfirstlane(__hip_atomic_load((LAS unsigned*)(lds0 + M_CTL) + (bt ? 5 : 8), __ATOMIC_RELAXED, __HIP_MEMORY_SCOPE_WORKGROUP)) >= (bt ? XGATE : 1);
    asm volatile("" ::: "memory");
    if (early) load_x();
    lds_wait(lds0, 5 + bt, 8);
    {
      f32x2 tv[8];
#pragma unroll
      for (int j = 0; j < 8; ++j) tv[j] = *(LAS const f32x2*)(lds + C_UOFF + xoff[j]);
      SCHED_BAR();
      float kv[8];
#pragma unroll
      for (int j = 0; j < 8; ++j) kv[j] = tv[j][0] + tv[j][1];
      *(LAS v4u*)(lds + C_KOFF + 64 + tid * 16) = v4u{pkbf(kv[0], kv[1]), pkbf(kv[2], kv[3]), pkbf(kv[4], kv[5]), pkbf(kv[6], kv[7])};
      S[0] = kv[0];
#pragma unroll
      for (int j = 1; j < 8; ++j) S[j] = S[j - 1] + kv[j];
      float inc = S[7];
      inc += dppf<0x111>(inc); inc += dppf<0x112>(inc); inc += dppf<0x114>(inc); inc += dppf<0x118>(inc);
      inc += __builtin_bit_cast(float, __builtin_amdgcn_update_dpp(0, __builtin_bit_cast(int, inc), 0x142, 0xa, 0xf, false));
      inc += __builtin_bit_cast(float, __builtin_amdgcn_update_dpp(0, __builtin_bit_cast(int, inc), 0x143, 0xc, 0xf, false));
      soff = inc - S[7];
      if (lane == 63) ((LAS float*)(lds + C_KOFF + 4160))[wave] = inc;
    }
    lds_post(lds0, 5 + bt, lane);
    if (!early) {
      if (bt == 0 && wave == 0) {
#pragma unroll 1
        for (unsigned spin = 0; spin < SPIN_CAP; ++spin) {
          asm volatile("" ::: "memory");
          const v4u f = ld128(rs, (unsigned)CTL_F0 + (wg & (F0_REP - 1)) * 1024 + lane * 16);
          if (wall(f.x == tag && f.y == ~tag && f.z == tag && f.w == ~tag)) break;
          __builtin_amdgcn_s_sleep(2);
        }
        __builtin_amdgcn_fence(__ATOMIC_ACQUIRE, "wavefront");
        lds_post(lds0, 8, lane);
      } else if (bt == 0) lds_wait(lds0, 8, 1);
      else lds_wait(lds0, 5, XGATE);
      load_x();
    }
    STAMP(2);
  }
  lds_wait(lds0, 5 + bt, 12);
  STAMP(3);
  float xf[8], xn[8];
  {
#pragma unroll
    for (int j = 0; j < 4; ++j) { xf[2 * j] = bflo(xv[j]); xf[2 * j + 1] = bfhi(xv[j]); }
    const f32x4 wt = *(LAS const f32x4*)(lds + C_KOFF + 4160);
    if (tid < 128) *(LAS v4u*)(lds + C_UOFF + (tid >> 5) * C_UP + (tid & 31) * 16) = v4u{0u, 0u, 0u, 0u};
#pragma unroll
    for (int j = 0; j < 8; ++j) {
      const unsigned sj = st[j >> 2][j & 3];
      xn[j] = (xf[j] - bflo(sj)) * __builtin_amdgcn_rcpf(bfhi(sj));
    }
#pragma unroll
    for (int e = 0; e < 4; ++e) *(LAS unsigned*)(lds + C_UOFF + e * C_UP + 512 + tid * 4) = pkbf(xn[e], xn[e + 4]);
    soff += (wave > 0 ? wt[0] : 0.f) + (wave > 1 ? wt[1] : 0.f) + (wave > 2 ? wt[2] : 0.f);
  }
  const u16 dskv = A.dskip[d];
  char* mgp = ws + WS_MOD + (size_t)((lane & 3) * 3840 + (lane < 12 ? lane >> 2 : 0) * D + d) * 8;
  const unsigned long long mg = __hip_atomic_load((gu64*)mgp, RLX_AGENT);
  STAMP(5);
  group_sync(lds0, 5 + bt, 16, lane);
  STAMP(6);
  f32x16 y0 = {}, y1 = {};
  {
    const int hh = lane >> 5, blk = (lane >> 4) & 1, qq = (lane & 15) >> 2, p = lane & 3, col = lane & 31;
    LAS const char* ab = lds + C_KOFF + (32 + 16 * blk + 4 * p - 4 * qq + 32 * hh) * 2 - 32;
    LAS const char* bb = lds + C_UOFF + wave * C_UP + (256 + 8 * (col - hh) - 240) * 2;
    struct CF { v2u a0l, a0h, a1l, a1h; v4u b0, b1; };
    CF cf[3];
    auto rdc = [&](int ks, CF& f) {
      f.a0l = trread(ab + ks * 128 + 32); f.a0h = trread(ab + ks * 128);
      f.b0 = *(LAS const v4u*)(bb + (15 - ks) * 32);
      f.b1 = *(LAS const v4u*)(bb + (31 - ks) * 32);
      f.a1l = trread(ab + (ks + 16) * 128 + 32); f.a1h = trread(ab + (ks + 16) * 128);
    };
    rdc(0, cf[0]); rdc(1, cf[1]);
    SCHED_BAR();
#pragma unroll
    for (int ks = 0; ks < 16; ++ks) {
      if (ks + 2 < 16) rdc(ks + 2, cf[(ks + 2) % 3]);
      const CF& f = cf[ks % 3];
      const v4u a0 = {f.a0l.x, f.a0l.y, f.a0h.x, f.a0h.y}, a1 = {f.a1l.x, f.a1l.y, f.a1h.x, f.a1h.y};
      y0 = mfma32(a0, f.b0, y0);
      y1 = mfma32(a0, f.b1, y1);
      y1 = mfma32(a1, f.b0, y1);
      INTERLEAVE(3, 2);
    }
    asm volatile("" :: "v"(y0), "v"(y1));
    STAMP(7);
    LAS char* cb = lds + C_COFF + (wave * 64 + col) * 144 + hh * 16;
#pragma unroll
    for (int g = 0; g < 4; ++g) {
      *(LAS f32x4*)(cb + g * 32) = f32x4{y0[4 * g], y0[4 * g + 1], y0[4 * g + 2], y0[4 * g + 3]};
      *(LAS f32x4*)(cb + 32 * 144 + g * 32) = f32x4{y1[4 * g], y1[4 * g + 1], y1[4 * g + 2], y1[4 * g + 3]};
    }
  }
  lds_post(lds0, 5 + bt, lane);
  unsigned mv[1] = {(unsigned)mg};
  if (!wall((unsigned)(mg >> 32) == tag)) get_granules<1>(mv, tag, [&](int) { return mgp; });
  float msum = u2f(mv[0]);
  msum += dppf<0xb1>(msum); msum += dppf<0x4e>(msum);
  const unsigned mb = f2u(bfr(msum));
  const float sa = u2f(__builtin_amdgcn_readlane(mb, 0)), c1a = bfr(1.0f + u2f(__builtin_amdgcn_readlane(mb, 4))), ga = u2f(__builtin_amdgcn_readlane(mb, 8)), dsk = bf2f(dskv);
  const float gc = ga * c1a, gs_ = ga * sa, gd = gc * dsk, g0 = gs_ * (soff + dsk);
#pragma unroll
  for (int j = 0; j < 8; ++j) xf[j] = __builtin_fmaf(gd, xn[j], __builtin_fmaf(gs_, S[j], xf[j] + g0));
  STAMP(8);
  lds_wait(lds0, 5 + bt, 20);
  STAMP(9);
  float y[8];
  {
    const int c1 = 2 * tid, c0 = tid ? c1 - 1 : 0;
    const int o0 = (c0 >> 3) * 144 + (c0 & 7) * 16, o1 = (c1 >> 3) * 144 + (c1 & 7) * 16;
    LAS const char* cb = lds + C_COFF;
    f32x4 w0[4], w1[4], w2[4];
#pragma unroll
    for (int e = 0; e < 4; ++e) {
      w1[e] = *(LAS const f32x4*)(cb + e * 64 * 144 + o1); w2[e] = *(LAS const f32x4*)(cb + e * 64 * 144 + o1 + 16);
      if (e) w0[e] = *(LAS const f32x4*)(cb + e * 64 * 144 + o0);
    }
    SCHED_BAR();
#pragma unroll
    for (int e = 0; e < 4; ++e) {
      float w[12];
#pragma unroll
      for (int j = 0; j < 4; ++j) { w[4 + j] = w1[e][j]; w[8 + j] = w2[e][j]; w[j] = (e && tid) ? w0[e][j] : 0.f; }
#pragma unroll
      for (int j = 0; j < 8; ++j) y[j] = e ? y[j] + w[4 + j - e] : w[4 + j];
    }
  }
  float o[8];
#pragma unroll
  for (int j = 0; j < 8; ++j) o[j] = __builtin_fmaf(gc, y[j], xf[j]);
  {
    const unsigned a0 = pkbf(o[0], o[1]), a1 = pkbf(o[2], o[3]), b0 = pkbf(o[4], o[5]), b1 = pkbf(o[6], o[7]);
    const bool odd = tid & 1;
    const unsigned s0 = odd ? a0 : b0, s1 = odd ? a1 : b1;
    const unsigned r0 = __builtin_amdgcn_update_dpp(0, s0, 0xb1, 0xf, 0xf, true), r1 = __builtin_amdgcn_update_dpp(0, s1, 0xb1, 0xf, 0xf, true);
    const v4u ov = odd ? v4u{r0, r1, b0, b1} : v4u{a0, a1, r0, r1};
    st128(rs, (unsigned)WS_X2T + d * (L * 2) + (tid >> 2) * 64 + (tid & 1) * 32 + (tid & 2) * 8, ov);
  }
  STAMP(10);
  asm volatile("" :: "v"(cpf), "v"(pv3));
  if (wave_arrive(lds0, bt, 4, lane)) signal<E1_REP, 8>(ws + CTL_E1 + bt * 65536, wg & 7, lane);
}

__device__ __forceinline__ void aux_group(LAS char* lds, LAS char* lds0, const Args& A, __amdgpu_buffer_rsrc_t rs, unsigned epoch, int u, int tid, int wave, int lane STAMP_ARG) {
  char* ws = A.ws;
  unsigned gs = 0;
  v4u wb[5], ov[3], mw[10];
  u16 mc;
  const int mpart = u & 3, mcol = (u < 120 ? u >> 2 : 0) * 128;
  unsigned tag;
  {
    const int r = tid >> 4, s = tid & 15, q = lane >> 4;
    v4u v[5];
    {
      const u16* xp = A.x + (size_t)(u * 16 + r) * D + (s ^ ((q | ((wave >> 1) << 2)) << 1)) * 8;
#pragma unroll
      for (int i = 0; i < 5; ++i) v[i] = NTLOAD((const v4u*)(xp + i * 128));
    }
    SCHED_BAR();
    mc = A.cvec[mpart * 160 + (tid < 160 ? tid : 0)];
#pragma unroll
    for (int i = 0; i < 10; ++i) mw[i] = NTLOAD((const v4u*)(A.wmod + (size_t)(mpart * 160 + i * 16 + (tid >> 4)) * (6 * D) + mcol + (tid & 15) * 8));
    SCHED_BAR();
    {
      const int zc = (u * 4 + wave) * 3 + lane - 32;
      st128(rs, lane < 32 ? (unsigned)CTL_ST3 + ((u * 4 + wave) * 32 + lane) * 16 : (lane < 35 && zc < 1280 ? (unsigned)zc * 256 : 0x7ffffff0u), v4u{0u, 0u, 0u, 0u});
    }
    LAS char* img = lds + 8192;
#pragma unroll
    for (int i = 0; i < 5; ++i) *(LAS v4u*)(img + (wave * 5 + i) * 1024 + lane * 16) = v[i];
    lds_post(lds0, 7, lane); gs += 4;
    STAMP(3);
    float s0 = 0.f, s1 = 0.f, q0 = 0.f, q1 = 0.f;
#pragma unroll
    for (int i = 0; i < 5; ++i) {
      s0 = dot2bf(v[i][0], 0x3f803f80u, s0); s1 = dot2bf(v[i][1], 0x3f803f80u, s1); s0 = dot2bf(v[i][2], 0x3f803f80u, s0); s1 = dot2bf(v[i][3], 0x3f803f80u, s1);
      q0 = dot2bf(v[i][0], v[i][0], q0); q1 = dot2bf(v[i][1], v[i][1], q1); q0 = dot2bf(v[i][2], v[i][2], q0); q1 = dot2bf(v[i][3], v[i][3], q1);
    }
    SCHED_BAR();
    lds_wait(lds0, 7, gs);
    const int rr = (lane & 15) >> 2, p = lane & 3, f = (rr | ((q & 1) << 2)) << 1;
    v2u lo[5], hi[5];
#pragma unroll
    for (int j = 0; j < 5; ++j) {
      const int a0 = (2 * (q & 1) * 5 + j) * 1024 + rr * 256 + ((((2 * wave + (q >> 1)) * 2 + (p >> 1)) ^ f) << 4) + (p & 1) * 8;
      lo[j] = trread(img + a0); hi[j] = trread(img + a0 + 5120);
    }
    float sum = s0 + s1, sq = q0 + q1;
    sum += dppf<0xb1>(sum); sq += dppf<0xb1>(sq); sum += dppf<0x4e>(sum); sq += dppf<0x4e>(sq);
    sum += dppf<0x141>(sum); sq += dppf<0x141>(sq); sum += dppf<0x140>(sum); sq += dppf<0x140>(sq);
#pragma unroll
    for (int j = 0; j < 5; ++j)
      st128(rs, (unsigned)WS_XT + ((u * 2 + (q & 1)) * D + (8 * j + 2 * wave + (q >> 1)) * 16 + (lane & 15)) * 16, v4u{lo[j].x, lo[j].y, hi[j].x, hi[j].y});
    const float mean = sum * (1.0f / D);
    const int t = u * 16 + r;
    st32(rs, s == 0 ? (unsigned)WS_ST1 + ((((t >> 2) & 1) * 256 + (t >> 3)) * 4 + (t & 3)) * 4 : 0x7ffffff0u, pkbf(mean, ln_std(__builtin_fmaxf(sq * (1.0f / D) - mean * mean, 0.f))));
    STAMP(8);
    SCHED_BAR();
    {
      const float c = bf2f(mc);
      ((LAS float*)lds)[tid] = bfr(c * __builtin_amdgcn_rcpf(1.0f + __builtin_amdgcn_exp2f(-1.4426950408889634f * c)));
    }
    lds_post(lds0, 7, lane); gs += 4;
    asm volatile("s_waitcnt vmcnt(0)\n\tv_readfirstlane_b32 %0, %1\n\ts_nop 3" : "=s"(tag) : "v"(epoch) : "memory");
    tag += 1u;
    if (wave_arrive(lds0, 9, 4, lane) && lane < F0_REP)
      __hip_atomic_store((gu64*)(ws + CTL_F0 + lane * 1024 + u * 8), ((unsigned long long)~tag << 32) | tag, RLX_AGENT);
    SCHED_BAR();
    {
      const int r8 = tid >> 3, s8 = tid & 7;
      const u16* bp = A.win + (size_t)(160 * (u & 3) + r8) * F + 64 * ((u < 108 ? u : 0) >> 2) + s8 * 8;
#pragma unroll
      for (int i = 0; i < 5; ++i) wb[i] = NTLOAD((const v4u*)(bp + (size_t)i * 32 * F));
      const int slab = u < 108 ? u >> 2 : 0;
      const int half = (r8 >> 2) & 1, jj = slab < 24 ? slab / 3 : 2 * (slab - 24) + half;
      const int sub = (jj < 6 ? 7 * jj : 42 + 6 * (jj - 6)) + (slab < 24 ? 2 * (slab - 3 * jj) + half : 6);
      const u16* src = A.wout + (size_t)(32 * (sub >> 1) + 8 * (r8 >> 3) + 4 * (sub & 1) + (r8 & 3)) * D + (u & 3) * 160;
#pragma unroll
      for (int i = 0; i < 3; ++i) ov[i] = NTLOAD((const v4u*)(src + (i < 2 || s8 < 4 ? s8 + 8 * i : 0) * 8));
    }
    SCHED_BAR();
    STAMP(1);
  }
  {
    LAS float* msc = (LAS float*)lds;
    lds_wait(lds0, 7, gs);
    float sv[10];
#pragma unroll
    for (int i = 0; i < 10; ++i) sv[i] = msc[(tid >> 4) + 16 * i];
    float acc[8] = {};
#pragma unroll
    for (int i = 0; i < 10; ++i)
#pragma unroll
      for (int j = 0; j < 4; ++j) { acc[2 * j] += sv[i] * bflo(mw[i][j]); acc[2 * j + 1] += sv[i] * bfhi(mw[i][j]); }
    treduce_xor<4>(acc, (lane & 32) != 0, 32);
    treduce_xor<2>(acc, (lane & 16) != 0, 16);
    LAS float* red = msc + 256;
    *(LAS f32x2*)(red + wave * 128 + (lane & 15) * 8 + (lane >> 4) * 2) = f32x2{acc[0], acc[1]};
    group_sync(lds0, 7, gs += 4, lane);
    if (tid < 128 && u < 120) put_granule(ws + WS_MOD + (size_t)(mpart * 3840 + mcol + tid) * 8, f2u((red[tid] + red[128 + tid]) + (red[256 + tid] + red[384 + tid])), tag);
  }
  __builtin_amdgcn_s_setprio(AUXB_PRIO);
  if (u < 108) {
    const int cbk = u >> 2, part = u & 3;
    const int c0 = 64 * cbk, r0 = 160 * part, seg = tid & 7, rg = tid >> 3;
    constexpr int OP = 400, WPITCH = 144;
    LAS char* oimg = lds + 32768;
    LAS char* img = lds + 4096;
#pragma unroll
    for (int i = 0; i < 5; ++i) *(LAS v4u*)(img + (rg + 32 * i) * WPITCH + seg * 16) = wb[i];
#pragma unroll
    for (int i = 0; i < 3; ++i) *(LAS v4u*)(oimg + rg * OP + (seg + 8 * i) * 16) = ov[i];
    group_sync(lds0, 7, gs += 4, lane);
    const int q = lane >> 4, rr = (lane & 15) >> 2, p = lane & 3;
    v4u wf[5];
    {
      const int colb = (32 * (wave >> 1) + 8 * p + 4 * (wave & 1)) * 2;
#pragma unroll
      for (int i = 0; i < 5; ++i) {
        const int a0 = (32 * i + 8 * q + rr) * WPITCH + colb;
        const v2u lo = trread(img + a0), hi = trread(img + a0 + 4 * WPITCH);
        wf[i] = v4u{lo.x, lo.y, hi.x, hi.y};
      }
      const int a0 = (8 * q + rr) * OP + p * 8 + wave * 32;
#pragma unroll
      for (int j = 0; j < 3; ++j) {
        const v2u lo = trread(oimg + a0 + j * 128), hi = trread(oimg + a0 + 4 * OP + j * 128);
        st128(rs, wave + 4 * j < 10 ? (unsigned)WS_WOUT + (((part * 10 + wave + 4 * j) * 27 + cbk) * 64 + lane) * 16 : 0x7ffffff0u, v4u{lo.x, lo.y, hi.x, hi.y});
      }
    }
    LAS float* tc = (LAS float*)(lds + 28672);
    LAS u16* ts = (LAS u16*)(lds + 29696);
    if (wave < 3) {
      unsigned mv[8];
      const int rw = r0 + (tid < 160 ? tid : 159);
      get_granules<8>(mv, tag, [&](int k) { return ws + WS_MOD + (size_t)((k & 3) * 3840 + (3 + (k >> 2)) * D + rw) * 8; });
      ts[tid] = (u16)pkbf((u2f(mv[0]) + u2f(mv[1])) + (u2f(mv[2]) + u2f(mv[3])), 0.f);
      tc[tid] = bfr(1.0f + bfr((u2f(mv[4]) + u2f(mv[5])) + (u2f(mv[6]) + u2f(mv[7]))));
    }
    group_sync(lds0, 7, gs += 4, lane);
    STAMP(2);
    f32x4 u1 = {0.f, 0.f, 0.f, 0.f}, u2 = {0.f, 0.f, 0.f, 0.f};
    const v4u ones = {0x3f803f80u, 0x3f803f80u, 0x3f803f80u, 0x3f803f80u};
#pragma unroll
    for (int i = 0; i < 5; ++i) {
      const f32x4 ca = *(LAS const f32x4*)(tc + 32 * i + 8 * q), cb = *(LAS const f32x4*)(tc + 32 * i + 8 * q + 4);
      const v4u w = wf[i];
      const v4u o = {pkbf(bflo(w.x) * ca[0], bfhi(w.x) * ca[1]), pkbf(bflo(w.y) * ca[2], bfhi(w.y) * ca[3]),
                     pkbf(bflo(w.z) * cb[0], bfhi(w.z) * cb[1]), pkbf(bflo(w.w) * cb[2], bfhi(w.w) * cb[3])};
      st128(rs, (unsigned)WS_WIN + (((((part * 5 + i) >> 1) * 108 + 4 * cbk + wave) * 2 + ((part * 5 + i) & 1)) * 64 + lane) * 16, o);
      u1 = mfma16(ones, o, u1);
      u2 = mfma16(*(LAS const v4u*)(ts + 32 * i + 8 * q), w, u2);
    }
    STAMP(3);
    st32(rs, lane < 32 ? (unsigned)WS_U + ((part * 2 + q) * F + c0 + wave * 16 + (lane & 15)) * 4 : 0x7ffffff0u, f2u(q ? u2[0] : u1[0]));
  }
  STAMP(8);
  lds_wait(lds0, 8, 1);
  if (wave_arrive(lds0, 2, 4, lane)) signal<E1_REP, 8>(ws + CTL_E1B, u & 7, lane);
}

__device__ __forceinline__ int g1_aoff(int row, int G, int ab, int p) {
  const int f = ((row >> 1) & 1) | (((row >> 3) & 1) << 1);
  return row * 128 + (((4 * G + 2 * ab + (p >> 1)) ^ (f << 1)) << 4) + (p & 1) * 8;
}

template <int NB>
__device__ __forceinline__ void loader_path(LAS char* lds, char* ws, __amdgpu_buffer_rsrc_t rs, const u16* __restrict__ wsp, int wg, int mt, int j, int sub0, int lw, int lane) {
  const unsigned lbase = (unsigned)(size_t)lds, vo = lane * 16;
  const bool CNT7 = j < 6;
  {
    unsigned avo[2];
#pragma unroll
    for (int h = 0; h < 2; ++h) {
      const int row = lane >> 3, pos = lane & 7, f = ((row >> 1) & 1) | (h << 1);
      avo[h] = (row + 8 * h) * (L * 2) + ((pos ^ (f << 1)) << 4) - h * 1024;
    }
    const char* asb = ws + WS_X2T + (size_t)(16 * lw) * (L * 2) + mt * 128;
    const int p0 = (NB == 4 || CNT7) ? (lw < 2 ? 4 * lw : 2 + 3 * lw) : 3 * lw;
    const char* bsb = ws + WS_WIN + (size_t)(sub0 * 2 + p0) * 1024;
    const unsigned bb = lbase + G1_BOFF;
    auto issueB = [&](int s, unsigned slot) {
      gldsL<NB>(bsb + (s < 10 ? 108 * 1024 + s * 216 * 1024 : (s - 10) * 216 * 1024), vo, slot + p0 * 1024);
    };
    auto issueA = [&](int s0, int s1) {
#pragma unroll 1
      for (int s = s0; s < s1; ++s) gldsN<2, true>(asb + (size_t)(s * 64) * (L * 2), avo[0], avo[1], 0, 0, lbase + s * 8192 + lw * 2048);
    };
    {
      const unsigned vb = (unsigned)WS_V + (mt * 4 + lw) * 27 * 1024 + lane * 16;
#pragma unroll
      for (int k = 0; k < 3; ++k) st128(rs, vb + (3 * j + k) * 1024, v4u{~0u, ~0u, ~0u, ~0u});
      if (j < 6) __builtin_amdgcn_raw_buffer_store_b64(v2u{~0u, ~0u}, rs, vb + (24 + (j >> 1)) * 1024 + (j & 1) * 8, 0, 16);
    }
    waitv<0>();
    group_sync(lds, 5, 24, lane);
    if (lw == 0) { wait_count<8>(ws + CTL_E1B, wg & (E1_REP - 1), 128, lane); lds_post(lds, 10, lane); }
    if (lw == 3) { wait_count<8>(ws + CTL_E1, wg & (E1_REP - 1), 256, lane); lds_post(lds, 11, lane); }
    lds_wait(lds, 10, 1);
    STAMP(13);
#pragma unroll
    for (int s = 0; s < 4; ++s) issueB(s, bb + (s + 1) * G1_BST);
    lds_wait(lds, 11, 1);
    lds_wait(lds, 1, 4);
    issueA(0, 4);
    STAMP(12);
    LBAR();
    LSTAMP(1); STAMP(14);
    issueB(4, bb);
    waitv<6 + NB>(); LBAR(); LSTAMP(2);
    waitv<4 + NB>(); LBAR(); issueB(5, bb + G1_BST);
    waitv<2 + 2 * NB>(); LBAR(); LSTAMP(3); issueA(4, 8); issueB(6, bb + 2 * G1_BST);
    waitv<8 + 3 * NB>(); LBAR(); issueB(7, bb + 3 * G1_BST);
    waitv<6 + 2 * NB>(); LBAR(); LSTAMP(4); issueB(8, bb + 4 * G1_BST);
    waitv<4 + 3 * NB>(); LBAR(); LSTAMP(5); issueA(8, 10); issueB(9, bb);
    waitv<4 + 3 * NB>(); LBAR(); issueB(10, bb + G1_BST);
    waitv<4 + 3 * NB>(); LBAR(); issueB(11, bb + 2 * G1_BST);
    waitv<2 + 3 * NB>(); LBAR(); LSTAMP(6); issueB(12, bb + 3 * G1_BST);
    int nxt = 4 * G1_BST;
#pragma unroll 1
    for (int i = 9; i <= 15; ++i) {
      waitv<NB * 3>(); LBAR();
      if (i >= 11) LSTAMP(i - 4);
      issueB(i + 4, bb + nxt);
      nxt += G1_BST; if (nxt == G1_NSLOT * G1_BST) nxt = 0;
    }
    waitv<NB * 3>(); LBAR(); LSTAMP(12);
    waitv<NB * 2>(); LBAR(); LSTAMP(13);
    waitv<NB * 1>(); LBAR();
    waitv<0>(); LBAR(); LSTAMP(14);
    LBAR();
  }
  const char* osb = ws + WS_WOUT + (size_t)j * 135 * 1024;
  if (lw >= 2) {
    unsigned wvo[4];
#pragma unroll
    for (int h = 0; h < 4; ++h) {
      const int i = 8 * (h & 1) + (lane >> 3);
      wvo[h] = (8 * (i >> 2) + (i & 3) + 4 * (h >> 1)) * (L * 2) + (((lane & 7) ^ (i >> 1)) << 4) - h * 1024;
    }
    const char* wsb = (const char*)wsp + (size_t)(mt * 64 + 32 * (lw & 1)) * (L * 2);
    auto issueW = [&](int s, unsigned slot) {
      gldsN<4, false>(wsb + s * 128, wvo[0], wvo[1], wvo[2], wvo[3], slot + (lw & 1) * 4096);
    };
#pragma unroll 1
    for (int s = 0; s < G2_NW; ++s) issueW(s, lbase + s * 8192);
    LBAR();
    waitv<32>(); LBAR();
    int wn = 0;
#pragma unroll 1
    for (int i = 1; i <= 23; ++i) { waitv<28>(); LBAR(); issueW(i + 8, lbase + wn); wn += 8192; if (wn == G2_NW * 8192) wn = 0; }
#pragma unroll 1
    for (int i = 24; i <= 32; ++i) {
      waitv<28>(); LBAR();
      gldsL<4>(osb + wn + (lw & 1) * 4096, vo, lbase + wn + (lw & 1) * 4096);
      wn += 8192; if (wn == G2_NW * 8192) wn = 0;
    }
  } else {
    constexpr int NZ = NB == 4 ? 7 : 6;
    const char* zsb = ws + WS_Z2 + (size_t)(sub0 * 2 + lw * NZ) * 1024;
    auto issueZ = [&](int s, unsigned slot) {
      gldsL<4>(zsb + s * 108 * 1024, vo, slot + lw * NZ * 1024);
      gldsL<NZ - 4>(zsb + s * 108 * 1024 + 4096, vo, slot + lw * NZ * 1024 + 4096);
    };
    const unsigned zb = lbase + G2_ZOFF;
    LBAR();
#pragma unroll 1
    for (int s = 0; s < G2_NZ; ++s) issueZ(s, zb + s * G2_ZST);
    waitv<NZ * 5>(); LBAR();
    int zn = 0;
#pragma unroll 1
    for (int i = 1; i <= 26; ++i) { waitv<NZ * 4>(); LBAR(); issueZ(i + 5, zb + zn); zn += G2_ZST; if (zn == G2_NZ * G2_ZST) zn = 0; }
    auto issueO = [&](int slot, int n) {
      const int o = G2_ZOFF + slot * G2_ZST + lw * n * 1024;
      if (n == 7) { gldsL<4>(osb + o, vo, lbase + o); gldsL<3>(osb + o + 4096, vo, lbase + o + 4096); }
      else gldsL<3>(osb + o, vo, lbase + o);
    };
    waitv<NZ * 4>(); LBAR(); issueO(2, 7);
    waitv<NZ * 3 + 7>(); LBAR(); issueO(3, 7);
    waitv<NZ * 2 + 14>(); LBAR(); issueO(4, 3);
    waitv<NZ + 17>(); LBAR(); if (lw == 0) gldsL<1>(osb + 134 * 1024, vo, lbase + 134 * 1024);
    waitv<17>(); LBAR(); issueO(0, 7);
    LBAR(); issueO(1, 7);
  }
  waitv<0>();
  LBAR();
}

template <int NSUB, bool STATS>
__device__ __forceinline__ void consumer_g12(LAS char* lds, char* ws, __amdgpu_buffer_rsrc_t rs, const u16* __restrict__ bsp, const u16* __restrict__ wsp, int wg, int mt, int j, int sub0, int cnt,
                                             int wave, int tid, int lane, v2u (&x2v)[3] STAMP_ARG) {
  constexpr int NS1 = NSUB > 0 ? NSUB : 1;
  const int G = wave >> 2, ng = G ? 3 - (wave & 3) : (wave & 3);
  const int q = lane >> 4, i16 = lane & 15, rr = i16 >> 2, p = lane & 3, n0 = 2 * ng;
  float bias[2];
#pragma unroll
  for (int m = 0; m < 2; ++m) bias[m] = bf2f(bsp[mt * 64 + 32 * G + 8 * (i16 >> 2) + 4 * m + (i16 & 3)]);
  STAMP(4);
  CBAR();
  v2u z1v[NS1][2];
  {
    const int aoff0 = g1_aoff(8 * q + rr, G, 0, p), aoff1 = g1_aoff(8 * q + rr, G, 1, p);
    const int boff = G1_BOFF + n0 * 2048 + lane * 16;
    struct Frags { v4u a[2][2]; v4u b[2][NS1]; };
    f32x4 acc2[NS1][2], acc1[NS1][2], asum[2], asq[2];
    int acur = 0, bcur = G1_BST;
    auto rd = [&](Frags& f) {
#pragma unroll
      for (int kk = 0; kk < 2; ++kk) {
        const v2u l0 = trread(lds + acur + aoff0 + kk * 4096), h0 = trread(lds + acur + aoff0 + kk * 4096 + 512);
        const v2u l1 = trread(lds + acur + aoff1 + kk * 4096), h1 = trread(lds + acur + aoff1 + kk * 4096 + 512);
        f.a[kk][0] = v4u{l0.x, l0.y, h0.x, h0.y}; f.a[kk][1] = v4u{l1.x, l1.y, h1.x, h1.y};
#pragma unroll
        for (int n = 0; n < NSUB; ++n) f.b[kk][n] = *(LAS const v4u*)(lds + bcur + boff + n * 2048 + kk * 1024);
      }
      acur += 8192; if (acur == 81920) acur = 0;
      bcur += G1_BST; if (bcur == G1_NSLOT * G1_BST) bcur = 0;
    };
    auto pin1 = [&]() {
#pragma unroll
      for (int n = 0; n < NSUB; ++n) asm volatile("" : "+v"(acc1[n][0]), "+v"(acc1[n][1]));
    };
#pragma unroll
    for (int ab = 0; ab < 2; ++ab) {
      asum[ab] = f32x4{0.f, 0.f, 0.f, 0.f}; asq[ab] = f32x4{0.f, 0.f, 0.f, 0.f};
#pragma unroll
      for (int n = 0; n < NS1; ++n) { acc2[n][ab] = f32x4{0.f, 0.f, 0.f, 0.f}; acc1[n][ab] = f32x4{0.f, 0.f, 0.f, 0.f}; }
    }
    const v4u ones = {0x3f803f80u, 0x3f803f80u, 0x3f803f80u, 0x3f803f80u};
    auto mm2 = [&](const Frags& f) {
#pragma unroll
      for (int kk = 0; kk < 2; ++kk)
#pragma unroll
        for (int ab = 0; ab < 2; ++ab) {
#pragma unroll
          for (int n = 0; n < NSUB; ++n) acc2[n][ab] = mfma16(f.a[ABL_KK][ab], f.b[ABL_KK][n], acc2[n][ab]);
          if (STATS) { asum[ab] = mfma16(f.a[kk][ab], ones, asum[ab]); asq[ab] = mfma16(f.a[kk][ab], f.a[kk][ab], asq[ab]); }
        }
      INTERLEAVE(6, 2);
    };
    auto mm1 = [&](const Frags& f) {
#pragma unroll
      for (int kk = 0; kk < 2; ++kk)
#pragma unroll
        for (int ab = 0; ab < 2; ++ab)
#pragma unroll
          for (int n = 0; n < NSUB; ++n) acc1[n][ab] = mfma16(f.b[kk][n], f.a[kk][ab], acc1[n][ab]);
      if (NSUB == 2) INTERLEAVE(6, 2); else INTERLEAVE(4, 3);
    };
    Frags f0, f1;
    CBAR(); rd(f0);
    SCHED_BAR();
    float up[4];
    {
      const int c = tid & 127, which = (tid >> 7) & 1, half = tid >> 8;
#pragma unroll
      for (int k = 0; k < 4; ++k) up[k] = ld32f(rs, c < cnt * 16 ? (unsigned)WS_U + ((k * 2 + which) * F + (sub0 + (half ? 0 : 54)) * 16 + c) * 4 : 0x7ffffff0u);
    }
    unsigned pv = 0;
    SCHED_BAR();
    STAMP(5);
    auto pin2 = [&]() {
#pragma unroll
      for (int n = 0; n < NSUB; ++n) asm volatile("" : "+v"(acc2[n][0]), "+v"(acc2[n][1]));
      asm volatile("" : "+v"(asum[0]), "+v"(asum[1]), "+v"(asq[0]), "+v"(asq[1]));
    };
    if (STATS) {
      char* cb = ws + CTL_E1 + (G + 1) * 65536;
      unsigned pc;
      poll_issue<8>(pc, cb, wg & (E1_REP - 1), lane);
      CBAR(); rd(f1); mm2(f0);
      if (G == 0) poll_finish<8>(pc, cb, wg & (E1_REP - 1), 256, lane);
      CBAR(); pin2(); rd(f0); mm2(f1);
      CBAR(); rd(f1); mm2(f0);
      poll_issue<8>(pc, cb, wg & (E1_REP - 1), lane);
      CBAR(); rd(f0); mm2(f1);
      if (G == 1) poll_finish<8>(pc, cb, wg & (E1_REP - 1), 128, lane);
      asm volatile("" :: "v"(pc));
      pin2();
    }
#pragma unroll 1
    for (int it = STATS ? 2 : 0; it < 5; ++it) {
      CBAR(); rd(f1); mm2(f0);
      CBAR(); rd(f0); mm2(f1);
    }
    ((LAS float*)(lds + G1_T_U))[tid] = (up[0] + up[1]) + (up[2] + up[3]);
    if (!STATS && ng < 2) pv = *(const unsigned*)((const char*)wsp + (size_t)(mt * 64 + (G * 2 + ng) * 16 + (lane >> 2)) * (L * 2) + j * 512 + (lane & 3) * 128);
    LAS f32x2* tab = (LAS f32x2*)(lds + G1_T_LN2);
    if (STATS) {
#pragma unroll
      for (int ab = 0; ab < 2; ++ab) {
        const float a0 = asum[ab][0], a1 = asum[ab][1], a2 = asum[ab][2], a3 = asum[ab][3];
        const float b0 = asq[ab][0], b1 = asq[ab][1], b2 = asq[ab][2], b3 = asq[ab][3];
        const float s1 = (p & 2) ? ((p & 1) ? a3 : a2) : ((p & 1) ? a1 : a0), s2 = (p & 2) ? ((p & 1) ? b3 : b2) : ((p & 1) ? b1 : b0);
        const float mean = s1 * (1.0f / D);
        float mb, rinv;
        ln_scalars(mean, __builtin_fmaxf(s2 * (1.0f / D) - mean * mean, 0.f), mb, rinv);
        if (rr == q) tab[(G * 2 + ab) * 16 + i16] = f32x2{rinv, rinv * mb};
      }
    }
    {
      const int nb = G ? 3 : 0;
#pragma unroll
      for (int n = 0; n < 3; ++n) {
        const int row = 80 * j + 16 * (nb + n < 5 ? nb + n : 4) + 4 * q + rr;
        x2v[n] = trread(lds + g1_aoff(row, (wave >> 1) & 1, wave & 1, p));
      }
    }
    STAMP(6);
    CBAR(); rd(f1); mm1(f0);
    float zq[NS1][8];
    {
      LAS const float* ut = (LAS const float*)(lds + G1_T_U);
      float v[16];
#pragma unroll
      for (int e = 0; e < 16; ++e) v[e] = 0.f;
      if (NSUB > 0) {
        f32x2 rv[8];
#pragma unroll
        for (int e = 0; e < 8; ++e) rv[e] = tab[(G * 2 + (e >> 2)) * 16 + 4 * q + (e & 3)];
#pragma unroll
        for (int n = 0; n < NSUB; ++n) {
          const float c1 = ut[(n0 + n) * 16 + i16], c2 = ut[128 + (n0 + n) * 16 + i16];
#pragma unroll
          for (int e = 0; e < 8; ++e) {
            const float z = gelu_f(__builtin_fmaf(rv[e].x, acc2[n][e >> 2][e & 3], __builtin_fmaf(-rv[e].y, c1, c2)));
            zq[n][e] = z;
            v[2 * e] = n ? v[2 * e] + z : z; v[2 * e + 1] = n ? __builtin_fmaf(z, z, v[2 * e + 1]) : z * z;
          }
        }
        treduce_dpp<8, 0x140>(v, (i16 & 8) != 0);
        treduce_dpp<4, 0x141>(v, (i16 & 4) != 0);
        treduce_dpp<2, 0x4e>(v, (i16 & 2) != 0);
        treduce_dpp<1, 0xb1>(v, (i16 & 1) != 0);
      }
      ((LAS float*)(lds + G1_T_P))[ng * 128 + G * 64 + lane] = v[0];
    }
    CBAR(); pin1();
    GAS unsigned long long* gr = (GAS unsigned long long*)(ws + CTL_ST3) + (size_t)mt * 1024 + G * 32;
    if (STATS) {
      LAS const float* pp = (LAS const float*)(lds + G1_T_P) + G * 64 + lane;
      const float sv = (pp[0] + pp[128]) + (pp[256] + pp[384]);
      __hip_atomic_store(gr + (j * 2 + (lane & 1)) * 64 + (lane >> 1), (1ull << 32) | f2u(sv), RLX_AGENT);
    }
    SCHED_BAR(); rd(f0); mm1(f1);
    CBAR(); pin1(); rd(f1); mm1(f0);
    CBAR(); pin1(); rd(f0); mm1(f1);
    unsigned long long gx[8];
    if (STATS) {
#pragma unroll
      for (int k = 0; k < 8; ++k) gx[k] = __hip_atomic_load(gr + (k * 2 + (lane >> 5)) * 64 + (lane & 31), RLX_AGENT);
    }
    CBAR(); pin1(); rd(f1); mm1(f0);
    if (STATS) {
      float sv;
      for (unsigned spin = 0;; ++spin) {
        bool ok = true;
        sv = 0.f;
#pragma unroll
        for (int k = 0; k < 8; ++k) { ok &= (gx[k] >> 32) == 1ull; sv += u2f((unsigned)gx[k]); }
        if (wall(ok) || spin > SPIN_CAP) break;
        __builtin_amdgcn_s_sleep(1);
#pragma unroll
        for (int k = 0; k < 8; ++k) gx[k] = __hip_atomic_load(gr + (k * 2 + (lane >> 5)) * 64 + (lane & 31), RLX_AGENT);
      }
      const float s2 = shx(sv, 32);
      const float mean = sv * (1.0f / FH);
      float mb, rinv;
      ln_scalars(mean, __builtin_fmaxf(s2 * (1.0f / FH) - mean * mean, 0.f), mb, rinv);
      if (lane < 32) ((LAS f32x2*)(lds + G1_T_Z))[32 * G + lane] = f32x2{-mb * rinv, rinv};
    }
    CBAR(); pin1();
    if (NSUB > 0) {
      f32x2 zs[8];
#pragma unroll
      for (int e = 0; e < 8; ++e) zs[e] = ((LAS const f32x2*)(lds + G1_T_Z))[32 * G + 8 * q + e];
#pragma unroll
      for (int n = 0; n < NSUB; ++n) {
        v4u o;
#pragma unroll
        for (int e = 0; e < 4; ++e)
          o[e] = pkbf(__builtin_fmaf(zq[n][2 * e], zs[2 * e].y, zs[2 * e].x), __builtin_fmaf(zq[n][2 * e + 1], zs[2 * e + 1].y, zs[2 * e + 1].x));
        st128(rs, (unsigned)WS_Z2 + (((mt * 54 + sub0 + n0 + n) * 2 + G) * 64 + lane) * 16, o);
      }
    }
    SCHED_BAR(); rd(f0); mm1(f1);
    CBAR(); pin1(); rd(f1); mm1(f0);
    CBAR(); pin1(); rd(f0); mm1(f1);
    if (wave_arrive(lds, 3, 8, lane)) signal<4, 4>(ws + CTL_E2 + j * 4096, mt & 3, lane);
    CBAR(); pin1(); rd(f1); mm1(f0);
    asm volatile("" :: "v"(pv));
    CBAR(); pin1();
    mm1(f1);
    STAMP(7);
    {
      LAS const float* ut = (LAS const float*)(lds + G1_T_U) + 256;
      f32x2 rv[2];
#pragma unroll
      for (int ab = 0; ab < 2; ++ab) rv[ab] = tab[(G * 2 + ab) * 16 + i16];
#pragma unroll
      for (int n = 0; n < NSUB; ++n) {
        const f32x4 u1 = *(LAS const f32x4*)(ut + (n0 + n) * 16 + 4 * q), u2 = *(LAS const f32x4*)(ut + 128 + (n0 + n) * 16 + 4 * q);
#pragma unroll
        for (int ab = 0; ab < 2; ++ab) {
          float z[4];
#pragma unroll
          for (int e = 0; e < 4; ++e) z[e] = gelu_f(__builtin_fmaf(rv[ab].x, acc1[n][ab][e], __builtin_fmaf(-rv[ab].y, u1[e], u2[e])));
          z1v[n][ab] = v2u{pkbf(z[0], z[1]), pkbf(z[2], z[3])};
          asm volatile("" : "+v"(z1v[n][ab]));
        }
      }
    }
  }
  if (wave == 1 || wave == 2) {
    const int c = tid - 64;
    if (c < 80) {
      const unsigned o = (unsigned)WS_MOD + (5 * D + 80 * j + c) * 8;
      ((LAS float*)(lds + G3_GB))[c] = bfr((ld32f(rs, o) + ld32f(rs, o + 30720)) + (ld32f(rs, o + 61440) + ld32f(rs, o + 92160)));
    }
  }
  if (wave == 3) wait_count<4>(ws + CTL_E2 + j * 4096, (mt >> 2) & 3, 32, lane);
  CBAR();
  STAMP(9);
  {
    f32x4 acc[NS1][2];
#pragma unroll
    for (int n = 0; n < NS1; ++n) { acc[n][0] = f32x4{0.f, 0.f, 0.f, 0.f}; acc[n][1] = f32x4{0.f, 0.f, 0.f, 0.f}; }
    int woff[2];
#pragma unroll
    for (int kk = 0; kk < 2; ++kk) woff[kk] = G * 4096 + i16 * 128 + (((4 * kk + q) ^ (i16 >> 1)) << 4);
    const int zoff = G2_ZOFF + n0 * 2048 + lane * 16;
    struct Frags { v4u w[2][2]; v4u z[2][NS1]; };
    int wc = 0, zc = 0;
    auto rd = [&](Frags& f) {
      if (NSUB > 0) {
#pragma unroll
        for (int kk = 0; kk < 2; ++kk) {
#pragma unroll
          for (int m = 0; m < 2; ++m) f.w[kk][m] = *(LAS const v4u*)(lds + wc + woff[kk] + m * 2048);
#pragma unroll
          for (int n = 0; n < NSUB; ++n) f.z[kk][n] = *(LAS const v4u*)(lds + zc + zoff + n * 2048 + kk * 1024);
        }
        wc += 8192; if (wc == G2_NW * 8192) wc = 0;
        zc += G2_ZST; if (zc == G2_NZ * G2_ZST) zc = 0;
      }
    };
    auto mm = [&](const Frags& f) {
#pragma unroll
      for (int kk = 0; kk < 2; ++kk)
#pragma unroll
        for (int n = 0; n < NSUB; ++n)
#pragma unroll
          for (int m = 0; m < 2; ++m) acc[n][m] = mfma16(f.z[ABL_KK][n], f.w[ABL_KK][m], acc[n][m]);
      INTERLEAVE(4, 2);
    };
    Frags f0, f1;
    CBAR(); rd(f0);
    STAMP(10);
#pragma unroll 1
    for (int it = 0; it < 15; ++it) {
      CBAR(); rd(f1); mm(f0);
      CBAR(); rd(f0); mm(f1);
    }
    CBAR(); rd(f1); mm(f0);
    mm(f1);
    STAMP(11);
#pragma unroll
    for (int m = 0; m < 2; ++m) {
      v2u o[NS1];
#pragma unroll
      for (int n = 0; n < NSUB; ++n) {
        float g[4];
#pragma unroll
        for (int e = 0; e < 4; ++e) g[e] = acc[n][m][e] + bias[m];
        const v2u zz = z1v[n][m];
        o[n] = v2u{pkbf(bflo(zz.x) * g[0], bfhi(zz.x) * g[1]), pkbf(bflo(zz.y) * g[2], bfhi(zz.y) * g[3])};
      }
      const unsigned vb = (unsigned)WS_V + (mt * 4 + G * 2 + m) * 27 * 1024 + lane * 16;
      if (NSUB == 2) st128(rs, vb + (3 * j + ng) * 1024, v4u{o[0].x, o[0].y, o[NS1 - 1].x, o[NS1 - 1].y});
      if (NSUB == 1) __builtin_amdgcn_raw_buffer_store_b64(o[0], rs, vb + (24 + (j >> 1)) * 1024 + (j & 1) * 8, 0, 16);
    }
    CBAR();
  }
}

template <int KH>
__device__ __forceinline__ void consumer_g3(LAS char* lds, char* ws, __amdgpu_buffer_rsrc_t rs, u16* __restrict__ out, int mt, int j, int m, int lane, const v2u (&x2v)[3] STAMP_ARG) {
  constexpr int K0 = KH ? 14 : 0, NK = KH ? 13 : 14, NF = KH ? 2 : 3, N0 = KH ? 3 : 0;
  const int q = lane >> 4, i16 = lane & 15;
  v4u vf[NK];
  __builtin_amdgcn_s_sleep(V_DELAY);
  CBAR();
  STAMP(12);
  const unsigned vo = (unsigned)WS_V + ((mt * 4 + m) * 27 + K0) * 1024 + lane * 16;
  constexpr int VW = V_WIN;
#pragma unroll
  for (int k = 0; k < VW; ++k) { vf[k] = ld128(rs, vo + k * 1024); SCHED_BAR(); }
  f32x4 acc[5];
#pragma unroll
  for (int n = 0; n < 5; ++n) acc[n] = f32x4{0.f, 0.f, 0.f, 0.f};
  LAS const char* wp = lds + K0 * 1024 + lane * 16;
  LAS const char* wp2 = wp + 54 * 1024;
  LAS const char* wp4 = wp + 108 * 1024;
  v4u wf[3][5];
  auto rdw = [&](int k, v4u (&w)[5]) {
    w[0] = *(LAS const v4u*)(wp + k * 1024); w[1] = *(LAS const v4u*)(wp + (27 + k) * 1024);
    w[2] = *(LAS const v4u*)(wp2 + k * 1024); w[3] = *(LAS const v4u*)(wp2 + (27 + k) * 1024);
    w[4] = *(LAS const v4u*)(wp4 + k * 1024);
  };
  rdw(0, wf[0]); rdw(1, wf[1]);
#pragma unroll
  for (int k = 0; k < NK; ++k) {
    if (k + VW < NK) vf[k + VW] = ld128(rs, vo + (k + VW) * 1024);
    if (k + 2 < NK) rdw(k + 2, wf[(k + 2) % 3]);
    SCHED_BAR();
    if (__builtin_expect(!wall(__builtin_elementwise_max(vf[k].x, vf[k].z) != ~0u), 0)) {
      unsigned spin = 0;
      do { asm volatile("" ::: "memory"); vf[k] = ld128(rs, vo + k * 1024); }
      while (!wall(__builtin_elementwise_max(vf[k].x, vf[k].z) != ~0u) && ++spin < SPIN_CAP);
    }
#pragma unroll
    for (int n = 0; n < 5; ++n) acc[n] = mfma16(wf[k % 3][n], vf[k], acc[n]);
    asm volatile("" : "+v"(acc[0]), "+v"(acc[1]), "+v"(acc[2]), "+v"(acc[3]), "+v"(acc[4]));
    SCHED_BAR();
  }
  STAMP(13);
  f32x4 gb[NF];
#pragma unroll
  for (int n = 0; n < NF; ++n) gb[n] = *(LAS const f32x4*)(lds + G3_GB + (16 * (N0 + n) + 4 * q) * 4);
  LAS char* red = lds + G3_RED + m * 5120 + lane * 16;
#pragma unroll
  for (int n = 0; n < 5; ++n) if ((n >= 3) != (KH != 0)) *(LAS f32x4*)(red + n * 1024) = acc[n];
  CBAR();
  const int tl = 32 * (m >> 1) + 8 * (i16 >> 2) + 4 * (m & 1) + (i16 & 3);
#pragma unroll
  for (int n = 0; n < NF; ++n) {
    const f32x4 o = *(LAS const f32x4*)(red + (N0 + n) * 1024);
    const f32x4 a = KH ? o + acc[N0 + n] : acc[N0 + n] + o;
    const float o0 = __builtin_fmaf(gb[n][0], a[0], bflo(x2v[n].x)), o1 = __builtin_fmaf(gb[n][1], a[1], bfhi(x2v[n].x));
    const float o2 = __builtin_fmaf(gb[n][2], a[2], bflo(x2v[n].y)), o3 = __builtin_fmaf(gb[n][3], a[3], bfhi(x2v[n].y));
    *(LAS v2u*)(lds + tl * 176 + 32 * (N0 + n) + 8 * q) = v2u{pkbf(o0, o1), pkbf(o2, o3)};
  }
  CBAR();
  {
    const int w8 = KH * 4 + m;
#pragma unroll
    for (int i = 0; i < 2; ++i) {
      const int c = lane + 64 * i, r = w8 * 8 + c / 10, pc = c % 10;
      if (c < 80) *(v4u*)((char*)out + ((size_t)(mt * 64 + r) * D + 80 * j) * 2 + pc * 16) = *(LAS const v4u*)(lds + r * 176 + pc * 16);
    }
  }
}

__global__ __launch_bounds__(768, 3) void k_all(Args A) {
  extern __shared__ __attribute__((aligned(16))) char smem[];
  unsigned long long pc;
  asm volatile("s_getpc_b64 %0" : "=s"(pc));
  LAS char* lds = (LAS char*)smem;
  char* ws = A.ws;
  const int tid = __builtin_amdgcn_workitem_id_x(), lane = tid & 63, wave = __builtin_amdgcn_readfirstlane(tid >> 6), wg = __builtin_amdgcn_workgroup_id_x();
  STAMP_DECL; STAMP(0);
  const __amdgpu_buffer_rsrc_t rs = __builtin_amdgcn_make_buffer_rsrc((void*)ws, 0, (int)WS_END, 0x00020000);
  asm volatile("" :: "s"(A.x), "s"(A.Are), "s"(A.Aim), "s"(A.Bre), "s"(A.Bim), "s"(A.Cre), "s"(A.Cim), "s"(A.logdt), "s"(A.dskip), "s"(A.win), "s"(A.wsp),
               "s"(A.bsp), "s"(A.wout), "s"(A.cvec), "s"(A.wmod), "s"(A.ws), "s"(A.out));
  const int grp = wave >> 2, bt = grp == 2 ? 0 : grp + 1, gw = wave & 3, gt = tid & 255;
  const bool isaux = bt == 2 && wg >= 128;
  const int u = wg - 128, d = 256 * bt + (wg & 7) * (bt < 2 ? 32 : 16) + (wg >> 3);
  unsigned epoch;
  asm volatile("global_load_dword %0, %1, %2" : "=v"(epoch) : "v"((unsigned)CTL_EPOCH), "s"(ws) : "memory");
  unsigned pre[7] = {};
  if (__builtin_expect(!isaux, 1)) {
    const unsigned o = (d * NS + (gw & 1) * 64 + lane) * 2;
#define LDU16(dst, base, voff) asm volatile("global_load_ushort %0, %1, %2" NT : "=v"(dst) : "v"(voff), "s"(base) : "memory")
    LDU16(pre[0], A.Are, o); LDU16(pre[1], A.Aim, o); LDU16(pre[2], A.logdt, d * 2); LDU16(pre[3], A.Bre, o);
    LDU16(pre[4], A.Bim, o); LDU16(pre[5], A.Cre, o); LDU16(pre[6], A.Cim, o);
  }
  STAMP(11);
  if (tid < 16) ((LAS unsigned*)(lds + M_CTL))[tid] = 0u;
  unsigned tag = 0;
  CBAR();
  {
    LAS char* gl = lds + (grp == 2 ? G1_TAB - C_GRP : grp * C_GRP);
    if (bt == 0) __builtin_amdgcn_s_setprio(2);
    if (bt == 1) __builtin_amdgcn_s_setprio(1);
    if (isaux) { __builtin_amdgcn_s_setprio(3); aux_group(gl, lds, A, rs, epoch, u, gt, gw, lane STAMP_PASS); }
    else conv_group(gl, lds, bt, A, rs, epoch, tag, pre, pc, wg, d, gt, gw, lane STAMP_PASS);
    __builtin_amdgcn_s_setprio(0);
  }
  const int xcd = wg & 7, idx = wg >> 3;
  const int mt = (xcd >> 1) * 8 + (idx & 7), j = (xcd & 1) * 4 + (idx >> 3);
  const int cnt = j < 6 ? 7 : 6, sub0 = j < 6 ? 7 * j : 42 + 6 * (j - 6);
  if (wave >= 8) {
    STAMP(4);
    const int lw = wave - 8;
    if (lw + 12 < 2 * cnt) loader_path<4>(lds, ws, rs, A.wsp, wg, mt, j, sub0, lw, lane);
    else loader_path<3>(lds, ws, rs, A.wsp, wg, mt, j, sub0, lw, lane);
    if (wg == 0 && tid == 512) *(volatile unsigned*)(ws + CTL_EPOCH) = tag;
    STAMP_FLUSH(1);
    return;
  }
  v2u x2v[3];
  const int ng = wave >> 2 ? 3 - (wave & 3) : (wave & 3);
  if (ng < 3) consumer_g12<2, false>(lds, ws, rs, A.bsp, A.wsp, wg, mt, j, sub0, cnt, wave, tid, lane, x2v STAMP_PASS);
  else if (cnt == 7) consumer_g12<1, true>(lds, ws, rs, A.bsp, A.wsp, wg, mt, j, sub0, cnt, wave, tid, lane, x2v STAMP_PASS);
  else consumer_g12<0, true>(lds, ws, rs, A.bsp, A.wsp, wg, mt, j, sub0, cnt, wave, tid, lane, x2v STAMP_PASS);
  if (wave < 4) consumer_g3<0>(lds, ws, rs, A.out, mt, j, wave & 3, lane, x2v STAMP_PASS);
  else consumer_g3<1>(lds, ws, rs, A.out, mt, j, wave & 3, lane, x2v STAMP_PASS);
  STAMP(14);
  STAMP_FLUSH(1);
}

#if defined(__HIP_DEVICE_COMPILE__) && !defined(KEEP_PK)
#pragma clang attribute pop
#endif

extern "C" void kernel_launch(void* const* d_in, const int* in_sizes, int n_in, void* d_out, int out_size, void* d_ws, size_t ws_size,
                              hipStream_t stream) {
  Args A;
  A.x = (const u16*)d_in[0]; A.Are = (const u16*)d_in[1]; A.Aim = (const u16*)d_in[2]; A.Bre = (const u16*)d_in[3]; A.Bim = (const u16*)d_in[4];
  A.Cre = (const u16*)d_in[5]; A.Cim = (const u16*)d_in[6]; A.logdt = (const u16*)d_in[7]; A.dskip = (const u16*)d_in[8];
  A.win = (const u16*)d_in[10]; A.wsp = (const u16*)d_in[11]; A.bsp = (const u16*)d_in[12]; A.wout = (const u16*)d_in[13];
  A.cvec = (const u16*)d_in[14]; A.wmod = (const u16*)d_in[15];
  A.ws = (char*)d_ws; A.out = (u16*)d_out;
  (void)hipFuncSetAttribute((const void*)k_all, hipFuncAttributeMaxDynamicSharedMemorySize, M_LDS);
  k_all<<<256, 768, M_LDS, stream>>>(A);
}
```

```cpp
#define MODE 0
#include <hip/hip_runtime.h>
#include <hip/hip_bf16.h>
#include <stdint.h>

#define NT ""
#define NTLOAD(p) (*(p))
#define GAS __attribute__((address_space(1)))
#define LAS __attribute__((address_space(3)))
typedef unsigned v4u __attribute__((ext_vector_type(4)));
typedef unsigned v2u __attribute__((ext_vector_type(2)));
typedef float f32x2 __attribute__((ext_vector_type(2)));
typedef float f32x4 __attribute__((ext_vector_type(4)));
typedef float f32x16 __attribute__((ext_vector_type(16)));
typedef __bf16 bf16x2 __attribute__((ext_vector_type(2)));
typedef __bf16 bf16x8 __attribute__((ext_vector_type(8)));
typedef _Float16 f16x2 __attribute__((ext_vector_type(2)));
typedef _Float16 f16x8 __attribute__((ext_vector_type(8)));
typedef short s16x4 __attribute__((ext_vector_type(4)));
typedef unsigned short u16;

#if defined(__HIP_DEVICE_COMPILE__) && !defined(KEEP_PK)
#pragma clang attribute push(__attribute__((target("no-packed-fp32-ops"))), apply_to = function)
#endif
__device__ __forceinline__ float u2f(unsigned u) { return __builtin_bit_cast(float, u); }
__device__ __forceinline__ unsigned f2u(float f) { return __builtin_bit_cast(unsigned, f); }
__device__ __forceinline__ int lane_id() { return __builtin_amdgcn_mbcnt_hi(~0u, __builtin_amdgcn_mbcnt_lo(~0u, 0u)); }
__device__ __forceinline__ unsigned shx(unsigned v, int m) { return __builtin_amdgcn_ds_bpermute((lane_id() ^ m) << 2, v); }
__device__ __forceinline__ float shx(float v, int m) { return u2f(shx(f2u(v), m)); }
__device__ __forceinline__ bool wall(bool p) { return __builtin_amdgcn_ballot_w64(!p) == 0ull; }
__device__ __forceinline__ float exp_acc(float x) {
  const float hi = x * 1.4426950408889634f;
  const float lo = __builtin_fmaf(x, 1.4426950408889634f, -hi) + x * 1.925963033500414e-8f;
  const float e = __builtin_amdgcn_exp2f(hi);
  return __builtin_fmaf(e, lo * 0.6931471805599453f, e);
}

constexpr int L = 2048, D = 640, NS = 128, F = 1728, FH = 864;

constexpr size_t CTL_E1 = 0;
constexpr size_t CTL_E1B = 196608;
constexpr size_t CTL_E2 = 262144;
constexpr size_t CTL_E3 = 294912;
constexpr size_t CTL_EW = 303104;
constexpr size_t CTL_ST3 = 327680;
constexpr size_t CTL_F0 = CTL_ST3 + 262144;
constexpr size_t CTL_EPOCH = CTL_F0 + 32768;
constexpr size_t WS_MOD = CTL_EPOCH + 4096;
constexpr size_t WS_ST1 = WS_MOD + 131072;
constexpr size_t WS_U = WS_ST1 + 16384;
constexpr size_t WS_XT = WS_U + 65536;
constexpr size_t WS_X2T = WS_XT + (size_t)D * L * 2;
constexpr size_t WS_WIN = WS_X2T + (size_t)D * L * 2;
constexpr size_t WS_WOUT = WS_WIN + (size_t)D * F * 2;
constexpr size_t WS_Z2 = WS_WOUT + (size_t)FH * D * 2;
constexpr size_t WS_V = WS_Z2 + (size_t)L * FH * 2;
constexpr size_t WS_END = WS_V + (size_t)L * FH * 2;

__device__ __forceinline__ float bflo(unsigned u) { return u2f(u << 16); }
__device__ __forceinline__ float bfhi(unsigned u) { return u2f(u & 0xffff0000u); }
__device__ __forceinline__ float bf2f(u16 v) { return u2f((unsigned)v << 16); }
__device__ __forceinline__ unsigned pkbf(float a, float b) {
  f32x2 v = {a, b};
  return __builtin_bit_cast(unsigned, __builtin_convertvector(v, bf16x2));
}
__device__ __forceinline__ unsigned pkh(float a, float b) {
  f32x2 v = {a, b};
  return __builtin_bit_cast(unsigned, __builtin_convertvector(v, f16x2));
}
__device__ __forceinline__ float bfr(float a) { return bflo(pkbf(a, a)); }
__device__ __forceinline__ v2u trread(LAS const char* p) {
  return __builtin_bit_cast(v2u, __builtin_amdgcn_ds_read_tr16_b64_v4i16((LAS s16x4*)p));
}
__device__ __forceinline__ f32x4 mfma16(v4u a, v4u b, f32x4 c) {
  return __builtin_amdgcn_mfma_f32_16x16x32_bf16(__builtin_bit_cast(bf16x8, a), __builtin_bit_cast(bf16x8, b), c, 0, 0, 0);
}
__device__ __forceinline__ f32x16 mfma32(v4u a, v4u b, f32x16 c) {
  return __builtin_amdgcn_mfma_f32_32x32x16_bf16(__builtin_bit_cast(bf16x8, a), __builtin_bit_cast(bf16x8, b), c, 0, 0, 0);
}
__device__ __forceinline__ f32x16 mfma32h(v4u a, v4u b, f32x16 c) {
  return __builtin_amdgcn_mfma_f32_32x32x16_f16(__builtin_bit_cast(f16x8, a), __builtin_bit_cast(f16x8, b), c, 0, 0, 0);
}
template <bool DEV>
__device__ __forceinline__ void glds16(const void* sbase, unsigned voff, unsigned dst) {
  unsigned keep;
  if (DEV) asm volatile(
"s_mov_b32 %0, m0\n\ts_mov_b32 m0, %3\n\ts_nop 0\n\tglobal_load_lds_dwordx4 %1, %2 sc1\n\ts_mov_b32 m0, %0"
                        : "=&s"(keep) : "v"(voff), "s"(sbase), "s"(dst) : "memory");
  else asm volatile("s_mov_b32 %0, m0\n\ts_mov_b32 m0, %3\n\ts_nop 0\n\tglobal_load_lds_dwordx4 %1, %2\n\ts_mov_b32 m0, %0"
                    : "=&s"(keep) : "v"(voff), "s"(sbase), "s"(dst) : "memory");
}
__device__ __forceinline__ void glds16nt(const void* sbase, unsigned voff, unsigned dst) {
  unsigned keep;
  asm volatile("s_mov_b32 %0, m0\n\ts_mov_b32 m0, %3\n\ts_nop 0\n\tglobal_load_lds_dwordx4 %1, %2" NT "\n\ts_mov_b32 m0, %0"
               : "=&s"(keep) : "v"(voff), "s"(sbase), "s"(dst) : "memory");
}
#define GL_(i, s) "global_load_lds_dwordx4 %" #i ", %5 offset:" s
template <int N, bool DEV>
__device__ __forceinline__ void gldsN(const void* sbase, unsigned v0, unsigned v1, unsigned v2, unsigned v3, unsigned dst) {
  unsigned keep;
#define GLN_(body) asm volatile("s_mov_b32 %0, m0\n\ts_mov_b32 m0, %6\n\ts_nop 0\n\t" body "s_mov_b32 m0, %0" : "=&s"(keep) : "v"(v0), "v"(v1), "v"(v2), "v"(v3), "s"(sbase), "s"(dst) : "memory")
  if (DEV) {
    if (N == 1) GLN_(GL_(1, "0 sc1\n\t"));
    if (N == 2) GLN_(GL_(1, "0 sc1\n\t") GL_(2, "1024 sc1\n\t"));
    if (N == 3) GLN_(GL_(1, "0 sc1\n\t") GL_(2, "1024 sc1\n\t") GL_(3, "2048 sc1\n\t"));
    if (N == 4) GLN_(GL_(1, "0 sc1\n\t") GL_(2, "1024 sc1\n\t") GL_(3, "2048 sc1\n\t") GL_(4, "3072 sc1\n\t"));
  } else {
    if (N == 2) GLN_(GL_(1, "0\n\t") GL_(2, "1024\n\t"));
    if (N == 4) GLN_(GL_(1, "0\n\t") GL_(2, "1024\n\t") GL_(3, "2048\n\t") GL_(4, "3072\n\t"));
  }
}
template <int N> __device__ __forceinline__ void gldsL(const void* sbase, unsigned vo, unsigned dst) { gldsN<N, true>(sbase, vo, vo, vo, vo, dst); }
template <int N> __device__ __forceinline__ void waitv() { asm volatile("s_waitcnt vmcnt(%0)" :: "i"(N) : "memory"); }
__device__ __forceinline__ float dot2bf(unsigned a, unsigned b, float c) {
  return __builtin_amdgcn_fdot2_f32_bf16(__builtin_bit_cast(bf16x2, a), __builtin_bit_cast(bf16x2, b), c, false);
}
#define SCHED_BAR() __builtin_amdgcn_sched_barrier(0)
#define STAMP_DECL
#define STAMP(i) do {} while (0)
#define STAMP_FLUSH(kid) do {} while (0)
#define STAMP_ARG
#define STAMP_PASS
#define CBAR() do { SCHED_BAR(); __builtin_amdgcn_s_waitcnt(0xC07F); asm volatile("" ::: "memory"); __builtin_amdgcn_s_barrier(); asm volatile("" ::: "memory"); } while (0)
#define LBAR() __builtin_amdgcn_s_barrier()

template <int CTRL> __device__ __forceinline__ float dppf(float v) {
  return __builtin_bit_cast(float, __builtin_amdgcn_update_dpp(0, __builtin_bit_cast(int, v), CTRL, 0xf, 0xf, true));
}
template <int N, int CTRL> __device__ __forceinline__ void treduce_dpp(float* v, bool up) {
#pragma unroll
  for (int e = 0; e < N; ++e) {
    const float a = v[e], b = v[e + N];
    const float send = up ? a : b, keep = up ? b : a;
    v[e] = keep + dppf<CTRL>(send);
  }
}
template <int N> __device__ __forceinline__ void treduce_xor(float* v, bool up, int mask) {
#pragma unroll
  for (int e = 0; e < N; ++e) {
    const float a = v[e], b = v[e + N];
    const float send = up ? a : b, keep = up ? b : a;
    v[e] = keep + shx(send, mask);
  }
}

__device__ __forceinline__ float ln_std(float var) { return __builtin_amdgcn_sqrtf(bfr(bfr(var) + 1e-5f)); }
__device__ __forceinline__ void ln_scalars(float mean, float var, float& m_b, float& rinv) {
  m_b = bfr(mean);
  rinv = __builtin_amdgcn_rcpf(bfr(ln_std(var)));
}

struct cplx { float r, i; };
__device__ __forceinline__ cplx cmul(cplx a, cplx b) { return {a.r * b.r - a.i * b.i, a.r * b.i + a.i * b.r}; }

typedef GAS unsigned gu32;
#define RLX_AGENT __ATOMIC_RELAXED, __HIP_MEMORY_SCOPE_AGENT
#define F0_REP 8
#define V_DELAY 24
#define V_WIN 5
#define LSTAMP(i) do {} while (0)
#define POLL_SLEEP 6
#define E1_REP 32
#define INTERLEAVE(n, r) do { _Pragma("unroll") for (int i_ = 0; i_ < (n); ++i_) { __builtin_amdgcn_sched_group_barrier(0x008, 1, 0); __builtin_amdgcn_sched_group_barrier(0x100, (r), 0); } \
    __builtin_amdgcn_sched_group_barrier(0x008, 8, 0); SCHED_BAR(); } while (0)
#define ABL_KK kk
#define AUXB_PRIO 2
#define XGATE 16
#define PV_POS 0
constexpr unsigned SPIN_CAP = 1u << 16;
constexpr int M_LDS = 163840, M_CTL = M_LDS - 64;
constexpr int CODE_LINES = 400;
constexpr int C_KOFF = 0, C_UOFF = 4224, C_UP = 1536, C_COFF = C_UOFF + 4 * C_UP, C_GRP = C_COFF + 4 * 64 * 144;
constexpr int G1_BOFF = 81920, G1_BST = 14336, G1_NSLOT = 5, G1_TAB = G1_BOFF + G1_BST * G1_NSLOT;
constexpr int G1_T_LN2 = G1_TAB, G1_T_U = G1_T_LN2 + 512, G1_T_P = G1_T_U + 2048, G1_T_Z = G1_T_P + 2048;
constexpr int G2_NW = 9, G2_NZ = 6, G2_ZOFF = G2_NW * 8192, G2_ZST = 14336;
constexpr int G3_RED = 135 * 1024, G3_GB = 161024;
static_assert(3 * C_GRP <= M_CTL && G1_T_Z + 512 <= M_CTL && G2_ZOFF + G2_NZ * G2_ZST <= M_CTL && G3_GB + 320 <= M_CTL, "lds");

__device__ __forceinline__ void st128(__amdgpu_buffer_rsrc_t rs, unsigned off, v4u v) { __builtin_amdgcn_raw_buffer_store_b128(v, rs, off, 0, 16); }
__device__ __forceinline__ v4u ld128(__amdgpu_buffer_rsrc_t rs, unsigned off) { return __builtin_amdgcn_raw_buffer_load_b128(rs, off, 0, 16); }

__device__ __forceinline__ bool wave_arrive(LAS char* lds, int word, unsigned n, int lane) {
  waitv<0>();
  unsigned old = 0;
  if (lane == 0) old = __hip_atomic_fetch_add((LAS unsigned*)(lds + M_CTL) + word, 1u, __ATOMIC_RELAXED, __HIP_MEMORY_SCOPE_WORKGROUP);
  return (unsigned)__builtin_amdgcn_readfirstlane(old) == n - 1;
}
__device__ __forceinline__ void lds_wait(LAS char* lds0, int word, unsigned target) {
  LAS unsigned* p = (LAS unsigned*)(lds0 + M_CTL) + word;
  while ((unsigned)__builtin_amdgcn_readfirstlane(__hip_atomic_load(p, __ATOMIC_RELAXED, __HIP_MEMORY_SCOPE_WORKGROUP)) < target) __builtin_amdgcn_s_sleep(0);
  asm volatile("" ::: "memory");
}
__device__ __forceinline__ void lds_post(LAS char* lds0, int word, int lane) {
  SCHED_BAR();
  asm volatile("" ::: "memory");
  if (lane == 0) __hip_atomic_fetch_add((LAS unsigned*)(lds0 + M_CTL) + word, 1u, __ATOMIC_RELAXED, __HIP_MEMORY_SCOPE_WORKGROUP);
  asm volatile("" ::: "memory");
}
__device__ __forceinline__ void group_sync(LAS char* lds0, int word, unsigned target, int lane) { lds_post(lds0, word, lane); lds_wait(lds0, word, target); }
template <int NREP, int NSH> __device__ __forceinline__ void signal(char* base, int shard, int lane) {
  if (lane < NREP) __hip_atomic_fetch_add((gu32*)(base + (lane * NSH + shard) * 256), 1u, RLX_AGENT);
}
template <int NSH> __device__ __forceinline__ bool count_ok(unsigned c, unsigned total) {
  if (NSH > 1) c += __builtin_amdgcn_update_dpp(0, c, 0xb1, 0xf, 0xf, true);
  if (NSH > 2) c += __builtin_amdgcn_update_dpp(0, c, 0x4e, 0xf, 0xf, true);
  if (NSH > 4) c += __builtin_amdgcn_update_dpp(0, c, 0x141, 0xf, 0xf, true);
  return (unsigned)__builtin_amdgcn_readfirstlane(c) >= total;
}
template <int NSH> __device__ __forceinline__ void wait_count(char* base, int rep, unsigned total, int lane) {
  gu32* p = (gu32*)(base + (rep * NSH + (lane & (NSH - 1))) * 256);
#pragma unroll 1
  for (unsigned spin = 0; spin < SPIN_CAP; ++spin) {
    if (count_ok<NSH>(__hip_atomic_load(p, RLX_AGENT), total)) break;
    __builtin_amdgcn_s_sleep(POLL_SLEEP);
  }
  __builtin_amdgcn_fence(__ATOMIC_ACQUIRE, "wavefront");
}
template <int NSH> __device__ __forceinline__ void poll_issue(unsigned& c, char* base, int rep, int lane) {
  c = __hip_atomic_load((gu32*)(base + (rep * NSH + (lane & (NSH - 1))) * 256), RLX_AGENT);
}
template <int NSH> __device__ __forceinline__ void poll_finish(unsigned c, char* base, int rep, unsigned total, int lane) {
  if (!count_ok<NSH>(c, total)) wait_count<NSH>(base, rep, total, lane);
  __builtin_amdgcn_fence(__ATOMIC_ACQUIRE, "wavefront");
}

__device__ __forceinline__ float gelu_f(float x) {
  const float p = __builtin_fmaf(x * x, -0.044715f * 2.302208198f, -2.302208198f);
  const float e = __builtin_amdgcn_exp2f(x * p);
  return x * __builtin_amdgcn_rcpf(1.0f + e);
}

__device__ __forceinline__ float ld32f(__amdgpu_buffer_rsrc_t rs, unsigned off) { return u2f(__builtin_amdgcn_raw_buffer_load_b32(rs, off, 0, 16)); }
__device__ __forceinline__ void st32(__amdgpu_buffer_rsrc_t rs, unsigned off, unsigned v) { __builtin_amdgcn_raw_buffer_store_b32(v, rs, off, 0, 16); }

struct Args {
  const u16 *x, *Are, *Aim, *Bre, *Bim, *Cre, *Cim, *logdt, *dskip, *win, *wsp, *bsp, *wout, *cvec, *wmod;
  char* ws; u16* out;
};

typedef GAS unsigned long long gu64;
__device__ __forceinline__ void put_granule(char* p, unsigned v, unsigned tag) { __hip_atomic_store((gu64*)p, ((unsigned long long)tag << 32) | v, RLX_AGENT); }
template <int N, class F> __device__ __forceinline__ void get_granules(unsigned (&v)[N], unsigned tag, F addr) {
#pragma unroll 1
  for (unsigned spin = 0; spin < SPIN_CAP; ++spin) {
    unsigned long long x[N];
#pragma unroll
    for (int k = 0; k < N; ++k) x[k] = __hip_atomic_load((gu64*)addr(k), RLX_AGENT);
    bool ok = true;
#pragma unroll
    for (int k = 0; k < N; ++k) { v[k] = (unsigned)x[k]; ok &= (unsigned)(x[k] >> 32) == tag; }
    if (wall(ok)) break;
    __builtin_amdgcn_s_sleep(3);
  }
}

__device__ __forceinline__ void conv_group(LAS char* lds, LAS char* lds0, int bt, const Args& A, __amdgpu_buffer_rsrc_t rs, unsigned epoch, unsigned& tag, unsigned (&pre)[7], unsigned long long pc,
                                           int wg, int d, int tid, int wave, int lane STAMP_ARG) {
  char* ws = A.ws;
  float S[8], soff;
  v4u xv, st[2];
  unsigned cpf = 0, pv3 = 0;
  {
    const int half = wave & 1;
    int xoff[8];
#pragma unroll
    for (int j = 0; j < 8; ++j) {
      const int l = tid * 8 + j;
      const int idx = l < 128 ? l : (int)(pkbf((float)l, 0.f) & 0xffffu) - 0x4300 + 128;
      xoff[j] = (idx + (idx >> 3)) * 8;
    }
    asm volatile("" : "+v"(xoff[0]), "+v"(xoff[1]), "+v"(xoff[2]), "+v"(xoff[3]), "+v"(xoff[4]), "+v"(xoff[5]), "+v"(xoff[6]), "+v"(xoff[7]));
    waitv<0>();
    asm volatile("v_readfirstlane_b32 %0, %1\n\ts_nop 3" : "=s"(tag) : "v"(epoch) : "memory");
    tag += 1u;
    asm volatile("" : "+v"(pre[0]), "+v"(pre[1]), "+v"(pre[2]), "+v"(pre[3]), "+v"(pre[4]), "+v"(pre[5]), "+v"(pre[6]));
    {
      const int so = ((wg >> 3) * 4 + wave) * 4 + (lane & 3);
      cpf = *(const GAS unsigned*)((pc & ~255ull) + (size_t)(bt == 1 && so < CODE_LINES ? so : 0) * 128);
    }
    const unsigned vare = pre[0], vaim = pre[1], vdt = pre[2], vbre = pre[3], vbim = pre[4], vcre = pre[5], vcim = pre[6];
    if (tid < 4) *(LAS v4u*)(lds + C_KOFF + tid * 16) = v4u{0u, 0u, 0u, 0u};
    const float dt = bfr(exp_acc(bflo(vdt)));
    const float ar2 = dt * bflo(vare) * 1.4426950408889634f, ai2 = dt * bflo(vaim) * 0.15915494309189535f;
    auto E = [&](float xx) {
      const float mag = __builtin_amdgcn_exp2f(ar2 * xx), ph = __builtin_amdgcn_fractf(ai2 * xx);
      return cplx{mag * __builtin_amdgcn_cosf(ph), mag * __builtin_amdgcn_sinf(ph)};
    };
    LAS char* img = lds + C_COFF + wave * 8192 + lane * 128;
    const int sw = lane & 7;
    float isc = 0.f;
    if (wave < 2) {
      const cplx e8 = E(8.f), e64 = E(64.f), e128 = E(128.f), e192 = E(192.f);
      cplx q[8];
      q[0] = {1.f, 0.f}; q[1] = e8; q[2] = cmul(e8, e8); q[3] = cmul(q[2], e8); q[4] = cmul(q[2], q[2]);
      q[5] = cmul(q[4], e8); q[6] = cmul(q[4], q[2]); q[7] = cmul(q[4], q[3]);
#pragma unroll
      for (int g = 0; g < 4; ++g) {
        cplx v[8];
#pragma unroll
        for (int j = 0; j < 8; ++j) v[j] = g == 0 ? q[j] : cmul(q[j], g == 1 ? e64 : (g == 2 ? e128 : e192));
        *(LAS v4u*)(img + ((g ^ sw) << 4)) = v4u{pkh(v[0].r, v[1].r), pkh(v[2].r, v[3].r), pkh(v[4].r, v[5].r), pkh(v[6].r, v[7].r)};
        *(LAS v4u*)(img + (((4 + g) ^ sw) << 4)) = v4u{pkh(v[0].i, v[1].i), pkh(v[2].i, v[3].i), pkh(v[4].i, v[5].i), pkh(v[6].i, v[7].i)};
      }
    } else {
      cplx W = cmul(cplx{bflo(vcre), bflo(vcim)}, cplx{dt * bflo(vbre), dt * bflo(vbim)});
      float mx = __builtin_fmaxf(__builtin_fabsf(W.r), __builtin_fabsf(W.i));
      mx = __builtin_fmaxf(mx, dppf<0xb1>(mx)); mx = __builtin_fmaxf(mx, dppf<0x4e>(mx)); mx = __builtin_fmaxf(mx, dppf<0x141>(mx)); mx = __builtin_fmaxf(mx, dppf<0x140>(mx));
      const unsigned mb = f2u(mx);
      const unsigned m01 = __builtin_elementwise_max((unsigned)__builtin_amdgcn_readlane(mb, 0), (unsigned)__builtin_amdgcn_readlane(mb, 16));
      const unsigned m23 = __builtin_elementwise_max((unsigned)__builtin_amdgcn_readlane(mb, 32), (unsigned)__builtin_amdgcn_readlane(mb, 48));
      int ex = (int)((__builtin_elementwise_max(m01, m23) >> 23) & 0xff);
      ex = ex < 20 ? 20 : (ex > 240 ? 240 : ex);
      const float sc = u2f((unsigned)(267 - ex) << 23);
      isc = u2f((unsigned)(ex - 13) << 23);
      W.r *= sc; W.i *= sc;
      const cplx e1 = E(1.f), e2 = cmul(e1, e1), e4 = cmul(e2, e2);
      cplx h[24];
      h[0] = W; h[1] = cmul(W, e1); h[2] = cmul(W, e2); h[3] = cmul(h[2], e1);
      h[4] = cmul(W, e4); h[5] = cmul(h[4], e1); h[6] = cmul(h[4], e2); h[7] = cmul(h[6], e1);
      const cplx g1 = E(256.f), g2 = E(512.f), g3 = E(768.f), g4 = E(1024.f);
      h[8] = cmul(h[0], g1); h[9] = cmul(h[2], g1); h[10] = cmul(h[4], g1); h[11] = cmul(h[6], g1);
      h[12] = cmul(h[0], g2); h[13] = cmul(h[4], g2); h[14] = cmul(h[0], g3); h[15] = cmul(h[4], g3);
      h[16] = cmul(h[0], g4); h[17] = cmul(h[8], g4); h[18] = cmul(h[12], g4); h[19] = cmul(h[14], g4);
      h[20] = cmul(h[16], g4); h[21] = {0.f, 0.f}; h[22] = {0.f, 0.f}; h[23] = {0.f, 0.f};
#pragma unroll
      for (int g = 0; g < 3; ++g) {
        const cplx* v = h + 8 * g;
        *(LAS v4u*)(img + ((g ^ sw) << 4)) = v4u{pkh(v[0].r, v[1].r), pkh(v[2].r, v[3].r), pkh(v[4].r, v[5].r), pkh(v[6].r, v[7].r)};
        *(LAS v4u*)(img + (((4 + g) ^ sw) << 4)) = v4u{pkh(-v[0].i, -v[1].i), pkh(-v[2].i, -v[3].i), pkh(-v[4].i, -v[5].i), pkh(-v[6].i, -v[7].i)};
      }
      *(LAS v4u*)(img + ((3 ^ sw) << 4)) = v4u{0u, 0u, 0u, 0u};
      *(LAS v4u*)(img + ((7 ^ sw) << 4)) = v4u{0u, 0u, 0u, 0u};
    }
    STAMP(1);
    group_sync(lds0, 5 + bt, 4, lane);
    if (wave >= 2) {
      const int hh = lane >> 5, blk = (lane >> 4) & 1, qq = (lane & 15) >> 2, p = lane & 3;
      LAS const char* Qi = lds + C_COFF + half * 8192;
      v2u a[8][2], bb[8][2];
#pragma unroll
      for (int ks = 0; ks < 8; ++ks)
#pragma unroll
        for (int t = 0; t < 2; ++t) {
          const int k = 16 * ks + 8 * hh + 4 * t + qq, mode = k >> 1, c = k & 1;
          const int off = mode * 128 + (((c * 4 + 2 * blk + (p >> 1)) ^ (mode & 7)) << 4) + (p & 1) * 8;
          a[ks][t] = trread(Qi + 16384 + off);
          bb[ks][t] = trread(Qi + off);
        }
      SCHED_BAR();
      f32x16 acc = {};
#pragma unroll
      for (int ks = 0; ks < 8; ++ks) acc = mfma32h(v4u{a[ks][0].x, a[ks][0].y, a[ks][1].x, a[ks][1].y}, v4u{bb[ks][0].x, bb[ks][0].y, bb[ks][1].x, bb[ks][1].y}, acc);
      LAS float* T = (LAS float*)(lds + C_UOFF) + half;
      const int q = lane & 31, hi = lane >> 5;
      auto put = [&](int idx, float v) { T[(idx + (idx >> 3)) * 2] = v * isc; };
#pragma unroll
      for (int r = 0; r < 4; ++r) put((r + 4 * hi) + 8 * q, acc[r]);
#pragma unroll
      for (int r = 0; r < 4; ++r) put(hi ? 384 + 64 * (r >> 1) + (r & 1) + 2 * q : 256 + r + 4 * q, acc[4 + r]);
      if (hi == 0) {
#pragma unroll
        for (int r = 0; r < 4; ++r) put(512 + 32 * r + q, acc[8 + r]);
      } else if (q == 0) put(640, acc[8]);
    }
    lds_post(lds0, 5 + bt, lane);
    auto load_x = [&]() {
      xv = ld128(rs, (unsigned)WS_XT + (tid * D + d) * 16);
#pragma unroll
      for (int i = 0; i < 2; ++i) st[i] = ld128(rs, (unsigned)WS_ST1 + (i * 256 + tid) * 16);
      SCHED_BAR();
    };
    const bool early = __builtin_amdgcn_readfirstlane(__hip_atomic_load((LAS unsigned*)(lds0 + M_CTL) + (bt ? 5 : 8), __ATOMIC_RELAXED, __HIP_MEMORY_SCOPE_WORKGROUP)) >= (bt ? XGATE : 1);
    asm volatile("" ::: "memory");
    if (early) load_x();
    lds_wait(lds0, 5 + bt, 8);
    {
      f32x2 tv[8];
#pragma unroll
      for (int j = 0; j < 8; ++j) tv[j] = *(LAS const f32x2*)(lds + C_UOFF + xoff[j]);
      SCHED_BAR();
      float kv[8];
#pragma unroll
      for (int j = 0; j < 8; ++j) kv[j] = tv[j][0] + tv[j][1];
      *(LAS v4u*)(lds + C_KOFF + 64 + tid * 16) = v4u{pkbf(kv[0], kv[1]), pkbf(kv[2], kv[3]), pkbf(kv[4], kv[5]), pkbf(kv[6], kv[7])};
      S[0] = kv[0];
#pragma unroll
      for (int j = 1; j < 8; ++j) S[j] = S[j - 1] + kv[j];
      float inc = S[7];
      inc += dppf<0x111>(inc); inc += dppf<0x112>(inc); inc += dppf<0x114>(inc); inc += dppf<0x118>(inc);
      inc += __builtin_bit_cast(float, __builtin_amdgcn_update_dpp(0, __builtin_bit_cast(int, inc), 0x142, 0xa, 0xf, false));
      inc += __builtin_bit_cast(float, __builtin_amdgcn_update_dpp(0, __builtin_bit_cast(int, inc), 0x143, 0xc, 0xf, false));
      soff = inc - S[7];
      if (lane == 63) ((LAS float*)(lds + C_KOFF + 4160))[wave] = inc;
    }
    lds_post(lds0, 5 + bt, lane);
    if (!early) {
      if (bt == 0 && wave == 0) {
#pragma unroll 1
        for (unsigned spin = 0; spin < SPIN_CAP; ++spin) {
          asm volatile("" ::: "memory");
          const v4u f = ld128(rs, (unsigned)CTL_F0 + (wg & (F0_REP - 1)) * 1024 + lane * 16);
          if (wall(f.x == tag && f.y == ~tag && f.z == tag && f.w == ~tag)) break;
          __builtin_amdgcn_s_sleep(2);
        }
        __builtin_amdgcn_fence(__ATOMIC_ACQUIRE, "wavefront");
        lds_post(lds0, 8, lane);
      } else if (bt == 0) lds_wait(lds0, 8, 1);
      else lds_wait(lds0, 5, XGATE);
      load_x();
    }
    STAMP(2);
  }
  lds_wait(lds0, 5 + bt, 12);
  STAMP(3);
  float xf[8], xn[8];
  {
#pragma unroll
    for (int j = 0; j < 4; ++j) { xf[2 * j] = bflo(xv[j]); xf[2 * j + 1] = bfhi(xv[j]); }
    const f32x4 wt = *(LAS const f32x4*)(lds + C_KOFF + 4160);
    if (tid < 128) *(LAS v4u*)(lds + C_UOFF + (tid >> 5) * C_UP + (tid & 31) * 16) = v4u{0u, 0u, 0u, 0u};
#pragma unroll
    for (int j = 0; j < 8; ++j) {
      const unsigned sj = st[j >> 2][j & 3];
      xn[j] = (xf[j] - bflo(sj)) * __builtin_amdgcn_rcpf(bfhi(sj));
    }
#pragma unroll
    for (int e = 0; e < 4; ++e) *(LAS unsigned*)(lds + C_UOFF + e * C_UP + 512 + tid * 4) = pkbf(xn[e], xn[e + 4]);
    soff += (wave > 0 ? wt[0] : 0.f) + (wave > 1 ? wt[1] : 0.f) + (wave > 2 ? wt[2] : 0.f);
  }
  const u16 dskv = A.dskip[d];
  char* mgp = ws + WS_MOD + (size_t)((lane & 3) * 3840 + (lane < 12 ? lane >> 2 : 0) * D + d) * 8;
  const unsigned long long mg = __hip_atomic_load((gu64*)mgp, RLX_AGENT);
  STAMP(5);
  group_sync(lds0, 5 + bt, 16, lane);
  STAMP(6);
  f32x16 y0 = {}, y1 = {};
  {
    const int hh = lane >> 5, blk = (lane >> 4) & 1, qq = (lane & 15) >> 2, p = lane & 3, col = lane & 31;
    LAS const char* ab = lds + C_KOFF + (32 + 16 * blk + 4 * p - 4 * qq + 32 * hh) * 2 - 32;
    LAS const char* bb = lds + C_UOFF + wave * C_UP + (256 + 8 * (col - hh) - 240) * 2;
    struct CF { v2u a0l, a0h, a1l, a1h; v4u b0, b1; };
    CF cf[3];
    auto rdc = [&](int ks, CF& f) {
      f.a0l = trread(ab + ks * 128 + 32); f.a0h = trread(ab + ks * 128);
      f.b0 = *(LAS const v4u*)(bb + (15 - ks) * 32);
      f.b1 = *(LAS const v4u*)(bb + (31 - ks) * 32);
      f.a1l = trread(ab + (ks + 16) * 128 + 32); f.a1h = trread(ab + (ks + 16) * 128);
    };
    rdc(0, cf[0]); rdc(1, cf[1]);
    SCHED_BAR();
#pragma unroll
    for (int ks = 0; ks < 16; ++ks) {
      if (ks + 2 < 16) rdc(ks + 2, cf[(ks + 2) % 3]);
      const CF& f = cf[ks % 3];
      const v4u a0 = {f.a0l.x, f.a0l.y, f.a0h.x, f.a0h.y}, a1 = {f.a1l.x, f.a1l.y, f.a1h.x, f.a1h.y};
      y0 = mfma32(a0, f.b0, y0);
      y1 = mfma32(a0, f.b1, y1);
      y1 = mfma32(a1, f.b0, y1);
      INTERLEAVE(3, 2);
    }
    asm volatile("" :: "v"(y0), "v"(y1));
    STAMP(7);
    LAS char* cb = lds + C_COFF + (wave * 64 + col) * 144 + hh * 16;
#pragma unroll
    for (int g = 0; g < 4; ++g) {
      *(LAS f32x4*)(cb + g * 32) = f32x4{y0[4 * g], y0[4 * g + 1], y0[4 * g + 2], y0[4 * g + 3]};
      *(LAS f32x4*)(cb + 32 * 144 + g * 32) = f32x4{y1[4 * g], y1[4 * g + 1], y1[4 * g + 2], y1[4 * g + 3]};
    }
  }
  lds_post(lds0, 5 + bt, lane);
  unsigned mv[1] = {(unsigned)mg};
  if (!wall((unsigned)(mg >> 32) == tag)) get_granules<1>(mv, tag, [&](int) { return mgp; });
  float msum = u2f(mv[0]);
  msum += dppf<0xb1>(msum); msum += dppf<0x4e>(msum);
  const unsigned mb = f2u(bfr(msum));
  const float sa = u2f(__builtin_amdgcn_readlane(mb, 0)), c1a = bfr(1.0f + u2f(__builtin_amdgcn_readlane(mb, 4))), ga = u2f(__builtin_amdgcn_readlane(mb, 8)), dsk = bf2f(dskv);
  const float gc = ga * c1a, gs_ = ga * sa, gd = gc * dsk, g0 = gs_ * (soff + dsk);
#pragma unroll
  for (int j = 0; j < 8; ++j) xf[j] = __builtin_fmaf(gd, xn[j], __builtin_fmaf(gs_, S[j], xf[j] + g0));
  STAMP(8);
  lds_wait(lds0, 5 + bt, 20);
  STAMP(9);
  float y[8];
  {
    const int c1 = 2 * tid, c0 = tid ? c1 - 1 : 0;
    const int o0 = (c0 >> 3) * 144 + (c0 & 7) * 16, o1 = (c1 >> 3) * 144 + (c1 & 7) * 16;
    LAS const char* cb = lds + C_COFF;
    f32x4 w0[4], w1[4], w2[4];
#pragma unroll
    for (int e = 0; e < 4; ++e) {
      w1[e] = *(LAS const f32x4*)(cb + e * 64 * 144 + o1); w2[e] = *(LAS const f32x4*)(cb + e * 64 * 144 + o1 + 16);
      if (e) w0[e] = *(LAS const f32x4*)(cb + e * 64 * 144 + o0);
    }
    SCHED_BAR();
#pragma unroll
    for (int e = 0; e < 4; ++e) {
      float w[12];
#pragma unroll
      for (int j = 0; j < 4; ++j) { w[4 + j] = w1[e][j]; w[8 + j] = w2[e][j]; w[j] = (e && tid) ? w0[e][j] : 0.f; }
#pragma unroll
      for (int j = 0; j < 8; ++j) y[j] = e ? y[j] + w[4 + j - e] : w[4 + j];
    }
  }
  float o[8];
#pragma unroll
  for (int j = 0; j < 8; ++j) o[j] = __builtin_fmaf(gc, y[j], xf[j]);
  {
    const unsigned a0 = pkbf(o[0], o[1]), a1 = pkbf(o[2], o[3]), b0 = pkbf(o[4], o[5]), b1 = pkbf(o[6], o[7]);
    const bool odd = tid & 1;
    const unsigned s0 = odd ? a0 : b0, s1 = odd ? a1 : b1;
    const unsigned r0 = __builtin_amdgcn_update_dpp(0, s0, 0xb1, 0xf, 0xf, true), r1 = __builtin_amdgcn_update_dpp(0, s1, 0xb1, 0xf, 0xf, true);
    const v4u ov = odd ? v4u{r0, r1, b0, b1} : v4u{a0, a1, r0, r1};
    st128(rs, (unsigned)WS_X2T + d * (L * 2) + (tid >> 2) * 64 + (tid & 1) * 32 + (tid & 2) * 8, ov);
  }
  STAMP(10);
  asm volatile("" :: "v"(cpf), "v"(pv3));
  if (wave_arrive(lds0, bt, 4, lane)) signal<E1_REP, 8>(ws + CTL_E1 + bt * 65536, wg & 7, lane);
}

__device__ __forceinline__ void aux_group(LAS char* lds, LAS char* lds0, const Args& A, __amdgpu_buffer_rsrc_t rs, unsigned epoch, int u, int tid, int wave, int lane STAMP_ARG) {
  char* ws = A.ws;
  unsigned gs = 0;
  v4u wb[5], ov[3], mw[10];
  u16 mc;
  const int mpart = u & 3, mcol = (u < 120 ? u >> 2 : 0) * 128;
  unsigned tag;
  {
    const int r = tid >> 4, s = tid & 15, q = lane >> 4;
    v4u v[5];
    {
      const u16* xp = A.x + (size_t)(u * 16 + r) * D + (s ^ ((q | ((wave >> 1) << 2)) << 1)) * 8;
#pragma unroll
      for (int i = 0; i < 5; ++i) v[i] = NTLOAD((const v4u*)(xp + i * 128));
    }
    SCHED_BAR();
    mc = A.cvec[mpart * 160 + (tid < 160 ? tid : 0)];
#pragma unroll
    for (int i = 0; i < 10; ++i) mw[i] = NTLOAD((const v4u*)(A.wmod + (size_t)(mpart * 160 + i * 16 + (tid >> 4)) * (6 * D) + mcol + (tid & 15) * 8));
    SCHED_BAR();
    {
      const int zc = (u * 4 + wave) * 3 + lane - 32;
      st128(rs, lane < 32 ? (unsigned)CTL_ST3 + ((u * 4 + wave) * 32 + lane) * 16 : (lane < 35 && zc < 1280 ? (unsigned)zc * 256 : 0x7ffffff0u), v4u{0u, 0u, 0u, 0u});
    }
    LAS char* img = lds + 8192;
#pragma unroll
    for (int i = 0; i < 5; ++i) *(LAS v4u*)(img + (wave * 5 + i) * 1024 + lane * 16) = v[i];
    lds_post(lds0, 7, lane); gs += 4;
    STAMP(3);
    float s0 = 0.f, s1 = 0.f, q0 = 0.f, q1 = 0.f;
#pragma unroll
    for (int i = 0; i < 5; ++i) {
      s0 = dot2bf(v[i][0], 0x3f803f80u, s0); s1 = dot2bf(v[i][1], 0x3f803f80u, s1); s0 = dot2bf(v[i][2], 0x3f803f80u, s0); s1 = dot2bf(v[i][3], 0x3f803f80u, s1);
      q0 = dot2bf(v[i][0], v[i][0], q0); q1 = dot2bf(v[i][1], v[i][1], q1); q0 = dot2bf(v[i][2], v[i][2], q0); q1 = dot2bf(v[i][3], v[i][3], q1);
    }
    SCHED_BAR();
    lds_wait(lds0, 7, gs);
    const int rr = (lane & 15) >> 2, p = lane & 3, f = (rr | ((q & 1) << 2)) << 1;
    v2u lo[5], hi[5];
#pragma unroll
    for (int j = 0; j < 5; ++j) {
      const int a0 = (2 * (q & 1) * 5 + j) * 1024 + rr * 256 + ((((2 * wave + (q >> 1)) * 2 + (p >> 1)) ^ f) << 4) + (p & 1) * 8;
      lo[j] = trread(img + a0); hi[j] = trread(img + a0 + 5120);
    }
    float sum = s0 + s1, sq = q0 + q1;
    sum += dppf<0xb1>(sum); sq += dppf<0xb1>(sq); sum += dppf<0x4e>(sum); sq += dppf<0x4e>(sq);
    sum += dppf<0x141>(sum); sq += dppf<0x141>(sq); sum += dppf<0x140>(sum); sq += dppf<0x140>(sq);
#pragma unroll
    for (int j = 0; j < 5; ++j)
      st128(rs, (unsigned)WS_XT + ((u * 2 + (q & 1)) * D + (8 * j + 2 * wave + (q >> 1)) * 16 + (lane & 15)) * 16, v4u{lo[j].x, lo[j].y, hi[j].x, hi[j].y});
    const float mean = sum * (1.0f / D);
    const int t = u * 16 + r;
    st32(rs, s == 0 ? (unsigned)WS_ST1 + ((((t >> 2) & 1) * 256 + (t >> 3)) * 4 + (t & 3)) * 4 : 0x7ffffff0u, pkbf(mean, ln_std(__builtin_fmaxf(sq * (1.0f / D) - mean * mean, 0.f))));
    STAMP(8);
    SCHED_BAR();
    {
      const float c = bf2f(mc);
      ((LAS float*)lds)[tid] = bfr(c * __builtin_amdgcn_rcpf(1.0f + __builtin_amdgcn_exp2f(-1.4426950408889634f * c)));
    }
    lds_post(lds0, 7, lane); gs += 4;
    asm volatile("s_waitcnt vmcnt(0)\n\tv_readfirstlane_b32 %0, %1\n\ts_nop 3" : "=s"(tag) : "v"(epoch) : "memory");
    tag += 1u;
    if (wave_arrive(lds0, 9, 4, lane) && lane < F0_REP)
      __hip_atomic_store((gu64*)(ws + CTL_F0 + lane * 1024 + u * 8), ((unsigned long long)~tag << 32) | tag, RLX_AGENT);
    SCHED_BAR();
    {
      const int r8 = tid >> 3, s8 = tid & 7;
      const u16* bp = A.win + (size_t)(160 * (u & 3) + r8) * F + 64 * ((u < 108 ? u : 0) >> 2) + s8 * 8;
#pragma unroll
      for (int i = 0; i < 5; ++i) wb[i] = NTLOAD((const v4u*)(bp + (size_t)i * 32 * F));
      const int slab = u < 108 ? u >> 2 : 0;
      const int half = (r8 >> 2) & 1, jj = slab < 24 ? slab / 3 : 2 * (slab - 24) + half;
      const int sub = (jj < 6 ? 7 * jj : 42 + 6 * (jj - 6)) + (slab < 24 ? 2 * (slab - 3 * jj) + half : 6);
      const u16* src = A.wout + (size_t)(32 * (sub >> 1) + 8 * (r8 >> 3) + 4 * (sub & 1) + (r8 & 3)) * D + (u & 3) * 160;
#pragma unroll
      for (int i = 0; i < 3; ++i) ov[i] = NTLOAD((const v4u*)(src + (i < 2 || s8 < 4 ? s8 + 8 * i : 0) * 8));
    }
    SCHED_BAR();
    STAMP(1);
  }
  {
    LAS float* msc = (LAS float*)lds;
    lds_wait(lds0, 7, gs);
    float sv[10];
#pragma unroll
    for (int i = 0; i < 10; ++i) sv[i] = msc[(tid >> 4) + 16 * i];
    float acc[8] = {};
#pragma unroll
    for (int i = 0; i < 10; ++i)
#pragma unroll
      for (int j = 0; j < 4; ++j) { acc[2 * j] += sv[i] * bflo(mw[i][j]); acc[2 * j + 1] += sv[i] * bfhi(mw[i][j]); }
    treduce_xor<4>(acc, (lane & 32) != 0, 32);
    treduce_xor<2>(acc, (lane & 16) != 0, 16);
    LAS float* red = msc + 256;
    *(LAS f32x2*)(red + wave * 128 + (lane & 15) * 8 + (lane >> 4) * 2) = f32x2{acc[0], acc[1]};
    group_sync(lds0, 7, gs += 4, lane);
    if (tid < 128 && u < 120) put_granule(ws + WS_MOD + (size_t)(mpart * 3840 + mcol + tid) * 8, f2u((red[tid] + red[128 + tid]) + (red[256 + tid] + red[384 + tid])), tag);
  }
  __builtin_amdgcn_s_setprio(AUXB_PRIO);
  if (u < 108) {
    const int cbk = u >> 2, part = u & 3;
    const int c0 = 64 * cbk, r0 = 160 * part, seg = tid & 7, rg = tid >> 3;
    constexpr int OP = 400, WPITCH = 144;
    LAS char* oimg = lds + 32768;
    LAS char* img = lds + 4096;
#pragma unroll
    for (int i = 0; i < 5; ++i) *(LAS v4u*)(img + (rg + 32 * i) * WPITCH + seg * 16) = wb[i];
#pragma unroll
    for (int i = 0; i < 3; ++i) *(LAS v4u*)(oimg + rg * OP + (seg + 8 * i) * 16) = ov[i];
    group_sync(lds0, 7, gs += 4, lane);
    const int q = lane >> 4, rr = (lane & 15) >> 2, p = lane & 3;
    v4u wf[5];
    {
      const int colb = (32 * (wave >> 1) + 8 * p + 4 * (wave & 1)) * 2;
#pragma unroll
      for (int i = 0; i < 5; ++i) {
        const int a0 = (32 * i + 8 * q + rr) * WPITCH + colb;
        const v2u lo = trread(img + a0), hi = trread(img + a0 + 4 * WPITCH);
        wf[i] = v4u{lo.x, lo.y, hi.x, hi.y};
      }
      const int a0 = (8 * q + rr) * OP + p * 8 + wave * 32;
#pragma unroll
      for (int j = 0; j < 3; ++j) {
        const v2u lo = trread(oimg + a0 + j * 128), hi = trread(oimg + a0 + 4 * OP + j * 128);
        st128(rs, wave + 4 * j < 10 ? (unsigned)WS_WOUT + (((part * 10 + wave + 4 * j) * 27 + cbk) * 64 + lane) * 16 : 0x7ffffff0u, v4u{lo.x, lo.y, hi.x, hi.y});
      }
    }
    LAS float* tc = (LAS float*)(lds + 28672);
    LAS u16* ts = (LAS u16*)(lds + 29696);
    if (wave < 3) {
      unsigned mv[8];
      const int rw = r0 + (tid < 160 ? tid : 159);
      get_granules<8>(mv, tag, [&](int k) { return ws + WS_MOD + (size_t)((k & 3) * 3840 + (3 + (k >> 2)) * D + rw) * 8; });
      ts[tid] = (u16)pkbf((u2f(mv[0]) + u2f(mv[1])) + (u2f(mv[2]) + u2f(mv[3])), 0.f);
      tc[tid] = bfr(1.0f + bfr((u2f(mv[4]) + u2f(mv[5])) + (u2f(mv[6]) + u2f(mv[7]))));
    }
    group_sync(lds0, 7, gs += 4, lane);
    STAMP(2);
    f32x4 u1 = {0.f, 0.f, 0.f, 0.f}, u2 = {0.f, 0.f, 0.f, 0.f};
    const v4u ones = {0x3f803f80u, 0x3f803f80u, 0x3f803f80u, 0x3f803f80u};
#pragma unroll
    for (int i = 0; i < 5; ++i) {
      const f32x4 ca = *(LAS const f32x4*)(tc + 32 * i + 8 * q), cb = *(LAS const f32x4*)(tc + 32 * i + 8 * q + 4);
      const v4u w = wf[i];
      const v4u o = {pkbf(bflo(w.x) * ca[0], bfhi(w.x) * ca[1]), pkbf(bflo(w.y) * ca[2], bfhi(w.y) * ca[3]),
                     pkbf(bflo(w.z) * cb[0], bfhi(w.z) * cb[1]), pkbf(bflo(w.w) * cb[2], bfhi(w.w) * cb[3])};
      st128(rs, (unsigned)WS_WIN + (((((part * 5 + i) >> 1) * 108 + 4 * cbk + wave) * 2 + ((part * 5 + i) & 1)) * 64 + lane) * 16, o);
      u1 = mfma16(ones, o, u1);
      u2 = mfma16(*(LAS const v4u*)(ts + 32 * i + 8 * q), w, u2);
    }
    STAMP(3);
    st32(rs, lane < 32 ? (unsigned)WS_U + ((part * 2 + q) * F + c0 + wave * 16 + (lane & 15)) * 4 : 0x7ffffff0u, f2u(q ? u2[0] : u1[0]));
  }
  STAMP(8);
  lds_wait(lds0, 8, 1);
  if (wave_arrive(lds0, 2, 4, lane)) signal<E1_REP, 8>(ws + CTL_E1B, u & 7, lane);
}

__device__ __forceinline__ int g1_aoff(int row, int G, int ab, int p) {
  const int f = ((row >> 1) & 1) | (((row >> 3) & 1) << 1);
  return row * 128 + (((4 * G + 2 * ab + (p >> 1)) ^ (f << 1)) << 4) + (p & 1) * 8;
}

template <int NB>
__device__ __forceinline__ void loader_path(LAS char* lds, char* ws, __amdgpu_buffer_rsrc_t rs, const u16* __restrict__ wsp, int wg, int mt, int j, int sub0, int lw, int lane) {
  const unsigned lbase = (unsigned)(size_t)lds, vo = lane * 16;
  const bool CNT7 = j < 6;
  {
    unsigned avo[2];
#pragma unroll
    for (int h = 0; h < 2; ++h) {
      const int row = lane >> 3, pos = lane & 7, f = ((row >> 1) & 1) | (h << 1);
      avo[h] = (row + 8 * h) * (L * 2) + ((pos ^ (f << 1)) << 4) - h * 1024;
    }
    const char* asb = ws + WS_X2T + (size_t)(16 * lw) * (L * 2) + mt * 128;
    const int p0 = (NB == 4 || CNT7) ? (lw < 2 ? 4 * lw : 2 + 3 * lw) : 3 * lw;
    const char* bsb = ws + WS_WIN + (size_t)(sub0 * 2 + p0) * 1024;
    const unsigned bb = lbase + G1_BOFF;
    auto issueB = [&](int s, unsigned slot) {
      gldsL<NB>(bsb + (s < 10 ? 108 * 1024 + s * 216 * 1024 : (s - 10) * 216 * 1024), vo, slot + p0 * 1024);
    };
    auto issueA = [&](int s0, int s1) {
#pragma unroll 1
      for (int s = s0; s < s1; ++s) gldsN<2, true>(asb + (size_t)(s * 64) * (L * 2), avo[0], avo[1], 0, 0, lbase + s * 8192 + lw * 2048);
    };
    {
      const unsigned vb = (unsigned)WS_V + (mt * 4 + lw) * 27 * 1024 + lane * 16;
#pragma unroll
      for (int k = 0; k < 3; ++k) st128(rs, vb + (3 * j + k) * 1024, v4u{~0u, ~0u, ~0u, ~0u});
      if (j < 6) __builtin_amdgcn_raw_buffer_store_b64(v2u{~0u, ~0u}, rs, vb + (24 + (j >> 1)) * 1024 + (j & 1) * 8, 0, 16);
    }
    waitv<0>();
    group_sync(lds, 5, 24, lane);
    if (lw == 0) { wait_count<8>(ws + CTL_E1B, wg & (E1_REP - 1), 128, lane); lds_post(lds, 10, lane); }
    if (lw == 3) { wait_count<8>(ws + CTL_E1, wg & (E1_REP - 1), 256, lane); lds_post(lds, 11, lane); }
    lds_wait(lds, 10, 1);
    STAMP(13);
#pragma unroll
    for (int s = 0; s < 4; ++s) issueB(s, bb + (s + 1) * G1_BST);
    lds_wait(lds, 11, 1);
    lds_wait(lds, 1, 4);
    issueA(0, 4);
    STAMP(12);
    LBAR();
    LSTAMP(1); STAMP(14);
    issueB(4, bb);
    waitv<6 + NB>(); LBAR(); LSTAMP(2);
    waitv<4 + NB>(); LBAR(); issueB(5, bb + G1_BST);
    waitv<2 + 2 * NB>(); LBAR(); LSTAMP(3); issueA(4, 8); issueB(6, bb + 2 * G1_BST);
    waitv<8 + 3 * NB>(); LBAR(); issueB(7, bb + 3 * G1_BST);
    waitv<6 + 2 * NB>(); LBAR(); LSTAMP(4); issueB(8, bb + 4 * G1_BST);
    waitv<4 + 3 * NB>(); LBAR(); LSTAMP(5); issueA(8, 10); issueB(9, bb);
    waitv<4 + 3 * NB>(); LBAR(); issueB(10, bb + G1_BST);
    waitv<4 + 3 * NB>(); LBAR(); issueB(11, bb + 2 * G1_BST);
    waitv<2 + 3 * NB>(); LBAR(); LSTAMP(6); issueB(12, bb + 3 * G1_BST);
    int nxt = 4 * G1_BST;
#pragma unroll 1
    for (int i = 9; i <= 15; ++i) {
      waitv<NB * 3>(); LBAR();
      if (i >= 11) LSTAMP(i - 4);
      issueB(i + 4, bb + nxt);
      nxt += G1_BST; if (nxt == G1_NSLOT * G1_BST) nxt = 0;
    }
    waitv<NB * 3>(); LBAR(); LSTAMP(12);
    waitv<NB * 2>(); LBAR(); LSTAMP(13);
    waitv<NB * 1>(); LBAR();
    waitv<0>(); LBAR(); LSTAMP(14);
    LBAR();
  }
  const char* osb = ws + WS_WOUT + (size_t)j * 135 * 1024;
  if (lw >= 2) {
    unsigned wvo[4];
#pragma unroll
    for (int h = 0; h < 4; ++h) {
      const int i = 8 * (h & 1) + (lane >> 3);
      wvo[h] = (8 * (i >> 2) + (i & 3) + 4 * (h >> 1)) * (L * 2) + (((lane & 7) ^ (i >> 1)) << 4) - h * 1024;
    }
    const char* wsb = (const char*)wsp + (size_t)(mt * 64 + 32 * (lw & 1)) * (L * 2);
    auto issueW = [&](int s, unsigned slot) {
      gldsN<4, false>(wsb + s * 128, wvo[0], wvo[1], wvo[2], wvo[3], slot + (lw & 1) * 4096);
    };
#pragma unroll 1
    for (int s = 0; s < G2_NW; ++s) issueW(s, lbase + s * 8192);
    LBAR();
    waitv<32>(); LBAR();
    int wn = 0;
#pragma unroll 1
    for (int i = 1; i <= 23; ++i) { waitv<28>(); LBAR(); issueW(i + 8, lbase + wn); wn += 8192; if (wn == G2_NW * 8192) wn = 0; }
#pragma unroll 1
    for (int i = 24; i <= 32; ++i) {
      waitv<28>(); LBAR();
      gldsL<4>(osb + wn + (lw & 1) * 4096, vo, lbase + wn + (lw & 1) * 4096);
      wn += 8192; if (wn == G2_NW * 8192) wn = 0;
    }
  } else {
    constexpr int NZ = NB == 4 ? 7 : 6;
    const char* zsb = ws + WS_Z2 + (size_t)(sub0 * 2 + lw * NZ) * 1024;
    auto issueZ = [&](int s, unsigned slot) {
      gldsL<4>(zsb + s * 108 * 1024, vo, slot + lw * NZ * 1024);
      gldsL<NZ - 4>(zsb + s * 108 * 1024 + 4096, vo, slot + lw * NZ * 1024 + 4096);
    };
    const unsigned zb = lbase + G2_ZOFF;
    LBAR();
#pragma unroll 1
    for (int s = 0; s < G2_NZ; ++s) issueZ(s, zb + s * G2_ZST);
    waitv<NZ * 5>(); LBAR();
    int zn = 0;
#pragma unroll 1
    for (int i = 1; i <= 26; ++i) { waitv<NZ * 4>(); LBAR(); issueZ(i + 5, zb + zn); zn += G2_ZST; if (zn == G2_NZ * G2_ZST) zn = 0; }
    auto issueO = [&](int slot, int n) {
      const int o = G2_ZOFF + slot * G2_ZST + lw * n * 1024;
      if (n == 7) { gldsL<4>(osb + o, vo, lbase + o); gldsL<3>(osb + o + 4096, vo, lbase + o + 4096); }
      else gldsL<3>(osb + o, vo, lbase + o);
    };
    waitv<NZ * 4>(); LBAR(); issueO(2, 7);
    waitv<NZ * 3 + 7>(); LBAR(); issueO(3, 7);
    waitv<NZ * 2 + 14>(); LBAR(); issueO(4, 3);
    waitv<NZ + 17>(); LBAR(); if (lw == 0) gldsL<1>(osb + 134 * 1024, vo, lbase + 134 * 1024);
    waitv<17>(); LBAR(); issueO(0, 7);
    LBAR(); issueO(1, 7);
  }
  waitv<0>();
  LBAR();
}

template <int NSUB, bool STATS>
__device__ __forceinline__ void consumer_g12(LAS char* lds, char* ws, __amdgpu_buffer_rsrc_t rs, const u16* __restrict__ bsp, const u16* __restrict__ wsp, int wg, int mt, int j, int sub0, int cnt,
                                             int wave, int tid, int lane, v2u (&x2v)[3] STAMP_ARG) {
  constexpr int NS1 = NSUB > 0 ? NSUB : 1;
  const int G = wave >> 2, ng = G ? 3 - (wave & 3) : (wave & 3);
  const int q = lane >> 4, i16 = lane & 15, rr = i16 >> 2, p = lane & 3, n0 = 2 * ng;
  float bias[2];
#pragma unroll
  for (int m = 0; m < 2; ++m) bias[m] = bf2f(bsp[mt * 64 + 32 * G + 8 * (i16 >> 2) + 4 * m + (i16 & 3)]);
  STAMP(4);
  CBAR();
  v2u z1v[NS1][2];
  {
    const int aoff0 = g1_aoff(8 * q + rr, G, 0, p), aoff1 = g1_aoff(8 * q + rr, G, 1, p);
    const int boff = G1_BOFF + n0 * 2048 + lane * 16;
    struct Frags { v4u a[2][2]; v4u b[2][NS1]; };
    f32x4 acc2[NS1][2], acc1[NS1][2], asum[2], asq[2];
    int acur = 0, bcur = G1_BST;
    auto rd = [&](Frags& f) {
#pragma unroll
      for (int kk = 0; kk < 2; ++kk) {
        const v2u l0 = trread(lds + acur + aoff0 + kk * 4096), h0 = trread(lds + acur + aoff0 + kk * 4096 + 512);
        const v2u l1 = trread(lds + acur + aoff1 + kk * 4096), h1 = trread(lds + acur + aoff1 + kk * 4096 + 512);
        f.a[kk][0] = v4u{l0.x, l0.y, h0.x, h0.y}; f.a[kk][1] = v4u{l1.x, l1.y, h1.x, h1.y};
#pragma unroll
        for (int n = 0; n < NSUB; ++n) f.b[kk][n] = *(LAS const v4u*)(lds + bcur + boff + n * 2048 + kk * 1024);
      }
      acur += 8192; if (acur == 81920) acur = 0;
      bcur += G1_BST; if (bcur == G1_NSLOT * G1_BST) bcur = 0;
    };
    auto pin1 = [&]() {
#pragma unroll
      for (int n = 0; n < NSUB; ++n) asm volatile("" : "+v"(acc1[n][0]), "+v"(acc1[n][1]));
    };
#pragma unroll
    for (int ab = 0; ab < 2; ++ab) {
      asum[ab] = f32x4{0.f, 0.f, 0.f, 0.f}; asq[ab] = f32x4{0.f, 0.f, 0.f, 0.f};
#pragma unroll
      for (int n = 0; n < NS1; ++n) { acc2[n][ab] = f32x4{0.f, 0.f, 0.f, 0.f}; acc1[n][ab] = f32x4{0.f, 0.f, 0.f, 0.f}; }
    }
    const v4u ones = {0x3f803f80u, 0x3f803f80u, 0x3f803f80u, 0x3f803f80u};
    auto mm2 = [&](const Frags& f) {
#pragma unroll
      for (int kk = 0; kk < 2; ++kk)
#pragma unroll
        for (int ab = 0; ab < 2; ++ab) {
#pragma unroll
          for (int n = 0; n < NSUB; ++n) acc2[n][ab] = mfma16(f.a[ABL_KK][ab], f.b[ABL_KK][n], acc2[n][ab]);
          if (STATS) { asum[ab] = mfma16(f.a[kk][ab], ones, asum[ab]); asq[ab] = mfma16(f.a[kk][ab], f.a[kk][ab], asq[ab]); }
        }
      INTERLEAVE(6, 2);
    };
    auto mm1 = [&](const Frags& f) {
#pragma unroll
      for (int kk = 0; kk < 2; ++kk)
#pragma unroll
        for (int ab = 0; ab < 2; ++ab)
#pragma unroll
          for (int n = 0; n < NSUB; ++n) acc1[n][ab] = mfma16(f.b[kk][n], f.a[kk][ab], acc1[n][ab]);
      if (NSUB == 2) INTERLEAVE(6, 2); else INTERLEAVE(4, 3);
    };
    Frags f0, f1;
    CBAR(); rd(f0);
    SCHED_BAR();
    float up[4];
    {
      const int c = tid & 127, which = (tid >> 7) & 1, half = tid >> 8;
#pragma unroll
      for (int k = 0; k < 4; ++k) up[k] = ld32f(rs, c < cnt * 16 ? (unsigned)WS_U + ((k * 2 + which) * F + (sub0 + (half ? 0 : 54)) * 16 + c) * 4 : 0x7ffffff0u);
    }
    unsigned pv = 0;
    SCHED_BAR();
    STAMP(5);
    auto pin2 = [&]() {
#pragma unroll
      for (int n = 0; n < NSUB; ++n) asm volatile("" : "+v"(acc2[n][0]), "+v"(acc2[n][1]));
      asm volatile("" : "+v"(asum[0]), "+v"(asum[1]), "+v"(asq[0]), "+v"(asq[1]));
    };
    if (STATS) {
      char* cb = ws + CTL_E1 + (G + 1) * 65536;
      unsigned pc;
      poll_issue<8>(pc, cb, wg & (E1_REP - 1), lane);
      CBAR(); rd(f1); mm2(f0);
      if (G == 0) poll_finish<8>(pc, cb, wg & (E1_REP - 1), 256, lane);
      CBAR(); pin2(); rd(f0); mm2(f1);
      CBAR(); rd(f1); mm2(f0);
      poll_issue<8>(pc, cb, wg & (E1_REP - 1), lane);
      CBAR(); rd(f0); mm2(f1);
      if (G == 1) poll_finish<8>(pc, cb, wg & (E1_REP - 1), 128, lane);
      asm volatile("" :: "v"(pc));
      pin2();
    }
#pragma unroll 1
    for (int it = STATS ? 2 : 0; it < 5; ++it) {
      CBAR(); rd(f1); mm2(f0);
      CBAR(); rd(f0); mm2(f1);
    }
    ((LAS float*)(lds + G1_T_U))[tid] = (up[0] + up[1]) + (up[2] + up[3]);
    if (!STATS && ng < 2) pv = *(const unsigned*)((const char*)wsp + (size_t)(mt * 64 + (G * 2 + ng) * 16 + (lane >> 2)) * (L * 2) + j * 512 + (lane & 3) * 128);
    LAS f32x2* tab = (LAS f32x2*)(lds + G1_T_LN2);
    if (STATS) {
#pragma unroll
      for (int ab = 0; ab < 2; ++ab) {
        const float a0 = asum[ab][0], a1 = asum[ab][1], a2 = asum[ab][2], a3 = asum[ab][3];
        const float b0 = asq[ab][0], b1 = asq[ab][1], b2 = asq[ab][2], b3 = asq[ab][3];
        const float s1 = (p & 2) ? ((p & 1) ? a3 : a2) : ((p & 1) ? a1 : a0), s2 = (p & 2) ? ((p & 1) ? b3 : b2) : ((p & 1) ? b1 : b0);
        const float mean = s1 * (1.0f / D);
        float mb, rinv;
        ln_scalars(mean, __builtin_fmaxf(s2 * (1.0f / D) - mean * mean, 0.f), mb, rinv);
        if (rr == q) tab[(G * 2 + ab) * 16 + i16] = f32x2{rinv, rinv * mb};
      }
    }
    {
      const int nb = G ? 3 : 0;
#pragma unroll
      for (int n = 0; n < 3; ++n) {
        const int row = 80 * j + 16 * (nb + n < 5 ? nb + n : 4) + 4 * q + rr;
        x2v[n] = trread(lds + g1_aoff(row, (wave >> 1) & 1, wave & 1, p));
      }
    }
    STAMP(6);
    CBAR(); rd(f1); mm1(f0);
    float zq[NS1][8];
    {
      LAS const float* ut = (LAS const float*)(lds + G1_T_U);
      float v[16];
#pragma unroll
      for (int e = 0; e < 16; ++e) v[e] = 0.f;
      if (NSUB > 0) {
        f32x2 rv[8];
#pragma unroll
        for (int e = 0; e < 8; ++e) rv[e] = tab[(G * 2 + (e >> 2)) * 16 + 4 * q + (e & 3)];
#pragma unroll
        for (int n = 0; n < NSUB; ++n) {
          const float c1 = ut[(n0 + n) * 16 + i16], c2 = ut[128 + (n0 + n) * 16 + i16];
#pragma unroll
          for (int e = 0; e < 8; ++e) {
            const float z = gelu_f(__builtin_fmaf(rv[e].x, acc2[n][e >> 2][e & 3], __builtin_fmaf(-rv[e].y, c1, c2)));
            zq[n][e] = z;
            v[2 * e] = n ? v[2 * e] + z : z; v[2 * e + 1] = n ? __builtin_fmaf(z, z, v[2 * e + 1]) : z * z;
          }
        }
        treduce_dpp<8, 0x140>(v, (i16 & 8) != 0);
        treduce_dpp<4, 0x141>(v, (i16 & 4) != 0);
        treduce_dpp<2, 0x4e>(v, (i16 & 2) != 0);
        treduce_dpp<1, 0xb1>(v, (i16 & 1) != 0);
      }
      ((LAS float*)(lds + G1_T_P))[ng * 128 + G * 64 + lane] = v[0];
    }
    CBAR(); pin1();
    GAS unsigned long long* gr = (GAS unsigned long long*)(ws + CTL_ST3) + (size_t)mt * 1024 + G * 32;
    if (STATS) {
      LAS const float* pp = (LAS const float*)(lds + G1_T_P) + G * 64 + lane;
      const float sv = (pp[0] + pp[128]) + (pp[256] + pp[384]);
      __hip_atomic_store(gr + (j * 2 + (lane & 1)) * 64 + (lane >> 1), (1ull << 32) | f2u(sv), RLX_AGENT);
    }
    SCHED_BAR(); rd(f0); mm1(f1);
    CBAR(); pin1(); rd(f1); mm1(f0);
    CBAR(); pin1(); rd(f0); mm1(f1);
    unsigned long long gx[8];
    if (STATS) {
#pragma unroll
      for (int k = 0; k < 8; ++k) gx[k] = __hip_atomic_load(gr + (k * 2 + (lane >> 5)) * 64 + (lane & 31), RLX_AGENT);
    }
    CBAR(); pin1(); rd(f1); mm1(f0);
    if (STATS) {
      float sv;
      for (unsigned spin = 0;; ++spin) {
        bool ok = true;
        sv = 0.f;
#pragma unroll
        for (int k = 0; k < 8; ++k) { ok &= (gx[k] >> 32) == 1ull; sv += u2f((unsigned)gx[k]); }
        if (wall(ok) || spin > SPIN_CAP) break;
        __builtin_amdgcn_s_sleep(1);
#pragma unroll
        for (int k = 0; k < 8; ++k) gx[k] = __hip_atomic_load(gr + (k * 2 + (lane >> 5)) * 64 + (lane & 31), RLX_AGENT);
      }
      const float s2 = shx(sv, 32);
      const float mean = sv * (1.0f / FH);
      float mb, rinv;
      ln_scalars(mean, __builtin_fmaxf(s2 * (1.0f / FH) - mean * mean, 0.f), mb, rinv);
      if (lane < 32) ((LAS f32x2*)(lds + G1_T_Z))[32 * G + lane] = f32x2{-mb * rinv, rinv};
    }
    CBAR(); pin1();
    if (NSUB > 0) {
      f32x2 zs[8];
#pragma unroll
      for (int e = 0; e < 8; ++e) zs[e] = ((LAS const f32x2*)(lds + G1_T_Z))[32 * G + 8 * q + e];
#pragma unroll
      for (int n = 0; n < NSUB; ++n) {
        v4u o;
#pragma unroll
        for (int e = 0; e < 4; ++e)
          o[e] = pkbf(__builtin_fmaf(zq[n][2 * e], zs[2 * e].y, zs[2 * e].x), __builtin_fmaf(zq[n][2 * e + 1], zs[2 * e + 1].y, zs[2 * e + 1].x));
        st128(rs, (unsigned)WS_Z2 + (((mt * 54 + sub0 + n0 + n) * 2 + G) * 64 + lane) * 16, o);
      }
    }
    SCHED_BAR(); rd(f0); mm1(f1);
    CBAR(); pin1(); rd(f1); mm1(f0);
    CBAR(); pin1(); rd(f0); mm1(f1);
    if (wave_arrive(lds, 3, 8, lane)) signal<4, 4>(ws + CTL_E2 + j * 4096, mt & 3, lane);
    CBAR(); pin1(); rd(f1); mm1(f0);
    asm volatile("" :: "v"(pv));
    CBAR(); pin1();
    mm1(f1);
    STAMP(7);
    {
      LAS const float* ut = (LAS const float*)(lds + G1_T_U) + 256;
      f32x2 rv[2];
#pragma unroll
      for (int ab = 0; ab < 2; ++ab) rv[ab] = tab[(G * 2 + ab) * 16 + i16];
#pragma unroll
      for (int n = 0; n < NSUB; ++n) {
        const f32x4 u1 = *(LAS const f32x4*)(ut + (n0 + n) * 16 + 4 * q), u2 = *(LAS const f32x4*)(ut + 128 + (n0 + n) * 16 + 4 * q);
#pragma unroll
        for (int ab = 0; ab < 2; ++ab) {
          float z[4];
#pragma unroll
          for (int e = 0; e < 4; ++e) z[e] = gelu_f(__builtin_fmaf(rv[ab].x, acc1[n][ab][e], __builtin_fmaf(-rv[ab].y, u1[e], u2[e])));
          z1v[n][ab] = v2u{pkbf(z[0], z[1]), pkbf(z[2], z[3])};
          asm volatile("" : "+v"(z1v[n][ab]));
        }
      }
    }
  }
  if (wave == 1 || wave == 2) {
    const int c = tid - 64;
    if (c < 80) {
      const unsigned o = (unsigned)WS_MOD + (5 * D + 80 * j + c) * 8;
      ((LAS float*)(lds + G3_GB))[c] = bfr((ld32f(rs, o) + ld32f(rs, o + 30720)) + (ld32f(rs, o + 61440) + ld32f(rs, o + 92160)));
    }
  }
  if (wave == 3) wait_count<4>(ws + CTL_E2 + j * 4096, (mt >> 2) & 3, 32, lane);
  CBAR();
  STAMP(9);
  {
    f32x4 acc[NS1][2];
#pragma unroll
    for (int n = 0; n < NS1; ++n) { acc[n][0] = f32x4{0.f, 0.f, 0.f, 0.f}; acc[n][1] = f32x4{0.f, 0.f, 0.f, 0.f}; }
    int woff[2];
#pragma unroll
    for (int kk = 0; kk < 2; ++kk) woff[kk] = G * 4096 + i16 * 128 + (((4 * kk + q) ^ (i16 >> 1)) << 4);
    const int zoff = G2_ZOFF + n0 * 2048 + lane * 16;
    struct Frags { v4u w[2][2]; v4u z[2][NS1]; };
    int wc = 0, zc = 0;
    auto rd = [&](Frags& f) {
      if (NSUB > 0) {
#pragma unroll
        for (int kk = 0; kk < 2; ++kk) {
#pragma unroll
          for (int m = 0; m < 2; ++m) f.w[kk][m] = *(LAS const v4u*)(lds + wc + woff[kk] + m * 2048);
#pragma unroll
          for (int n = 0; n < NSUB; ++n) f.z[kk][n] = *(LAS const v4u*)(lds + zc + zoff + n * 2048 + kk * 1024);
        }
        wc += 8192; if (wc == G2_NW * 8192) wc = 0;
        zc += G2_ZST; if (zc == G2_NZ * G2_ZST) zc = 0;
      }
    };
    auto mm = [&](const Frags& f) {
#pragma unroll
      for (int kk = 0; kk < 2; ++kk)
#pragma unroll
        for (int n = 0; n < NSUB; ++n)
#pragma unroll
          for (int m = 0; m < 2; ++m) acc[n][m] = mfma16(f.z[ABL_KK][n], f.w[ABL_KK][m], acc[n][m]);
      INTERLEAVE(4, 2);
    };
    Frags f0, f1;
    CBAR(); rd(f0);
    STAMP(10);
#pragma unroll 1
    for (int it = 0; it < 15; ++it) {
      CBAR(); rd(f1); mm(f0);
      CBAR(); rd(f0); mm(f1);
    }
    CBAR(); rd(f1); mm(f0);
    mm(f1);
    STAMP(11);
#pragma unroll
    for (int m = 0; m < 2; ++m) {
      v2u o[NS1];
#pragma unroll
      for (int n = 0; n < NSUB; ++n) {
        float g[4];
#pragma unroll
        for (int e = 0; e < 4; ++e) g[e] = acc[n][m][e] + bias[m];
        const v2u zz = z1v[n][m];
        o[n] = v2u{pkbf(bflo(zz.x) * g[0], bfhi(zz.x) * g[1]), pkbf(bflo(zz.y) * g[2], bfhi(zz.y) * g[3])};
      }
      const unsigned vb = (unsigned)WS_V + (mt * 4 + G * 2 + m) * 27 * 1024 + lane * 16;
      if (NSUB == 2) st128(rs, vb + (3 * j + ng) * 1024, v4u{o[0].x, o[0].y, o[NS1 - 1].x, o[NS1 - 1].y});
      if (NSUB == 1) __builtin_amdgcn_raw_buffer_store_b64(o[0], rs, vb + (24 + (j >> 1)) * 1024 + (j & 1) * 8, 0, 16);
    }
    CBAR();
  }
}

template <int KH>
__device__ __forceinline__ void consumer_g3(LAS char* lds, char* ws, __amdgpu_buffer_rsrc_t rs, u16* __restrict__ out, int mt, int j, int m, int lane, const v2u (&x2v)[3] STAMP_ARG) {
  constexpr int K0 = KH ? 14 : 0, NK = KH ? 13 : 14, NF = KH ? 2 : 3, N0 = KH ? 3 : 0;
  const int q = lane >> 4, i16 = lane & 15;
  v4u vf[NK];
  __builtin_amdgcn_s_sleep(V_DELAY);
  CBAR();
  STAMP(12);
  const unsigned vo = (unsigned)WS_V + ((mt * 4 + m) * 27 + K0) * 1024 + lane * 16;
  constexpr int VW = V_WIN;
#pragma unroll
  for (int k = 0; k < VW; ++k) { vf[k] = ld128(rs, vo + k * 1024); SCHED_BAR(); }
  f32x4 acc[5];
#pragma unroll
  for (int n = 0; n < 5; ++n) acc[n] = f32x4{0.f, 0.f, 0.f, 0.f};
  LAS const char* wp = lds + K0 * 1024 + lane * 16;
  LAS const char* wp2 = wp + 54 * 1024;
  LAS const char* wp4 = wp + 108 * 1024;
  v4u wf[3][5];
  auto rdw = [&](int k, v4u (&w)[5]) {
    w[0] = *(LAS const v4u*)(wp + k * 1024); w[1] = *(LAS const v4u*)(wp + (27 + k) * 1024);
    w[2] = *(LAS const v4u*)(wp2 + k * 1024); w[3] = *(LAS const v4u*)(wp2 + (27 + k) * 1024);
    w[4] = *(LAS const v4u*)(wp4 + k * 1024);
  };
  rdw(0, wf[0]); rdw(1, wf[1]);
#pragma unroll
  for (int k = 0; k < NK; ++k) {
    if (k + VW < NK) vf[k + VW] = ld128(rs, vo + (k + VW) * 1024);
    if (k + 2 < NK) rdw(k + 2, wf[(k + 2) % 3]);
    SCHED_BAR();
    if (__builtin_expect(!wall(__builtin_elementwise_max(vf[k].x, vf[k].z) != ~0u), 0)) {
      unsigned spin = 0;
      do { asm volatile("" ::: "memory"); vf[k] = ld128(rs, vo + k * 1024); }
      while (!wall(__builtin_elementwise_max(vf[k].x, vf[k].z) != ~0u) && ++spin < SPIN_CAP);
    }
#pragma unroll
    for (int n = 0; n < 5; ++n) acc[n] = mfma16(wf[k % 3][n], vf[k], acc[n]);
    asm volatile("" : "+v"(acc[0]), "+v"(acc[1]), "+v"(acc[2]), "+v"(acc[3]), "+v"(acc[4]));
    SCHED_BAR();
  }
  STAMP(13);
  f32x4 gb[NF];
#pragma unroll
  for (int n = 0; n < NF; ++n) gb[n] = *(LAS const f32x4*)(lds + G3_GB + (16 * (N0 + n) + 4 * q) * 4);
  LAS char* red = lds + G3_RED + m * 5120 + lane * 16;
#pragma unroll
  for (int n = 0; n < 5; ++n) if ((n >= 3) != (KH != 0)) *(LAS f32x4*)(red + n * 1024) = acc[n];
  CBAR();
  const int tl = 32 * (m >> 1) + 8 * (i16 >> 2) + 4 * (m & 1) + (i16 & 3);
#pragma unroll
  for (int n = 0; n < NF; ++n) {
    const f32x4 o = *(LAS const f32x4*)(red + (N0 + n) * 1024);
    const f32x4 a = KH ? o + acc[N0 + n] : acc[N0 + n] + o;
    const float o0 = __builtin_fmaf(gb[n][0], a[0], bflo(x2v[n].x)), o1 = __builtin_fmaf(gb[n][1], a[1], bfhi(x2v[n].x));
    const float o2 = __builtin_fmaf(gb[n][2], a[2], bflo(x2v[n].y)), o3 = __builtin_fmaf(gb[n][3], a[3], bfhi(x2v[n].y));
    *(LAS v2u*)(lds + tl * 176 + 32 * (N0 + n) + 8 * q) = v2u{pkbf(o0, o1), pkbf(o2, o3)};
  }
  CBAR();
  {
    const int w8 = KH * 4 + m;
#pragma unroll
    for (int i = 0; i < 2; ++i) {
      const int c = lane + 64 * i, r = w8 * 8 + c / 10, pc = c % 10;
      if (c < 80) *(v4u*)((char*)out + ((size_t)(mt * 64 + r) * D + 80 * j) * 2 + pc * 16) = *(LAS const v4u*)(lds + r * 176 + pc * 16);
    }
  }
}

__global__ __launch_bounds__(768, 3) void k_all(Args A) {
  extern __shared__ __attribute__((aligned(16))) char smem[];
  const unsigned long long T0_ = __builtin_amdgcn_s_memrealtime();
  unsigned long long pc;
  asm volatile("s_getpc_b64 %0" : "=s"(pc));
  LAS char* lds = (LAS char*)smem;
  char* ws = A.ws;
  const int tid = __builtin_amdgcn_workitem_id_x(), lane = tid & 63, wave = __builtin_amdgcn_readfirstlane(tid >> 6), wg = __builtin_amdgcn_workgroup_id_x();
  STAMP_DECL; STAMP(0);
  const __amdgpu_buffer_rsrc_t rs = __builtin_amdgcn_make_buffer_rsrc((void*)ws, 0, (int)WS_END, 0x00020000);
  asm volatile("" :: "s"(A.x), "s"(A.Are), "s"(A.Aim), "s"(A.Bre), "s"(A.Bim), "s"(A.Cre), "s"(A.Cim), "s"(A.logdt), "s"(A.dskip), "s"(A.win), "s"(A.wsp),
               "s"(A.bsp), "s"(A.wout), "s"(A.cvec), "s"(A.wmod), "s"(A.ws), "s"(A.out));
  const int grp = wave >> 2, bt = grp == 2 ? 0 : grp + 1, gw = wave & 3, gt = tid & 255;
  const bool isaux = bt == 2 && wg >= 128;
  const int u = wg - 128, d = 256 * bt + (wg & 7) * (bt < 2 ? 32 : 16) + (wg >> 3);
  unsigned epoch;
  asm volatile("global_load_dword %0, %1, %2" : "=v"(epoch) : "v"((unsigned)CTL_EPOCH), "s"(ws) : "memory");
  unsigned pre[7] = {};
  if (__builtin_expect(!isaux, 1)) {
    const unsigned o = (d * NS + (gw & 1) * 64 + lane) * 2;
#define LDU16(dst, base, voff) asm volatile("global_load_ushort %0, %1, %2" NT : "=v"(dst) : "v"(voff), "s"(base) : "memory")
    LDU16(pre[0], A.Are, o); LDU16(pre[1], A.Aim, o); LDU16(pre[2], A.logdt, d * 2); LDU16(pre[3], A.Bre, o);
    LDU16(pre[4], A.Bim, o); LDU16(pre[5], A.Cre, o); LDU16(pre[6], A.Cim, o);
  }
  if (tid == 0) __hip_atomic_store((gu64*)(ws + WS_END + wg * 8), T0_, RLX_AGENT);
  if (tid < 16) ((LAS unsigned*)(lds + M_CTL))[tid] = 0u;
  unsigned tag = 0;
  CBAR();
  {
    LAS char* gl = lds + (grp == 2 ? G1_TAB - C_GRP : grp * C_GRP);
    if (bt == 0) __builtin_amdgcn_s_setprio(2);
    if (bt == 1) __builtin_amdgcn_s_setprio(1);
    if (isaux) { __builtin_amdgcn_s_setprio(3); aux_group(gl, lds, A, rs, epoch, u, gt, gw, lane STAMP_PASS); }
    else conv_group(gl, lds, bt, A, rs, epoch, tag, pre, pc, wg, d, gt, gw, lane STAMP_PASS);
    __builtin_amdgcn_s_setprio(0);
  }
  const int xcd = wg & 7, idx = wg >> 3;
  const int mt = (xcd >> 1) * 8 + (idx & 7), j = (xcd & 1) * 4 + (idx >> 3);
  const int cnt = j < 6 ? 7 : 6, sub0 = j < 6 ? 7 * j : 42 + 6 * (j - 6);
  if (wave >= 8) {
    STAMP(4);
    const int lw = wave - 8;
    if (lw + 12 < 2 * cnt) loader_path<4>(lds, ws, rs, A.wsp, wg, mt, j, sub0, lw, lane);
    else loader_path<3>(lds, ws, rs, A.wsp, wg, mt, j, sub0, lw, lane);
    if (wg == 0 && tid == 512) *(volatile unsigned*)(ws + CTL_EPOCH) = tag;
    STAMP_FLUSH(1);
    return;
  }
  v2u x2v[3];
  const int ng = wave >> 2 ? 3 - (wave & 3) : (wave & 3);
  if (ng < 3) consumer_g12<2, false>(lds, ws, rs, A.bsp, A.wsp, wg, mt, j, sub0, cnt, wave, tid, lane, x2v STAMP_PASS);
  else if (cnt == 7) consumer_g12<1, true>(lds, ws, rs, A.bsp, A.wsp, wg, mt, j, sub0, cnt, wave, tid, lane, x2v STAMP_PASS);
  else consumer_g12<0, true>(lds, ws, rs, A.bsp, A.wsp, wg, mt, j, sub0, cnt, wave, tid, lane, x2v STAMP_PASS);
  if (wave < 4) consumer_g3<0>(lds, ws, rs, A.out, mt, j, wave & 3, lane, x2v STAMP_PASS);
  else consumer_g3<1>(lds, ws, rs, A.out, mt, j, wave & 3, lane, x2v STAMP_PASS);
  if (wg == 5 && tid == 0) {
    unsigned long long mn = ~0ull, mx = 0;
    for (int i = 0; i < 256; ++i) { const unsigned long long t = __hip_atomic_load((gu64*)(ws + WS_END + i * 8), RLX_AGENT); mn = t < mn ? t : mn; mx = t > mx ? t : mx; }
    const unsigned long long d_ = MODE == 0 ? 0 : (MODE == 1 ? (mx - mn) * 10 : (T0_ - mn) * 10), tgt = T0_ + 20000 + d_;
    while (__builtin_amdgcn_s_memrealtime() < tgt) __builtin_amdgcn_s_sleep(1);
  }
}

#if defined(__HIP_DEVICE_COMPILE__) && !defined(KEEP_PK)
#pragma clang attribute pop
#endif

extern "C" void kernel_launch(void* const* d_in, const int* in_sizes, int n_in, void* d_out, int out_size, void* d_ws, size_t ws_size,
                              hipStream_t stream) {
  Args A;
  A.x = (const u16*)d_in[0]; A.Are = (const u16*)d_in[1]; A.Aim = (const u16*)d_in[2]; A.Bre = (const u16*)d_in[3]; A.Bim = (const u16*)d_in[4];
  A.Cre = (const u16*)d_in[5]; A.Cim = (const u16*)d_in[6]; A.logdt = (const u16*)d_in[7]; A.dskip = (const u16*)d_in[8];
  A.win = (const u16*)d_in[10]; A.wsp = (const u16*)d_in[11]; A.bsp = (const u16*)d_in[12]; A.wout = (const u16*)d_in[13];
  A.cvec = (const u16*)d_in[14]; A.wmod = (const u16*)d_in[15];
  A.ws = (char*)d_ws; A.out = (u16*)d_out;
  (void)hipFuncSetAttribute((const void*)k_all, hipFuncAttributeMaxDynamicSharedMemorySize, M_LDS);
  k_all<<<256, 768, M_LDS, stream>>>(A);
}
```

```cpp
#include <hip/hip_runtime.h>
#include <hip/hip_bf16.h>
#include <stdint.h>

#define GAS __attribute__((address_space(1)))
#define LAS __attribute__((address_space(3)))
typedef unsigned v4u __attribute__((ext_vector_type(4)));
typedef unsigned v2u __attribute__((ext_vector_type(2)));
typedef float f32x2 __attribute__((ext_vector_type(2)));
typedef float f32x4 __attribute__((ext_vector_type(4)));
typedef float f32x16 __attribute__((ext_vector_type(16)));
typedef __bf16 bf16x2 __attribute__((ext_vector_type(2)));
typedef __bf16 bf16x8 __attribute__((ext_vector_type(8)));
typedef _Float16 f16x2 __attribute__((ext_vector_type(2)));
typedef _Float16 f16x8 __attribute__((ext_vector_type(8)));
typedef short s16x4 __attribute__((ext_vector_type(4)));
typedef unsigned short u16;

#if defined(__HIP_DEVICE_COMPILE__)
#pragma clang attribute push(__attribute__((target("no-packed-fp32-ops"))), apply_to = function)
#endif
__device__ __forceinline__ float u2f(unsigned u) { return __builtin_bit_cast(float, u); }
__device__ __forceinline__ unsigned f2u(float f) { return __builtin_bit_cast(unsigned, f); }
__device__ __forceinline__ int lane_id() { return __builtin_amdgcn_mbcnt_hi(~0u, __builtin_amdgcn_mbcnt_lo(~0u, 0u)); }
__device__ __forceinline__ unsigned shx(unsigned v, int m) { return __builtin_amdgcn_ds_bpermute((lane_id() ^ m) << 2, v); }
__device__ __forceinline__ float shx(float v, int m) { return u2f(shx(f2u(v), m)); }
__device__ __forceinline__ bool wall(bool p) { return __builtin_amdgcn_ballot_w64(!p) == 0ull; }
__device__ __forceinline__ float exp_acc(float x) {
  const float hi = x * 1.4426950408889634f;
  const float lo = __builtin_fmaf(x, 1.4426950408889634f, -hi) + x * 1.925963033500414e-8f;
  const float e = __builtin_amdgcn_exp2f(hi);
  return __builtin_fmaf(e, lo * 0.6931471805599453f, e);
}

constexpr int L = 2048, D = 640, NS = 128, F = 1728, FH = 864;

constexpr size_t CTL_E1 = 0;
constexpr size_t CTL_E1B = 196608;
constexpr size_t CTL_E2 = 262144;
constexpr size_t CTL_ST3 = 327680;
constexpr size_t CTL_F0 = CTL_ST3 + 262144;
constexpr size_t CTL_EPOCH = CTL_F0 + 32768;
constexpr size_t WS_MOD = CTL_EPOCH + 4096;
constexpr size_t WS_ST1 = WS_MOD + 131072;
constexpr size_t WS_U = WS_ST1 + 16384;
constexpr size_t WS_XT = WS_U + 65536;
constexpr size_t WS_X2T = WS_XT + (size_t)D * L * 2;
constexpr size_t WS_WIN = WS_X2T + (size_t)D * L * 2;
constexpr size_t WS_WOUT = WS_WIN + (size_t)D * F * 2;
constexpr size_t WS_Z2 = WS_WOUT + (size_t)FH * D * 2;
constexpr size_t WS_V = WS_Z2 + (size_t)L * FH * 2;
constexpr size_t WS_END = WS_V + (size_t)L * FH * 2;

__device__ __forceinline__ float bflo(unsigned u) { return u2f(u << 16); }
__device__ __forceinline__ float bfhi(unsigned u) { return u2f(u & 0xffff0000u); }
__device__ __forceinline__ float bf2f(u16 v) { return u2f((unsigned)v << 16); }
__device__ __forceinline__ unsigned pkbf(float a, float b) {
  f32x2 v = {a, b};
  return __builtin_bit_cast(unsigned, __builtin_convertvector(v, bf16x2));
}
__device__ __forceinline__ unsigned pkh(float a, float b) {
  f32x2 v = {a, b};
  return __builtin_bit_cast(unsigned, __builtin_convertvector(v, f16x2));
}
__device__ __forceinline__ float bfr(float a) { return bflo(pkbf(a, a)); }
__device__ __forceinline__ v2u trread(LAS const char* p) {
  return __builtin_bit_cast(v2u, __builtin_amdgcn_ds_read_tr16_b64_v4i16((LAS s16x4*)p));
}
__device__ __forceinline__ f32x4 mfma16(v4u a, v4u b, f32x4 c) {
  return __builtin_amdgcn_mfma_f32_16x16x32_bf16(__builtin_bit_cast(bf16x8, a), __builtin_bit_cast(bf16x8, b), c, 0, 0, 0);
}
__device__ __forceinline__ f32x16 mfma32(v4u a, v4u b, f32x16 c) {
  return __builtin_amdgcn_mfma_f32_32x32x16_bf16(__builtin_bit_cast(bf16x8, a), __builtin_bit_cast(bf16x8, b), c, 0, 0, 0);
}
__device__ __forceinline__ f32x16 mfma32h(v4u a, v4u b, f32x16 c) {
  return __builtin_amdgcn_mfma_f32_32x32x16_f16(__builtin_bit_cast(f16x8, a), __builtin_bit_cast(f16x8, b), c, 0, 0, 0);
}
#define GL_(i, s) "global_load_lds_dwordx4 %" #i ", %5 offset:" s
template <int N, bool DEV>
__device__ __forceinline__ void gldsN(const void* sbase, unsigned v0, unsigned v1, unsigned v2, unsigned v3, unsigned dst) {
  unsigned keep;
#define GLN_(body) asm volatile("s_mov_b32 %0, m0\n\ts_mov_b32 m0, %6\n\ts_nop 0\n\t" body "s_mov_b32 m0, %0" : "=&s"(keep) : "v"(v0), "v"(v1), "v"(v2), "v"(v3), "s"(sbase), "s"(dst) : "memory")
  if (DEV) {
    if (N == 1) GLN_(GL_(1, "0 sc1\n\t"));
    if (N == 2) GLN_(GL_(1, "0 sc1\n\t") GL_(2, "1024 sc1\n\t"));
    if (N == 3) GLN_(GL_(1, "0 sc1\n\t") GL_(2, "1024 sc1\n\t") GL_(3, "2048 sc1\n\t"));
    if (N == 4) GLN_(GL_(1, "0 sc1\n\t") GL_(2, "1024 sc1\n\t") GL_(3, "2048 sc1\n\t") GL_(4, "3072 sc1\n\t"));
  } else {
    if (N == 2) GLN_(GL_(1, "0\n\t") GL_(2, "1024\n\t"));
    if (N == 4) GLN_(GL_(1, "0\n\t") GL_(2, "1024\n\t") GL_(3, "2048\n\t") GL_(4, "3072\n\t"));
  }
}
template <int N> __device__ __forceinline__ void gldsL(const void* sbase, unsigned vo, unsigned dst) { gldsN<N, true>(sbase, vo, vo, vo, vo, dst); }
template <int N> __device__ __forceinline__ void waitv() { asm volatile("s_waitcnt vmcnt(%0)" :: "i"(N) : "memory"); }
__device__ __forceinline__ float dot2bf(unsigned a, unsigned b, float c) {
  return __builtin_amdgcn_fdot2_f32_bf16(__builtin_bit_cast(bf16x2, a), __builtin_bit_cast(bf16x2, b), c, false);
}
#define SCHED_BAR() __builtin_amdgcn_sched_barrier(0)
#define CBAR() do { SCHED_BAR(); __builtin_amdgcn_s_waitcnt(0xC07F); asm volatile("" ::: "memory"); __builtin_amdgcn_s_barrier(); asm volatile("" ::: "memory"); } while (0)
#define LBAR() __builtin_amdgcn_s_barrier()

template <int CTRL> __device__ __forceinline__ float dppf(float v) {
  return __builtin_bit_cast(float, __builtin_amdgcn_update_dpp(0, __builtin_bit_cast(int, v), CTRL, 0xf, 0xf, true));
}
template <int N, int CTRL> __device__ __forceinline__ void treduce_dpp(float* v, bool up) {
#pragma unroll
  for (int e = 0; e < N; ++e) {
    const float a = v[e], b = v[e + N];
    const float send = up ? a : b, keep = up ? b : a;
    v[e] = keep + dppf<CTRL>(send);
  }
}
template <int N> __device__ __forceinline__ void treduce_xor(float* v, bool up, int mask) {
#pragma unroll
  for (int e = 0; e < N; ++e) {
    const float a = v[e], b = v[e + N];
    const float send = up ? a : b, keep = up ? b : a;
    v[e] = keep + shx(send, mask);
  }
}

__device__ __forceinline__ float ln_std(float var) { return __builtin_amdgcn_sqrtf(bfr(bfr(var) + 1e-5f)); }
__device__ __forceinline__ void ln_scalars(float mean, float var, float& m_b, float& rinv) {
  m_b = bfr(mean);
  rinv = __builtin_amdgcn_rcpf(bfr(ln_std(var)));
}

struct cplx { float r, i; };
__device__ __forceinline__ cplx cmul(cplx a, cplx b) { return {a.r * b.r - a.i * b.i, a.r * b.i + a.i * b.r}; }

typedef GAS unsigned gu32;
#define RLX_AGENT __ATOMIC_RELAXED, __HIP_MEMORY_SCOPE_AGENT
#define INTERLEAVE(n, r) do { _Pragma("unroll") for (int i_ = 0; i_ < (n); ++i_) { __builtin_amdgcn_sched_group_barrier(0x008, 1, 0); __builtin_amdgcn_sched_group_barrier(0x100, (r), 0); } \
    __builtin_amdgcn_sched_group_barrier(0x008, 8, 0); SCHED_BAR(); } while (0)
constexpr int F0_REP = 8, V_WIN = 5, E1_REP = 32, XGATE = 16;
constexpr unsigned SPIN_CAP = 1u << 16;
constexpr int M_LDS = 163840, M_CTL = M_LDS - 64;
constexpr int CODE_LINES = 400;
constexpr int C_KOFF = 0, C_UOFF = 4224, C_UP = 1536, C_COFF = C_UOFF + 4 * C_UP, C_GRP = C_COFF + 4 * 64 * 144;
constexpr int G1_BOFF = 81920, G1_BST = 14336, G1_NSLOT = 5, G1_TAB = G1_BOFF + G1_BST * G1_NSLOT;
constexpr int G1_T_LN2 = G1_TAB, G1_T_U = G1_T_LN2 + 512, G1_T_P = G1_T_U + 2048, G1_T_Z = G1_T_P + 2048;
constexpr int G2_NW = 9, G2_NZ = 6, G2_ZOFF = G2_NW * 8192, G2_ZST = 14336;
constexpr int G3_RED = 135 * 1024, G3_GB = 161024;
static_assert(3 * C_GRP <= M_CTL && G1_T_Z + 512 <= M_CTL && G2_ZOFF + G2_NZ * G2_ZST <= M_CTL && G3_GB + 320 <= M_CTL, "lds");

__device__ __forceinline__ void st128(__amdgpu_buffer_rsrc_t rs, unsigned off, v4u v) { __builtin_amdgcn_raw_buffer_store_b128(v, rs, off, 0, 16); }
__device__ __forceinline__ v4u ld128(__amdgpu_buffer_rsrc_t rs, unsigned off) { return __builtin_amdgcn_raw_buffer_load_b128(rs, off, 0, 16); }

__device__ __forceinline__ bool wave_arrive(LAS char* lds, int word, unsigned n, int lane) {
  waitv<0>();
  unsigned old = 0;
  if (lane == 0) old = __hip_atomic_fetch_add((LAS unsigned*)(lds + M_CTL) + word, 1u, __ATOMIC_RELAXED, __HIP_MEMORY_SCOPE_WORKGROUP);
  return (unsigned)__builtin_amdgcn_readfirstlane(old) == n - 1;
}
__device__ __forceinline__ void lds_wait(LAS char* lds0, int word, unsigned target) {
  LAS unsigned* p = (LAS unsigned*)(lds0 + M_CTL) + word;
  while ((unsigned)__builtin_amdgcn_readfirstlane(__hip_atomic_load(p, __ATOMIC_RELAXED, __HIP_MEMORY_SCOPE_WORKGROUP)) < target) __builtin_amdgcn_s_sleep(1);
  asm volatile("" ::: "memory");
}
__device__ __forceinline__ void lds_post(LAS char* lds0, int word, int lane) {
  SCHED_BAR();
  asm volatile("" ::: "memory");
  if (lane == 0) __hip_atomic_fetch_add((LAS unsigned*)(lds0 + M_CTL) + word, 1u, __ATOMIC_RELAXED, __HIP_MEMORY_SCOPE_WORKGROUP);
  asm volatile("" ::: "memory");
}
__device__ __forceinline__ void group_sync(LAS char* lds0, int word, unsigned target, int lane) { lds_post(lds0, word, lane); lds_wait(lds0, word, target); }
template <int NREP, int NSH> __device__ __forceinline__ void signal(char* base, int shard, int lane) {
  if (lane < NREP) __hip_atomic_fetch_add((gu32*)(base + (lane * NSH + shard) * 256), 1u, RLX_AGENT);
}
template <int NSH> __device__ __forceinline__ bool count_ok(unsigned c, unsigned total) {
  if (NSH > 1) c += __builtin_amdgcn_update_dpp(0, c, 0xb1, 0xf, 0xf, true);
  if (NSH > 2) c += __builtin_amdgcn_update_dpp(0, c, 0x4e, 0xf, 0xf, true);
  if (NSH > 4) c += __builtin_amdgcn_update_dpp(0, c, 0x141, 0xf, 0xf, true);
  return (unsigned)__builtin_amdgcn_readfirstlane(c) >= total;
}
template <int NSH> __device__ __forceinline__ void wait_count(char* base, int rep, unsigned total, int lane) {
  gu32* p = (gu32*)(base + (rep * NSH + (lane & (NSH - 1))) * 256);
#pragma unroll 1
  for (unsigned spin = 0; spin < SPIN_CAP; ++spin) {
    if (count_ok<NSH>(__hip_atomic_load(p, RLX_AGENT), total)) break;
    __builtin_amdgcn_s_sleep(6);
  }
  __builtin_amdgcn_fence(__ATOMIC_ACQUIRE, "wavefront");
}
template <int NSH> __device__ __forceinline__ void poll_issue(unsigned& c, char* base, int rep, int lane) {
  c = __hip_atomic_load((gu32*)(base + (rep * NSH + (lane & (NSH - 1))) * 256), RLX_AGENT);
}
template <int NSH> __device__ __forceinline__ void poll_finish(unsigned c, char* base, int rep, unsigned total, int lane) {
  if (!count_ok<NSH>(c, total)) wait_count<NSH>(base, rep, total, lane);
  __builtin_amdgcn_fence(__ATOMIC_ACQUIRE, "wavefront");
}

__device__ __forceinline__ float gelu_f(float x) {
  const float p = __builtin_fmaf(x * x, -0.044715f * 2.302208198f, -2.302208198f);
  const float e = __builtin_amdgcn_exp2f(x * p);
  return x * __builtin_amdgcn_rcpf(1.0f + e);
}

__device__ __forceinline__ float ld32f(__amdgpu_buffer_rsrc_t rs, unsigned off) { return u2f(__builtin_amdgcn_raw_buffer_load_b32(rs, off, 0, 16)); }
__device__ __forceinline__ void st32(__amdgpu_buffer_rsrc_t rs, unsigned off, unsigned v) { __builtin_amdgcn_raw_buffer_store_b32(v, rs, off, 0, 16); }

struct Args {
  const u16 *x, *Are, *Aim, *Bre, *Bim, *Cre, *Cim, *logdt, *dskip, *win, *wsp, *bsp, *wout, *cvec, *wmod;
  char* ws; u16* out;
};

typedef GAS unsigned long long gu64;
__device__ __forceinline__ void put_granule(char* p, unsigned v, unsigned tag) { __hip_atomic_store((gu64*)p, ((unsigned long long)tag << 32) | v, RLX_AGENT); }
template <int N, class F> __device__ __forceinline__ void get_granules(unsigned (&v)[N], unsigned tag, F addr) {
#pragma unroll 1
  for (unsigned spin = 0; spin < SPIN_CAP; ++spin) {
    unsigned long long x[N];
#pragma unroll
    for (int k = 0; k < N; ++k) x[k] = __hip_atomic_load((gu64*)addr(k), RLX_AGENT);
    bool ok = true;
#pragma unroll
    for (int k = 0; k < N; ++k) { v[k] = (unsigned)x[k]; ok &= (unsigned)(x[k] >> 32) == tag; }
    if (wall(ok)) break;
    __builtin_amdgcn_s_sleep(3);
  }
}

__device__ __forceinline__ void conv_group(LAS char* lds, LAS char* lds0, int bt, const Args& A, __amdgpu_buffer_rsrc_t rs, unsigned epoch, unsigned& tag, unsigned (&pre)[7], unsigned long long pc,
                                           int wg, int d, int tid, int wave, int lane) {
  char* ws = A.ws;
  float S[8], soff;
  v4u xv, st[2];
  unsigned cpf = 0;
  {
    const int half = wave & 1;
    int xoff[8];
#pragma unroll
    for (int j = 0; j < 8; ++j) {
      const int l = tid * 8 + j;
      const int idx = l < 128 ? l : (int)(pkbf((float)l, 0.f) & 0xffffu) - 0x4300 + 128;
      xoff[j] = (idx + (idx >> 3)) * 8;
    }
    asm volatile("" : "+v"(xoff[0]), "+v"(xoff[1]), "+v"(xoff[2]), "+v"(xoff[3]), "+v"(xoff[4]), "+v"(xoff[5]), "+v"(xoff[6]), "+v"(xoff[7]));
    waitv<0>();
    asm volatile("v_readfirstlane_b32 %0, %1\n\ts_nop 3" : "=s"(tag) : "v"(epoch) : "memory");
    tag += 1u;
    asm volatile("" : "+v"(pre[0]), "+v"(pre[1]), "+v"(pre[2]), "+v"(pre[3]), "+v"(pre[4]), "+v"(pre[5]), "+v"(pre[6]));
    {
      const int so = ((wg >> 3) * 4 + wave) * 4 + (lane & 3);
      cpf = *(const GAS unsigned*)((pc & ~255ull) + (size_t)(bt == 1 && so < CODE_LINES ? so : 0) * 128);
    }
    const unsigned vare = pre[0], vaim = pre[1], vdt = pre[2], vbre = pre[3], vbim = pre[4], vcre = pre[5], vcim = pre[6];
    if (tid < 4) *(LAS v4u*)(lds + C_KOFF + tid * 16) = v4u{0u, 0u, 0u, 0u};
    const float dt = bfr(exp_acc(bflo(vdt)));
    const float ar2 = dt * bflo(vare) * 1.4426950408889634f, ai2 = dt * bflo(vaim) * 0.15915494309189535f;
    auto E = [&](float xx) {
      const float mag = __builtin_amdgcn_exp2f(ar2 * xx), ph = __builtin_amdgcn_fractf(ai2 * xx);
      return cplx{mag * __builtin_amdgcn_cosf(ph), mag * __builtin_amdgcn_sinf(ph)};
    };
    LAS char* img = lds + C_COFF + wave * 8192 + lane * 128;
    const int sw = lane & 7;
    float isc = 0.f;
    if (wave < 2) {
      const cplx e8 = E(8.f), e64 = E(64.f), e128 = E(128.f), e192 = E(192.f);
      cplx q[8];
      q[0] = {1.f, 0.f}; q[1] = e8; q[2] = cmul(e8, e8); q[3] = cmul(q[2], e8); q[4] = cmul(q[2], q[2]);
      q[5] = cmul(q[4], e8); q[6] = cmul(q[4], q[2]); q[7] = cmul(q[4], q[3]);
#pragma unroll
      for (int g = 0; g < 4; ++g) {
        cplx v[8];
#pragma unroll
        for (int j = 0; j < 8; ++j) v[j] = g == 0 ? q[j] : cmul(q[j], g == 1 ? e64 : (g == 2 ? e128 : e192));
        *(LAS v4u*)(img + ((g ^ sw) << 4)) = v4u{pkh(v[0].r, v[1].r), pkh(v[2].r, v[3].r), pkh(v[4].r, v[5].r), pkh(v[6].r, v[7].r)};
        *(LAS v4u*)(img + (((4 + g) ^ sw) << 4)) = v4u{pkh(v[0].i, v[1].i), pkh(v[2].i, v[3].i), pkh(v[4].i, v[5].i), pkh(v[6].i, v[7].i)};
      }
    } else {
      cplx W = cmul(cplx{bflo(vcre), bflo(vcim)}, cplx{dt * bflo(vbre), dt * bflo(vbim)});
      float mx = __builtin_fmaxf(__builtin_fabsf(W.r), __builtin_fabsf(W.i));
      mx = __builtin_fmaxf(mx, dppf<0xb1>(mx)); mx = __builtin_fmaxf(mx, dppf<0x4e>(mx)); mx = __builtin_fmaxf(mx, dppf<0x141>(mx)); mx = __builtin_fmaxf(mx, dppf<0x140>(mx));
      const unsigned mb = f2u(mx);
      const unsigned m01 = __builtin_elementwise_max((unsigned)__builtin_amdgcn_readlane(mb, 0), (unsigned)__builtin_amdgcn_readlane(mb, 16));
      const unsigned m23 = __builtin_elementwise_max((unsigned)__builtin_amdgcn_readlane(mb, 32), (unsigned)__builtin_amdgcn_readlane(mb, 48));
      int ex = (int)((__builtin_elementwise_max(m01, m23) >> 23) & 0xff);
      ex = ex < 20 ? 20 : (ex > 240 ? 240 : ex);
      const float sc = u2f((unsigned)(267 - ex) << 23);
      isc = u2f((unsigned)(ex - 13) << 23);
      W.r *= sc; W.i *= sc;
      const cplx e1 = E(1.f), e2 = cmul(e1, e1), e4 = cmul(e2, e2);
      cplx h[24];
      h[0] = W; h[1] = cmul(W, e1); h[2] = cmul(W, e2); h[3] = cmul(h[2], e1);
      h[4] = cmul(W, e4); h[5] = cmul(h[4], e1); h[6] = cmul(h[4], e2); h[7] = cmul(h[6], e1);
      const cplx g1 = E(256.f), g2 = E(512.f), g3 = E(768.f), g4 = E(1024.f);
      h[8] = cmul(h[0], g1); h[9] = cmul(h[2], g1); h[10] = cmul(h[4], g1); h[11] = cmul(h[6], g1);
      h[12] = cmul(h[0], g2); h[13] = cmul(h[4], g2); h[14] = cmul(h[0], g3); h[15] = cmul(h[4], g3);
      h[16] = cmul(h[0], g4); h[17] = cmul(h[8], g4); h[18] = cmul(h[12], g4); h[19] = cmul(h[14], g4);
      h[20] = cmul(h[16], g4); h[21] = {0.f, 0.f}; h[22] = {0.f, 0.f}; h[23] = {0.f, 0.f};
#pragma unroll
      for (int g = 0; g < 3; ++g) {
        const cplx* v = h + 8 * g;
        *(LAS v4u*)(img + ((g ^ sw) << 4)) = v4u{pkh(v[0].r, v[1].r), pkh(v[2].r, v[3].r), pkh(v[4].r, v[5].r), pkh(v[6].r, v[7].r)};
        *(LAS v4u*)(img + (((4 + g) ^ sw) << 4)) = v4u{pkh(-v[0].i, -v[1].i), pkh(-v[2].i, -v[3].i), pkh(-v[4].i, -v[5].i), pkh(-v[6].i, -v[7].i)};
      }
      *(LAS v4u*)(img + ((3 ^ sw) << 4)) = v4u{0u, 0u, 0u, 0u};
      *(LAS v4u*)(img + ((7 ^ sw) << 4)) = v4u{0u, 0u, 0u, 0u};
    }
    group_sync(lds0, 5 + bt, 4, lane);
    if (wave >= 2) {
      const int hh = lane >> 5, blk = (lane >> 4) & 1, qq = (lane & 15) >> 2, p = lane & 3;
      LAS const char* Qi = lds + C_COFF + half * 8192;
      v2u a[8][2], bb[8][2];
#pragma unroll
      for (int ks = 0; ks < 8; ++ks)
#pragma unroll
        for (int t = 0; t < 2; ++t) {
          const int k = 16 * ks + 8 * hh + 4 * t + qq, mode = k >> 1, c = k & 1;
          const int off = mode * 128 + (((c * 4 + 2 * blk + (p >> 1)) ^ (mode & 7)) << 4) + (p & 1) * 8;
          a[ks][t] = trread(Qi + 16384 + off);
          bb[ks][t] = trread(Qi + off);
        }
      SCHED_BAR();
      f32x16 acc = {};
#pragma unroll
      for (int ks = 0; ks < 8; ++ks) acc = mfma32h(v4u{a[ks][0].x, a[ks][0].y, a[ks][1].x, a[ks][1].y}, v4u{bb[ks][0].x, bb[ks][0].y, bb[ks][1].x, bb[ks][1].y}, acc);
      LAS float* T = (LAS float*)(lds + C_UOFF) + half;
      const int q = lane & 31, hi = lane >> 5;
      auto put = [&](int idx, float v) { T[(idx + (idx >> 3)) * 2] = v * isc; };
#pragma unroll
      for (int r = 0; r < 4; ++r) put((r + 4 * hi) + 8 * q, acc[r]);
#pragma unroll
      for (int r = 0; r < 4; ++r) put(hi ? 384 + 64 * (r >> 1) + (r & 1) + 2 * q : 256 + r + 4 * q, acc[4 + r]);
      if (hi == 0) {
#pragma unroll
        for (int r = 0; r < 4; ++r) put(512 + 32 * r + q, acc[8 + r]);
      } else if (q == 0) put(640, acc[8]);
    }
    lds_post(lds0, 5 + bt, lane);
    auto load_x = [&]() {
      xv = ld128(rs, (unsigned)WS_XT + (tid * D + d) * 16);
#pragma unroll
      for (int i = 0; i < 2; ++i) st[i] = ld128(rs, (unsigned)WS_ST1 + (i * 256 + tid) * 16);
      SCHED_BAR();
    };
    const bool early = __builtin_amdgcn_readfirstlane(__hip_atomic_load((LAS unsigned*)(lds0 + M_CTL) + (bt ? 5 : 8), __ATOMIC_RELAXED, __HIP_MEMORY_SCOPE_WORKGROUP)) >= (bt ? XGATE : 1);
    asm volatile("" ::: "memory");
    if (early) load_x();
    lds_wait(lds0, 5 + bt, 8);
    {
      f32x2 tv[8];
#pragma unroll
      for (int j = 0; j < 8; ++j) tv[j] = *(LAS const f32x2*)(lds + C_UOFF + xoff[j]);
      SCHED_BAR();
      float kv[8];
#pragma unroll
      for (int j = 0; j < 8; ++j) kv[j] = tv[j][0] + tv[j][1];
      *(LAS v4u*)(lds + C_KOFF + 64 + tid * 16) = v4u{pkbf(kv[0], kv[1]), pkbf(kv[2], kv[3]), pkbf(kv[4], kv[5]), pkbf(kv[6], kv[7])};
      S[0] = kv[0];
#pragma unroll
      for (int j = 1; j < 8; ++j) S[j] = S[j - 1] + kv[j];
      float inc = S[7];
      inc += dppf<0x111>(inc); inc += dppf<0x112>(inc); inc += dppf<0x114>(inc); inc += dppf<0x118>(inc);
      inc += __builtin_bit_cast(float, __builtin_amdgcn_update_dpp(0, __builtin_bit_cast(int, inc), 0x142, 0xa, 0xf, false));
      inc += __builtin_bit_cast(float, __builtin_amdgcn_update_dpp(0, __builtin_bit_cast(int, inc), 0x143, 0xc, 0xf, false));
      soff = inc - S[7];
      if (lane == 63) ((LAS float*)(lds + C_KOFF + 4160))[wave] = inc;
    }
    lds_post(lds0, 5 + bt, lane);
    if (!early) {
      if (bt == 0 && wave == 0) {
#pragma unroll 1
        for (unsigned spin = 0; spin < SPIN_CAP; ++spin) {
          asm volatile("" ::: "memory");
          const v4u f = ld128(rs, (unsigned)CTL_F0 + (wg & (F0_REP - 1)) * 1024 + lane * 16);
          if (wall(f.x == tag && f.y == ~tag && f.z == tag && f.w == ~tag)) break;
          __builtin_amdgcn_s_sleep(2);
        }
        __builtin_amdgcn_fence(__ATOMIC_ACQUIRE, "wavefront");
        lds_post(lds0, 8, lane);
      } else if (bt == 0) lds_wait(lds0, 8, 1);
      else lds_wait(lds0, 5, XGATE);
      load_x();
    }
  }
  lds_wait(lds0, 5 + bt, 12);
  float xf[8], xn[8];
  {
#pragma unroll
    for (int j = 0; j < 4; ++j) { xf[2 * j] = bflo(xv[j]); xf[2 * j + 1] = bfhi(xv[j]); }
    const f32x4 wt = *(LAS const f32x4*)(lds + C_KOFF + 4160);
    if (tid < 128) *(LAS v4u*)(lds + C_UOFF + (tid >> 5) * C_UP + (tid & 31) * 16) = v4u{0u, 0u, 0u, 0u};
#pragma unroll
    for (int j = 0; j < 8; ++j) {
      const unsigned sj = st[j >> 2][j & 3];
      xn[j] = (xf[j] - bflo(sj)) * __builtin_amdgcn_rcpf(bfhi(sj));
    }
#pragma unroll
    for (int e = 0; e < 4; ++e) *(LAS unsigned*)(lds + C_UOFF + e * C_UP + 512 + tid * 4) = pkbf(xn[e], xn[e + 4]);
    soff += (wave > 0 ? wt[0] : 0.f) + (wave > 1 ? wt[1] : 0.f) + (wave > 2 ? wt[2] : 0.f);
  }
  const u16 dskv = A.dskip[d];
  char* mgp = ws + WS_MOD + (size_t)((lane & 3) * 3840 + (lane < 12 ? lane >> 2 : 0) * D + d) * 8;
  const unsigned long long mg = __hip_atomic_load((gu64*)mgp, RLX_AGENT);
  group_sync(lds0, 5 + bt, 16, lane);
  f32x16 y0 = {}, y1 = {};
  {
    const int hh = lane >> 5, blk = (lane >> 4) & 1, qq = (lane & 15) >> 2, p = lane & 3, col = lane & 31;
    LAS const char* ab = lds + C_KOFF + (32 + 16 * blk + 4 * p - 4 * qq + 32 * hh) * 2 - 32;
    LAS const char* bb = lds + C_UOFF + wave * C_UP + (256 + 8 * (col - hh) - 240) * 2;
    struct CF { v2u a0l, a0h, a1l, a1h; v4u b0, b1; };
    CF cf[3];
    auto rdc = [&](int ks, CF& f) {
      f.a0l = trread(ab + ks * 128 + 32); f.a0h = trread(ab + ks * 128);
      f.b0 = *(LAS const v4u*)(bb + (15 - ks) * 32);
      f.b1 = *(LAS const v4u*)(bb + (31 - ks) * 32);
      f.a1l = trread(ab + (ks + 16) * 128 + 32); f.a1h = trread(ab + (ks + 16) * 128);
    };
    rdc(0, cf[0]); rdc(1, cf[1]);
    SCHED_BAR();
#pragma unroll
    for (int ks = 0; ks < 16; ++ks) {
      if (ks + 2 < 16) rdc(ks + 2, cf[(ks + 2) % 3]);
      const CF& f = cf[ks % 3];
      const v4u a0 = {f.a0l.x, f.a0l.y, f.a0h.x, f.a0h.y}, a1 = {f.a1l.x, f.a1l.y, f.a1h.x, f.a1h.y};
      y0 = mfma32(a0, f.b0, y0);
      y1 = mfma32(a0, f.b1, y1);
      y1 = mfma32(a1, f.b0, y1);
      INTERLEAVE(3, 2);
    }
    LAS char* cb = lds + C_COFF + (wave * 64 + col) * 144 + hh * 16;
#pragma unroll
    for (int g = 0; g < 4; ++g) {
      *(LAS f32x4*)(cb + g * 32) = f32x4{y0[4 * g], y0[4 * g + 1], y0[4 * g + 2], y0[4 * g + 3]};
      *(LAS f32x4*)(cb + 32 * 144 + g * 32) = f32x4{y1[4 * g], y1[4 * g + 1], y1[4 * g + 2], y1[4 * g + 3]};
    }
  }
  lds_post(lds0, 5 + bt, lane);
  unsigned mv[1] = {(unsigned)mg};
  if (!wall((unsigned)(mg >> 32) == tag)) get_granules<1>(mv, tag, [&](int) { return mgp; });
  float msum = u2f(mv[0]);
  msum += dppf<0xb1>(msum); msum += dppf<0x4e>(msum);
  const unsigned mb = f2u(bfr(msum));
  const float sa = u2f(__builtin_amdgcn_readlane(mb, 0)), c1a = bfr(1.0f + u2f(__builtin_amdgcn_readlane(mb, 4))), ga = u2f(__builtin_amdgcn_readlane(mb, 8)), dsk = bf2f(dskv);
  const float gc = ga * c1a, gs_ = ga * sa, gd = gc * dsk, g0 = gs_ * (soff + dsk);
#pragma unroll
  for (int j = 0; j < 8; ++j) xf[j] = __builtin_fmaf(gd, xn[j], __builtin_fmaf(gs_, S[j], xf[j] + g0));
  lds_wait(lds0, 5 + bt, 20);
  float y[8];
  {
    const int c1 = 2 * tid, c0 = tid ? c1 - 1 : 0;
    const int o0 = (c0 >> 3) * 144 + (c0 & 7) * 16, o1 = (c1 >> 3) * 144 + (c1 & 7) * 16;
    LAS const char* cb = lds + C_COFF;
    f32x4 w0[4], w1[4], w2[4];
#pragma unroll
    for (int e = 0; e < 4; ++e) {
      w1[e] = *(LAS const f32x4*)(cb + e * 64 * 144 + o1); w2[e] = *(LAS const f32x4*)(cb + e * 64 * 144 + o1 + 16);
      if (e) w0[e] = *(LAS const f32x4*)(cb + e * 64 * 144 + o0);
    }
    SCHED_BAR();
#pragma unroll
    for (int e = 0; e < 4; ++e) {
      float w[12];
#pragma unroll
      for (int j = 0; j < 4; ++j) { w[4 + j] = w1[e][j]; w[8 + j] = w2[e][j]; w[j] = (e && tid) ? w0[e][j] : 0.f; }
#pragma unroll
      for (int j = 0; j < 8; ++j) y[j] = e ? y[j] + w[4 + j - e] : w[4 + j];
    }
  }
  float o[8];
#pragma unroll
  for (int j = 0; j < 8; ++j) o[j] = __builtin_fmaf(gc, y[j], xf[j]);
  {
    const unsigned a0 = pkbf(o[0], o[1]), a1 = pkbf(o[2], o[3]), b0 = pkbf(o[4], o[5]), b1 = pkbf(o[6], o[7]);
    const bool odd = tid & 1;
    const unsigned s0 = odd ? a0 : b0, s1 = odd ? a1 : b1;
    const unsigned r0 = __builtin_amdgcn_update_dpp(0, s0, 0xb1, 0xf, 0xf, true), r1 = __builtin_amdgcn_update_dpp(0, s1, 0xb1, 0xf, 0xf, true);
    const v4u ov = odd ? v4u{r0, r1, b0, b1} : v4u{a0, a1, r0, r1};
    st128(rs, (unsigned)WS_X2T + d * (L * 2) + (tid >> 2) * 64 + (tid & 1) * 32 + (tid & 2) * 8, ov);
  }
  asm volatile("" :: "v"(cpf));
  if (wave_arrive(lds0, bt, 4, lane)) signal<E1_REP, 8>(ws + CTL_E1 + bt * 65536, wg & 7, lane);
}

__device__ __forceinline__ void aux_group(LAS char* lds, LAS char* lds0, const Args& A, __amdgpu_buffer_rsrc_t rs, unsigned epoch, int u, int tid, int wave, int lane) {
  char* ws = A.ws;
  unsigned gs = 0;
  v4u wb[5], ov[3], mw[10];
  u16 mc;
  const int mpart = u & 3, mcol = (u < 120 ? u >> 2 : 0) * 128;
  unsigned tag;
  {
    const int r = tid >> 4, s = tid & 15, q = lane >> 4;
    v4u v[5];
    {
      const u16* xp = A.x + (size_t)(u * 16 + r) * D + (s ^ ((q | ((wave >> 1) << 2)) << 1)) * 8;
#pragma unroll
      for (int i = 0; i < 5; ++i) v[i] = *((const v4u*)(xp + i * 128));
    }
    SCHED_BAR();
    mc = A.cvec[mpart * 160 + (tid < 160 ? tid : 0)];
#pragma unroll
    for (int i = 0; i < 10; ++i) mw[i] = *((const v4u*)(A.wmod + (size_t)(mpart * 160 + i * 16 + (tid >> 4)) * (6 * D) + mcol + (tid & 15) * 8));
    SCHED_BAR();
    {
      const int zc = (u * 4 + wave) * 3 + lane - 32;
      st128(rs, lane < 32 ? (unsigned)CTL_ST3 + ((u * 4 + wave) * 32 + lane) * 16 : (lane < 35 && zc < 1280 ? (unsigned)zc * 256 : 0x7ffffff0u), v4u{0u, 0u, 0u, 0u});
    }
    LAS char* img = lds + 8192;
#pragma unroll
    for (int i = 0; i < 5; ++i) *(LAS v4u*)(img + (wave * 5 + i) * 1024 + lane * 16) = v[i];
    lds_post(lds0, 7, lane); gs += 4;
    float s0 = 0.f, s1 = 0.f, q0 = 0.f, q1 = 0.f;
#pragma unroll
    for (int i = 0; i < 5; ++i) {
      s0 = dot2bf(v[i][0], 0x3f803f80u, s0); s1 = dot2bf(v[i][1], 0x3f803f80u, s1); s0 = dot2bf(v[i][2], 0x3f803f80u, s0); s1 = dot2bf(v[i][3], 0x3f803f80u, s1);
      q0 = dot2bf(v[i][0], v[i][0], q0); q1 = dot2bf(v[i][1], v[i][1], q1); q0 = dot2bf(v[i][2], v[i][2], q0); q1 = dot2bf(v[i][3], v[i][3], q1);
    }
    SCHED_BAR();
    lds_wait(lds0, 7, gs);
    const int rr = (lane & 15) >> 2, p = lane & 3, f = (rr | ((q & 1) << 2)) << 1;
    v2u lo[5], hi[5];
#pragma unroll
    for (int j = 0; j < 5; ++j) {
      const int a0 = (2 * (q & 1) * 5 + j) * 1024 + rr * 256 + ((((2 * wave + (q >> 1)) * 2 + (p >> 1)) ^ f) << 4) + (p & 1) * 8;
      lo[j] = trread(img + a0); hi[j] = trread(img + a0 + 5120);
    }
    float sum = s0 + s1, sq = q0 + q1;
    sum += dppf<0xb1>(sum); sq += dppf<0xb1>(sq); sum += dppf<0x4e>(sum); sq += dppf<0x4e>(sq);
    sum += dppf<0x141>(sum); sq += dppf<0x141>(sq); sum += dppf<0x140>(sum); sq += dppf<0x140>(sq);
#pragma unroll
    for (int j = 0; j < 5; ++j)
      st128(rs, (unsigned)WS_XT + ((u * 2 + (q & 1)) * D + (8 * j + 2 * wave + (q >> 1)) * 16 + (lane & 15)) * 16, v4u{lo[j].x, lo[j].y, hi[j].x, hi[j].y});
    const float mean = sum * (1.0f / D);
    const int t = u * 16 + r;
    st32(rs, s == 0 ? (unsigned)WS_ST1 + ((((t >> 2) & 1) * 256 + (t >> 3)) * 4 + (t & 3)) * 4 : 0x7ffffff0u, pkbf(mean, ln_std(__builtin_fmaxf(sq * (1.0f / D) - mean * mean, 0.f))));
    SCHED_BAR();
    {
      const float c = bf2f(mc);
      ((LAS float*)lds)[tid] = bfr(c * __builtin_amdgcn_rcpf(1.0f + __builtin_amdgcn_exp2f(-1.4426950408889634f * c)));
    }
    lds_post(lds0, 7, lane); gs += 4;
    asm volatile("s_waitcnt vmcnt(0)\n\tv_readfirstlane_b32 %0, %1\n\ts_nop 3" : "=s"(tag) : "v"(epoch) : "memory");
    tag += 1u;
    if (wave_arrive(lds0, 9, 4, lane) && lane < F0_REP)
      __hip_atomic_store((gu64*)(ws + CTL_F0 + lane * 1024 + u * 8), ((unsigned long long)~tag << 32) | tag, RLX_AGENT);
    SCHED_BAR();
    {
      const int r8 = tid >> 3, s8 = tid & 7;
      const u16* bp = A.win + (size_t)(160 * (u & 3) + r8) * F + 64 * ((u < 108 ? u : 0) >> 2) + s8 * 8;
#pragma unroll
      for (int i = 0; i < 5; ++i) wb[i] = *((const v4u*)(bp + (size_t)i * 32 * F));
      const int slab = u < 108 ? u >> 2 : 0;
      const int half = (r8 >> 2) & 1, jj = slab < 24 ? slab / 3 : 2 * (slab - 24) + half;
      const int sub = (jj < 6 ? 7 * jj : 42 + 6 * (jj - 6)) + (slab < 24 ? 2 * (slab - 3 * jj) + half : 6);
      const u16* src = A.wout + (size_t)(32 * (sub >> 1) + 8 * (r8 >> 3) + 4 * (sub & 1) + (r8 & 3)) * D + (u & 3) * 160;
#pragma unroll
      for (int i = 0; i < 3; ++i) ov[i] = *((const v4u*)(src + (i < 2 || s8 < 4 ? s8 + 8 * i : 0) * 8));
    }
    SCHED_BAR();
  }
  {
    LAS float* msc = (LAS float*)lds;
    lds_wait(lds0, 7, gs);
    float sv[10];
#pragma unroll
    for (int i = 0; i < 10; ++i) sv[i] = msc[(tid >> 4) + 16 * i];
    float acc[8] = {};
#pragma unroll
    for (int i = 0; i < 10; ++i)
#pragma unroll
      for (int j = 0; j < 4; ++j) { acc[2 * j] += sv[i] * bflo(mw[i][j]); acc[2 * j + 1] += sv[i] * bfhi(mw[i][j]); }
    treduce_xor<4>(acc, (lane & 32) != 0, 32);
    treduce_xor<2>(acc, (lane & 16) != 0, 16);
    LAS float* red = msc + 256;
    *(LAS f32x2*)(red + wave * 128 + (lane & 15) * 8 + (lane >> 4) * 2) = f32x2{acc[0], acc[1]};
    group_sync(lds0, 7, gs += 4, lane);
    if (tid < 128 && u < 120) put_granule(ws + WS_MOD + (size_t)(mpart * 3840 + mcol + tid) * 8, f2u((red[tid] + red[128 + tid]) + (red[256 + tid] + red[384 + tid])), tag);
  }
  __builtin_amdgcn_s_setprio(2);
  if (u < 108) {
    const int cbk = u >> 2, part = u & 3;
    const int c0 = 64 * cbk, r0 = 160 * part, seg = tid & 7, rg = tid >> 3;
    constexpr int OP = 400, WPITCH = 144;
    LAS char* oimg = lds + 32768;
    LAS char* img = lds + 4096;
#pragma unroll
    for (int i = 0; i < 5; ++i) *(LAS v4u*)(img + (rg + 32 * i) * WPITCH + seg * 16) = wb[i];
#pragma unroll
    for (int i = 0; i < 3; ++i) *(LAS v4u*)(oimg + rg * OP + (seg + 8 * i) * 16) = ov[i];
    group_sync(lds0, 7, gs += 4, lane);
    const int q = lane >> 4, rr = (lane & 15) >> 2, p = lane & 3;
    v4u wf[5];
    {
      const int colb = (32 * (wave >> 1) + 8 * p + 4 * (wave & 1)) * 2;
#pragma unroll
      for (int i = 0; i < 5; ++i) {
        const int a0 = (32 * i + 8 * q + rr) * WPITCH + colb;
        const v2u lo = trread(img + a0), hi = trread(img + a0 + 4 * WPITCH);
        wf[i] = v4u{lo.x, lo.y, hi.x, hi.y};
      }
      const int a0 = (8 * q + rr) * OP + p * 8 + wave * 32;
#pragma unroll
      for (int j = 0; j < 3; ++j) {
        const v2u lo = trread(oimg + a0 + j * 128), hi = trread(oimg + a0 + 4 * OP + j * 128);
        st128(rs, wave + 4 * j < 10 ? (unsigned)WS_WOUT + (((part * 10 + wave + 4 * j) * 27 + cbk) * 64 + lane) * 16 : 0x7ffffff0u, v4u{lo.x, lo.y, hi.x, hi.y});
      }
    }
    LAS float* tc = (LAS float*)(lds + 28672);
    LAS u16* ts = (LAS u16*)(lds + 29696);
    if (wave < 3) {
      unsigned mv[8];
      const int rw = r0 + (tid < 160 ? tid : 159);
      get_granules<8>(mv, tag, [&](int k) { return ws + WS_MOD + (size_t)((k & 3) * 3840 + (3 + (k >> 2)) * D + rw) * 8; });
      ts[tid] = (u16)pkbf((u2f(mv[0]) + u2f(mv[1])) + (u2f(mv[2]) + u2f(mv[3])), 0.f);
      tc[tid] = bfr(1.0f + bfr((u2f(mv[4]) + u2f(mv[5])) + (u2f(mv[6]) + u2f(mv[7]))));
    }
    group_sync(lds0, 7, gs += 4, lane);
    f32x4 u1 = {0.f, 0.f, 0.f, 0.f}, u2 = {0.f, 0.f, 0.f, 0.f};
    const v4u ones = {0x3f803f80u, 0x3f803f80u, 0x3f803f80u, 0x3f803f80u};
#pragma unroll
    for (int i = 0; i < 5; ++i) {
      const f32x4 ca = *(LAS const f32x4*)(tc + 32 * i + 8 * q), cb = *(LAS const f32x4*)(tc + 32 * i + 8 * q + 4);
      const v4u w = wf[i];
      const v4u o = {pkbf(bflo(w.x) * ca[0], bfhi(w.x) * ca[1]), pkbf(bflo(w.y) * ca[2], bfhi(w.y) * ca[3]),
                     pkbf(bflo(w.z) * cb[0], bfhi(w.z) * cb[1]), pkbf(bflo(w.w) * cb[2], bfhi(w.w) * cb[3])};
      st128(rs, (unsigned)WS_WIN + (((((part * 5 + i) >> 1) * 108 + 4 * cbk + wave) * 2 + ((part * 5 + i) & 1)) * 64 + lane) * 16, o);
      u1 = mfma16(ones, o, u1);
      u2 = mfma16(*(LAS const v4u*)(ts + 32 * i + 8 * q), w, u2);
    }
    st32(rs, lane < 32 ? (unsigned)WS_U + ((part * 2 + q) * F + c0 + wave * 16 + (lane & 15)) * 4 : 0x7ffffff0u, f2u(q ? u2[0] : u1[0]));
  }
  lds_wait(lds0, 8, 1);
  if (wave_arrive(lds0, 2, 4, lane)) signal<E1_REP, 8>(ws + CTL_E1B, u & 7, lane);
}

__device__ __forceinline__ int g1_aoff(int row, int G, int ab, int p) {
  const int f = ((row >> 1) & 1) | (((row >> 3) & 1) << 1);
  return row * 128 + (((4 * G + 2 * ab + (p >> 1)) ^ (f << 1)) << 4) + (p & 1) * 8;
}

template <int NB>
__device__ __forceinline__ void loader_path(LAS char* lds, char* ws, __amdgpu_buffer_rsrc_t rs, const u16* __restrict__ wsp, int wg, int mt, int j, int sub0, int lw, int lane) {
  const unsigned lbase = (unsigned)(size_t)lds, vo = lane * 16;
  const bool CNT7 = j < 6;
  {
    unsigned avo[2];
#pragma unroll
    for (int h = 0; h < 2; ++h) {
      const int row = lane >> 3, pos = lane & 7, f = ((row >> 1) & 1) | (h << 1);
      avo[h] = (row + 8 * h) * (L * 2) + ((pos ^ (f << 1)) << 4) - h * 1024;
    }
    const char* asb = ws + WS_X2T + (size_t)(16 * lw) * (L * 2) + mt * 128;
    const int p0 = (NB == 4 || CNT7) ? (lw < 2 ? 4 * lw : 2 + 3 * lw) : 3 * lw;
    const char* bsb = ws + WS_WIN + (size_t)(sub0 * 2 + p0) * 1024;
    const unsigned bb = lbase + G1_BOFF;
    auto issueB = [&](int s, unsigned slot) {
      gldsL<NB>(bsb + (s < 10 ? 108 * 1024 + s * 216 * 1024 : (s - 10) * 216 * 1024), vo, slot + p0 * 1024);
    };
    auto issueA = [&](int s0, int s1) {
#pragma unroll 1
      for (int s = s0; s < s1; ++s) gldsN<2, true>(asb + (size_t)(s * 64) * (L * 2), avo[0], avo[1], 0, 0, lbase + s * 8192 + lw * 2048);
    };
    {
      const unsigned vb = (unsigned)WS_V + (mt * 4 + lw) * 27 * 1024 + lane * 16;
#pragma unroll
      for (int k = 0; k < 3; ++k) st128(rs, vb + (3 * j + k) * 1024, v4u{~0u, ~0u, ~0u, ~0u});
      if (j < 6) __builtin_amdgcn_raw_buffer_store_b64(v2u{~0u, ~0u}, rs, vb + (24 + (j >> 1)) * 1024 + (j & 1) * 8, 0, 16);
    }
    waitv<0>();
    group_sync(lds, 5, 24, lane);
    if (lw == 0) { wait_count<8>(ws + CTL_E1B, wg & (E1_REP - 1), 128, lane); lds_post(lds, 10, lane); }
    if (lw == 3) { wait_count<8>(ws + CTL_E1, wg & (E1_REP - 1), 256, lane); lds_post(lds, 11, lane); }
    lds_wait(lds, 10, 1);
#pragma unroll
    for (int s = 0; s < 4; ++s) issueB(s, bb + (s + 1) * G1_BST);
    lds_wait(lds, 11, 1);
    lds_wait(lds, 1, 4);
    issueA(0, 4);
    LBAR();
    issueB(4, bb);
    waitv<6 + NB>(); LBAR();
    waitv<4 + NB>(); LBAR(); issueB(5, bb + G1_BST);
    waitv<2 + 2 * NB>(); LBAR(); issueA(4, 8); issueB(6, bb + 2 * G1_BST);
    waitv<8 + 3 * NB>(); LBAR(); issueB(7, bb + 3 * G1_BST);
    waitv<6 + 2 * NB>(); LBAR(); issueB(8, bb + 4 * G1_BST);
    waitv<4 + 3 * NB>(); LBAR(); issueA(8, 10); issueB(9, bb);
    waitv<4 + 3 * NB>(); LBAR(); issueB(10, bb + G1_BST);
    waitv<4 + 3 * NB>(); LBAR(); issueB(11, bb + 2 * G1_BST);
    waitv<2 + 3 * NB>(); LBAR(); issueB(12, bb + 3 * G1_BST);
    int nxt = 4 * G1_BST;
#pragma unroll 1
    for (int i = 9; i <= 15; ++i) {
      waitv<NB * 3>(); LBAR();
      issueB(i + 4, bb + nxt);
      nxt += G1_BST; if (nxt == G1_NSLOT * G1_BST) nxt = 0;
    }
    waitv<NB * 3>(); LBAR();
    waitv<NB * 2>(); LBAR();
    waitv<NB * 1>(); LBAR();
    waitv<0>(); LBAR();
    LBAR();
  }
  const char* osb = ws + WS_WOUT + (size_t)j * 135 * 1024;
  if (lw >= 2) {
    unsigned wvo[4];
#pragma unroll
    for (int h = 0; h < 4; ++h) {
      const int i = 8 * (h & 1) + (lane >> 3);
      wvo[h] = (8 * (i >> 2) + (i & 3) + 4 * (h >> 1)) * (L * 2) + (((lane & 7) ^ (i >> 1)) << 4) - h * 1024;
    }
    const char* wsb = (const char*)wsp + (size_t)(mt * 64 + 32 * (lw & 1)) * (L * 2);
    auto issueW = [&](int s, unsigned slot) {
      gldsN<4, false>(wsb + s * 128, wvo[0], wvo[1], wvo[2], wvo[3], slot + (lw & 1) * 4096);
    };
#pragma unroll 1
    for (int s = 0; s < G2_NW; ++s) issueW(s, lbase + s * 8192);
    LBAR();
    waitv<32>(); LBAR();
    int wn = 0;
#pragma unroll 1
    for (int i = 1; i <= 23; ++i) { waitv<28>(); LBAR(); issueW(i + 8, lbase + wn); wn += 8192; if (wn == G2_NW * 8192) wn = 0; }
#pragma unroll 1
    for (int i = 24; i <= 32; ++i) {
      waitv<28>(); LBAR();
      gldsL<4>(osb + wn + (lw & 1) * 4096, vo, lbase + wn + (lw & 1) * 4096);
      wn += 8192; if (wn == G2_NW * 8192) wn = 0;
    }
  } else {
    constexpr int NZ = NB == 4 ? 7 : 6;
    const char* zsb = ws + WS_Z2 + (size_t)(sub0 * 2 + lw * NZ) * 1024;
    auto issueZ = [&](int s, unsigned slot) {
      gldsL<4>(zsb + s * 108 * 1024, vo, slot + lw * NZ * 1024);
      gldsL<NZ - 4>(zsb + s * 108 * 1024 + 4096, vo, slot + lw * NZ * 1024 + 4096);
    };
    const unsigned zb = lbase + G2_ZOFF;
    LBAR();
#pragma unroll 1
    for (int s = 0; s < G2_NZ; ++s) issueZ(s, zb + s * G2_ZST);
    waitv<NZ * 5>(); LBAR();
    int zn = 0;
#pragma unroll 1
    for (int i = 1; i <= 26; ++i) { waitv<NZ * 4>(); LBAR(); issueZ(i + 5, zb + zn); zn += G2_ZST; if (zn == G2_NZ * G2_ZST) zn = 0; }
    auto issueO = [&](int slot, int n) {
      const int o = G2_ZOFF + slot * G2_ZST + lw * n * 1024;
      if (n == 7) { gldsL<4>(osb + o, vo, lbase + o); gldsL<3>(osb + o + 4096, vo, lbase + o + 4096); }
      else gldsL<3>(osb + o, vo, lbase + o);
    };
    waitv<NZ * 4>(); LBAR(); issueO(2, 7);
    waitv<NZ * 3 + 7>(); LBAR(); issueO(3, 7);
    waitv<NZ * 2 + 14>(); LBAR(); issueO(4, 3);
    waitv<NZ + 17>(); LBAR(); if (lw == 0) gldsL<1>(osb + 134 * 1024, vo, lbase + 134 * 1024);
    waitv<17>(); LBAR(); issueO(0, 7);
    LBAR(); issueO(1, 7);
  }
  waitv<0>();
  LBAR();
}

template <int NSUB, bool STATS>
__device__ __forceinline__ void consumer_g12(LAS char* lds, char* ws, __amdgpu_buffer_rsrc_t rs, const u16* __restrict__ bsp, const u16* __restrict__ wsp, int wg, int mt, int j, int sub0, int cnt,
                                             int wave, int tid, int lane, v2u (&x2v)[3]) {
  constexpr int NS1 = NSUB > 0 ? NSUB : 1;
  const int G = wave >> 2, ng = G ? 3 - (wave & 3) : (wave & 3);
  const int q = lane >> 4, i16 = lane & 15, rr = i16 >> 2, p = lane & 3, n0 = 2 * ng;
  float bias[2];
#pragma unroll
  for (int m = 0; m < 2; ++m) bias[m] = bf2f(bsp[mt * 64 + 32 * G + 8 * (i16 >> 2) + 4 * m + (i16 & 3)]);
  CBAR();
  v2u z1v[NS1][2];
  {
    const int aoff0 = g1_aoff(8 * q + rr, G, 0, p), aoff1 = g1_aoff(8 * q + rr, G, 1, p);
    const int boff = G1_BOFF + n0 * 2048 + lane * 16;
    struct Frags { v4u a[2][2]; v4u b[2][NS1]; };
    f32x4 acc2[NS1][2], acc1[NS1][2], asum[2], asq[2];
    int acur = 0, bcur = G1_BST;
    auto rd = [&](Frags& f) {
#pragma unroll
      for (int kk = 0; kk < 2; ++kk) {
        const v2u l0 = trread(lds + acur + aoff0 + kk * 4096), h0 = trread(lds + acur + aoff0 + kk * 4096 + 512);
        const v2u l1 = trread(lds + acur + aoff1 + kk * 4096), h1 = trread(lds + acur + aoff1 + kk * 4096 + 512);
        f.a[kk][0] = v4u{l0.x, l0.y, h0.x, h0.y}; f.a[kk][1] = v4u{l1.x, l1.y, h1.x, h1.y};
#pragma unroll
        for (int n = 0; n < NSUB; ++n) f.b[kk][n] = *(LAS const v4u*)(lds + bcur + boff + n * 2048 + kk * 1024);
      }
      acur += 8192; if (acur == 81920) acur = 0;
      bcur += G1_BST; if (bcur == G1_NSLOT * G1_BST) bcur = 0;
    };
    auto pin1 = [&]() {
#pragma unroll
      for (int n = 0; n < NSUB; ++n) asm volatile("" : "+v"(acc1[n][0]), "+v"(acc1[n][1]));
    };
#pragma unroll
    for (int ab = 0; ab < 2; ++ab) {
      asum[ab] = f32x4{0.f, 0.f, 0.f, 0.f}; asq[ab] = f32x4{0.f, 0.f, 0.f, 0.f};
#pragma unroll
      for (int n = 0; n < NS1; ++n) { acc2[n][ab] = f32x4{0.f, 0.f, 0.f, 0.f}; acc1[n][ab] = f32x4{0.f, 0.f, 0.f, 0.f}; }
    }
    const v4u ones = {0x3f803f80u, 0x3f803f80u, 0x3f803f80u, 0x3f803f80u};
    auto mm2 = [&](const Frags& f) {
#pragma unroll
      for (int kk = 0; kk < 2; ++kk)
#pragma unroll
        for (int ab = 0; ab < 2; ++ab) {
#pragma unroll
          for (int n = 0; n < NSUB; ++n) acc2[n][ab] = mfma16(f.a[kk][ab], f.b[kk][n], acc2[n][ab]);
          if (STATS) { asum[ab] = mfma16(f.a[kk][ab], ones, asum[ab]); asq[ab] = mfma16(f.a[kk][ab], f.a[kk][ab], asq[ab]); }
        }
      INTERLEAVE(6, 2);
    };
    auto mm1 = [&](const Frags& f) {
#pragma unroll
      for (int kk = 0; kk < 2; ++kk)
#pragma unroll
        for (int ab = 0; ab < 2; ++ab)
#pragma unroll
          for (int n = 0; n < NSUB; ++n) acc1[n][ab] = mfma16(f.b[kk][n], f.a[kk][ab], acc1[n][ab]);
      if (NSUB == 2) INTERLEAVE(6, 2); else INTERLEAVE(4, 3);
    };
    Frags f0, f1;
    CBAR(); rd(f0);
    SCHED_BAR();
    float up[4];
    {
      const int c = tid & 127, which = (tid >> 7) & 1, half = tid >> 8;
#pragma unroll
      for (int k = 0; k < 4; ++k) up[k] = ld32f(rs, c < cnt * 16 ? (unsigned)WS_U + ((k * 2 + which) * F + (sub0 + (half ? 0 : 54)) * 16 + c) * 4 : 0x7ffffff0u);
    }
    unsigned pv = 0;
    SCHED_BAR();
    auto pin2 = [&]() {
#pragma unroll
      for (int n = 0; n < NSUB; ++n) asm volatile("" : "+v"(acc2[n][0]), "+v"(acc2[n][1]));
      asm volatile("" : "+v"(asum[0]), "+v"(asum[1]), "+v"(asq[0]), "+v"(asq[1]));
    };
    if (STATS) {
      char* cb = ws + CTL_E1 + (G + 1) * 65536;
      unsigned pc;
      poll_issue<8>(pc, cb, wg & (E1_REP - 1), lane);
      CBAR(); rd(f1); mm2(f0);
      if (G == 0) poll_finish<8>(pc, cb, wg & (E1_REP - 1), 256, lane);
      CBAR(); pin2(); rd(f0); mm2(f1);
      CBAR(); rd(f1); mm2(f0);
      poll_issue<8>(pc, cb, wg & (E1_REP - 1), lane);
      CBAR(); rd(f0); mm2(f1);
      if (G == 1) poll_finish<8>(pc, cb, wg & (E1_REP - 1), 128, lane);
      asm volatile("" :: "v"(pc));
      pin2();
    }
#pragma unroll 1
    for (int it = STATS ? 2 : 0; it < 5; ++it) {
      CBAR(); rd(f1); mm2(f0);
      CBAR(); rd(f0); mm2(f1);
    }
    ((LAS float*)(lds + G1_T_U))[tid] = (up[0] + up[1]) + (up[2] + up[3]);
    if (!STATS && ng < 2) pv = *(const unsigned*)((const char*)wsp + (size_t)(mt * 64 + (G * 2 + ng) * 16 + (lane >> 2)) * (L * 2) + j * 512 + (lane & 3) * 128);
    LAS f32x2* tab = (LAS f32x2*)(lds + G1_T_LN2);
    if (STATS) {
#pragma unroll
      for (int ab = 0; ab < 2; ++ab) {
        const float a0 = asum[ab][0], a1 = asum[ab][1], a2 = asum[ab][2], a3 = asum[ab][3];
        const float b0 = asq[ab][0], b1 = asq[ab][1], b2 = asq[ab][2], b3 = asq[ab][3];
        const float s1 = (p & 2) ? ((p & 1) ? a3 : a2) : ((p & 1) ? a1 : a0), s2 = (p & 2) ? ((p & 1) ? b3 : b2) : ((p & 1) ? b1 : b0);
        const float mean = s1 * (1.0f / D);
        float mb, rinv;
        ln_scalars(mean, __builtin_fmaxf(s2 * (1.0f / D) - mean * mean, 0.f), mb, rinv);
        if (rr == q) tab[(G * 2 + ab) * 16 + i16] = f32x2{rinv, rinv * mb};
      }
    }
    {
      const int nb = G ? 3 : 0;
#pragma unroll
      for (int n = 0; n < 3; ++n) {
        const int row = 80 * j + 16 * (nb + n < 5 ? nb + n : 4) + 4 * q + rr;
        x2v[n] = trread(lds + g1_aoff(row, (wave >> 1) & 1, wave & 1, p));
      }
    }
    CBAR(); rd(f1); mm1(f0);
    float zq[NS1][8];
    {
      LAS const float* ut = (LAS const float*)(lds + G1_T_U);
      float v[16];
#pragma unroll
      for (int e = 0; e < 16; ++e) v[e] = 0.f;
      if (NSUB > 0) {
        f32x2 rv[8];
#pragma unroll
        for (int e = 0; e < 8; ++e) rv[e] = tab[(G * 2 + (e >> 2)) * 16 + 4 * q + (e & 3)];
#pragma unroll
        for (int n = 0; n < NSUB; ++n) {
          const float c1 = ut[(n0 + n) * 16 + i16], c2 = ut[128 + (n0 + n) * 16 + i16];
#pragma unroll
          for (int e = 0; e < 8; ++e) {
            const float z = gelu_f(__builtin_fmaf(rv[e].x, acc2[n][e >> 2][e & 3], __builtin_fmaf(-rv[e].y, c1, c2)));
            zq[n][e] = z;
            v[2 * e] = n ? v[2 * e] + z : z; v[2 * e + 1] = n ? __builtin_fmaf(z, z, v[2 * e + 1]) : z * z;
          }
        }
        treduce_dpp<8, 0x140>(v, (i16 & 8) != 0);
        treduce_dpp<4, 0x141>(v, (i16 & 4) != 0);
        treduce_dpp<2, 0x4e>(v, (i16 & 2) != 0);
        treduce_dpp<1, 0xb1>(v, (i16 & 1) != 0);
      }
      ((LAS float*)(lds + G1_T_P))[ng * 128 + G * 64 + lane] = v[0];
    }
    CBAR(); pin1();
    GAS unsigned long long* gr = (GAS unsigned long long*)(ws + CTL_ST3) + (size_t)mt * 1024 + G * 32;
    if (STATS) {
      LAS const float* pp = (LAS const float*)(lds + G1_T_P) + G * 64 + lane;
      const float sv = (pp[0] + pp[128]) + (pp[256] + pp[384]);
      __hip_atomic_store(gr + (j * 2 + (lane & 1)) * 64 + (lane >> 1), (1ull << 32) | f2u(sv), RLX_AGENT);
    }
    SCHED_BAR(); rd(f0); mm1(f1);
    CBAR(); pin1(); rd(f1); mm1(f0);
    CBAR(); pin1(); rd(f0); mm1(f1);
    unsigned long long gx[8];
    if (STATS) {
#pragma unroll
      for (int k = 0; k < 8; ++k) gx[k] = __hip_atomic_load(gr + (k * 2 + (lane >> 5)) * 64 + (lane & 31), RLX_AGENT);
    }
    CBAR(); pin1(); rd(f1); mm1(f0);
    if (STATS) {
      float sv;
      for (unsigned spin = 0;; ++spin) {
        bool ok = true;
        sv = 0.f;
#pragma unroll
        for (int k = 0; k < 8; ++k) { ok &= (gx[k] >> 32) == 1ull; sv += u2f((unsigned)gx[k]); }
        if (wall(ok) || spin > SPIN_CAP) break;
        __builtin_amdgcn_s_sleep(1);
#pragma unroll
        for (int k = 0; k < 8; ++k) gx[k] = __hip_atomic_load(gr + (k * 2 + (lane >> 5)) * 64 + (lane & 31), RLX_AGENT);
      }
      const float s2 = shx(sv, 32);
      const float mean = sv * (1.0f / FH);
      float mb, rinv;
      ln_scalars(mean, __builtin_fmaxf(s2 * (1.0f / FH) - mean * mean, 0.f), mb, rinv);
      if (lane < 32) ((LAS f32x2*)(lds + G1_T_Z))[32 * G + lane] = f32x2{-mb * rinv, rinv};
    }
    CBAR(); pin1();
    if (NSUB > 0) {
      f32x2 zs[8];
#pragma unroll
      for (int e = 0; e < 8; ++e) zs[e] = ((LAS const f32x2*)(lds + G1_T_Z))[32 * G + 8 * q + e];
#pragma unroll
      for (int n = 0; n < NSUB; ++n) {
        v4u o;
#pragma unroll
        for (int e = 0; e < 4; ++e)
          o[e] = pkbf(__builtin_fmaf(zq[n][2 * e], zs[2 * e].y, zs[2 * e].x), __builtin_fmaf(zq[n][2 * e + 1], zs[2 * e + 1].y, zs[2 * e + 1].x));
        st128(rs, (unsigned)WS_Z2 + (((mt * 54 + sub0 + n0 + n) * 2 + G) * 64 + lane) * 16, o);
      }
    }
    SCHED_BAR(); rd(f0); mm1(f1);
    CBAR(); pin1(); rd(f1); mm1(f0);
    CBAR(); pin1(); rd(f0); mm1(f1);
    if (wave_arrive(lds, 3, 8, lane)) signal<4, 4>(ws + CTL_E2 + j * 4096, mt & 3, lane);
    CBAR(); pin1(); rd(f1); mm1(f0);
    asm volatile("" :: "v"(pv));
    CBAR(); pin1();
    mm1(f1);
    {
      LAS const float* ut = (LAS const float*)(lds + G1_T_U) + 256;
      f32x2 rv[2];
#pragma unroll
      for (int ab = 0; ab < 2; ++ab) rv[ab] = tab[(G * 2 + ab) * 16 + i16];
#pragma unroll
      for (int n = 0; n < NSUB; ++n) {
        const f32x4 u1 = *(LAS const f32x4*)(ut + (n0 + n) * 16 + 4 * q), u2 = *(LAS const f32x4*)(ut + 128 + (n0 + n) * 16 + 4 * q);
#pragma unroll
        for (int ab = 0; ab < 2; ++ab) {
          float z[4];
#pragma unroll
          for (int e = 0; e < 4; ++e) z[e] = gelu_f(__builtin_fmaf(rv[ab].x, acc1[n][ab][e], __builtin_fmaf(-rv[ab].y, u1[e], u2[e])));
          z1v[n][ab] = v2u{pkbf(z[0], z[1]), pkbf(z[2], z[3])};
          asm volatile("" : "+v"(z1v[n][ab]));
        }
      }
    }
  }
  if (wave == 1 || wave == 2) {
    const int c = tid - 64;
    if (c < 80) {
      const unsigned o = (unsigned)WS_MOD + (5 * D + 80 * j + c) * 8;
      ((LAS float*)(lds + G3_GB))[c] = bfr((ld32f(rs, o) + ld32f(rs, o + 30720)) + (ld32f(rs, o + 61440) + ld32f(rs, o + 92160)));
    }
  }
  if (wave == 3) wait_count<4>(ws + CTL_E2 + j * 4096, (mt >> 2) & 3, 32, lane);
  CBAR();
  {
    f32x4 acc[NS1][2];
#pragma unroll
    for (int n = 0; n < NS1; ++n) { acc[n][0] = f32x4{0.f, 0.f, 0.f, 0.f}; acc[n][1] = f32x4{0.f, 0.f, 0.f, 0.f}; }
    int woff[2];
#pragma unroll
    for (int kk = 0; kk < 2; ++kk) woff[kk] = G * 4096 + i16 * 128 + (((4 * kk + q) ^ (i16 >> 1)) << 4);
    const int zoff = G2_ZOFF + n0 * 2048 + lane * 16;
    struct Frags { v4u w[2][2]; v4u z[2][NS1]; };
    int wc = 0, zc = 0;
    auto rd = [&](Frags& f) {
      if (NSUB > 0) {
#pragma unroll
        for (int kk = 0; kk < 2; ++kk) {
#pragma unroll
          for (int m = 0; m < 2; ++m) f.w[kk][m] = *(LAS const v4u*)(lds + wc + woff[kk] + m * 2048);
#pragma unroll
          for (int n = 0; n < NSUB; ++n) f.z[kk][n] = *(LAS const v4u*)(lds + zc + zoff + n * 2048 + kk * 1024);
        }
        wc += 8192; if (wc == G2_NW * 8192) wc = 0;
        zc += G2_ZST; if (zc == G2_NZ * G2_ZST) zc = 0;
      }
    };
    auto mm = [&](const Frags& f) {
#pragma unroll
      for (int kk = 0; kk < 2; ++kk)
#pragma unroll
        for (int n = 0; n < NSUB; ++n)
#pragma unroll
          for (int m = 0; m < 2; ++m) acc[n][m] = mfma16(f.z[kk][n], f.w[kk][m], acc[n][m]);
      INTERLEAVE(4, 2);
    };
    Frags f0, f1;
    CBAR(); rd(f0);
#pragma unroll 1
    for (int it = 0; it < 15; ++it) {
      CBAR(); rd(f1); mm(f0);
      CBAR(); rd(f0); mm(f1);
    }
    CBAR(); rd(f1); mm(f0);
    mm(f1);
#pragma unroll
    for (int m = 0; m < 2; ++m) {
      v2u o[NS1];
#pragma unroll
      for (int n = 0; n < NSUB; ++n) {
        float g[4];
#pragma unroll
        for (int e = 0; e < 4; ++e) g[e] = acc[n][m][e] + bias[m];
        const v2u zz = z1v[n][m];
        o[n] = v2u{pkbf(bflo(zz.x) * g[0], bfhi(zz.x) * g[1]), pkbf(bflo(zz.y) * g[2], bfhi(zz.y) * g[3])};
      }
      const unsigned vb = (unsigned)WS_V + (mt * 4 + G * 2 + m) * 27 * 1024 + lane * 16;
      if (NSUB == 2) st128(rs, vb + (3 * j + ng) * 1024, v4u{o[0].x, o[0].y, o[NS1 - 1].x, o[NS1 - 1].y});
      if (NSUB == 1) __builtin_amdgcn_raw_buffer_store_b64(o[0], rs, vb + (24 + (j >> 1)) * 1024 + (j & 1) * 8, 0, 16);
    }
    CBAR();
  }
}

template <int KH>
__device__ __forceinline__ void consumer_g3(LAS char* lds, char* ws, __amdgpu_buffer_rsrc_t rs, u16* __restrict__ out, int mt, int j, int m, int lane, const v2u (&x2v)[3]) {
  constexpr int K0 = KH ? 14 : 0, NK = KH ? 13 : 14, NF = KH ? 2 : 3, N0 = KH ? 3 : 0;
  const int q = lane >> 4, i16 = lane & 15;
  v4u vf[NK];
  __builtin_amdgcn_s_sleep(24);
  CBAR();
  const unsigned vo = (unsigned)WS_V + ((mt * 4 + m) * 27 + K0) * 1024 + lane * 16;
  constexpr int VW = V_WIN;
#pragma unroll
  for (int k = 0; k < VW; ++k) { vf[k] = ld128(rs, vo + k * 1024); SCHED_BAR(); }
  f32x4 acc[5];
#pragma unroll
  for (int n = 0; n < 5; ++n) acc[n] = f32x4{0.f, 0.f, 0.f, 0.f};
  LAS const char* wp = lds + K0 * 1024 + lane * 16;
  LAS const char* wp2 = wp + 54 * 1024;
  LAS const char* wp4 = wp + 108 * 1024;
  v4u wf[3][5];
  auto rdw = [&](int k, v4u (&w)[5]) {
    w[0] = *(LAS const v4u*)(wp + k * 1024); w[1] = *(LAS const v4u*)(wp + (27 + k) * 1024);
    w[2] = *(LAS const v4u*)(wp2 + k * 1024); w[3] = *(LAS const v4u*)(wp2 + (27 + k) * 1024);
    w[4] = *(LAS const v4u*)(wp4 + k * 1024);
  };
  rdw(0, wf[0]); rdw(1, wf[1]);
#pragma unroll
  for (int k = 0; k < NK; ++k) {
    if (k + VW < NK) vf[k + VW] = ld128(rs, vo + (k + VW) * 1024);
    if (k + 2 < NK) rdw(k + 2, wf[(k + 2) % 3]);
    SCHED_BAR();
    if (__builtin_expect(!wall(__builtin_elementwise_max(vf[k].x, vf[k].z) != ~0u), 0)) {
      unsigned spin = 0;
      do { asm volatile("" ::: "memory"); vf[k] = ld128(rs, vo + k * 1024); }
      while (!wall(__builtin_elementwise_max(vf[k].x, vf[k].z) != ~0u) && ++spin < SPIN_CAP);
    }
#pragma unroll
    for (int n = 0; n < 5; ++n) acc[n] = mfma16(wf[k % 3][n], vf[k], acc[n]);
    asm volatile("" : "+v"(acc[0]), "+v"(acc[1]), "+v"(acc[2]), "+v"(acc[3]), "+v"(acc[4]));
    SCHED_BAR();
  }
  f32x4 gb[NF];
#pragma unroll
  for (int n = 0; n < NF; ++n) gb[n] = *(LAS const f32x4*)(lds + G3_GB + (16 * (N0 + n) + 4 * q) * 4);
  LAS char* red = lds + G3_RED + m * 5120 + lane * 16;
#pragma unroll
  for (int n = 0; n < 5; ++n) if ((n >= 3) != (KH != 0)) *(LAS f32x4*)(red + n * 1024) = acc[n];
  CBAR();
  const int tl = 32 * (m >> 1) + 8 * (i16 >> 2) + 4 * (m & 1) + (i16 & 3);
#pragma unroll
  for (int n = 0; n < NF; ++n) {
    const f32x4 o = *(LAS const f32x4*)(red + (N0 + n) * 1024);
    const f32x4 a = KH ? o + acc[N0 + n] : acc[N0 + n] + o;
    const float o0 = __builtin_fmaf(gb[n][0], a[0], bflo(x2v[n].x)), o1 = __builtin_fmaf(gb[n][1], a[1], bfhi(x2v[n].x));
    const float o2 = __builtin_fmaf(gb[n][2], a[2], bflo(x2v[n].y)), o3 = __builtin_fmaf(gb[n][3], a[3], bfhi(x2v[n].y));
    *(LAS v2u*)(lds + tl * 176 + 32 * (N0 + n) + 8 * q) = v2u{pkbf(o0, o1), pkbf(o2, o3)};
  }
  CBAR();
  {
    const int w8 = KH * 4 + m;
#pragma unroll
    for (int i = 0; i < 2; ++i) {
      const int c = lane + 64 * i, r = w8 * 8 + c / 10, pc = c % 10;
      if (c < 80) *(v4u*)((char*)out + ((size_t)(mt * 64 + r) * D + 80 * j) * 2 + pc * 16) = *(LAS const v4u*)(lds + r * 176 + pc * 16);
    }
  }
}

__global__ __launch_bounds__(768, 3) void k_all(Args A) {
  extern __shared__ __attribute__((aligned(16))) char smem[];
  unsigned long long pc;
  asm volatile("s_getpc_b64 %0" : "=s"(pc));
  LAS char* lds = (LAS char*)smem;
  char* ws = A.ws;
  const int tid = __builtin_amdgcn_workitem_id_x(), lane = tid & 63, wave = __builtin_amdgcn_readfirstlane(tid >> 6), wg = __builtin_amdgcn_workgroup_id_x();
  const __amdgpu_buffer_rsrc_t rs = __builtin_amdgcn_make_buffer_rsrc((void*)ws, 0, (int)WS_END, 0x00020000);
  asm volatile("" :: "s"(A.x), "s"(A.Are), "s"(A.Aim), "s"(A.Bre), "s"(A.Bim), "s"(A.Cre), "s"(A.Cim), "s"(A.logdt), "s"(A.dskip), "s"(A.win), "s"(A.wsp),
               "s"(A.bsp), "s"(A.wout), "s"(A.cvec), "s"(A.wmod), "s"(A.ws), "s"(A.out));
  const int grp = wave >> 2, bt = grp == 2 ? 0 : grp + 1, gw = wave & 3, gt = tid & 255;
  const bool isaux = bt == 2 && wg >= 128;
  const int u = wg - 128, d = 256 * bt + (wg & 7) * (bt < 2 ? 32 : 16) + (wg >> 3);
  unsigned epoch;
  asm volatile("global_load_dword %0, %1, %2" : "=v"(epoch) : "v"((unsigned)CTL_EPOCH), "s"(ws) : "memory");
  unsigned pre[7] = {};
  if (__builtin_expect(!isaux, 1)) {
    const unsigned o = (d * NS + (gw & 1) * 64 + lane) * 2;
#define LDU16(dst, base, voff) asm volatile("global_load_ushort %0, %1, %2" : "=v"(dst) : "v"(voff), "s"(base) : "memory")
    LDU16(pre[0], A.Are, o); LDU16(pre[1], A.Aim, o); LDU16(pre[2], A.logdt, d * 2); LDU16(pre[3], A.Bre, o);
    LDU16(pre[4], A.Bim, o); LDU16(pre[5], A.Cre, o); LDU16(pre[6], A.Cim, o);
  }
  if (tid < 16) ((LAS unsigned*)(lds + M_CTL))[tid] = 0u;
  unsigned tag = 0;
  CBAR();
  {
    LAS char* gl = lds + (grp == 2 ? G1_TAB - C_GRP : grp * C_GRP);
    if (bt == 0) __builtin_amdgcn_s_setprio(2);
    if (bt == 1) __builtin_amdgcn_s_setprio(1);
    if (isaux) { __builtin_amdgcn_s_setprio(3); aux_group(gl, lds, A, rs, epoch, u, gt, gw, lane); }
    else conv_group(gl, lds, bt, A, rs, epoch, tag, pre, pc, wg, d, gt, gw, lane);
    __builtin_amdgcn_s_setprio(0);
  }
  const int xcd = wg & 7, idx = wg >> 3;
  const int mt = (xcd >> 1) * 8 + (idx & 7), j = (xcd & 1) * 4 + (idx >> 3);
  const int cnt = j < 6 ? 7 : 6, sub0 = j < 6 ? 7 * j : 42 + 6 * (j - 6);
  if (wave >= 8) {
    const int lw = wave - 8;
    if (lw + 12 < 2 * cnt) loader_path<4>(lds, ws, rs, A.wsp, wg, mt, j, sub0, lw, lane);
    else loader_path<3>(lds, ws, rs, A.wsp, wg, mt, j, sub0, lw, lane);
    if (wg == 0 && tid == 512) *(volatile unsigned*)(ws + CTL_EPOCH) = tag;
    return;
  }
  v2u x2v[3];
  const int ng = wave >> 2 ? 3 - (wave & 3) : (wave & 3);
  if (ng < 3) consumer_g12<2, false>(lds, ws, rs, A.bsp, A.wsp, wg, mt, j, sub0, cnt, wave, tid, lane, x2v);
  else if (cnt == 7) consumer_g12<1, true>(lds, ws, rs, A.bsp, A.wsp, wg, mt, j, sub0, cnt, wave, tid, lane, x2v);
  else consumer_g12<0, true>(lds, ws, rs, A.bsp, A.wsp, wg, mt, j, sub0, cnt, wave, tid, lane, x2v);
  if (wave < 4) consumer_g3<0>(lds, ws, rs, A.out, mt, j, wave & 3, lane, x2v);
  else consumer_g3<1>(lds, ws, rs, A.out, mt, j, wave & 3, lane, x2v);
}

#if defined(__HIP_DEVICE_COMPILE__)
#pragma clang attribute pop
#endif

extern "C" void kernel_launch(void* const* d_in, const int* in_sizes, int n_in, void* d_out, int out_size, void* d_ws, size_t ws_size,
                              hipStream_t stream) {
  Args A;
  A.x = (const u16*)d_in[0]; A.Are = (const u16*)d_in[1]; A.Aim = (const u16*)d_in[2]; A.Bre = (const u16*)d_in[3]; A.Bim = (const u16*)d_in[4];
  A.Cre = (const u16*)d_in[5]; A.Cim = (const u16*)d_in[6]; A.logdt = (const u16*)d_in[7]; A.dskip = (const u16*)d_in[8];
  A.win = (const u16*)d_in[10]; A.wsp = (const u16*)d_in[11]; A.bsp = (const u16*)d_in[12]; A.wout = (const u16*)d_in[13];
  A.cvec = (const u16*)d_in[14]; A.wmod = (const u16*)d_in[15];
  A.ws = (char*)d_ws; A.out = (u16*)d_out;
  (void)hipFuncSetAttribute((const void*)k_all, hipFuncAttributeMaxDynamicSharedMemorySize, M_LDS);
  k_all<<<256, 768, M_LDS, stream>>>(A);
}
```
